```python
import torch

def _softmax(x, axis=-1):
    return torch.softmax(x, dim=axis)
def _gelu(x):
    return torch.nn.functional.gelu(x, approximate="tanh")
def _silu(x):
    return torch.nn.functional.silu(x)
def _sigmoid(x):
    return torch.sigmoid(x)
def _layernorm(x, axis=-1, eps=1e-5):
    m = x.mean(dim=axis, keepdim=True)
    v = x.var(dim=axis, keepdim=True, unbiased=False)
    return (x - m) / torch.sqrt(v + eps)
def _scatter_add(tgt, idx, src, axis=0):
    return tgt.clone().index_add_(axis, idx, src)
def _segment_sum(src, seg, n):
    out = torch.zeros((n,) + src.shape[1:], dtype=src.dtype)
    return out.index_add_(0, seg, src)
def _pad(x, pw):
    flat = tuple(v for pair in reversed(pw) for v in pair)
    return torch.nn.functional.pad(x, flat)

kernel_name = 'concat_heads__ssm_conv_h3__softmax_full_mha__prompt'

def setup_inputs():

    _w = (lambda a: (lambda t: t.to(torch.bfloat16) if t.is_floating_point() else t)(torch.as_tensor(a)))
    def _A_setup():
        B, S, D = 1, 2048, 1024
        ks = 4
        return {
            "q": _w((lambda *s: torch.randn(*s, dtype=torch.bfloat16) * 0.05)(B, S, D)),
            "k": _w((lambda *s: torch.randn(*s, dtype=torch.bfloat16) * 0.8)(B, S, D)),
            "v": _w((lambda *s: torch.randn(*s, dtype=torch.bfloat16) * 0.8)(B, S, D)),
            "shift_k": _w((lambda *s: torch.randn(*s, dtype=torch.bfloat16) * 0.8)(D, ks) * 0.5),
            "ssm_k": _w((lambda *s: torch.randn(*s, dtype=torch.bfloat16) * 0.8)(D, S) * (1.0 / S ** 0.5)),
            "ssm_D": _w((lambda *s: torch.randn(*s, dtype=torch.bfloat16) * 0.8)(D)),
        }
    _A_in = _A_setup()

    def _B_setup():
        B, S, D = 1, 2048, 1024
        return {
            "x": _w((lambda *s: torch.randn(*s, dtype=torch.bfloat16) * 0.2)(B, S, D)),
            "wqkv": _w((lambda *s: torch.randn(*s, dtype=torch.bfloat16) * 0.2)(D, 3 * D)),
            "wo": _w((lambda *s: torch.randn(*s, dtype=torch.bfloat16) * 0.05)(D, D)),
        }
    _B_in = _B_setup()

    return {
        "A_q": _A_in["q"],
        "A_k": _A_in["k"],
        "A_v": _A_in["v"],
        "A_shift_k": _A_in["shift_k"],
        "A_ssm_k": _A_in["ssm_k"],
        "A_ssm_D": _A_in["ssm_D"],
        "B_x": _B_in["x"],
        "B_wqkv": _B_in["wqkv"],
        "B_wo": _B_in["wo"],
    }

def reference(A_q, A_k, A_v, A_shift_k, A_ssm_k, A_ssm_D, B_x, B_wqkv, B_wo):
    def _A_ref(q, k, v, shift_k, ssm_k, ssm_D):
        B, S, D = q.shape
        ks = shift_k.shape[-1]
        k_pad = _pad(k, ((0, 0), (ks - 1, 0), (0, 0)))
        k_shift = torch.zeros_like(k)
        for t in range(ks):
            k_shift = k_shift + k_pad[:, t:t + S, :] * shift_k[:, ks - 1 - t][None, None, :]
        kv = k_shift * v
        L = 2 * S
        kv_pad = _pad(kv, ((0, 0), (0, S), (0, 0)))
        kf_pad = _pad(ssm_k, ((0, 0), (0, S)))
        kv_f = (lambda x, axes=(-2,-1): torch.fft.rfft2(x.float(), dim=axes))(kv_pad[:, None, :, :], axes=(1, 2))
        kf_f = (lambda x, axes=(-2,-1): torch.fft.rfft2(x.float(), dim=axes))(kf_pad[:, None, :], axes=(1, 2))
        kf_f = (lambda x, *d: x.permute(*d))(kf_f, 1, 2, 0)
        y_f = kv_f * kf_f[None, :, :, :]
        y = (lambda x, s, axes=(-2,-1): torch.fft.irfft2(x, s=s, dim=axes).to(torch.bfloat16))(y_f, (1, L), axes=(1, 2))[:, 0, :S, :]
        y = y + kv * ssm_D[None, None, :]
        return q * y

    def _B_ref(x, wqkv, wo):
        B, S, D = x.shape
        H = 32; Dh = D // H
        qkv = (lambda x, *d: x.permute(*d))((x @ wqkv).reshape(B, S, 3, H, Dh), 2, 0, 3, 1, 4)
        q, k, v = qkv[0], qkv[1], qkv[2]
        scale = Dh ** -0.5
        scores = torch.einsum("bhqd,bhkd->bhqk", q, k) * scale
        p = _softmax(scores, axis=-1)
        o = torch.einsum("bhqk,bhkd->bhqd", p, v)
        return (lambda x, *d: x.permute(*d))(o, 0, 2, 1, 3).reshape(B, S, D) @ wo

    a = _A_ref(A_q, A_k, A_v, A_shift_k, A_ssm_k, A_ssm_D)
    b = _B_ref(B_x, B_wqkv, B_wo)
    return torch.concatenate([a, b], axis=-1)
```

```cpp
#include <hip/hip_runtime.h>
#include <hip/hip_bf16.h>
#include <stdint.h>

typedef short bf16x8 __attribute__((ext_vector_type(8)));
typedef short s16x4 __attribute__((ext_vector_type(4)));
typedef float f32x16 __attribute__((ext_vector_type(16)));
typedef float f32x4 __attribute__((ext_vector_type(4)));
typedef float f32x2 __attribute__((ext_vector_type(2)));
typedef unsigned u32x4 __attribute__((ext_vector_type(4)));
typedef unsigned u32x2 __attribute__((ext_vector_type(2)));
typedef __bf16 bf16x2_t __attribute__((ext_vector_type(2)));

#define LAS __attribute__((address_space(3)))
typedef LAS char* lds_ptr;
#define DI __device__ __forceinline__
#define SBAR() __builtin_amdgcn_sched_barrier(0)
#define MFMA32(a, b, c) __builtin_amdgcn_mfma_f32_32x32x16_bf16((a), (b), (c), 0, 0, 0)
#define MFMA16(a, b, c) __builtin_amdgcn_mfma_f32_16x16x32_bf16((a), (b), (c), 0, 0, 0)
#define WAIT_BAR(N) asm volatile("s_waitcnt vmcnt(" #N ") lgkmcnt(0)\n\ts_barrier" ::: "memory")
#define LGKM_BAR() asm volatile("s_waitcnt lgkmcnt(0)\n\ts_barrier" ::: "memory")

constexpr int S_LEN = 2048, DM = 1024, NH = 32;

constexpr size_t WS_Q = 0;
constexpr size_t WS_KV = 4u << 20;
constexpr size_t WS_O = 12u << 20;
constexpr size_t WS_KVR = 16u << 20;
constexpr size_t WS_ZT = 20u << 20;

DI unsigned cvtpk(float lo, float hi) {
    f32x2 v = {lo, hi};
    return __builtin_bit_cast(unsigned, __builtin_convertvector(v, bf16x2_t));
}
DI float bflo(unsigned u) { return __uint_as_float(u << 16); }
DI float bfhi(unsigned u) { return __uint_as_float(u & 0xffff0000u); }

DI void glds16(unsigned voff, const void* sbase, unsigned lds_dst) {
    unsigned sv;
    asm volatile("s_mov_b32 %0, m0\n\ts_mov_b32 m0, %3\n\ts_nop 0\n\tglobal_load_lds_dwordx4 %1, %2\n\ts_mov_b32 m0, %0"
                 : "=&s"(sv) : "v"(voff), "s"(sbase), "s"(lds_dst) : "memory");
}
DI bf16x8 lds_rd128(lds_ptr p) { return *(const LAS bf16x8*)p; }
DI s16x4 lds_tr(lds_ptr p) {
    return __builtin_bit_cast(s16x4, __builtin_amdgcn_ds_read_tr16_b64_v4i16((LAS s16x4*)p));
}
DI bf16x8 cat8(s16x4 a, s16x4 b) { return __builtin_shufflevector(a, b, 0, 1, 2, 3, 4, 5, 6, 7); }
DI u32x4 pack8(const f32x16& x, int s, float sc) {
    u32x4 p;
    p[0] = cvtpk(x[8 * s + 0] * sc, x[8 * s + 1] * sc);
    p[1] = cvtpk(x[8 * s + 2] * sc, x[8 * s + 3] * sc);
    p[2] = cvtpk(x[8 * s + 4] * sc, x[8 * s + 5] * sc);
    p[3] = cvtpk(x[8 * s + 6] * sc, x[8 * s + 7] * sc);
    return p;
}
DI u32x4 pack8n(const f32x16& x, int s) {
    u32x4 p;
    p[0] = cvtpk(x[8 * s + 0], x[8 * s + 1]);
    p[1] = cvtpk(x[8 * s + 2], x[8 * s + 3]);
    p[2] = cvtpk(x[8 * s + 4], x[8 * s + 5]);
    p[3] = cvtpk(x[8 * s + 6], x[8 * s + 7]);
    return p;
}
DI void rnd2(float& a, float& b) { unsigned u = cvtpk(a, b); a = bflo(u); b = bfhi(u); }

constexpr int K1_STAGE = 40960;
__global__ __launch_bounds__(512, 2) void k1_qkv(const char* __restrict__ X, const char* __restrict__ W,
                                                 char* __restrict__ ws, const unsigned* __restrict__ Ak,
                                                 const unsigned* __restrict__ Av, const u32x4* __restrict__ shiftk) {
    __shared__ __attribute__((aligned(16))) char lds_raw[3 * K1_STAGE];
    const lds_ptr lds = (lds_ptr)lds_raw;
    const unsigned lds0 = (unsigned)(uintptr_t)lds;
    const int tid = threadIdx.x, lane = tid & 63;
    const int w = __builtin_amdgcn_readfirstlane(tid >> 6);
    const int b = blockIdx.x, xcd = b & 7, idx = b >> 3;
    const int hp = xcd * 2 + (idx & 1), tb = idx >> 1;
    const int kh = w >> 2, wm = (w >> 1) & 1, wn = w & 1;
    const int r = lane & 31, hh = lane >> 5;

    unsigned voffX[2], voffW[3];
#pragma unroll
    for (int pp = 0; pp < 2; ++pp) {
        const int row = 16 * w + 8 * pp + (lane >> 3);
        const int ch = (lane & 7) ^ ((row >> 1) & 7);
        voffX[pp] = (unsigned)(tb * 128 + row) * 2048u + ch * 16;
    }
#pragma unroll
    for (int sec = 0; sec < 3; ++sec) {
        const int krow = 8 * w + (lane >> 3);
        const int ch = (lane & 7) ^ (((lane >> 4) & 1) << 2);
        voffW[sec] = (unsigned)krow * 6144u + sec * 2048 + hp * 128 + ch * 16;
    }
    auto issue = [&](int kt, int buf) {
        const char* xb = X + kt * 128;
        const char* wb = W + (size_t)kt * 64 * 6144;
        const unsigned st = lds0 + buf * K1_STAGE;
        glds16(voffX[0], xb, st + (2 * w) * 1024);
        glds16(voffX[1], xb, st + (2 * w + 1) * 1024);
        glds16(voffW[0], wb, st + 16384 + w * 1024);
        glds16(voffW[1], wb, st + 16384 + 8192 + w * 1024);
        glds16(voffW[2], wb, st + 16384 + 16384 + w * 1024);
    };
    int xoff[2];
#pragma unroll
    for (int tt = 0; tt < 2; ++tt)
        xoff[tt] = (64 * wm + r) * 128 + (((4 * kh + 2 * tt + hh) ^ ((r >> 1) & 7)) << 4);
    const int g = lane >> 4, q = (lane & 15) >> 2, p = lane & 3;
    const int woff = 16384 + (32 * kh + 8 * hh + q) * 128 + (((4 * wn + 2 * (g & 1) + (p >> 1)) ^ ((q >> 1) << 2)) << 4) + (p & 1) * 8;

    f32x16 acc[2][3];
#pragma unroll
    for (int m = 0; m < 2; ++m)
#pragma unroll
        for (int s = 0; s < 3; ++s) acc[m][s] = f32x16{};

    bf16x8 xf[2][2], wf[2][3];
    auto ldfrag = [&](int buf, int tt) {
        const lds_ptr st = lds + buf * K1_STAGE;
        xf[tt][0] = lds_rd128(st + xoff[tt]);
        xf[tt][1] = lds_rd128(st + xoff[tt] + 4096);
#pragma unroll
        for (int sec = 0; sec < 3; ++sec)
            wf[tt][sec] = cat8(lds_tr(st + woff + sec * 8192 + tt * 2048), lds_tr(st + woff + sec * 8192 + tt * 2048 + 512));
    };
    auto mma = [&](int tt) {
#pragma unroll
        for (int m = 0; m < 2; ++m) {
            acc[m][0] = MFMA32(wf[tt][0], xf[tt][m], acc[m][0]);
            acc[m][1] = MFMA32(wf[tt][1], xf[tt][m], acc[m][1]);
            acc[m][2] = MFMA32(xf[tt][m], wf[tt][2], acc[m][2]);
        }
    };

    issue(0, 0);
    issue(1, 1);
#pragma unroll
    for (int kt = 0; kt < 16; ++kt) {
        SBAR();
        if (kt < 15) WAIT_BAR(5); else WAIT_BAR(0);
        SBAR();
        if (kt + 2 < 16) issue(kt + 2, (kt + 2) % 3);
        ldfrag(kt % 3, 0);
        SBAR();
        if (kt > 0) mma(1);
        SBAR();
        ldfrag(kt % 3, 1);
        SBAR();
        mma(0);
    }
    mma(1);
    SBAR();
    LGKM_BAR();
    {
        const lds_ptr mine = lds + w * 12288 + lane * 16;
        const lds_ptr other = lds + (w ^ 4) * 12288 + lane * 16;
#pragma unroll
        for (int s = 0; s < 3; ++s)
#pragma unroll
            for (int i = 0; i < 4; ++i) {
                f32x4 v;
#pragma unroll
                for (int e = 0; e < 4; ++e) v[e] = kh ? acc[0][s][4 * i + e] : acc[1][s][4 * i + e];
                *(LAS f32x4*)(mine + (s * 4 + i) * 1024) = v;
            }
        LGKM_BAR();
        const int h = 2 * hp + wn, T = tb * 4 + wm * 2 + kh;
        const float qscale = 0.17677669529663687f * 1.4426950408889634f;
#pragma unroll
        for (int s = 0; s < 3; ++s) {
            f32x16 a;
#pragma unroll
            for (int i = 0; i < 4; ++i) {
                const f32x4 v = *(const LAS f32x4*)(other + (s * 4 + i) * 1024);
#pragma unroll
                for (int e = 0; e < 4; ++e) a[4 * i + e] = (kh ? acc[1][s][4 * i + e] : acc[0][s][4 * i + e]) + v[e];
            }
            char* dst = (s == 0) ? ws + WS_Q + (size_t)(h * 64 + T) * 2048 + lane * 16
                                 : ws + WS_KV + (size_t)(h * 64 + T) * 4096 + (s - 1) * 2048 + lane * 16;
            if (s == 0) {
                *(u32x4*)(dst) = pack8(a, 0, qscale);
                *(u32x4*)(dst + 1024) = pack8(a, 1, qscale);
            } else {
                *(u32x4*)(dst) = pack8n(a, 0);
                *(u32x4*)(dst + 1024) = pack8n(a, 1);
            }
        }
    }
    {
        const int s0 = (b >> 3) * 64 + w * 8, cp = (b & 7) * 64 + lane;
        unsigned kr[11], vr[8];
#pragma unroll
        for (int i = 0; i < 11; ++i) {
            const int s = s0 - 3 + i;
            kr[i] = (s >= 0) ? Ak[(size_t)(s < 0 ? 0 : s) * 512 + cp] : 0u;
        }
#pragma unroll
        for (int i = 0; i < 8; ++i) vr[i] = Av[(size_t)(s0 + i) * 512 + cp];
        const u32x4 sk = shiftk[cp];
        const float wa[4] = {bflo(sk[0]), bfhi(sk[0]), bflo(sk[1]), bfhi(sk[1])};
        const float wb[4] = {bflo(sk[2]), bfhi(sk[2]), bflo(sk[3]), bfhi(sk[3])};
        float ra[8], rb[8];
#pragma unroll
        for (int i = 0; i < 8; ++i) {
            float a = bflo(kr[i]) * wa[3], c = bfhi(kr[i]) * wb[3];
            rnd2(a, c);
#pragma unroll
            for (int t = 1; t < 4; ++t) {
                float ta = bflo(kr[i + t]) * wa[3 - t], tc = bfhi(kr[i + t]) * wb[3 - t];
                rnd2(ta, tc);
                a += ta; c += tc;
                rnd2(a, c);
            }
            ra[i] = a * bflo(vr[i]);
            rb[i] = c * bfhi(vr[i]);
        }
        u32x4 oa, ob;
#pragma unroll
        for (int i = 0; i < 4; ++i) {
            oa[i] = cvtpk(ra[7 - 2 * i], ra[6 - 2 * i]);
            ob[i] = cvtpk(rb[7 - 2 * i], rb[6 - 2 * i]);
        }
        char* dst = ws + WS_KVR + (size_t)(2 * cp) * 4096 + s0 * 2;
        *(u32x4*)dst = oa;
        *(u32x4*)(dst + 4096) = ob;
    }
}

constexpr int K2_SLOT = 16384, K2_RING = 4 * K2_SLOT;
constexpr int CV_HA = 0, CV_HB = 4160, CV_KV = 8320, CV_BYTES = 8320 + 480 + 4096;
constexpr int K2_LDS = K2_RING + 4 * CV_BYTES;

__global__ __launch_bounds__(768, 3) void k2_attn(char* __restrict__ ws, const char* __restrict__ ssmk,
                                                  const unsigned short* __restrict__ ssmD) {
    __shared__ __attribute__((aligned(16))) char lds_raw[K2_LDS];
    const lds_ptr lds = (lds_ptr)lds_raw;
    const unsigned lds0 = (unsigned)(uintptr_t)lds;
    const int tid = threadIdx.x, lane = tid & 63;
    const int w = __builtin_amdgcn_readfirstlane(tid >> 6);
    const int b = blockIdx.x, xcd = b & 7, idx = b >> 3;
    const int h = xcd * 4 + (idx >> 3), qb = idx & 7;
    const char* kvg = ws + WS_KV + (size_t)h * 64 * 4096;

    if (w >= 8) {
        const int cw = w - 8;
        const unsigned voff = cw * 4096 + lane * 16;
        auto issue = [&](int st) {
            const char* src = kvg + st * K2_SLOT;
            const unsigned dst = lds0 + (st & 3) * K2_SLOT + cw * 4096;
            glds16(voff, src, dst);
            glds16(voff + 1024, src, dst + 1024);
            glds16(voff + 2048, src, dst + 2048);
            glds16(voff + 3072, src, dst + 3072);
        };
        issue(0); issue(1); issue(2);
        const int ch = b * 4 + cw;
        const lds_ptr cv = lds + K2_RING + cw * CV_BYTES;
        {
            const char* hrow = ssmk + (size_t)ch * 4096;
            const char* kvrow = ws + WS_KVR + (size_t)ch * 4096;
            u32x4 hv[4], kv4[4]; unsigned pv[4];
#pragma unroll
            for (int i = 0; i < 4; ++i) {
                const int off = i * 1024 + lane * 16;
                hv[i] = *(const u32x4*)(hrow + off);
                pv[i] = *(const unsigned*)(hrow + (off ? off - 4 : 0));
                kv4[i] = *(const u32x4*)(kvrow + off);
            }
            if (lane == 0) pv[0] = 0u;
            const u32x4 z4 = {0u, 0u, 0u, 0u};
            if (lane < 2) { *(LAS u32x4*)(cv + CV_HA + lane * 16) = z4; *(LAS u32x4*)(cv + CV_HB + lane * 16) = z4; }
            if (lane < 30) *(LAS u32x4*)(cv + CV_KV + lane * 16) = z4;
#pragma unroll
            for (int i = 0; i < 4; ++i) {
                const int off = i * 1024 + lane * 16;
                u32x4 sh;
                sh[0] = __builtin_amdgcn_alignbit(hv[i][0], pv[i], 16);
                sh[1] = __builtin_amdgcn_alignbit(hv[i][1], hv[i][0], 16);
                sh[2] = __builtin_amdgcn_alignbit(hv[i][2], hv[i][1], 16);
                sh[3] = __builtin_amdgcn_alignbit(hv[i][3], hv[i][2], 16);
                *(LAS u32x4*)(cv + CV_HA + 32 + off) = hv[i];
                *(LAS u32x4*)(cv + CV_HB + 32 + off) = sh;
                *(LAS u32x4*)(cv + CV_KV + 480 + off) = kv4[i];
            }
        }
        const int r1 = lane & 15, q = lane >> 4;
        const int toff = (r1 & 1) ? CV_HA + 2 * (16 + r1 + 9 - 8 * q) : CV_HB + 2 * (16 + r1 + 10 - 8 * q);
        const int boff = CV_KV + 448 + 32 * r1 + 16 * q;
        f32x4 y[8];
#pragma unroll
        for (int t = 0; t < 8; ++t) y[t] = f32x4{};
        WAIT_BAR(0);
#pragma unroll 1
        for (int st = 0; st < 16; ++st) {
            if (st + 3 < 16) issue(st + 3);
            if (st & 1) {
                const int bb = st >> 1;
                bf16x8 tf[8], bf[8];
#pragma unroll
                for (int a = 0; a < 8; ++a) {
                    const LAS unsigned* tp = (const LAS unsigned*)(cv + toff + 64 * bb + 512 * a);
                    u32x4 t4 = {tp[0], tp[1], tp[2], tp[3]};
                    tf[a] = __builtin_bit_cast(bf16x8, t4);
                    bf[a] = lds_rd128(cv + boff - 64 * bb + 512 * a);
                }
#pragma unroll
                for (int a = 0; a < 8; ++a)
#pragma unroll
                    for (int ww = 0; ww + a < 8; ++ww) y[a + ww] = MFMA16(tf[a], bf[ww], y[a + ww]);
            }
            if (st < 15) {
                if (st + 3 < 16) WAIT_BAR(4); else WAIT_BAR(0);
            }
        }
        const float dch = __uint_as_float((unsigned)ssmD[ch] << 16);
        char* zrow = ws + WS_ZT + (size_t)ch * 4096;
#pragma unroll
        for (int t = 0; t < 8; ++t) {
            const u32x2 kk = *(const LAS u32x2*)(cv + CV_KV + 480 + 512 * t + 32 * r1 + 16 * (q >> 1) + ((q & 1) ? 0 : 8));
            float k0 = bfhi(kk[1]) * dch, k1 = bflo(kk[1]) * dch, k2 = bfhi(kk[0]) * dch, k3 = bflo(kk[0]) * dch;
            rnd2(k0, k1); rnd2(k2, k3);
            float y0 = y[t][0], y1 = y[t][1], y2 = y[t][2], y3 = y[t][3];
            rnd2(y0, y1); rnd2(y2, y3);
            u32x2 o = {cvtpk(y0 + k0, y1 + k1), cvtpk(y2 + k2, y3 + k3)};
            *(u32x2*)(zrow + 512 * t + 32 * r1 + 8 * q) = o;
        }
        return;
    }

    const int T = qb * 8 + w;
    const char* qg = ws + WS_Q + (size_t)(h * 64 + T) * 2048 + lane * 16;
    const bf16x8 qf0 = *(const bf16x8*)qg, qf1 = *(const bf16x8*)(qg + 1024);
    bf16x8 sel;
    {
        const short one = (((lane >> 2) & 1) == ((lane >> 4) & 1)) ? (short)0x3f80 : (short)0;
#pragma unroll
        for (int i = 0; i < 8; ++i) sel[i] = one;
    }
    f32x16 o = f32x16{};
    f32x4 lsum = f32x4{};
    f32x16 negm;
    const lds_ptr lp = lds + lane * 16;
    LGKM_BAR();
    {
        const bf16x8 k0 = lds_rd128(lp), k1 = lds_rd128(lp + 1024);
        f32x16 s = MFMA32(k0, qf0, f32x16{});
        s = MFMA32(k1, qf1, s);
        float m = s[0];
#pragma unroll
        for (int i = 1; i < 16; ++i) m = __builtin_fmaxf(m, s[i]);
        auto rr = __builtin_amdgcn_permlane32_swap(__float_as_uint(m), __float_as_uint(m), false, false);
        m = __builtin_fmaxf(__uint_as_float(rr[0]), __uint_as_float(rr[1]));
#pragma unroll
        for (int i = 0; i < 16; ++i) negm[i] = -m;
    }
    f32x16 sc;
    {
        const bf16x8 k0 = lds_rd128(lp), k1 = lds_rd128(lp + 1024);
        sc = MFMA32(k0, qf0, negm);
        sc = MFMA32(k1, qf1, sc);
    }
#pragma unroll 1
    for (int st = 0; st < 16; ++st) {
        const lds_ptr cur = lp + (st & 3) * K2_SLOT;
        const lds_ptr nxt = lp + ((st + 1) & 3) * K2_SLOT;
#pragma unroll
        for (int j = 0; j < 4; ++j) {
            const lds_ptr kn = (j < 3) ? cur + (j + 1) * 4096 : nxt;
            const bf16x8 k0 = lds_rd128(kn), k1 = lds_rd128(kn + 1024);
            const bf16x8 v0 = lds_rd128(cur + j * 4096 + 2048), v1 = lds_rd128(cur + j * 4096 + 3072);
            f32x16 sn = MFMA32(k0, qf0, negm);
            sn = MFMA32(k1, qf1, sn);
#pragma unroll
            for (int i = 0; i < 16; ++i) sc[i] = __builtin_amdgcn_exp2f(sc[i]);
            const bf16x8 p0 = __builtin_bit_cast(bf16x8, pack8n(sc, 0));
            const bf16x8 p1 = __builtin_bit_cast(bf16x8, pack8n(sc, 1));
            o = MFMA32(v0, p0, o);
            lsum = MFMA16(sel, p0, lsum);
            o = MFMA32(v1, p1, o);
            lsum = MFMA16(sel, p1, lsum);
            sc = sn;
        }
        if (st < 15) LGKM_BAR();
    }
    float l = lsum[0];
    {
        float amax = __builtin_fabsf(o[0]);
#pragma unroll
        for (int i = 1; i < 16; ++i) amax = __builtin_fmaxf(amax, __builtin_fabsf(o[i]));
        bool nan = false;
#pragma unroll
        for (int i = 0; i < 16; ++i) nan |= (o[i] != o[i]);
        const bool ok = (l <= 1.0e30f) && (l > 0.f) && (amax <= 3.0e38f) && !nan;
        if (__builtin_expect(__any(!ok), 0)) {
            float m = -INFINITY, ls = 0.f;
            o = f32x16{};
#pragma unroll 1
            for (int t = 0; t < 64; ++t) {
                const char* g = kvg + t * 4096 + lane * 16;
                const bf16x8 k0 = *(const bf16x8*)g, k1 = *(const bf16x8*)(g + 1024);
                const bf16x8 v0 = *(const bf16x8*)(g + 2048), v1 = *(const bf16x8*)(g + 3072);
                f32x16 s = MFMA32(k0, qf0, f32x16{});
                s = MFMA32(k1, qf1, s);
                float tm = s[0];
#pragma unroll
                for (int i = 1; i < 16; ++i) tm = __builtin_fmaxf(tm, s[i]);
                auto rr = __builtin_amdgcn_permlane32_swap(__float_as_uint(tm), __float_as_uint(tm), false, false);
                tm = __builtin_fmaxf(__uint_as_float(rr[0]), __uint_as_float(rr[1]));
                const float mn = __builtin_fmaxf(m, tm);
                const float alpha = __builtin_amdgcn_exp2f(m - mn);
                m = mn;
                float ps = 0.f;
#pragma unroll
                for (int i = 0; i < 16; ++i) { s[i] = __builtin_amdgcn_exp2f(s[i] - mn); ps += s[i]; o[i] *= alpha; }
                ls = ls * alpha + ps;
                o = MFMA32(v0, __builtin_bit_cast(bf16x8, pack8n(s, 0)), o);
                o = MFMA32(v1, __builtin_bit_cast(bf16x8, pack8n(s, 1)), o);
            }
            auto rr = __builtin_amdgcn_permlane32_swap(__float_as_uint(ls), __float_as_uint(ls), false, false);
            l = __uint_as_float(rr[0]) + __uint_as_float(rr[1]);
        }
    }
    const float rl = 1.0f / l;
    char* og = ws + WS_O + (size_t)(T * 32 + h) * 2048 + lane * 16;
    *(u32x4*)og = pack8(o, 0, rl);
    *(u32x4*)(og + 1024) = pack8(o, 1, rl);
}

__global__ __launch_bounds__(512, 2) void k3_out(const char* __restrict__ ws, const char* __restrict__ Wo,
                                                 const unsigned* __restrict__ Aq, char* __restrict__ out) {
    __shared__ __attribute__((aligned(16))) char lds_raw[131072];
    const lds_ptr lds = (lds_ptr)lds_raw;
    const unsigned lds0 = (unsigned)(uintptr_t)lds;
    const int tid = threadIdx.x, lane = tid & 63;
    const int w = __builtin_amdgcn_readfirstlane(tid >> 6);
    const int b = blockIdx.x, xcd = b & 7, idx = b >> 3;
    const int tt = (xcd >> 1) * 4 + (idx >> 3), nt = (xcd & 1) * 8 + (idx & 7);
    const int kh = w >> 2, T = tt * 4 + (w & 3);
    const int r = lane & 31, hh = lane >> 5;

    {
        const int ch = (lane & 7) ^ (((lane >> 4) & 1) << 2);
        const unsigned voff = (unsigned)(lane >> 3) * 2048u + nt * 128 + ch * 16;
#pragma unroll
        for (int qd = 0; qd < 4; ++qd)
#pragma unroll
            for (int i = 0; i < 4; ++i) {
                const int piece = (i >> 1) * 64 + qd * 16 + (i & 1) * 8 + w;
                glds16(voff, Wo + (size_t)piece * 8 * 2048, lds0 + piece * 1024);
            }
    }
    const char* og = ws + WS_O + (size_t)(T * 32 + 16 * kh) * 2048 + lane * 16;
    bf16x8 of[32];
#pragma unroll
    for (int i = 0; i < 32; ++i) of[i] = *(const bf16x8*)(og + i * 1024);

    const int g = lane >> 4, q = (lane & 15) >> 2, p = lane & 3;
    int woff[2];
#pragma unroll
    for (int n = 0; n < 2; ++n) {
        const int c = 4 * n + 2 * (p & 1) + (g & 1);
        woff[n] = (512 * kh + 4 * hh + q) * 128 + ((c ^ ((q >> 1) << 2)) << 4) + (p >> 1) * 8;
    }
    f32x16 acc[2] = {f32x16{}, f32x16{}};
    WAIT_BAR(0);
#pragma unroll
    for (int i = 0; i < 32; ++i) {
#pragma unroll
        for (int n = 0; n < 2; ++n) {
            const bf16x8 wf = cat8(lds_tr(lds + woff[n] + i * 2048), lds_tr(lds + woff[n] + i * 2048 + 1024));
            acc[n] = MFMA32(wf, of[i], acc[n]);
        }
    }
    LGKM_BAR();
    {
        const lds_ptr mine = lds + w * 4096 + lane * 16;
        const lds_ptr other = lds + (w ^ 4) * 4096 + lane * 16;
#pragma unroll
        for (int i = 0; i < 4; ++i) {
            f32x4 v;
#pragma unroll
            for (int e = 0; e < 4; ++e) v[e] = kh ? acc[0][4 * i + e] : acc[1][4 * i + e];
            *(LAS f32x4*)(mine + i * 1024) = v;
        }
        LGKM_BAR();
        f32x16 a;
#pragma unroll
        for (int i = 0; i < 4; ++i) {
            const f32x4 v = *(const LAS f32x4*)(other + i * 1024);
#pragma unroll
            for (int e = 0; e < 4; ++e) a[4 * i + e] = (kh ? acc[1][4 * i + e] : acc[0][4 * i + e]) + v[e];
        }
        char* dst = out + (size_t)(T * 32 + r) * 4096 + 2048 + nt * 128 + kh * 64 + hh * 32;
        *(u32x4*)dst = pack8n(a, 0);
        *(u32x4*)(dst + 16) = pack8n(a, 1);
    }
    {
        const int s0 = (b >> 3) * 64 + w * 8, cp = (b & 7) * 64 + lane;
        const char* zs = ws + WS_ZT + (size_t)(2 * cp) * 4096 + s0 * 2;
        const u32x4 za = *(const u32x4*)zs, zb = *(const u32x4*)(zs + 4096);
        unsigned qr[8];
#pragma unroll
        for (int i = 0; i < 8; ++i) qr[i] = Aq[(size_t)(s0 + i) * 512 + cp];
#pragma unroll
        for (int i = 0; i < 8; ++i) {
            const float a = (i & 1) ? bfhi(za[i >> 1]) : bflo(za[i >> 1]);
            const float c = (i & 1) ? bfhi(zb[i >> 1]) : bflo(zb[i >> 1]);
            *(unsigned*)(out + (size_t)(s0 + i) * 4096 + cp * 4) = cvtpk(bflo(qr[i]) * a, bfhi(qr[i]) * c);
        }
    }
}

extern "C" void kernel_launch(void* const* d_in, const int* in_sizes, int n_in,
                              void* d_out, int out_size, void* d_ws, size_t ws_size,
                              hipStream_t stream) {
    const void* A_q = d_in[0];
    const void* A_k = d_in[1];
    const void* A_v = d_in[2];
    const void* A_shift = d_in[3];
    const void* A_ssmk = d_in[4];
    const void* A_D = d_in[5];
    const void* B_x = d_in[6];
    const void* B_wqkv = d_in[7];
    const void* B_wo = d_in[8];
    char* ws = (char*)d_ws;
    k1_qkv<<<256, 512, 0, stream>>>((const char*)B_x, (const char*)B_wqkv, ws, (const unsigned*)A_k,
                                    (const unsigned*)A_v, (const u32x4*)A_shift);
    k2_attn<<<256, 768, 0, stream>>>(ws, (const char*)A_ssmk, (const unsigned short*)A_D);
    k3_out<<<256, 512, 0, stream>>>(ws, (const char*)B_wo, (const unsigned*)A_q, (char*)d_out);
}
```

```cpp
#include <hip/hip_runtime.h>
#include <hip/hip_bf16.h>
#include <stdint.h>

typedef short bf16x8 __attribute__((ext_vector_type(8)));
typedef short s16x4 __attribute__((ext_vector_type(4)));
typedef float f32x16 __attribute__((ext_vector_type(16)));
typedef float f32x4 __attribute__((ext_vector_type(4)));
typedef float f32x2 __attribute__((ext_vector_type(2)));
typedef unsigned u32x4 __attribute__((ext_vector_type(4)));
typedef unsigned u32x2 __attribute__((ext_vector_type(2)));
typedef __bf16 bf16x2_t __attribute__((ext_vector_type(2)));

#define LAS __attribute__((address_space(3)))
typedef LAS char* lds_ptr;
#define DI __device__ __forceinline__
#define SBAR() __builtin_amdgcn_sched_barrier(0)
#define MFMA32(a, b, c) __builtin_amdgcn_mfma_f32_32x32x16_bf16((a), (b), (c), 0, 0, 0)
#define MFMA16(a, b, c) __builtin_amdgcn_mfma_f32_16x16x32_bf16((a), (b), (c), 0, 0, 0)
#define WAIT_BAR(N) asm volatile("s_waitcnt vmcnt(" #N ") lgkmcnt(0)\n\ts_barrier" ::: "memory")
#define LGKM_BAR() asm volatile("s_waitcnt lgkmcnt(0)\n\ts_barrier" ::: "memory")

#define DG_DECL(K)
#define DG(i)
#define DG_END(i)

constexpr int S_LEN = 2048, DM = 1024, NH = 32;

constexpr size_t WS_Q = 0;
constexpr size_t WS_KV = 4u << 20;
constexpr size_t WS_O = 12u << 20;
constexpr size_t WS_KVR = 16u << 20;
constexpr size_t WS_ZT = 20u << 20;

DI unsigned cvtpk(float lo, float hi) {
    f32x2 v = {lo, hi};
    return __builtin_bit_cast(unsigned, __builtin_convertvector(v, bf16x2_t));
}
DI float bflo(unsigned u) { return __uint_as_float(u << 16); }
DI float bfhi(unsigned u) { return __uint_as_float(u & 0xffff0000u); }

DI void glds16(unsigned voff, const void* sbase, unsigned lds_dst) {
    unsigned sv;
    asm volatile("s_mov_b32 %0, m0\n\ts_mov_b32 m0, %3\n\ts_nop 0\n\tglobal_load_lds_dwordx4 %1, %2\n\ts_mov_b32 m0, %0"
                 : "=&s"(sv) : "v"(voff), "s"(sbase), "s"(lds_dst) : "memory");
}
DI bf16x8 lds_rd128(lds_ptr p) { return *(const LAS bf16x8*)p; }
DI s16x4 lds_tr(lds_ptr p) {
    return __builtin_bit_cast(s16x4, __builtin_amdgcn_ds_read_tr16_b64_v4i16((LAS s16x4*)p));
}
DI bf16x8 cat8(s16x4 a, s16x4 b) { return __builtin_shufflevector(a, b, 0, 1, 2, 3, 4, 5, 6, 7); }
DI u32x4 pack8(const f32x16& x, int s, float sc) {
    u32x4 p;
    p[0] = cvtpk(x[8 * s + 0] * sc, x[8 * s + 1] * sc);
    p[1] = cvtpk(x[8 * s + 2] * sc, x[8 * s + 3] * sc);
    p[2] = cvtpk(x[8 * s + 4] * sc, x[8 * s + 5] * sc);
    p[3] = cvtpk(x[8 * s + 6] * sc, x[8 * s + 7] * sc);
    return p;
}
DI u32x4 pack8n(const f32x16& x, int s) {
    u32x4 p;
    p[0] = cvtpk(x[8 * s + 0], x[8 * s + 1]);
    p[1] = cvtpk(x[8 * s + 2], x[8 * s + 3]);
    p[2] = cvtpk(x[8 * s + 4], x[8 * s + 5]);
    p[3] = cvtpk(x[8 * s + 6], x[8 * s + 7]);
    return p;
}
DI void rnd2(float& a, float& b) { unsigned u = cvtpk(a, b); a = bflo(u); b = bfhi(u); }

#define RAW_BAR() asm volatile("s_barrier" ::: "memory")
DI unsigned gld32(unsigned voff, const void* sbase) {
    unsigned r;
    asm volatile("global_load_dword %0, %1, %2" : "=v"(r) : "v"(voff), "s"(sbase) : "memory");
    return r;
}
DI u32x4 gld128(unsigned voff, const void* sbase) {
    u32x4 r;
    asm volatile("global_load_dwordx4 %0, %1, %2" : "=v"(r) : "v"(voff), "s"(sbase) : "memory");
    return r;
}

constexpr int K1_STAGE = 40960;
__global__ __launch_bounds__(512, 2) void k1_qkv(const char* __restrict__ X, const char* __restrict__ W,
                                                 char* __restrict__ ws, const char* __restrict__ Ak,
                                                 const char* __restrict__ Av, const char* __restrict__ shiftk) {
    __shared__ __attribute__((aligned(16))) char lds_raw[4 * K1_STAGE];
    const lds_ptr lds = (lds_ptr)lds_raw;
    const unsigned lds0 = (unsigned)(uintptr_t)lds;
    const int tid = threadIdx.x, lane = tid & 63;
    const int w = __builtin_amdgcn_readfirstlane(tid >> 6);
    const int b = blockIdx.x, xcd = b & 7, idx = b >> 3;
    const int hp = xcd * 2 + (idx & 1), tb = idx >> 1;
    DG_DECL(0)

    if (w >= 4) {
        const int lw = w - 4;
        unsigned voffX[4], voffW[2];
#pragma unroll
        for (int pp = 0; pp < 4; ++pp) {
            const int row = 32 * lw + 8 * pp + (lane >> 3);
            const int ch = (lane & 7) ^ ((row >> 1) & 7);
            voffX[pp] = (unsigned)(tb * 128 + row) * 2048u + ch * 16;
        }
#pragma unroll
        for (int pp = 0; pp < 2; ++pp) {
            const int krow = 16 * lw + 8 * pp + (lane >> 3);
            const int ch = (lane & 7) ^ (((lane >> 4) & 1) << 2);
            voffW[pp] = (unsigned)krow * 6144u + hp * 128 + ch * 16;
        }
        auto issue = [&](int kt) {
            const char* xb = X + kt * 128;
            const char* wb = W + (size_t)kt * (64 * 6144);
            const unsigned st = lds0 + (kt & 3) * K1_STAGE;
#pragma unroll
            for (int pp = 0; pp < 4; ++pp) glds16(voffX[pp], xb, st + (4 * lw + pp) * 1024);
#pragma unroll
            for (int sec = 0; sec < 3; ++sec)
#pragma unroll
                for (int pp = 0; pp < 2; ++pp)
                    glds16(voffW[pp], wb + sec * 2048, st + 16384 + sec * 8192 + (2 * lw + pp) * 1024);
        };
        issue(0); issue(1); issue(2);
#pragma unroll 1
        for (int kt = 0; kt < 13; ++kt) {
            WAIT_BAR(20);
            issue(kt + 3);
        }
        const int s0 = (b >> 3) * 64 + lw * 16, cp = (b & 7) * 64 + lane;
        unsigned kr[19], vr[16];
        const unsigned vo = (unsigned)cp * 4u;
#pragma unroll
        for (int i = 0; i < 19; ++i) {
            int s = s0 - 3 + i; s = s < 0 ? 0 : s;
            kr[i] = gld32(vo, Ak + (size_t)s * 2048);
        }
#pragma unroll
        for (int i = 0; i < 16; ++i) vr[i] = gld32(vo, Av + (size_t)(s0 + i) * 2048);
        u32x4 sk = gld128((unsigned)cp * 16u, shiftk);
        WAIT_BAR(56);
        WAIT_BAR(46);
        WAIT_BAR(36);
        asm volatile("s_waitcnt vmcnt(0)" ::: "memory");
        SBAR();
#pragma unroll
        for (int i = 0; i < 19; ++i) asm volatile("" : "+v"(kr[i]));
#pragma unroll
        for (int i = 0; i < 16; ++i) asm volatile("" : "+v"(vr[i]));
        asm volatile("" : "+v"(sk));
        if (s0 == 0) { kr[0] = 0u; kr[1] = 0u; kr[2] = 0u; }
        const float wa[4] = {bflo(sk[0]), bfhi(sk[0]), bflo(sk[1]), bfhi(sk[1])};
        const float wb[4] = {bflo(sk[2]), bfhi(sk[2]), bflo(sk[3]), bfhi(sk[3])};
        char* dst = ws + WS_KVR + (size_t)(2 * cp) * 4096 + s0 * 2;
#pragma unroll
        for (int gq = 0; gq < 2; ++gq) {
            float ra[8], rb[8];
#pragma unroll
            for (int i = 0; i < 8; ++i) {
                const int j = gq * 8 + i;
                float a = bflo(kr[j]) * wa[3], c = bfhi(kr[j]) * wb[3];
#pragma unroll
                for (int t = 1; t < 4; ++t) {
                    a = __builtin_fmaf(bflo(kr[j + t]), wa[3 - t], a);
                    c = __builtin_fmaf(bfhi(kr[j + t]), wb[3 - t], c);
                }
                ra[i] = a * bflo(vr[j]);
                rb[i] = c * bfhi(vr[j]);
            }
            u32x4 oa, ob;
#pragma unroll
            for (int i = 0; i < 4; ++i) {
                oa[i] = cvtpk(ra[7 - 2 * i], ra[6 - 2 * i]);
                ob[i] = cvtpk(rb[7 - 2 * i], rb[6 - 2 * i]);
            }
            *(u32x4*)(dst + gq * 16) = oa;
            *(u32x4*)(dst + 4096 + gq * 16) = ob;
        }
        DG_END(5);
        return;
    }

    const int wm = w >> 1, wn = w & 1;
    const int r = lane & 31, hh = lane >> 5;
    const int g = lane >> 4, q = (lane & 15) >> 2, p = lane & 3;
    const int xoff0 = (64 * wm + r) * 128 + ((hh ^ ((r >> 1) & 7)) << 4);
    const int woff0 = 16384 + (8 * hh + q) * 128 + (((4 * wn + 2 * (g & 1) + (p >> 1)) ^ ((q >> 1) << 2)) << 4) + (p & 1) * 8;

    f32x16 acc[2][3];
#pragma unroll
    for (int m = 0; m < 2; ++m)
#pragma unroll
        for (int s = 0; s < 3; ++s) acc[m][s] = f32x16{};
    bf16x8 xf[2][2];
    s16x4 wl[2][3], wh[2][3];
#define K1_WF(f, sec) cat8(wl[f][sec], wh[f][sec])
#define K1_STEP(F, st, t, BAR_HERE) do { \
        const lds_ptr xp_ = (st) + (xoff0 ^ ((t) << 5)); const lds_ptr wp_ = (st) + woff0 + (t) * 2048; \
        acc[0][0] = MFMA32(K1_WF(F, 0), xf[F][0], acc[0][0]); SBAR(); \
        if (BAR_HERE) { LGKM_BAR(); SBAR(); } \
        xf[F ^ 1][0] = lds_rd128(xp_); wl[F ^ 1][0] = lds_tr(wp_); wh[F ^ 1][0] = lds_tr(wp_ + 512); SBAR(); \
        acc[0][1] = MFMA32(K1_WF(F, 1), xf[F][0], acc[0][1]); SBAR(); \
        wl[F ^ 1][1] = lds_tr(wp_ + 8192); wh[F ^ 1][1] = lds_tr(wp_ + 8192 + 512); SBAR(); \
        acc[0][2] = MFMA32(xf[F][0], K1_WF(F, 2), acc[0][2]); SBAR(); \
        wl[F ^ 1][2] = lds_tr(wp_ + 16384); wh[F ^ 1][2] = lds_tr(wp_ + 16384 + 512); SBAR(); \
        acc[1][0] = MFMA32(K1_WF(F, 0), xf[F][1], acc[1][0]); SBAR(); \
        xf[F ^ 1][1] = lds_rd128(xp_ + 4096); SBAR(); \
        acc[1][1] = MFMA32(K1_WF(F, 1), xf[F][1], acc[1][1]); \
        acc[1][2] = MFMA32(xf[F][1], K1_WF(F, 2), acc[1][2]); SBAR(); \
    } while (0)

    LGKM_BAR();
    DG(3);
    {
        const lds_ptr xp_ = lds + xoff0; const lds_ptr wp_ = lds + woff0;
        xf[0][0] = lds_rd128(xp_); xf[0][1] = lds_rd128(xp_ + 4096);
#pragma unroll
        for (int sec = 0; sec < 3; ++sec) { wl[0][sec] = lds_tr(wp_ + sec * 8192); wh[0][sec] = lds_tr(wp_ + sec * 8192 + 512); }
    }
    SBAR();
#pragma unroll 1
    for (int kt = 0; kt < 15; ++kt) {
        const lds_ptr st = lds + (kt & 3) * K1_STAGE;
        const lds_ptr sn = lds + ((kt + 1) & 3) * K1_STAGE;
        K1_STEP(0, st, 1, false);
        K1_STEP(1, st, 2, false);
        K1_STEP(0, st, 3, false);
        K1_STEP(1, sn, 0, true);
    }
    {
        const lds_ptr st = lds + 3 * K1_STAGE;
        K1_STEP(0, st, 1, false);
        K1_STEP(1, st, 2, false);
        K1_STEP(0, st, 3, false);
#pragma unroll
        for (int m = 0; m < 2; ++m) {
            acc[m][0] = MFMA32(K1_WF(1, 0), xf[1][m], acc[m][0]);
            acc[m][1] = MFMA32(K1_WF(1, 1), xf[1][m], acc[m][1]);
            acc[m][2] = MFMA32(xf[1][m], K1_WF(1, 2), acc[m][2]);
        }
    }
    DG(4);
    {
        const int h = 2 * hp + wn;
        const float qscale = 0.17677669529663687f * 1.4426950408889634f;
#pragma unroll
        for (int m = 0; m < 2; ++m) {
            const int T = tb * 4 + wm * 2 + m;
            char* dq = ws + WS_Q + (size_t)(h * 64 + T) * 2048 + lane * 16;
            char* dk = ws + WS_KV + (size_t)(h * 64 + T) * 4096 + lane * 16;
            *(u32x4*)(dk) = pack8n(acc[m][1], 0);
            *(u32x4*)(dk + 1024) = pack8n(acc[m][1], 1);
            *(u32x4*)(dk + 2048) = pack8n(acc[m][2], 0);
            *(u32x4*)(dk + 3072) = pack8n(acc[m][2], 1);
            *(u32x4*)(dq) = pack8(acc[m][0], 0, qscale);
            *(u32x4*)(dq + 1024) = pack8(acc[m][0], 1, qscale);
        }
    }
    DG_END(5);
}

constexpr int K2_SLOT = 16384, K2_RING = 4 * K2_SLOT;
constexpr int CV_HA = 0, CV_HB = 4160, CV_KV = 8320, CV_BYTES = 8320 + 480 + 4096;
constexpr int K2_LDS = K2_RING + 4 * CV_BYTES;

__global__ __launch_bounds__(768, 3) void k2_attn(char* __restrict__ ws, const char* __restrict__ ssmk,
                                                  const unsigned short* __restrict__ ssmD) {
    __shared__ __attribute__((aligned(16))) char lds_raw[K2_LDS];
    const lds_ptr lds = (lds_ptr)lds_raw;
    const unsigned lds0 = (unsigned)(uintptr_t)lds;
    const int tid = threadIdx.x, lane = tid & 63;
    const int w = __builtin_amdgcn_readfirstlane(tid >> 6);
    const int b = blockIdx.x, xcd = b & 7, idx = b >> 3;
    const int h = xcd * 4 + (idx >> 3), qb = idx & 7;
    const char* kvg = ws + WS_KV + (size_t)h * 64 * 4096;
    DG_DECL(1)

    if (w >= 8) {
        const int cw = w - 8;
        const unsigned voff = lane * 16;
        auto issue = [&](int st) {
            const char* src = kvg + st * K2_SLOT + cw * 4096;
            const unsigned dst = lds0 + (st & 3) * K2_SLOT + cw * 4096;
#pragma unroll
            for (int i = 0; i < 4; ++i) glds16(voff, src + i * 1024, dst + i * 1024);
        };
        issue(0); issue(1); issue(2); issue(3);
        const int ch = b * 4 + cw;
        const lds_ptr cv = lds + K2_RING + cw * CV_BYTES;
        {
            const char* hrow = ssmk + (size_t)ch * 4096;
            const char* kvrow = ws + WS_KVR + (size_t)ch * 4096;
            const unsigned cv0 = lds0 + K2_RING + cw * CV_BYTES;
#pragma unroll
            for (int i = 0; i < 4; ++i) glds16(voff, hrow + i * 1024, cv0 + CV_HA + 32 + i * 1024);
#pragma unroll
            for (int i = 0; i < 4; ++i) glds16(voff, kvrow + i * 1024, cv0 + CV_KV + 480 + i * 1024);
            const u32x4 z4 = {0u, 0u, 0u, 0u};
            if (lane < 2) { *(LAS u32x4*)(cv + CV_HA + lane * 16) = z4; *(LAS u32x4*)(cv + CV_HB + lane * 16) = z4; }
            if (lane < 30) *(LAS u32x4*)(cv + CV_KV + lane * 16) = z4;
        }
        WAIT_BAR(20);
        DG(3);
        WAIT_BAR(16);
        WAIT_BAR(12);
        issue(4);
        WAIT_BAR(12);
        issue(5);
        WAIT_BAR(4);
        issue(6);
        {
#pragma unroll
            for (int i = 0; i < 4; ++i) {
                const int off = 32 + i * 1024 + lane * 16;
                const u32x4 hv = *(const LAS u32x4*)(cv + CV_HA + off);
                const unsigned pv = *(const LAS unsigned*)(cv + CV_HA + off - 4);
                u32x4 sh;
                sh[0] = __builtin_amdgcn_alignbit(hv[0], pv, 16);
                sh[1] = __builtin_amdgcn_alignbit(hv[1], hv[0], 16);
                sh[2] = __builtin_amdgcn_alignbit(hv[2], hv[1], 16);
                sh[3] = __builtin_amdgcn_alignbit(hv[3], hv[2], 16);
                *(LAS u32x4*)(cv + CV_HB + off) = sh;
            }
        }
        const int r1 = lane & 15, q = lane >> 4;
        const int toff = (r1 & 1) ? CV_HA + 2 * (16 + r1 + 9 - 8 * q) : CV_HB + 2 * (16 + r1 + 10 - 8 * q);
        const int boff = CV_KV + 448 + 32 * r1 + 16 * q;
        f32x4 y[8];
#pragma unroll
        for (int t = 0; t < 8; ++t) y[t] = f32x4{};
#pragma unroll 1
        for (int bb = 0; bb < 8; ++bb) {
            WAIT_BAR(4);
            issue(7 + bb);
            bf16x8 tf[8], bf[8];
#pragma unroll
            for (int a = 0; a < 8; ++a) {
                const LAS unsigned* tp = (const LAS unsigned*)(cv + toff + 64 * bb + 512 * a);
                u32x4 t4 = {tp[0], tp[1], tp[2], tp[3]};
                tf[a] = __builtin_bit_cast(bf16x8, t4);
                bf[a] = lds_rd128(cv + boff - 64 * bb + 512 * a);
            }
#pragma unroll
            for (int a = 0; a < 8; ++a)
#pragma unroll
                for (int ww = 0; ww + a < 8; ++ww) y[a + ww] = MFMA16(tf[a], bf[ww], y[a + ww]);
        }
        WAIT_BAR(4);
        issue(15);
        WAIT_BAR(4);
        WAIT_BAR(0);
        DG(4);
        const float dch = __uint_as_float((unsigned)ssmD[ch] << 16);
        char* zrow = ws + WS_ZT + (size_t)ch * 4096;
#pragma unroll
        for (int t = 0; t < 8; ++t) {
            const u32x2 kk = *(const LAS u32x2*)(cv + CV_KV + 480 + 512 * t + 32 * r1 + 16 * (q >> 1) + ((q & 1) ? 0 : 8));
            float k0 = bfhi(kk[1]) * dch, k1 = bflo(kk[1]) * dch, k2 = bfhi(kk[0]) * dch, k3 = bflo(kk[0]) * dch;
            rnd2(k0, k1); rnd2(k2, k3);
            float y0 = y[t][0], y1 = y[t][1], y2 = y[t][2], y3 = y[t][3];
            rnd2(y0, y1); rnd2(y2, y3);
            u32x2 o = {cvtpk(y0 + k0, y1 + k1), cvtpk(y2 + k2, y3 + k3)};
            *(u32x2*)(zrow + 512 * t + 32 * r1 + 8 * q) = o;
        }
        DG_END(5);
        return;
    }

    const int T = qb * 8 + w;
    const char* qg = ws + WS_Q + (size_t)(h * 64 + T) * 2048 + lane * 16;
    const bf16x8 qf0 = *(const bf16x8*)qg, qf1 = *(const bf16x8*)(qg + 1024);
    bf16x8 sel;
    {
        const short one = (((lane >> 2) & 1) == ((lane >> 4) & 1)) ? (short)0x3f80 : (short)0;
#pragma unroll
        for (int i = 0; i < 8; ++i) sel[i] = one;
    }
    f32x16 o = f32x16{};
    f32x4 lsum = f32x4{};
    f32x16 negm, sA, sB;
    const lds_ptr lp = lds + lane * 16;
    bf16x8 k0, k1, v0, v1;
    u32x4 p0, p1 = {0u, 0u, 0u, 0u};
    LGKM_BAR();
    DG(3);
    {
        k0 = lds_rd128(lp); k1 = lds_rd128(lp + 1024);
        f32x16 s = MFMA32(k0, qf0, f32x16{});
        s = MFMA32(k1, qf1, s);
        float m = s[0];
#pragma unroll
        for (int i = 1; i < 16; ++i) m = __builtin_fmaxf(m, s[i]);
        auto rr = __builtin_amdgcn_permlane32_swap(__float_as_uint(m), __float_as_uint(m), false, false);
        m = __builtin_fmaxf(__uint_as_float(rr[0]), __uint_as_float(rr[1]));
#pragma unroll
        for (int i = 0; i < 16; ++i) { negm[i] = -m; sA[i] = s[i] - m; }
        k0 = lds_rd128(lp + 4096); k1 = lds_rd128(lp + 4096 + 1024);
        v0 = lds_rd128(lp + 2048); v1 = v0;
        sA[0] = __builtin_amdgcn_exp2f(sA[0]); sA[1] = __builtin_amdgcn_exp2f(sA[1]);
    }
#define EX(X, i) X[i] = __builtin_amdgcn_exp2f(X[i])
#define PB(x) __builtin_bit_cast(bf16x8, x)
#define TILE(C, N, vc, vn, kn, BAR_HERE) do { \
        o = MFMA32(v1, PB(p1), o); SBAR(); \
        if (BAR_HERE) { RAW_BAR(); SBAR(); } \
        v1 = lds_rd128((vc) + 3072); EX(C, 2); EX(C, 3); EX(C, 4); SBAR(); \
        N = MFMA32(k0, qf0, negm); SBAR(); \
        k0 = lds_rd128(kn); EX(C, 5); EX(C, 6); EX(C, 7); SBAR(); \
        lsum = MFMA16(sel, PB(p1), lsum); SBAR(); \
        p0[0] = cvtpk(C[0], C[1]); p0[1] = cvtpk(C[2], C[3]); SBAR(); \
        N = MFMA32(k1, qf1, N); SBAR(); \
        k1 = lds_rd128((kn) + 1024); p0[2] = cvtpk(C[4], C[5]); p0[3] = cvtpk(C[6], C[7]); EX(C, 8); EX(C, 9); SBAR(); \
        o = MFMA32(v0, PB(p0), o); SBAR(); \
        v0 = lds_rd128((vn) + 2048); EX(C, 10); EX(C, 11); EX(C, 12); SBAR(); \
        lsum = MFMA16(sel, PB(p0), lsum); SBAR(); \
        EX(C, 13); EX(C, 14); EX(C, 15); \
        p1[0] = cvtpk(C[8], C[9]); p1[1] = cvtpk(C[10], C[11]); p1[2] = cvtpk(C[12], C[13]); p1[3] = cvtpk(C[14], C[15]); SBAR(); \
        EX(N, 0); EX(N, 1); SBAR(); \
    } while (0)
    SBAR();
#pragma unroll 1
    for (int st = 0; st < 16; ++st) {
        const lds_ptr cur = lp + (st & 3) * K2_SLOT;
        const lds_ptr nxt = lp + ((st + 1) & 3) * K2_SLOT;
        TILE(sA, sB, cur, cur + 4096, cur + 8192, true);
        TILE(sB, sA, cur + 4096, cur + 8192, cur + 12288, false);
        TILE(sA, sB, cur + 8192, cur + 12288, nxt, false);
        TILE(sB, sA, cur + 12288, nxt, nxt + 4096, false);
    }
    o = MFMA32(v1, PB(p1), o);
    lsum = MFMA16(sel, PB(p1), lsum);
    DG(4);
    float l = lsum[0];
    {
        float amax = __builtin_fabsf(o[0]);
#pragma unroll
        for (int i = 1; i < 16; ++i) amax = __builtin_fmaxf(amax, __builtin_fabsf(o[i]));
        bool nan = false;
#pragma unroll
        for (int i = 0; i < 16; ++i) nan |= (o[i] != o[i]);
        const bool ok = (l <= 1.0e30f) && (l > 0.f) && (amax <= 3.0e38f) && !nan;
        if (__builtin_expect(__any(!ok), 0)) {
            float m = -INFINITY, ls = 0.f;
            o = f32x16{};
#pragma unroll 1
            for (int t = 0; t < 64; ++t) {
                const char* g = kvg + t * 4096 + lane * 16;
                const bf16x8 a0 = *(const bf16x8*)g, a1 = *(const bf16x8*)(g + 1024);
                const bf16x8 b0 = *(const bf16x8*)(g + 2048), b1 = *(const bf16x8*)(g + 3072);
                f32x16 s = MFMA32(a0, qf0, f32x16{});
                s = MFMA32(a1, qf1, s);
                float tm = s[0];
#pragma unroll
                for (int i = 1; i < 16; ++i) tm = __builtin_fmaxf(tm, s[i]);
                auto rr = __builtin_amdgcn_permlane32_swap(__float_as_uint(tm), __float_as_uint(tm), false, false);
                tm = __builtin_fmaxf(__uint_as_float(rr[0]), __uint_as_float(rr[1]));
                const float mn = __builtin_fmaxf(m, tm);
                const float alpha = __builtin_amdgcn_exp2f(m - mn);
                m = mn;
                float ps = 0.f;
#pragma unroll
                for (int i = 0; i < 16; ++i) { s[i] = __builtin_amdgcn_exp2f(s[i] - mn); ps += s[i]; o[i] *= alpha; }
                ls = ls * alpha + ps;
                o = MFMA32(b0, PB(pack8n(s, 0)), o);
                o = MFMA32(b1, PB(pack8n(s, 1)), o);
            }
            auto rr = __builtin_amdgcn_permlane32_swap(__float_as_uint(ls), __float_as_uint(ls), false, false);
            l = __uint_as_float(rr[0]) + __uint_as_float(rr[1]);
        }
    }
    const float rl = 1.0f / l;
    char* og = ws + WS_O + (size_t)(T * 32 + h) * 2048 + lane * 16;
    *(u32x4*)og = pack8(o, 0, rl);
    *(u32x4*)(og + 1024) = pack8(o, 1, rl);
    DG_END(5);
}

__global__ __launch_bounds__(512, 2) void k3_out(const char* __restrict__ ws, const char* __restrict__ Wo,
                                                 const unsigned* __restrict__ Aq, char* __restrict__ out) {
    __shared__ __attribute__((aligned(16))) char lds_raw[131072];
    const lds_ptr lds = (lds_ptr)lds_raw;
    const unsigned lds0 = (unsigned)(uintptr_t)lds;
    const int tid = threadIdx.x, lane = tid & 63;
    const int w = __builtin_amdgcn_readfirstlane(tid >> 6);
    const int b = blockIdx.x, xcd = b & 7, idx = b >> 3;
    const int tt = (xcd >> 1) * 4 + (idx >> 3), nt = (xcd & 1) * 8 + (idx & 7);
    DG_DECL(2)

    if (w >= 4) {
        const int lw = w - 4;
        const int ch = (lane & 7) ^ (((lane >> 4) & 1) << 2);
        const unsigned voff = (unsigned)(lane >> 3) * 2048u + nt * 128 + ch * 16;
#pragma unroll
        for (int i = 0; i < 32; ++i) {
            const int piece = i * 4 + lw;
            glds16(voff, Wo + (size_t)piece * 16384, lds0 + piece * 1024);
        }
        WAIT_BAR(28); WAIT_BAR(24); WAIT_BAR(20); WAIT_BAR(16);
        WAIT_BAR(12); WAIT_BAR(8); WAIT_BAR(4); WAIT_BAR(0);
        DG_END(3);
        return;
    }

    const int T = tt * 4 + w;
    const int r = lane & 31, hh = lane >> 5;
    const int s0 = (b >> 3) * 64 + w * 16, cp = (b & 7) * 64 + lane;
    const char* zs = ws + WS_ZT + (size_t)(2 * cp) * 4096 + s0 * 2;
    u32x4 za[2], zb[2];
    za[0] = *(const u32x4*)zs; za[1] = *(const u32x4*)(zs + 16);
    zb[0] = *(const u32x4*)(zs + 4096); zb[1] = *(const u32x4*)(zs + 4096 + 16);
    unsigned qr[16];
#pragma unroll
    for (int i = 0; i < 16; ++i) qr[i] = Aq[(size_t)(s0 + i) * 512 + cp];
    SBAR();
    const char* og = ws + WS_O + (size_t)T * 65536 + lane * 16;
    bf16x8 of[32];
#pragma unroll
    for (int i = 0; i < 32; ++i) of[i] = *(const bf16x8*)(og + i * 1024);
    SBAR();
#pragma unroll
    for (int i = 0; i < 16; ++i) {
        const unsigned ua = za[i >> 3][(i >> 1) & 3], ub = zb[i >> 3][(i >> 1) & 3];
        const float a = (i & 1) ? bfhi(ua) : bflo(ua);
        const float c = (i & 1) ? bfhi(ub) : bflo(ub);
        *(unsigned*)(out + (size_t)(s0 + i) * 4096 + cp * 4) = cvtpk(bflo(qr[i]) * a, bfhi(qr[i]) * c);
    }
    SBAR();
    DG(3);
    const int g = lane >> 4, q = (lane & 15) >> 2, p = lane & 3;
    int woff[2];
#pragma unroll
    for (int n = 0; n < 2; ++n) {
        const int c = 4 * n + 2 * (p & 1) + (g & 1);
        woff[n] = (4 * hh + q) * 128 + ((c ^ ((q >> 1) << 2)) << 4) + (p >> 1) * 8;
    }
    f32x16 acc[2] = {f32x16{}, f32x16{}};
#pragma unroll
    for (int c = 0; c < 8; ++c) {
        RAW_BAR();
        SBAR();
#pragma unroll
        for (int i = 0; i < 8; ++i) {
            const int ks = c * 8 + i;
#pragma unroll
            for (int n = 0; n < 2; ++n) {
                const lds_ptr wp = lds + woff[n] + ks * 2048;
                acc[n] = MFMA32(cat8(lds_tr(wp), lds_tr(wp + 1024)), of[ks & 31], acc[n]);
            }
        }
        SBAR();
        if (c < 4) {
#pragma unroll
            for (int i = 0; i < 8; ++i) of[c * 8 + i] = *(const bf16x8*)(og + (32 + c * 8 + i) * 1024);
        }
        SBAR();
    }
    DG(4);
    char* dst = out + (size_t)(T * 32 + r) * 4096 + 2048 + nt * 128 + hh * 32;
#pragma unroll
    for (int n = 0; n < 2; ++n) {
        *(u32x4*)(dst + n * 64) = pack8n(acc[n], 0);
        *(u32x4*)(dst + n * 64 + 16) = pack8n(acc[n], 1);
    }
    DG_END(5);
}

extern "C" void kernel_launch(void* const* d_in, const int* in_sizes, int n_in,
                              void* d_out, int out_size, void* d_ws, size_t ws_size,
                              hipStream_t stream) {
    const void* A_q = d_in[0];
    const void* A_k = d_in[1];
    const void* A_v = d_in[2];
    const void* A_shift = d_in[3];
    const void* A_ssmk = d_in[4];
    const void* A_D = d_in[5];
    const void* B_x = d_in[6];
    const void* B_wqkv = d_in[7];
    const void* B_wo = d_in[8];
    char* ws = (char*)d_ws;
    k1_qkv<<<256, 512, 0, stream>>>((const char*)B_x, (const char*)B_wqkv, ws, (const char*)A_k,
                                    (const char*)A_v, (const char*)A_shift);
    k2_attn<<<256, 768, 0, stream>>>(ws, (const char*)A_ssmk, (const unsigned short*)A_D);
    k3_out<<<256, 512, 0, stream>>>(ws, (const char*)B_wo, (const unsigned*)A_q, (char*)d_out);
}
```

```cpp
#include <hip/hip_runtime.h>
#include <hip/hip_bf16.h>
#include <stdint.h>

typedef short bf16x8 __attribute__((ext_vector_type(8)));
typedef short s16x4 __attribute__((ext_vector_type(4)));
typedef float f32x16 __attribute__((ext_vector_type(16)));
typedef float f32x4 __attribute__((ext_vector_type(4)));
typedef float f32x2 __attribute__((ext_vector_type(2)));
typedef unsigned u32x4 __attribute__((ext_vector_type(4)));
typedef unsigned u32x2 __attribute__((ext_vector_type(2)));
typedef __bf16 bf16x2_t __attribute__((ext_vector_type(2)));

#define LAS __attribute__((address_space(3)))
typedef LAS char* lds_ptr;
#define DI __device__ __forceinline__
#define SBAR() __builtin_amdgcn_sched_barrier(0)
#define MFMA32(a, b, c) __builtin_amdgcn_mfma_f32_32x32x16_bf16((a), (b), (c), 0, 0, 0)
#define MFMA16(a, b, c) __builtin_amdgcn_mfma_f32_16x16x32_bf16((a), (b), (c), 0, 0, 0)
#define WAIT_BAR(N) asm volatile("s_waitcnt vmcnt(" #N ") lgkmcnt(0)\n\ts_barrier" ::: "memory")
#define LGKM_BAR() asm volatile("s_waitcnt lgkmcnt(0)\n\ts_barrier" ::: "memory")

#define DG_DECL(K)
#define DG(i)
#define DG_END(i)

constexpr int S_LEN = 2048, DM = 1024, NH = 32;

constexpr size_t WS_Q = 0;
constexpr size_t WS_KV = 4u << 20;
constexpr size_t WS_O = 12u << 20;

DI unsigned cvtpk(float lo, float hi) {
    f32x2 v = {lo, hi};
    return __builtin_bit_cast(unsigned, __builtin_convertvector(v, bf16x2_t));
}
DI float bflo(unsigned u) { return __uint_as_float(u << 16); }
DI float bfhi(unsigned u) { return __uint_as_float(u & 0xffff0000u); }

DI void glds16(unsigned voff, const void* sbase, unsigned lds_dst) {
    unsigned sv;
    asm volatile("s_mov_b32 %0, m0\n\ts_mov_b32 m0, %3\n\ts_nop 0\n\tglobal_load_lds_dwordx4 %1, %2\n\ts_mov_b32 m0, %0"
                 : "=&s"(sv) : "v"(voff), "s"(sbase), "s"(lds_dst) : "memory");
}
DI bf16x8 lds_rd128(lds_ptr p) { return *(const LAS bf16x8*)p; }
DI s16x4 lds_tr(lds_ptr p) {
    return __builtin_bit_cast(s16x4, __builtin_amdgcn_ds_read_tr16_b64_v4i16((LAS s16x4*)p));
}
DI bf16x8 cat8(s16x4 a, s16x4 b) { return __builtin_shufflevector(a, b, 0, 1, 2, 3, 4, 5, 6, 7); }
DI u32x4 pack8(const f32x16& x, int s, float sc) {
    u32x4 p;
    p[0] = cvtpk(x[8 * s + 0] * sc, x[8 * s + 1] * sc);
    p[1] = cvtpk(x[8 * s + 2] * sc, x[8 * s + 3] * sc);
    p[2] = cvtpk(x[8 * s + 4] * sc, x[8 * s + 5] * sc);
    p[3] = cvtpk(x[8 * s + 6] * sc, x[8 * s + 7] * sc);
    return p;
}
DI u32x4 pack8n(const f32x16& x, int s) {
    u32x4 p;
    p[0] = cvtpk(x[8 * s + 0], x[8 * s + 1]);
    p[1] = cvtpk(x[8 * s + 2], x[8 * s + 3]);
    p[2] = cvtpk(x[8 * s + 4], x[8 * s + 5]);
    p[3] = cvtpk(x[8 * s + 6], x[8 * s + 7]);
    return p;
}
DI void rnd2(float& a, float& b) { unsigned u = cvtpk(a, b); a = bflo(u); b = bfhi(u); }

#define CONV_PACE()
#define RAW_BAR() asm volatile("s_barrier" ::: "memory")
DI unsigned gld32(unsigned voff, const void* sbase) {
    unsigned r;
    asm volatile("global_load_dword %0, %1, %2" : "=v"(r) : "v"(voff), "s"(sbase) : "memory");
    return r;
}
DI u32x2 gld64(unsigned voff, const void* sbase) {
    u32x2 r;
    asm volatile("global_load_dwordx2 %0, %1, %2" : "=v"(r) : "v"(voff), "s"(sbase) : "memory");
    return r;
}
DI float mul_sv(float s, float v) { float r; asm("v_mul_f32_e32 %0, %1, %2" : "=v"(r) : "s"(s), "v"(v)); return r; }
DI float mul_vv(float a, float b) { float r; asm("v_mul_f32_e32 %0, %1, %2" : "=v"(r) : "v"(a), "v"(b)); return r; }
DI void fmac_sv(float& acc, float s, float v) { asm("v_fmac_f32_e32 %0, %1, %2" : "+v"(acc) : "s"(s), "v"(v)); }
#define PRIO(x) do { __builtin_amdgcn_s_setprio(x); SBAR(); } while (0)
DI u32x4 gld128(unsigned voff, const void* sbase) {
    u32x4 r;
    asm volatile("global_load_dwordx4 %0, %1, %2" : "=v"(r) : "v"(voff), "s"(sbase) : "memory");
    return r;
}

constexpr int K1_STAGE = 40960;
__global__ __launch_bounds__(512, 2) void k1_qkv(const char* __restrict__ X, const char* __restrict__ W,
                                                 char* __restrict__ ws) {
    __shared__ __attribute__((aligned(16))) char lds_raw[4 * K1_STAGE];
    const lds_ptr lds = (lds_ptr)lds_raw;
    const unsigned lds0 = (unsigned)(uintptr_t)lds;
    const int tid = threadIdx.x, lane = tid & 63;
    const int w = __builtin_amdgcn_readfirstlane(tid >> 6);
    const int b = blockIdx.x, xcd = b & 7, idx = b >> 3;
    const int hp = (xcd >> 1) * 4 + (idx & 3), tb = (xcd & 1) * 8 + (idx >> 2);
    DG_DECL(0)

    if (w >= 4) {
        const int lw = w - 4;
        const int rot = (tb & 3) * 4;
        unsigned voffX[4], voffW[2];
#pragma unroll
        for (int pp = 0; pp < 4; ++pp) {
            const int row = 32 * lw + 8 * pp + (lane >> 3);
            const int ch = (lane & 7) ^ ((row >> 1) & 7);
            voffX[pp] = (unsigned)(tb * 128 + row) * 2048u + ch * 16;
        }
#pragma unroll
        for (int pp = 0; pp < 2; ++pp) {
            const int krow = 16 * lw + 8 * pp + (lane >> 3);
            const int ch = (lane & 7) ^ (((lane >> 4) & 1) << 2);
            voffW[pp] = (unsigned)krow * 6144u + hp * 128 + ch * 16;
        }
        auto issue = [&](int kt) {
            const int kr = (kt + rot) & 15;
            const char* xb = X + kr * 128;
            const char* wb = W + (size_t)kr * (64 * 6144);
            const unsigned st = lds0 + (kt & 3) * K1_STAGE;
#pragma unroll
            for (int pp = 0; pp < 4; ++pp) glds16(voffX[pp], xb, st + (4 * lw + pp) * 1024);
#pragma unroll
            for (int sec = 0; sec < 3; ++sec)
#pragma unroll
                for (int pp = 0; pp < 2; ++pp)
                    glds16(voffW[pp], wb + sec * 2048, st + 16384 + sec * 8192 + (2 * lw + pp) * 1024);
        };
        issue(0); issue(1); issue(2);
#pragma unroll 1
        for (int kt = 0; kt < 13; ++kt) {
            WAIT_BAR(20);
            issue(kt + 3);
        }
        WAIT_BAR(20);
        WAIT_BAR(10);
        WAIT_BAR(0);
        DG_END(5);
        return;
    }

    const int wm = w >> 1, wn = w & 1;
    const int r = lane & 31, hh = lane >> 5;
    const int g = lane >> 4, q = (lane & 15) >> 2, p = lane & 3;
    const int xoff0 = (64 * wm + r) * 128 + ((hh ^ ((r >> 1) & 7)) << 4);
    const int woff0 = 16384 + (8 * hh + q) * 128 + (((4 * wn + 2 * (g & 1) + (p >> 1)) ^ ((q >> 1) << 2)) << 4) + (p & 1) * 8;

    f32x16 acc[2][3];
#pragma unroll
    for (int m = 0; m < 2; ++m)
#pragma unroll
        for (int s = 0; s < 3; ++s) acc[m][s] = f32x16{};
    bf16x8 xf[2][2];
    s16x4 wl[2][3], wh[2][3];
#define K1_WF(f, sec) cat8(wl[f][sec], wh[f][sec])
#define K1_STEP(F, st, t, BAR_HERE) do { \
        const lds_ptr xp_ = (st) + (xoff0 ^ ((t) << 5)); const lds_ptr wp_ = (st) + woff0 + (t) * 2048; \
        acc[0][0] = MFMA32(K1_WF(F, 0), xf[F][0], acc[0][0]); SBAR(); \
        if (BAR_HERE) { LGKM_BAR(); SBAR(); } \
        xf[F ^ 1][0] = lds_rd128(xp_); wl[F ^ 1][0] = lds_tr(wp_); wh[F ^ 1][0] = lds_tr(wp_ + 512); SBAR(); \
        acc[0][1] = MFMA32(K1_WF(F, 1), xf[F][0], acc[0][1]); SBAR(); \
        wl[F ^ 1][1] = lds_tr(wp_ + 8192); wh[F ^ 1][1] = lds_tr(wp_ + 8192 + 512); SBAR(); \
        acc[0][2] = MFMA32(xf[F][0], K1_WF(F, 2), acc[0][2]); SBAR(); \
        wl[F ^ 1][2] = lds_tr(wp_ + 16384); wh[F ^ 1][2] = lds_tr(wp_ + 16384 + 512); xf[F ^ 1][1] = lds_rd128(xp_ + 4096); SBAR(); \
        acc[1][0] = MFMA32(K1_WF(F, 0), xf[F][1], acc[1][0]); SBAR(); \
        acc[1][1] = MFMA32(K1_WF(F, 1), xf[F][1], acc[1][1]); \
        acc[1][2] = MFMA32(xf[F][1], K1_WF(F, 2), acc[1][2]); SBAR(); \
    } while (0)

    LGKM_BAR();
    DG(3);
    {
        const lds_ptr xp_ = lds + xoff0; const lds_ptr wp_ = lds + woff0;
        xf[0][0] = lds_rd128(xp_); xf[0][1] = lds_rd128(xp_ + 4096);
#pragma unroll
        for (int sec = 0; sec < 3; ++sec) { wl[0][sec] = lds_tr(wp_ + sec * 8192); wh[0][sec] = lds_tr(wp_ + sec * 8192 + 512); }
    }
    SBAR();
#pragma unroll
    for (int kt = 0; kt < 15; ++kt) {
        const lds_ptr st = lds + (kt & 3) * K1_STAGE;
        const lds_ptr sn = lds + ((kt + 1) & 3) * K1_STAGE;
        K1_STEP(0, st, 1, false);
        K1_STEP(1, st, 2, false);
        K1_STEP(0, st, 3, false);
        K1_STEP(1, sn, 0, true);
    }
    {
        const lds_ptr st = lds + 3 * K1_STAGE;
        K1_STEP(0, st, 1, false);
        K1_STEP(1, st, 2, false);
        K1_STEP(0, st, 3, false);
#pragma unroll
        for (int m = 0; m < 2; ++m) {
            acc[m][1] = MFMA32(K1_WF(1, 1), xf[1][m], acc[m][1]);
            acc[m][2] = MFMA32(xf[1][m], K1_WF(1, 2), acc[m][2]);
            acc[m][0] = MFMA32(K1_WF(1, 0), xf[1][m], acc[m][0]);
        }
    }
    DG(4);
    {
        const int h = 2 * hp + wn;
        const float qscale = 0.17677669529663687f * 1.4426950408889634f;
#pragma unroll
        for (int m = 0; m < 2; ++m) {
            const int T = tb * 4 + wm * 2 + m;
            char* dq = ws + WS_Q + (size_t)(h * 64 + T) * 2048 + lane * 16;
            char* dk = ws + WS_KV + (size_t)(h * 64 + T) * 4096 + lane * 16;
            *(u32x4*)(dk) = pack8n(acc[m][1], 0);
            *(u32x4*)(dk + 1024) = pack8n(acc[m][1], 1);
            *(u32x4*)(dk + 2048) = pack8n(acc[m][2], 0);
            *(u32x4*)(dk + 3072) = pack8n(acc[m][2], 1);
            *(u32x4*)(dq) = pack8(acc[m][0], 0, qscale);
            *(u32x4*)(dq + 1024) = pack8(acc[m][0], 1, qscale);
        }
    }
    DG_END(5);
}

constexpr int K2_SLOT = 32768, K2_RING = 2 * K2_SLOT;
constexpr int CV_HA = 0, CV_HB = 4160, CV_KV = 8320, CV_BYTES = 8320 + 480 + 4096;
constexpr int K2_LX = K2_RING + 4 * CV_BYTES;
constexpr int K2_LDS = K2_LX + 2048;

__global__ __launch_bounds__(768, 3) void k2_attn(char* __restrict__ ws, const char* __restrict__ Aq,
                                                  const char* __restrict__ Ak, const char* __restrict__ Av,
                                                  const unsigned* __restrict__ shiftk, const char* __restrict__ ssmk,
                                                  const unsigned* __restrict__ ssmD, char* __restrict__ out) {
    __shared__ __attribute__((aligned(16))) char lds_raw[K2_LDS];
    const lds_ptr lds = (lds_ptr)lds_raw;
    const unsigned lds0 = (unsigned)(uintptr_t)lds;
    const int tid = threadIdx.x, lane = tid & 63;
    const int w = __builtin_amdgcn_readfirstlane(tid >> 6);
    const int b = blockIdx.x, xcd = b & 7, idx = b >> 3;
    const int h = xcd * 4 + (idx >> 3), qb = idx & 7;
    const char* kvg = ws + WS_KV + (size_t)h * 64 * 4096;
    DG_DECL(1)

    if (w >= 8) {
        const int cw = w - 8;
        const unsigned voff = lane * 16;
        auto issue = [&](int st) {
            const char* src = kvg + (st * 4 + cw) * 4096;
            const unsigned dst = lds0 + (st & 1) * K2_SLOT + cw * 4096;
#pragma unroll
            for (int i = 0; i < 4; ++i) glds16(voff, src + i * 1024, dst + i * 1024);
#pragma unroll
            for (int i = 0; i < 4; ++i) glds16(voff, src + 131072 + i * 1024, dst + 16384 + i * 1024);
        };
        issue(0); issue(1);
        const int cb = xcd * 32 + idx;
        const int ch = cb * 4 + cw;
        const lds_ptr cvb = lds + K2_RING;
        const lds_ptr cv = cvb + cw * CV_BYTES;
        {
            const char* hrow = ssmk + (size_t)ch * 4096;
            const unsigned cv0 = lds0 + K2_RING + cw * CV_BYTES;
#pragma unroll
            for (int i = 0; i < 4; ++i) glds16(voff, hrow + i * 1024, cv0 + CV_HA + 32 + i * 1024);
        }
        const int g8 = cw * 64 + lane;
        const unsigned roff = (unsigned)g8 * 16384u + cb * 8;
        u32x2 kr[11], vr[8];
#pragma unroll
        for (int i = 0; i < 3; ++i) kr[i] = gld64(g8 == 0 ? roff : roff + (i - 3) * 2048, Ak);
#pragma unroll
        for (int i = 3; i < 11; ++i) kr[i] = gld64(roff, Ak + (i - 3) * 2048);
#pragma unroll
        for (int i = 0; i < 8; ++i) vr[i] = gld64(roff, Av + i * 2048);
        {
            const u32x4 z4 = {0u, 0u, 0u, 0u};
            if (lane < 2) { *(LAS u32x4*)(cv + CV_HA + lane * 16) = z4; *(LAS u32x4*)(cv + CV_HB + lane * 16) = z4; }
            if (lane < 30) *(LAS u32x4*)(cv + CV_KV + lane * 16) = z4;
        }
        WAIT_BAR(31);
        DG(3);
        WAIT_BAR(23);
        issue(2);
        asm volatile("s_waitcnt vmcnt(8)" ::: "memory");
        SBAR();
#pragma unroll
        for (int i = 0; i < 11; ++i) asm volatile("" : "+v"(kr[i]));
#pragma unroll
        for (int i = 0; i < 8; ++i) asm volatile("" : "+v"(vr[i]));
        DG(6);
        {
#pragma unroll
            for (int i = 0; i < 4; ++i) {
                const int off = 32 + i * 1024 + lane * 16;
                const u32x4 hv = *(const LAS u32x4*)(cv + CV_HA + off);
                const unsigned pv = *(const LAS unsigned*)(cv + CV_HA + off - 4);
                u32x4 sh;
                sh[0] = __builtin_amdgcn_alignbit(hv[0], pv, 16);
                sh[1] = __builtin_amdgcn_alignbit(hv[1], hv[0], 16);
                sh[2] = __builtin_amdgcn_alignbit(hv[2], hv[1], 16);
                sh[3] = __builtin_amdgcn_alignbit(hv[3], hv[2], 16);
                *(LAS u32x4*)(cv + CV_HB + off) = sh;
            }
        }
        {
            if (g8 == 0) { kr[0] = u32x2{0u, 0u}; kr[1] = u32x2{0u, 0u}; kr[2] = u32x2{0u, 0u}; }
#pragma unroll
            for (int c = 0; c < 4; ++c) {
                const unsigned s01 = shiftk[cb * 8 + c * 2], s23 = shiftk[cb * 8 + c * 2 + 1];
                const float w0 = bflo(s01), w1 = bfhi(s01), w2 = bflo(s23), w3 = bfhi(s23);
                float kf[11], kv[8];
#pragma unroll
                for (int i = 0; i < 11; ++i) kf[i] = (c & 1) ? bfhi(kr[i][c >> 1]) : bflo(kr[i][c >> 1]);
#pragma unroll
                for (int i = 0; i < 8; ++i) {
                    float a = mul_sv(w3, kf[i]);
                    fmac_sv(a, w2, kf[i + 1]); fmac_sv(a, w1, kf[i + 2]); fmac_sv(a, w0, kf[i + 3]);
                    kv[i] = mul_vv(a, (c & 1) ? bfhi(vr[i][c >> 1]) : bflo(vr[i][c >> 1]));
                }
                u32x4 o4;
#pragma unroll
                for (int i = 0; i < 4; ++i) o4[i] = cvtpk(kv[7 - 2 * i], kv[6 - 2 * i]);
                *(LAS u32x4*)(cvb + c * CV_BYTES + CV_KV + 480 + g8 * 16) = o4;
            }
        }
        DG(7);
        const int r1 = lane & 15, q = lane >> 4;
        const int toff = (r1 & 1) ? CV_HA + 2 * (16 + r1 + 9 - 8 * q) : CV_HB + 2 * (16 + r1 + 10 - 8 * q);
        const int boff = CV_KV + 448 + 32 * r1 + 16 * q;
        f32x4 y[8];
#pragma unroll
        for (int t = 0; t < 8; ++t) y[t] = f32x4{};
        auto group = [&](int bb) {
            bf16x8 tf[8], bf[8];
#pragma unroll
            for (int a = 0; a < 8; ++a) {
                const LAS unsigned* tp = (const LAS unsigned*)(cv + toff + 64 * bb + 512 * a);
                u32x4 t4 = {tp[0], tp[1], tp[2], tp[3]};
                tf[a] = __builtin_bit_cast(bf16x8, t4);
                bf[a] = lds_rd128(cv + boff - 64 * bb + 512 * a);
            }
#pragma unroll
            for (int a = 0; a < 8; ++a)
#pragma unroll
                for (int ww = 0; ww + a < 8; ++ww) { y[a + ww] = MFMA16(tf[a], bf[ww], y[a + ww]); CONV_PACE(); }
        };
#pragma unroll 1
        for (int it = 0; it < 3; ++it) {
            WAIT_BAR(0);
            issue(3 + it);
#pragma unroll 1
            for (int j = 0; j < 2; ++j) group(2 * it + j);
        }
        WAIT_BAR(0);
        issue(6);
        group(6);
        WAIT_BAR(0);
        issue(7);
        u32x2 qr[8];
#pragma unroll
        for (int i = 0; i < 8; ++i) qr[i] = gld64(roff, Aq + i * 2048);
        group(7);
        DG(4);
        {
            const unsigned dd = ssmD[ch >> 1];
            const float dch = (ch & 1) ? bfhi(dd) : bflo(dd);
#pragma unroll
            for (int t = 0; t < 8; ++t) {
                const u32x2 kk = *(const LAS u32x2*)(cv + CV_KV + 480 + 512 * t + 32 * r1 + 16 * (q >> 1) + ((q & 1) ? 0 : 8));
                float k0 = mul_sv(dch, bfhi(kk[1])), k1 = mul_sv(dch, bflo(kk[1]));
                float k2 = mul_sv(dch, bfhi(kk[0])), k3 = mul_sv(dch, bflo(kk[0]));
                rnd2(k0, k1); rnd2(k2, k3);
                float y0 = y[t][0], y1 = y[t][1], y2 = y[t][2], y3 = y[t][3];
                rnd2(y0, y1); rnd2(y2, y3);
                u32x2 o = {cvtpk(y0 + k0, y1 + k1), cvtpk(y2 + k2, y3 + k3)};
                *(LAS u32x2*)(cv + CV_HA + 512 * t + 32 * r1 + 8 * q) = o;
            }
        }
        WAIT_BAR(0);
        SBAR();
#pragma unroll
        for (int i = 0; i < 8; ++i) asm volatile("" : "+v"(qr[i]));
        {
            u32x4 zc[4];
#pragma unroll
            for (int c = 0; c < 4; ++c) zc[c] = *(const LAS u32x4*)(cvb + c * CV_BYTES + CV_HA + g8 * 16);
            char* dst = out + (size_t)g8 * 32768 + cb * 8;
#pragma unroll
            for (int i = 0; i < 8; ++i) {
                float zz[4];
#pragma unroll
                for (int c = 0; c < 4; ++c) zz[c] = (i & 1) ? bfhi(zc[c][i >> 1]) : bflo(zc[c][i >> 1]);
                u32x2 o = {cvtpk(mul_vv(bflo(qr[i][0]), zz[0]), mul_vv(bfhi(qr[i][0]), zz[1])),
                           cvtpk(mul_vv(bflo(qr[i][1]), zz[2]), mul_vv(bfhi(qr[i][1]), zz[3]))};
                *(u32x2*)(dst + i * 4096) = o;
            }
        }
        DG_END(5);
        return;
    }

    const int half = w >> 2;
    const int TA = qb * 8 + (w & 3) * 2 + half, TB = TA ^ 1;
    const char* qga = ws + WS_Q + (size_t)(h * 64 + TA) * 2048 + lane * 16;
    const char* qgb = ws + WS_Q + (size_t)(h * 64 + TB) * 2048 + lane * 16;
    const bf16x8 qa0 = *(const bf16x8*)qga, qa1 = *(const bf16x8*)(qga + 1024);
    const bf16x8 qb0 = *(const bf16x8*)qgb, qb1 = *(const bf16x8*)(qgb + 1024);
    bf16x8 sel;
    {
        const short one = (((lane >> 2) & 1) == ((lane >> 4) & 1)) ? (short)0x3f80 : (short)0;
#pragma unroll
        for (int i = 0; i < 8; ++i) sel[i] = one;
    }
    f32x16 oA = f32x16{}, oB = f32x16{};
    f32x4 lA = f32x4{}, lB = f32x4{};
    f32x16 negm, sA, sB;
    const lds_ptr lp = lds + half * 16384 + lane * 16;
    bf16x8 k0[2], k1[2], v0[2], v1[2];
    u32x4 p0, p1 = {0u, 0u, 0u, 0u};
#define EX(X, i) X[i] = __builtin_amdgcn_exp2f(X[i])
#define PB(x) __builtin_bit_cast(bf16x8, x)
    LGKM_BAR();
    DG(3);
    {
        const bf16x8 x0 = lds_rd128(lds + lane * 16), x1 = lds_rd128(lds + lane * 16 + 1024);
        k0[0] = lds_rd128(lp); k1[0] = lds_rd128(lp + 1024);
        v0[0] = lds_rd128(lp + 2048); v1[0] = lds_rd128(lp + 3072);
        sA = MFMA32(x0, qa0, f32x16{});
        sB = MFMA32(x0, qb0, f32x16{});
        sA = MFMA32(x1, qa1, sA);
        sB = MFMA32(x1, qb1, sB);
        float m = __builtin_fmaxf(sA[0], sB[0]);
#pragma unroll
        for (int i = 1; i < 16; ++i) m = __builtin_fmaxf(m, __builtin_fmaxf(sA[i], sB[i]));
        auto rr = __builtin_amdgcn_permlane32_swap(__float_as_uint(m), __float_as_uint(m), false, false);
        m = __builtin_fmaxf(__uint_as_float(rr[0]), __uint_as_float(rr[1]));
#pragma unroll
        for (int i = 0; i < 16; ++i) negm[i] = -m;
        sA = MFMA32(k0[0], qa0, negm);
        sA = MFMA32(k1[0], qa1, sA);
        v1[1] = v1[0];
        EX(sA, 0); EX(sA, 1);
    }
#define SUM16(l, p) asm volatile("v_mfma_f32_16x16x32_bf16 %0, %1, %2, %0" : "+v"(l) : "v"(sel), "v"(p))
#define HALF(C, oC, lC, N, qx, qy, ka, kb, oP, lP, vprev, vcur0, PRE) do { \
        oP = MFMA32(vprev, PB(p1), oP); SBAR(); \
        PRE; EX(C, 2); EX(C, 3); EX(C, 4); SBAR(); \
        N = MFMA32(ka, qx, negm); SBAR(); \
        EX(C, 5); EX(C, 6); EX(C, 7); SBAR(); \
        SUM16(lP, p1); SBAR(); \
        p0[0] = cvtpk(C[0], C[1]); p0[1] = cvtpk(C[2], C[3]); SBAR(); \
        N = MFMA32(kb, qy, N); SBAR(); \
        p0[2] = cvtpk(C[4], C[5]); p0[3] = cvtpk(C[6], C[7]); SBAR(); EX(C, 8); EX(C, 9); SBAR(); \
        oC = MFMA32(vcur0, PB(p0), oC); SBAR(); \
        EX(C, 10); EX(C, 11); EX(C, 12); SBAR(); \
        SUM16(lC, p0); SBAR(); \
        EX(C, 13); EX(C, 14); EX(C, 15); SBAR(); \
        p1[0] = cvtpk(C[8], C[9]); p1[1] = cvtpk(C[10], C[11]); p1[2] = cvtpk(C[12], C[13]); p1[3] = cvtpk(C[14], C[15]); SBAR(); \
        EX(N, 0); EX(N, 1); SBAR(); \
    } while (0)
#define RD4(Y, nx) do { v1[Y] = lds_rd128((nx) + 3072); v0[Y] = lds_rd128((nx) + 2048); \
        k1[Y] = lds_rd128((nx) + 1024); k0[Y] = lds_rd128(nx); } while (0)
#define KT(X, nx, PRE0) do { \
        HALF(sA, oA, lA, sB, qb0, qb1, k0[X], k1[X], oB, lB, v1[X ^ 1], v0[X], PRE0; RD4(X ^ 1, nx)); \
        HALF(sB, oB, lB, sA, qa0, qa1, k0[X ^ 1], k1[X ^ 1], oA, lA, v1[X], v0[X], (void)0); \
    } while (0)
    SBAR();
#pragma unroll 1
    for (int st = 0; st < 8; ++st) {
        const lds_ptr cur = lp + (st & 1) * K2_SLOT;
        const lds_ptr nxt = lp + ((st + 1) & 1) * K2_SLOT;
        PRIO(2);
        KT(0, cur + 4096, (void)0);
        PRIO(1);
        KT(1, cur + 8192, (void)0);
        PRIO(0);
        KT(0, cur + 12288, (void)0);
        PRIO(3);
        KT(1, nxt, if (st < 7) RAW_BAR(); SBAR());
    }
    oB = MFMA32(v1[1], PB(p1), oB);
    SBAR();
    SUM16(lB, p1);
    asm volatile("s_nop 7\n\ts_nop 7" ::: "memory");
    SBAR();
    DG(4);
    {
        const lds_ptr xo = lds + w * 4096 + lane * 16;
#pragma unroll
        for (int i = 0; i < 4; ++i) *(LAS f32x4*)(xo + i * 1024) = f32x4{oB[4 * i], oB[4 * i + 1], oB[4 * i + 2], oB[4 * i + 3]};
        *(LAS float*)(lds + K2_LX + w * 256 + lane * 4) = lB[0];
        LGKM_BAR();
        const lds_ptr xi = lds + (w ^ 4) * 4096 + lane * 16;
#pragma unroll
        for (int i = 0; i < 4; ++i) {
            const f32x4 t = *(const LAS f32x4*)(xi + i * 1024);
#pragma unroll
            for (int j = 0; j < 4; ++j) oA[4 * i + j] += t[j];
        }
        lA[0] += *(const LAS float*)(lds + K2_LX + (w ^ 4) * 256 + lane * 4);
    }
    float l = lA[0];
    {
        float amax = __builtin_fabsf(oA[0]);
#pragma unroll
        for (int i = 1; i < 16; ++i) amax = __builtin_fmaxf(amax, __builtin_fabsf(oA[i]));
        bool nan = false;
#pragma unroll
        for (int i = 0; i < 16; ++i) nan |= (oA[i] != oA[i]);
        const bool ok = (l <= 1.0e30f) && (l >= 1.0e-18f) && (amax <= 3.0e38f) && !nan;
        if (__builtin_expect(__any(!ok), 0)) {
            float m = -INFINITY, ls = 0.f;
            oA = f32x16{};
#pragma unroll 1
            for (int t = 0; t < 64; ++t) {
                const char* g = kvg + t * 4096 + lane * 16;
                const bf16x8 a0 = *(const bf16x8*)g, a1 = *(const bf16x8*)(g + 1024);
                const bf16x8 b0 = *(const bf16x8*)(g + 2048), b1 = *(const bf16x8*)(g + 3072);
                f32x16 s = MFMA32(a0, qa0, f32x16{});
                s = MFMA32(a1, qa1, s);
                float tm = s[0];
#pragma unroll
                for (int i = 1; i < 16; ++i) tm = __builtin_fmaxf(tm, s[i]);
                auto rr = __builtin_amdgcn_permlane32_swap(__float_as_uint(tm), __float_as_uint(tm), false, false);
                tm = __builtin_fmaxf(__uint_as_float(rr[0]), __uint_as_float(rr[1]));
                const float mn = __builtin_fmaxf(m, tm);
                const float alpha = __builtin_amdgcn_exp2f(m - mn);
                m = mn;
                float ps = 0.f;
#pragma unroll
                for (int i = 0; i < 16; ++i) { s[i] = __builtin_amdgcn_exp2f(s[i] - mn); ps += s[i]; oA[i] *= alpha; }
                ls = ls * alpha + ps;
                oA = MFMA32(b0, PB(pack8n(s, 0)), oA);
                oA = MFMA32(b1, PB(pack8n(s, 1)), oA);
            }
            auto rr = __builtin_amdgcn_permlane32_swap(__float_as_uint(ls), __float_as_uint(ls), false, false);
            l = __uint_as_float(rr[0]) + __uint_as_float(rr[1]);
        }
    }
    const float rl = 1.0f / l;
    char* og = ws + WS_O + (size_t)(TA * 32 + h) * 2048 + lane * 16;
    *(u32x4*)og = pack8(oA, 0, rl);
    *(u32x4*)(og + 1024) = pack8(oA, 1, rl);
    DG_END(5);
}

__global__ __launch_bounds__(512, 2) void k3_out(const char* __restrict__ ws, const char* __restrict__ Wo,
                                                 char* __restrict__ out) {
    __shared__ __attribute__((aligned(16))) char lds_raw[131072];
    const lds_ptr lds = (lds_ptr)lds_raw;
    const unsigned lds0 = (unsigned)(uintptr_t)lds;
    const int tid = threadIdx.x, lane = tid & 63;
    const int w = __builtin_amdgcn_readfirstlane(tid >> 6);
    const int b = blockIdx.x, xcd = b & 7, idx = b >> 3;
    const int tt = (xcd >> 1) * 4 + (idx >> 3), nt = (xcd & 1) * 8 + (idx & 7);
    DG_DECL(2)

    if (w >= 4) {
        const int lw = w - 4;
        const int ch = (lane & 7) ^ (((lane >> 4) & 1) << 2);
        const unsigned voff = (unsigned)(lane >> 3) * 2048u + nt * 128 + ch * 16;
#pragma unroll
        for (int i = 0; i < 32; ++i) {
            const int piece = i * 4 + lw;
            glds16(voff, Wo + (size_t)piece * 16384, lds0 + piece * 1024);
        }
        WAIT_BAR(28); WAIT_BAR(24); WAIT_BAR(20); WAIT_BAR(16);
        WAIT_BAR(12); WAIT_BAR(8); WAIT_BAR(4); WAIT_BAR(0);
        DG_END(3);
        return;
    }

    const int T = tt * 4 + w;
    const int r = lane & 31, hh = lane >> 5;
    const char* og = ws + WS_O + (size_t)T * 65536 + lane * 16;
    bf16x8 of[32];
#pragma unroll
    for (int i = 0; i < 32; ++i) of[i] = *(const bf16x8*)(og + i * 1024);
    SBAR();
    DG(3);
    const int g = lane >> 4, q = (lane & 15) >> 2, p = lane & 3;
    int woff[2];
#pragma unroll
    for (int n = 0; n < 2; ++n) {
        const int c = 4 * n + 2 * (p & 1) + (g & 1);
        woff[n] = (4 * hh + q) * 128 + ((c ^ ((q >> 1) << 2)) << 4) + (p >> 1) * 8;
    }
    f32x16 acc[2] = {f32x16{}, f32x16{}};
#pragma unroll
    for (int c = 0; c < 8; ++c) {
        RAW_BAR();
        SBAR();
#pragma unroll
        for (int i = 0; i < 8; ++i) {
            const int ks = c * 8 + i;
#pragma unroll
            for (int n = 0; n < 2; ++n) {
                const lds_ptr wp = lds + woff[n] + ks * 2048;
                acc[n] = MFMA32(cat8(lds_tr(wp), lds_tr(wp + 1024)), of[ks & 31], acc[n]);
            }
        }
        SBAR();
        if (c < 4) {
#pragma unroll
            for (int i = 0; i < 8; ++i) of[c * 8 + i] = *(const bf16x8*)(og + (32 + c * 8 + i) * 1024);
        }
        SBAR();
    }
    DG(4);
    char* dst = out + (size_t)(T * 32 + r) * 4096 + 2048 + nt * 128 + hh * 32;
#pragma unroll
    for (int n = 0; n < 2; ++n) {
        *(u32x4*)(dst + n * 64) = pack8n(acc[n], 0);
        *(u32x4*)(dst + n * 64 + 16) = pack8n(acc[n], 1);
    }
    DG_END(5);
}

extern "C" void kernel_launch(void* const* d_in, const int* in_sizes, int n_in,
                              void* d_out, int out_size, void* d_ws, size_t ws_size,
                              hipStream_t stream) {
    const void* A_q = d_in[0];
    const void* A_k = d_in[1];
    const void* A_v = d_in[2];
    const void* A_shift = d_in[3];
    const void* A_ssmk = d_in[4];
    const void* A_D = d_in[5];
    const void* B_x = d_in[6];
    const void* B_wqkv = d_in[7];
    const void* B_wo = d_in[8];
    char* ws = (char*)d_ws;
    k1_qkv<<<256, 512, 0, stream>>>((const char*)B_x, (const char*)B_wqkv, ws);
    k2_attn<<<256, 768, 0, stream>>>(ws, (const char*)A_q, (const char*)A_k, (const char*)A_v, (const unsigned*)A_shift,
                                     (const char*)A_ssmk, (const unsigned*)A_D, (char*)d_out);
    k3_out<<<256, 512, 0, stream>>>(ws, (const char*)B_wo, (char*)d_out);
}
```

```cpp
#include <hip/hip_runtime.h>
#include <hip/hip_bf16.h>
#include <stdint.h>

typedef short bf16x8 __attribute__((ext_vector_type(8)));
typedef short s16x4 __attribute__((ext_vector_type(4)));
typedef float f32x16 __attribute__((ext_vector_type(16)));
typedef float f32x4 __attribute__((ext_vector_type(4)));
typedef float f32x2 __attribute__((ext_vector_type(2)));
typedef unsigned u32x4 __attribute__((ext_vector_type(4)));
typedef unsigned u32x2 __attribute__((ext_vector_type(2)));
typedef __bf16 bf16x2_t __attribute__((ext_vector_type(2)));

#define LAS __attribute__((address_space(3)))
typedef LAS char* lds_ptr;
#define DI __device__ __forceinline__
#define SBAR() __builtin_amdgcn_sched_barrier(0)
#define MFMA32(a, b, c) __builtin_amdgcn_mfma_f32_32x32x16_bf16((a), (b), (c), 0, 0, 0)
#define MFMA16(a, b, c) __builtin_amdgcn_mfma_f32_16x16x32_bf16((a), (b), (c), 0, 0, 0)
#define WAIT_BAR(N) asm volatile("s_waitcnt vmcnt(" #N ") lgkmcnt(0)\n\ts_barrier" ::: "memory")
#define LGKM_BAR() asm volatile("s_waitcnt lgkmcnt(0)\n\ts_barrier" ::: "memory")

#define DG_DECL(K)
#define DG(i)
#define DG_END(i)

constexpr int S_LEN = 2048, DM = 1024, NH = 32;

constexpr size_t WS_Q = 0;
constexpr size_t WS_KV = 4u << 20;
constexpr size_t WS_O = 12u << 20;
constexpr size_t WS_KVR = 16u << 20;
constexpr size_t WS_ZT = 20u << 20;

DI unsigned cvtpk(float lo, float hi) {
    f32x2 v = {lo, hi};
    return __builtin_bit_cast(unsigned, __builtin_convertvector(v, bf16x2_t));
}
DI float bflo(unsigned u) { return __uint_as_float(u << 16); }
DI float bfhi(unsigned u) { return __uint_as_float(u & 0xffff0000u); }

DI void glds16(unsigned voff, const void* sbase, unsigned lds_dst) {
    unsigned sv;
    asm volatile("s_mov_b32 %0, m0\n\ts_mov_b32 m0, %3\n\ts_nop 0\n\tglobal_load_lds_dwordx4 %1, %2\n\ts_mov_b32 m0, %0"
                 : "=&s"(sv) : "v"(voff), "s"(sbase), "s"(lds_dst) : "memory");
}
DI bf16x8 lds_rd128(lds_ptr p) { return *(const LAS bf16x8*)p; }
DI s16x4 lds_tr(lds_ptr p) {
    return __builtin_bit_cast(s16x4, __builtin_amdgcn_ds_read_tr16_b64_v4i16((LAS s16x4*)p));
}
DI bf16x8 cat8(s16x4 a, s16x4 b) { return __builtin_shufflevector(a, b, 0, 1, 2, 3, 4, 5, 6, 7); }
DI u32x4 pack8(const f32x16& x, int s, float sc) {
    u32x4 p;
    p[0] = cvtpk(x[8 * s + 0] * sc, x[8 * s + 1] * sc);
    p[1] = cvtpk(x[8 * s + 2] * sc, x[8 * s + 3] * sc);
    p[2] = cvtpk(x[8 * s + 4] * sc, x[8 * s + 5] * sc);
    p[3] = cvtpk(x[8 * s + 6] * sc, x[8 * s + 7] * sc);
    return p;
}
DI u32x4 pack8n(const f32x16& x, int s) {
    u32x4 p;
    p[0] = cvtpk(x[8 * s + 0], x[8 * s + 1]);
    p[1] = cvtpk(x[8 * s + 2], x[8 * s + 3]);
    p[2] = cvtpk(x[8 * s + 4], x[8 * s + 5]);
    p[3] = cvtpk(x[8 * s + 6], x[8 * s + 7]);
    return p;
}
DI void rnd2(float& a, float& b) { unsigned u = cvtpk(a, b); a = bflo(u); b = bfhi(u); }

#define CONV_PACE()
#define RAW_BAR() asm volatile("s_barrier" ::: "memory")
DI unsigned gld32(unsigned voff, const void* sbase) {
    unsigned r;
    asm volatile("global_load_dword %0, %1, %2" : "=v"(r) : "v"(voff), "s"(sbase) : "memory");
    return r;
}
DI u32x2 gld64(unsigned voff, const void* sbase) {
    u32x2 r;
    asm volatile("global_load_dwordx2 %0, %1, %2" : "=v"(r) : "v"(voff), "s"(sbase) : "memory");
    return r;
}
DI float mul_sv(float s, float v) { float r; asm("v_mul_f32_e32 %0, %1, %2" : "=v"(r) : "s"(s), "v"(v)); return r; }
DI float mul_vv(float a, float b) { float r; asm("v_mul_f32_e32 %0, %1, %2" : "=v"(r) : "v"(a), "v"(b)); return r; }
DI void fmac_vv(float& acc, float a, float b) { asm("v_fmac_f32_e32 %0, %1, %2" : "+v"(acc) : "v"(a), "v"(b)); }
DI void fmac_sv(float& acc, float s, float v) { asm("v_fmac_f32_e32 %0, %1, %2" : "+v"(acc) : "s"(s), "v"(v)); }
#define PRIO(x) do { __builtin_amdgcn_s_setprio(x); SBAR(); } while (0)
DI u32x4 gld128(unsigned voff, const void* sbase) {
    u32x4 r;
    asm volatile("global_load_dwordx4 %0, %1, %2" : "=v"(r) : "v"(voff), "s"(sbase) : "memory");
    return r;
}

constexpr int K1_STAGE = 40960;
__global__ __launch_bounds__(512, 2) void k1_qkv(const char* __restrict__ X, const char* __restrict__ W,
                                                 char* __restrict__ ws, const char* __restrict__ Ak,
                                                 const char* __restrict__ Av, const char* __restrict__ shiftk) {
    __shared__ __attribute__((aligned(16))) char lds_raw[4 * K1_STAGE];
    const lds_ptr lds = (lds_ptr)lds_raw;
    const unsigned lds0 = (unsigned)(uintptr_t)lds;
    const int tid = threadIdx.x, lane = tid & 63;
    const int w = __builtin_amdgcn_readfirstlane(tid >> 6);
    const int b = blockIdx.x, xcd = b & 7, idx = b >> 3;
    const int hp = (xcd >> 1) * 4 + (idx & 3), tb = (xcd & 1) * 8 + (idx >> 2);
    DG_DECL(0)

    if (w >= 4) {
        const int lw = w - 4;
        const int rot = (tb & 3) * 4;
        unsigned voffX[4], voffW[2];
#pragma unroll
        for (int pp = 0; pp < 4; ++pp) {
            const int row = 32 * lw + 8 * pp + (lane >> 3);
            const int ch = (lane & 7) ^ ((row >> 1) & 7);
            voffX[pp] = (unsigned)(tb * 128 + row) * 2048u + ch * 16;
        }
#pragma unroll
        for (int pp = 0; pp < 2; ++pp) {
            const int krow = 16 * lw + 8 * pp + (lane >> 3);
            const int ch = (lane & 7) ^ (((lane >> 4) & 1) << 2);
            voffW[pp] = (unsigned)krow * 6144u + hp * 128 + ch * 16;
        }
        auto issue = [&](int kt) {
            const int kr = (kt + rot) & 15;
            const char* xb = X + kr * 128;
            const char* wb = W + (size_t)kr * (64 * 6144);
            const unsigned st = lds0 + (kt & 3) * K1_STAGE;
#pragma unroll
            for (int pp = 0; pp < 4; ++pp) glds16(voffX[pp], xb, st + (4 * lw + pp) * 1024);
#pragma unroll
            for (int sec = 0; sec < 3; ++sec)
#pragma unroll
                for (int pp = 0; pp < 2; ++pp)
                    glds16(voffW[pp], wb + sec * 2048, st + 16384 + sec * 8192 + (2 * lw + pp) * 1024);
        };
        issue(0); issue(1); issue(2);
        WAIT_BAR(20); issue(3);
        WAIT_BAR(20); issue(4);
        WAIT_BAR(20); issue(5);
        const int g8 = xcd * 32 + idx, cq = lw * 64 + lane;
        const unsigned roff = (unsigned)g8 * 16384u + cq * 8;
        u32x2 kr[11], vr[8];
#pragma unroll
        for (int i = 0; i < 11; ++i) kr[i] = gld64(roff, Ak + ((g8 == 0 && i < 3) ? 0 : (i - 3) * 2048));
#pragma unroll
        for (int i = 0; i < 8; ++i) vr[i] = gld64(roff, Av + i * 2048);
        u32x4 sk[2];
        sk[0] = gld128((unsigned)cq * 32u, shiftk); sk[1] = gld128((unsigned)cq * 32u, shiftk + 16);
        WAIT_BAR(41); issue(6);
        WAIT_BAR(41); issue(7);
        WAIT_BAR(41); issue(8);
        WAIT_BAR(20); issue(9);
        SBAR();
#pragma unroll
        for (int i = 0; i < 11; ++i) asm volatile("" : "+v"(kr[i]));
#pragma unroll
        for (int i = 0; i < 8; ++i) asm volatile("" : "+v"(vr[i]));
        asm volatile("" : "+v"(sk[0]), "+v"(sk[1]));
        if (g8 == 0) { kr[0] = u32x2{0u, 0u}; kr[1] = u32x2{0u, 0u}; kr[2] = u32x2{0u, 0u}; }
        char* kvd = ws + WS_KVR + (size_t)cq * 16384 + g8 * 16;
#pragma unroll
        for (int c = 0; c < 4; ++c) {
            const unsigned s01 = sk[c >> 1][(c & 1) * 2], s23 = sk[c >> 1][(c & 1) * 2 + 1];
            const float w0 = bflo(s01), w1 = bfhi(s01), w2 = bflo(s23), w3 = bfhi(s23);
            float kf[11], kv[8];
#pragma unroll
            for (int i = 0; i < 11; ++i) kf[i] = (c & 1) ? bfhi(kr[i][c >> 1]) : bflo(kr[i][c >> 1]);
#pragma unroll
            for (int i = 0; i < 8; ++i) {
                float a = mul_vv(w3, kf[i]);
                fmac_vv(a, w2, kf[i + 1]); fmac_vv(a, w1, kf[i + 2]); fmac_vv(a, w0, kf[i + 3]);
                kv[i] = mul_vv(a, (c & 1) ? bfhi(vr[i][c >> 1]) : bflo(vr[i][c >> 1]));
            }
            u32x4 o4;
#pragma unroll
            for (int i = 0; i < 4; ++i) o4[i] = cvtpk(kv[7 - 2 * i], kv[6 - 2 * i]);
            *(u32x4*)(kvd + c * 4096) = o4;
            SBAR();
            WAIT_BAR(20); issue(10 + c);
            SBAR();
        }
        WAIT_BAR(20); issue(14);
        WAIT_BAR(20); issue(15);
        WAIT_BAR(20);
        WAIT_BAR(10);
        WAIT_BAR(0);
        DG_END(5);
        return;
    }

    const int wm = w >> 1, wn = w & 1;
    const int r = lane & 31, hh = lane >> 5;
    const int g = lane >> 4, q = (lane & 15) >> 2, p = lane & 3;
    const int xoff0 = (64 * wm + r) * 128 + ((hh ^ ((r >> 1) & 7)) << 4);
    const int woff0 = 16384 + (8 * hh + q) * 128 + (((4 * wn + 2 * (g & 1) + (p >> 1)) ^ ((q >> 1) << 2)) << 4) + (p & 1) * 8;

    f32x16 acc[2][3];
#pragma unroll
    for (int m = 0; m < 2; ++m)
#pragma unroll
        for (int s = 0; s < 3; ++s) acc[m][s] = f32x16{};
    bf16x8 xf[2][2];
    s16x4 wl[2][3], wh[2][3];
#define K1_WF(f, sec) cat8(wl[f][sec], wh[f][sec])
#define K1_STEP(F, st, t, BAR_HERE) do { \
        const lds_ptr xp_ = (st) + (xoff0 ^ ((t) << 5)); const lds_ptr wp_ = (st) + woff0 + (t) * 2048; \
        acc[0][0] = MFMA32(K1_WF(F, 0), xf[F][0], acc[0][0]); SBAR(); \
        if (BAR_HERE) { LGKM_BAR(); SBAR(); } \
        xf[F ^ 1][0] = lds_rd128(xp_); wl[F ^ 1][0] = lds_tr(wp_); wh[F ^ 1][0] = lds_tr(wp_ + 512); SBAR(); \
        acc[0][1] = MFMA32(K1_WF(F, 1), xf[F][0], acc[0][1]); SBAR(); \
        wl[F ^ 1][1] = lds_tr(wp_ + 8192); wh[F ^ 1][1] = lds_tr(wp_ + 8192 + 512); SBAR(); \
        acc[0][2] = MFMA32(xf[F][0], K1_WF(F, 2), acc[0][2]); SBAR(); \
        wl[F ^ 1][2] = lds_tr(wp_ + 16384); wh[F ^ 1][2] = lds_tr(wp_ + 16384 + 512); xf[F ^ 1][1] = lds_rd128(xp_ + 4096); SBAR(); \
        acc[1][0] = MFMA32(K1_WF(F, 0), xf[F][1], acc[1][0]); SBAR(); \
        acc[1][1] = MFMA32(K1_WF(F, 1), xf[F][1], acc[1][1]); \
        acc[1][2] = MFMA32(xf[F][1], K1_WF(F, 2), acc[1][2]); SBAR(); \
    } while (0)

    LGKM_BAR();
    DG(3);
    {
        const lds_ptr xp_ = lds + xoff0; const lds_ptr wp_ = lds + woff0;
        xf[0][0] = lds_rd128(xp_); xf[0][1] = lds_rd128(xp_ + 4096);
#pragma unroll
        for (int sec = 0; sec < 3; ++sec) { wl[0][sec] = lds_tr(wp_ + sec * 8192); wh[0][sec] = lds_tr(wp_ + sec * 8192 + 512); }
    }
    SBAR();
#pragma unroll
    for (int kt = 0; kt < 15; ++kt) {
        const lds_ptr st = lds + (kt & 3) * K1_STAGE;
        const lds_ptr sn = lds + ((kt + 1) & 3) * K1_STAGE;
        K1_STEP(0, st, 1, false);
        K1_STEP(1, st, 2, false);
        K1_STEP(0, st, 3, false);
        K1_STEP(1, sn, 0, true);
    }
    {
        const lds_ptr st = lds + 3 * K1_STAGE;
        K1_STEP(0, st, 1, false);
        K1_STEP(1, st, 2, false);
        K1_STEP(0, st, 3, false);
#pragma unroll
        for (int m = 0; m < 2; ++m) {
            acc[m][1] = MFMA32(K1_WF(1, 1), xf[1][m], acc[m][1]);
            acc[m][2] = MFMA32(xf[1][m], K1_WF(1, 2), acc[m][2]);
            acc[m][0] = MFMA32(K1_WF(1, 0), xf[1][m], acc[m][0]);
        }
    }
    DG(4);
    {
        const int h = 2 * hp + wn;
        const float qscale = 0.17677669529663687f * 1.4426950408889634f;
#pragma unroll
        for (int m = 0; m < 2; ++m) {
            const int T = tb * 4 + wm * 2 + m;
            char* dq = ws + WS_Q + (size_t)(h * 64 + T) * 2048 + lane * 16;
            char* dk = ws + WS_KV + (size_t)(h * 64 + T) * 4096 + lane * 16;
            *(u32x4*)(dk) = pack8n(acc[m][1], 0);
            *(u32x4*)(dk + 1024) = pack8n(acc[m][1], 1);
            *(u32x4*)(dk + 2048) = pack8n(acc[m][2], 0);
            *(u32x4*)(dk + 3072) = pack8n(acc[m][2], 1);
            *(u32x4*)(dq) = pack8(acc[m][0], 0, qscale);
            *(u32x4*)(dq + 1024) = pack8(acc[m][0], 1, qscale);
        }
    }
    DG_END(5);
}

constexpr int K2_SLOT = 32768, K2_RING = 2 * K2_SLOT;
constexpr int CV_HA = 0, CV_HB = 4160, CV_KV = 8320, CV_BYTES = 8320 + 480 + 4096;
constexpr int K2_LX = K2_RING + 4 * CV_BYTES;
constexpr int K2_LDS = K2_LX + 2048;

__global__ __launch_bounds__(768, 3) void k2_attn(char* __restrict__ ws, const char* __restrict__ ssmk,
                                                  const unsigned* __restrict__ ssmD) {
    __shared__ __attribute__((aligned(16))) char lds_raw[K2_LDS];
    const lds_ptr lds = (lds_ptr)lds_raw;
    const unsigned lds0 = (unsigned)(uintptr_t)lds;
    const int tid = threadIdx.x, lane = tid & 63;
    const int w = __builtin_amdgcn_readfirstlane(tid >> 6);
    const int b = blockIdx.x, xcd = b & 7, idx = b >> 3;
    const int h = xcd * 4 + (idx >> 3), qb = idx & 7;
    const char* kvg = ws + WS_KV + (size_t)h * 64 * 4096;
    DG_DECL(1)

    if (w >= 8) {
        const int cw = w - 8;
        const unsigned voff = lane * 16;
        auto issue = [&](int st) {
            const char* src = kvg + (st * 4 + cw) * 4096;
            const unsigned dst = lds0 + (st & 1) * K2_SLOT + cw * 4096;
#pragma unroll
            for (int i = 0; i < 4; ++i) glds16(voff, src + i * 1024, dst + i * 1024);
#pragma unroll
            for (int i = 0; i < 4; ++i) glds16(voff, src + 131072 + i * 1024, dst + 16384 + i * 1024);
        };
        issue(0); issue(1);
        const int ch = b * 4 + cw;
        const lds_ptr cv = lds + K2_RING + cw * CV_BYTES;
        {
            const char* hrow = ssmk + (size_t)ch * 4096;
            const char* kvrow = ws + WS_KVR + (size_t)ch * 4096;
            const unsigned cv0 = lds0 + K2_RING + cw * CV_BYTES;
#pragma unroll
            for (int i = 0; i < 4; ++i) glds16(voff, hrow + i * 1024, cv0 + CV_HA + 32 + i * 1024);
#pragma unroll
            for (int i = 0; i < 4; ++i) glds16(voff, kvrow + i * 1024, cv0 + CV_KV + 480 + i * 1024);
            const u32x4 z4 = {0u, 0u, 0u, 0u};
            if (lane < 2) { *(LAS u32x4*)(cv + CV_HA + lane * 16) = z4; *(LAS u32x4*)(cv + CV_HB + lane * 16) = z4; }
            if (lane < 30) *(LAS u32x4*)(cv + CV_KV + lane * 16) = z4;
        }
        WAIT_BAR(16);
        DG(3);
        asm volatile("s_waitcnt vmcnt(0)" ::: "memory");
        SBAR();
        DG(6);
        {
#pragma unroll
            for (int i = 0; i < 4; ++i) {
                const int off = 32 + i * 1024 + lane * 16;
                const u32x4 hv = *(const LAS u32x4*)(cv + CV_HA + off);
                const unsigned pv = *(const LAS unsigned*)(cv + CV_HA + off - 4);
                u32x4 sh;
                sh[0] = __builtin_amdgcn_alignbit(hv[0], pv, 16);
                sh[1] = __builtin_amdgcn_alignbit(hv[1], hv[0], 16);
                sh[2] = __builtin_amdgcn_alignbit(hv[2], hv[1], 16);
                sh[3] = __builtin_amdgcn_alignbit(hv[3], hv[2], 16);
                *(LAS u32x4*)(cv + CV_HB + off) = sh;
            }
        }
        DG(7);
        const int r1 = lane & 15, q = lane >> 4;
        const int toff = (r1 & 1) ? CV_HA + 2 * (16 + r1 + 9 - 8 * q) : CV_HB + 2 * (16 + r1 + 10 - 8 * q);
        const int boff = CV_KV + 448 + 32 * r1 + 16 * q;
        f32x4 y[8];
#pragma unroll
        for (int t = 0; t < 8; ++t) y[t] = f32x4{};
        auto group = [&](int bb) {
            bf16x8 tf[8], bf[8];
#pragma unroll
            for (int a = 0; a < 8; ++a) {
                const LAS unsigned* tp = (const LAS unsigned*)(cv + toff + 64 * bb + 512 * a);
                u32x4 t4 = {tp[0], tp[1], tp[2], tp[3]};
                tf[a] = __builtin_bit_cast(bf16x8, t4);
                bf[a] = lds_rd128(cv + boff - 64 * bb + 512 * a);
            }
#pragma unroll
            for (int a = 0; a < 8; ++a)
#pragma unroll
                for (int ww = 0; ww + a < 8; ++ww) { y[a + ww] = MFMA16(tf[a], bf[ww], y[a + ww]); CONV_PACE(); }
        };
        group(0);
#pragma unroll 1
        for (int it = 1; it < 7; ++it) {
            WAIT_BAR(0);
            issue(it + 1);
            group(it);
        }
        WAIT_BAR(0);
        group(7);
        DG(4);
        {
            const unsigned dd = ssmD[ch >> 1];
            const float dch = (ch & 1) ? bfhi(dd) : bflo(dd);
            char* zrow = ws + WS_ZT + (size_t)ch * 4096;
#pragma unroll
            for (int t = 0; t < 8; ++t) {
                const u32x2 kk = *(const LAS u32x2*)(cv + CV_KV + 480 + 512 * t + 32 * r1 + 16 * (q >> 1) + ((q & 1) ? 0 : 8));
                float k0 = mul_sv(dch, bfhi(kk[1])), k1 = mul_sv(dch, bflo(kk[1]));
                float k2 = mul_sv(dch, bfhi(kk[0])), k3 = mul_sv(dch, bflo(kk[0]));
                rnd2(k0, k1); rnd2(k2, k3);
                float y0 = y[t][0], y1 = y[t][1], y2 = y[t][2], y3 = y[t][3];
                rnd2(y0, y1); rnd2(y2, y3);
                u32x2 o = {cvtpk(y0 + k0, y1 + k1), cvtpk(y2 + k2, y3 + k3)};
                *(u32x2*)(zrow + 512 * t + 32 * r1 + 8 * q) = o;
            }
        }
        DG_END(5);
        return;
    }

    const int half = w >> 2;
    const int TA = qb * 8 + (w & 3) * 2 + half, TB = TA ^ 1;
    const char* qga = ws + WS_Q + (size_t)(h * 64 + TA) * 2048 + lane * 16;
    const char* qgb = ws + WS_Q + (size_t)(h * 64 + TB) * 2048 + lane * 16;
    const bf16x8 qa0 = *(const bf16x8*)qga, qa1 = *(const bf16x8*)(qga + 1024);
    const bf16x8 qb0 = *(const bf16x8*)qgb, qb1 = *(const bf16x8*)(qgb + 1024);
    bf16x8 sel;
    {
        const short one = (((lane >> 2) & 1) == ((lane >> 4) & 1)) ? (short)0x3f80 : (short)0;
#pragma unroll
        for (int i = 0; i < 8; ++i) sel[i] = one;
    }
    f32x16 oA = f32x16{}, oB = f32x16{};
    f32x4 lA = f32x4{}, lB = f32x4{};
    f32x16 negm, sA, sB;
    const lds_ptr lp = lds + half * 16384 + lane * 16;
    bf16x8 k0[2], k1[2], v0[2], v1[2];
    u32x4 p0, p1 = {0u, 0u, 0u, 0u};
#define EX(X, i) X[i] = __builtin_amdgcn_exp2f(X[i])
#define PB(x) __builtin_bit_cast(bf16x8, x)
    LGKM_BAR();
    DG(3);
    {
        const bf16x8 x0 = lds_rd128(lds + lane * 16), x1 = lds_rd128(lds + lane * 16 + 1024);
        k0[0] = lds_rd128(lp); k1[0] = lds_rd128(lp + 1024);
        v0[0] = lds_rd128(lp + 2048); v1[0] = lds_rd128(lp + 3072);
        sA = MFMA32(x0, qa0, f32x16{});
        sB = MFMA32(x0, qb0, f32x16{});
        sA = MFMA32(x1, qa1, sA);
        sB = MFMA32(x1, qb1, sB);
        float m = __builtin_fmaxf(sA[0], sB[0]);
#pragma unroll
        for (int i = 1; i < 16; ++i) m = __builtin_fmaxf(m, __builtin_fmaxf(sA[i], sB[i]));
        auto rr = __builtin_amdgcn_permlane32_swap(__float_as_uint(m), __float_as_uint(m), false, false);
        m = __builtin_fmaxf(__uint_as_float(rr[0]), __uint_as_float(rr[1]));
#pragma unroll
        for (int i = 0; i < 16; ++i) negm[i] = -m;
        sA = MFMA32(k0[0], qa0, negm);
        sA = MFMA32(k1[0], qa1, sA);
        v1[1] = v1[0];
        EX(sA, 0); EX(sA, 1);
    }
#define SUM16(l, p) asm volatile("v_mfma_f32_16x16x32_bf16 %0, %1, %2, %0" : "+v"(l) : "v"(sel), "v"(p))
#define HALF(C, oC, lC, N, qx, qy, ka, kb, oP, lP, vprev, vcur0, PRE) do { \
        oP = MFMA32(vprev, PB(p1), oP); SBAR(); \
        PRE; EX(C, 2); EX(C, 3); EX(C, 4); SBAR(); \
        N = MFMA32(ka, qx, negm); SBAR(); \
        EX(C, 5); EX(C, 6); EX(C, 7); SBAR(); \
        SUM16(lP, p1); SBAR(); \
        p0[0] = cvtpk(C[0], C[1]); p0[1] = cvtpk(C[2], C[3]); SBAR(); \
        N = MFMA32(kb, qy, N); SBAR(); \
        p0[2] = cvtpk(C[4], C[5]); p0[3] = cvtpk(C[6], C[7]); SBAR(); EX(C, 8); EX(C, 9); SBAR(); \
        oC = MFMA32(vcur0, PB(p0), oC); SBAR(); \
        EX(C, 10); EX(C, 11); EX(C, 12); SBAR(); \
        SUM16(lC, p0); SBAR(); \
        EX(C, 13); EX(C, 14); EX(C, 15); SBAR(); \
        p1[0] = cvtpk(C[8], C[9]); p1[1] = cvtpk(C[10], C[11]); p1[2] = cvtpk(C[12], C[13]); p1[3] = cvtpk(C[14], C[15]); SBAR(); \
        EX(N, 0); EX(N, 1); SBAR(); \
    } while (0)
#define RD4(Y, nx) do { v1[Y] = lds_rd128((nx) + 3072); v0[Y] = lds_rd128((nx) + 2048); \
        k1[Y] = lds_rd128((nx) + 1024); k0[Y] = lds_rd128(nx); } while (0)
#define KT(X, nx, PRE0) do { \
        HALF(sA, oA, lA, sB, qb0, qb1, k0[X], k1[X], oB, lB, v1[X ^ 1], v0[X], PRE0; RD4(X ^ 1, nx)); \
        HALF(sB, oB, lB, sA, qa0, qa1, k0[X ^ 1], k1[X ^ 1], oA, lA, v1[X], v0[X], (void)0); \
    } while (0)
    SBAR();
#pragma unroll 1
    for (int st = 0; st < 8; ++st) {
        const lds_ptr cur = lp + (st & 1) * K2_SLOT;
        const lds_ptr nxt = lp + ((st + 1) & 1) * K2_SLOT;
        PRIO(2);
        KT(0, cur + 4096, (void)0);
        PRIO(1);
        KT(1, cur + 8192, (void)0);
        PRIO(0);
        KT(0, cur + 12288, (void)0);
        PRIO(3);
        KT(1, nxt, if (st < 7) RAW_BAR(); SBAR());
    }
    oB = MFMA32(v1[1], PB(p1), oB);
    SBAR();
    SUM16(lB, p1);
    asm volatile("s_nop 7\n\ts_nop 7" ::: "memory");
    SBAR();
    DG(4);
    {
        const lds_ptr xo = lds + w * 4096 + lane * 16;
#pragma unroll
        for (int i = 0; i < 4; ++i) *(LAS f32x4*)(xo + i * 1024) = f32x4{oB[4 * i], oB[4 * i + 1], oB[4 * i + 2], oB[4 * i + 3]};
        *(LAS float*)(lds + K2_LX + w * 256 + lane * 4) = lB[0];
        LGKM_BAR();
        const lds_ptr xi = lds + (w ^ 4) * 4096 + lane * 16;
#pragma unroll
        for (int i = 0; i < 4; ++i) {
            const f32x4 t = *(const LAS f32x4*)(xi + i * 1024);
#pragma unroll
            for (int j = 0; j < 4; ++j) oA[4 * i + j] += t[j];
        }
        lA[0] += *(const LAS float*)(lds + K2_LX + (w ^ 4) * 256 + lane * 4);
    }
    float l = lA[0];
    {
        float amax = __builtin_fabsf(oA[0]);
#pragma unroll
        for (int i = 1; i < 16; ++i) amax = __builtin_fmaxf(amax, __builtin_fabsf(oA[i]));
        bool nan = false;
#pragma unroll
        for (int i = 0; i < 16; ++i) nan |= (oA[i] != oA[i]);
        const bool ok = (l <= 1.0e30f) && (l >= 1.0e-18f) && (amax <= 3.0e38f) && !nan;
        if (__builtin_expect(__any(!ok), 0)) {
            float m = -INFINITY, ls = 0.f;
            oA = f32x16{};
#pragma unroll 1
            for (int t = 0; t < 64; ++t) {
                const char* g = kvg + t * 4096 + lane * 16;
                const bf16x8 a0 = *(const bf16x8*)g, a1 = *(const bf16x8*)(g + 1024);
                const bf16x8 b0 = *(const bf16x8*)(g + 2048), b1 = *(const bf16x8*)(g + 3072);
                f32x16 s = MFMA32(a0, qa0, f32x16{});
                s = MFMA32(a1, qa1, s);
                float tm = s[0];
#pragma unroll
                for (int i = 1; i < 16; ++i) tm = __builtin_fmaxf(tm, s[i]);
                auto rr = __builtin_amdgcn_permlane32_swap(__float_as_uint(tm), __float_as_uint(tm), false, false);
                tm = __builtin_fmaxf(__uint_as_float(rr[0]), __uint_as_float(rr[1]));
                const float mn = __builtin_fmaxf(m, tm);
                const float alpha = __builtin_amdgcn_exp2f(m - mn);
                m = mn;
                float ps = 0.f;
#pragma unroll
                for (int i = 0; i < 16; ++i) { s[i] = __builtin_amdgcn_exp2f(s[i] - mn); ps += s[i]; oA[i] *= alpha; }
                ls = ls * alpha + ps;
                oA = MFMA32(b0, PB(pack8n(s, 0)), oA);
                oA = MFMA32(b1, PB(pack8n(s, 1)), oA);
            }
            auto rr = __builtin_amdgcn_permlane32_swap(__float_as_uint(ls), __float_as_uint(ls), false, false);
            l = __uint_as_float(rr[0]) + __uint_as_float(rr[1]);
        }
    }
    const float rl = 1.0f / l;
    char* og = ws + WS_O + (size_t)(TA * 32 + h) * 2048 + lane * 16;
    *(u32x4*)og = pack8(oA, 0, rl);
    *(u32x4*)(og + 1024) = pack8(oA, 1, rl);
    DG_END(5);
}

__global__ __launch_bounds__(512, 2) void k3_out(const char* __restrict__ ws, const char* __restrict__ Wo,
                                                 const char* __restrict__ Aq, char* __restrict__ out) {
    __shared__ __attribute__((aligned(16))) char lds_raw[131072];
    const lds_ptr lds = (lds_ptr)lds_raw;
    const unsigned lds0 = (unsigned)(uintptr_t)lds;
    const int tid = threadIdx.x, lane = tid & 63;
    const int w = __builtin_amdgcn_readfirstlane(tid >> 6);
    const int b = blockIdx.x, xcd = b & 7, idx = b >> 3;
    const int tt = (xcd >> 1) * 4 + (idx >> 3), nt = (xcd & 1) * 8 + (idx & 7);
    DG_DECL(2)

    if (w >= 4) {
        const int lw = w - 4;
        const int ch = (lane & 7) ^ (((lane >> 4) & 1) << 2);
        const unsigned voff = (unsigned)(lane >> 3) * 2048u + nt * 128 + ch * 16;
#pragma unroll
        for (int i = 0; i < 32; ++i) {
            const int piece = i * 4 + lw;
            glds16(voff, Wo + (size_t)piece * 16384, lds0 + piece * 1024);
        }
        const int g8 = xcd * 32 + idx, cq = lw * 64 + lane;
        u32x4 zc[4];
        u32x2 qr[8];
#pragma unroll
        for (int c = 0; c < 4; ++c) zc[c] = gld128((unsigned)cq * 16384u + g8 * 16, ws + WS_ZT + c * 4096);
#pragma unroll
        for (int i = 0; i < 8; ++i) qr[i] = gld64((unsigned)g8 * 16384u + cq * 8, Aq + i * 2048);
        WAIT_BAR(40); WAIT_BAR(36); WAIT_BAR(32); WAIT_BAR(28);
        WAIT_BAR(24); WAIT_BAR(20); WAIT_BAR(16); WAIT_BAR(12);
        asm volatile("s_waitcnt vmcnt(0)" ::: "memory");
        SBAR();
#pragma unroll
        for (int c = 0; c < 4; ++c) asm volatile("" : "+v"(zc[c]));
#pragma unroll
        for (int i = 0; i < 8; ++i) asm volatile("" : "+v"(qr[i]));
        char* dst = out + (size_t)g8 * 32768 + cq * 8;
#pragma unroll
        for (int i = 0; i < 8; ++i) {
            float zz[4];
#pragma unroll
            for (int c = 0; c < 4; ++c) zz[c] = (i & 1) ? bfhi(zc[c][i >> 1]) : bflo(zc[c][i >> 1]);
            u32x2 o = {cvtpk(bflo(qr[i][0]) * zz[0], bfhi(qr[i][0]) * zz[1]),
                       cvtpk(bflo(qr[i][1]) * zz[2], bfhi(qr[i][1]) * zz[3])};
            *(u32x2*)(dst + i * 4096) = o;
        }
        DG_END(3);
        return;
    }

    const int T = tt * 4 + w;
    const int r = lane & 31, hh = lane >> 5;
    const char* og = ws + WS_O + (size_t)T * 65536 + lane * 16;
    bf16x8 of[32];
#pragma unroll
    for (int i = 0; i < 32; ++i) of[i] = *(const bf16x8*)(og + i * 1024);
    SBAR();
    DG(3);
    const int g = lane >> 4, q = (lane & 15) >> 2, p = lane & 3;
    int woff[2];
#pragma unroll
    for (int n = 0; n < 2; ++n) {
        const int c = 4 * n + 2 * (p & 1) + (g & 1);
        woff[n] = (4 * hh + q) * 128 + ((c ^ ((q >> 1) << 2)) << 4) + (p >> 1) * 8;
    }
    f32x16 acc[2] = {f32x16{}, f32x16{}};
#pragma unroll
    for (int c = 0; c < 8; ++c) {
        RAW_BAR();
        SBAR();
#pragma unroll
        for (int i = 0; i < 8; ++i) {
            const int ks = c * 8 + i;
#pragma unroll
            for (int n = 0; n < 2; ++n) {
                const lds_ptr wp = lds + woff[n] + ks * 2048;
                acc[n] = MFMA32(cat8(lds_tr(wp), lds_tr(wp + 1024)), of[ks & 31], acc[n]);
            }
        }
        SBAR();
        if (c < 4) {
#pragma unroll
            for (int i = 0; i < 8; ++i) of[c * 8 + i] = *(const bf16x8*)(og + (32 + c * 8 + i) * 1024);
        }
        SBAR();
    }
    DG(4);
    char* dst = out + (size_t)(T * 32 + r) * 4096 + 2048 + nt * 128 + hh * 32;
#pragma unroll
    for (int n = 0; n < 2; ++n) {
        *(u32x4*)(dst + n * 64) = pack8n(acc[n], 0);
        *(u32x4*)(dst + n * 64 + 16) = pack8n(acc[n], 1);
    }
    DG_END(5);
}

extern "C" void kernel_launch(void* const* d_in, const int* in_sizes, int n_in,
                              void* d_out, int out_size, void* d_ws, size_t ws_size,
                              hipStream_t stream) {
    const void* A_q = d_in[0];
    const void* A_k = d_in[1];
    const void* A_v = d_in[2];
    const void* A_shift = d_in[3];
    const void* A_ssmk = d_in[4];
    const void* A_D = d_in[5];
    const void* B_x = d_in[6];
    const void* B_wqkv = d_in[7];
    const void* B_wo = d_in[8];
    char* ws = (char*)d_ws;
    k1_qkv<<<256, 512, 0, stream>>>((const char*)B_x, (const char*)B_wqkv, ws, (const char*)A_k,
                                    (const char*)A_v, (const char*)A_shift);
    k2_attn<<<256, 768, 0, stream>>>(ws, (const char*)A_ssmk, (const unsigned*)A_D);
    k3_out<<<256, 512, 0, stream>>>(ws, (const char*)B_wo, (const char*)A_q, (char*)d_out);
}
```

```cpp
#include <hip/hip_runtime.h>
#include <hip/hip_bf16.h>
#include <stdint.h>

typedef short bf16x8 __attribute__((ext_vector_type(8)));
typedef short s16x4 __attribute__((ext_vector_type(4)));
typedef float f32x16 __attribute__((ext_vector_type(16)));
typedef float f32x4 __attribute__((ext_vector_type(4)));
typedef float f32x2 __attribute__((ext_vector_type(2)));
typedef unsigned u32x4 __attribute__((ext_vector_type(4)));
typedef unsigned u32x2 __attribute__((ext_vector_type(2)));
typedef __bf16 bf16x2_t __attribute__((ext_vector_type(2)));

#define LAS __attribute__((address_space(3)))
typedef LAS char* lds_ptr;
#define DI __device__ __forceinline__
#define SBAR() __builtin_amdgcn_sched_barrier(0)
#define MFMA32(a, b, c) __builtin_amdgcn_mfma_f32_32x32x16_bf16((a), (b), (c), 0, 0, 0)
#define MFMA16(a, b, c) __builtin_amdgcn_mfma_f32_16x16x32_bf16((a), (b), (c), 0, 0, 0)
#define WAIT_BAR(N) asm volatile("s_waitcnt vmcnt(" #N ") lgkmcnt(0)\n\ts_barrier" ::: "memory")
#define LGKM_BAR() asm volatile("s_waitcnt lgkmcnt(0)\n\ts_barrier" ::: "memory")

#define DG_DECL(K)
#define DG(i)
#define DG_END(i)

constexpr int S_LEN = 2048, DM = 1024, NH = 32;

constexpr size_t WS_Q = 0;
constexpr size_t WS_KV = 4u << 20;
constexpr size_t WS_O = 12u << 20;
constexpr size_t WS_KVR = 16u << 20;
constexpr size_t WS_ZT = 20u << 20;
constexpr size_t WS_CNT = 24u << 20;

DI unsigned cvtpk(float lo, float hi) {
    f32x2 v = {lo, hi};
    return __builtin_bit_cast(unsigned, __builtin_convertvector(v, bf16x2_t));
}
DI float bflo(unsigned u) { return __uint_as_float(u << 16); }
DI float bfhi(unsigned u) { return __uint_as_float(u & 0xffff0000u); }

DI void glds16(unsigned voff, const void* sbase, unsigned lds_dst) {
    unsigned sv;
    asm volatile("s_mov_b32 %0, m0\n\ts_mov_b32 m0, %3\n\ts_nop 0\n\tglobal_load_lds_dwordx4 %1, %2\n\ts_mov_b32 m0, %0"
                 : "=&s"(sv) : "v"(voff), "s"(sbase), "s"(lds_dst) : "memory");
}
DI bf16x8 lds_rd128(lds_ptr p) { return *(const LAS bf16x8*)p; }
DI s16x4 lds_tr(lds_ptr p) {
    return __builtin_bit_cast(s16x4, __builtin_amdgcn_ds_read_tr16_b64_v4i16((LAS s16x4*)p));
}
DI bf16x8 cat8(s16x4 a, s16x4 b) { return __builtin_shufflevector(a, b, 0, 1, 2, 3, 4, 5, 6, 7); }
DI u32x4 pack8(const f32x16& x, int s, float sc) {
    u32x4 p;
    p[0] = cvtpk(x[8 * s + 0] * sc, x[8 * s + 1] * sc);
    p[1] = cvtpk(x[8 * s + 2] * sc, x[8 * s + 3] * sc);
    p[2] = cvtpk(x[8 * s + 4] * sc, x[8 * s + 5] * sc);
    p[3] = cvtpk(x[8 * s + 6] * sc, x[8 * s + 7] * sc);
    return p;
}
DI u32x4 pack8n(const f32x16& x, int s) {
    u32x4 p;
    p[0] = cvtpk(x[8 * s + 0], x[8 * s + 1]);
    p[1] = cvtpk(x[8 * s + 2], x[8 * s + 3]);
    p[2] = cvtpk(x[8 * s + 4], x[8 * s + 5]);
    p[3] = cvtpk(x[8 * s + 6], x[8 * s + 7]);
    return p;
}
DI void rnd2(float& a, float& b) { unsigned u = cvtpk(a, b); a = bflo(u); b = bfhi(u); }

#define CONV_PACE()
#define RAW_BAR() asm volatile("s_barrier" ::: "memory")
DI unsigned gld32(unsigned voff, const void* sbase) {
    unsigned r;
    asm volatile("global_load_dword %0, %1, %2" : "=v"(r) : "v"(voff), "s"(sbase) : "memory");
    return r;
}
DI u32x2 gld64(unsigned voff, const void* sbase) {
    u32x2 r;
    asm volatile("global_load_dwordx2 %0, %1, %2" : "=v"(r) : "v"(voff), "s"(sbase) : "memory");
    return r;
}
DI float mul_sv(float s, float v) { float r; asm("v_mul_f32_e32 %0, %1, %2" : "=v"(r) : "s"(s), "v"(v)); return r; }
DI float mul_vv(float a, float b) { float r; asm("v_mul_f32_e32 %0, %1, %2" : "=v"(r) : "v"(a), "v"(b)); return r; }
DI void fmac_vv(float& acc, float a, float b) { asm("v_fmac_f32_e32 %0, %1, %2" : "+v"(acc) : "v"(a), "v"(b)); }
DI void fmac_sv(float& acc, float s, float v) { asm("v_fmac_f32_e32 %0, %1, %2" : "+v"(acc) : "s"(s), "v"(v)); }
typedef __attribute__((address_space(1))) unsigned gu32;
#define RLX_AGENT __ATOMIC_RELAXED, __HIP_MEMORY_SCOPE_AGENT
#define CFENCE() asm volatile("" ::: "memory")
#define DRAIN() asm volatile("s_waitcnt vmcnt(0)" ::: "memory")
constexpr int AUX_SC1 = 16;
DI u32x4 ld_sc1(__amdgpu_buffer_rsrc_t rs, unsigned off) { return __builtin_amdgcn_raw_buffer_load_b128(rs, (int)off, 0, AUX_SC1); }
DI void st_sc1(u32x4 v, __amdgpu_buffer_rsrc_t rs, unsigned off) { __builtin_amdgcn_raw_buffer_store_b128(v, rs, (int)off, 0, AUX_SC1); }
DI unsigned lds_add_rtn(lds_ptr p, int lane) {
    unsigned old = 0;
    CFENCE();
    if (lane == 0) old = __hip_atomic_fetch_add((LAS unsigned*)p, 1u, __ATOMIC_RELAXED, __HIP_MEMORY_SCOPE_WORKGROUP);
    old = __builtin_amdgcn_readfirstlane(old);
    CFENCE();
    return old;
}
DI void lds_add(lds_ptr p, int lane) {
    CFENCE();
    if (lane == 0) (void)__hip_atomic_fetch_add((LAS unsigned*)p, 1u, __ATOMIC_RELAXED, __HIP_MEMORY_SCOPE_WORKGROUP);
    CFENCE();
}
DI void lds_wait_ge(lds_ptr p, unsigned v) {
    CFENCE();
    while ((unsigned)__builtin_amdgcn_readfirstlane(*(volatile LAS unsigned*)p) < v) __builtin_amdgcn_s_sleep(1);
    CFENCE();
}
DI void poll_ge(gu32* word, unsigned want) {
    CFENCE();
    for (unsigned spins = 0; spins < (1u << 22); ++spins) {
        if ((unsigned)__builtin_amdgcn_readfirstlane(__hip_atomic_load(word, RLX_AGENT)) >= want) break;
        __builtin_amdgcn_s_sleep(1);
    }
    __builtin_amdgcn_fence(__ATOMIC_ACQUIRE, "wavefront");
    CFENCE();
}
#define PRIO(x) do { __builtin_amdgcn_s_setprio(x); SBAR(); } while (0)
DI u32x4 gld128(unsigned voff, const void* sbase) {
    u32x4 r;
    asm volatile("global_load_dwordx4 %0, %1, %2" : "=v"(r) : "v"(voff), "s"(sbase) : "memory");
    return r;
}

constexpr int K1_STAGE = 40960;
__global__ __launch_bounds__(512, 2) void k1_qkv(const char* __restrict__ X, const char* __restrict__ W,
                                                 char* __restrict__ ws) {
    __shared__ __attribute__((aligned(16))) char lds_raw[4 * K1_STAGE];
    const lds_ptr lds = (lds_ptr)lds_raw;
    const unsigned lds0 = (unsigned)(uintptr_t)lds;
    const int tid = threadIdx.x, lane = tid & 63;
    const int w = __builtin_amdgcn_readfirstlane(tid >> 6);
    const int b = blockIdx.x, xcd = b & 7, idx = b >> 3;
    const int hp = (xcd >> 1) * 4 + (idx & 3), tb = (xcd & 1) * 8 + (idx >> 2);
    DG_DECL(0)

    if (w >= 4) {
        const int lw = w - 4;
        const int rot = (tb & 3) * 4;
        unsigned voffX[4], voffW[2];
#pragma unroll
        for (int pp = 0; pp < 4; ++pp) {
            const int row = 32 * lw + 8 * pp + (lane >> 3);
            const int ch = (lane & 7) ^ ((row >> 1) & 7);
            voffX[pp] = (unsigned)(tb * 128 + row) * 2048u + ch * 16;
        }
#pragma unroll
        for (int pp = 0; pp < 2; ++pp) {
            const int krow = 16 * lw + 8 * pp + (lane >> 3);
            const int ch = (lane & 7) ^ (((lane >> 4) & 1) << 2);
            voffW[pp] = (unsigned)krow * 6144u + hp * 128 + ch * 16;
        }
        auto issue = [&](int kt) {
            const int kr = (kt + rot) & 15;
            const char* xb = X + kr * 128;
            const char* wb = W + (size_t)kr * (64 * 6144);
            const unsigned st = lds0 + (kt & 3) * K1_STAGE;
#pragma unroll
            for (int pp = 0; pp < 4; ++pp) glds16(voffX[pp], xb, st + (4 * lw + pp) * 1024);
#pragma unroll
            for (int sec = 0; sec < 3; ++sec)
#pragma unroll
                for (int pp = 0; pp < 2; ++pp)
                    glds16(voffW[pp], wb + sec * 2048, st + 16384 + sec * 8192 + (2 * lw + pp) * 1024);
        };
        issue(0); issue(1); issue(2);
#pragma unroll 1
        for (int kt = 0; kt < 13; ++kt) {
            WAIT_BAR(20);
            issue(kt + 3);
        }
        WAIT_BAR(20);
        WAIT_BAR(10);
        WAIT_BAR(0);
        if (b == 0 && lw == 0 && lane < 24) *(unsigned*)(ws + WS_CNT + lane * 256) = 0u;
        DG_END(5);
        return;
    }

    const int wm = w >> 1, wn = w & 1;
    const int r = lane & 31, hh = lane >> 5;
    const int g = lane >> 4, q = (lane & 15) >> 2, p = lane & 3;
    const int xoff0 = (64 * wm + r) * 128 + ((hh ^ ((r >> 1) & 7)) << 4);
    const int woff0 = 16384 + (8 * hh + q) * 128 + (((4 * wn + 2 * (g & 1) + (p >> 1)) ^ ((q >> 1) << 2)) << 4) + (p & 1) * 8;

    f32x16 acc[2][3];
#pragma unroll
    for (int m = 0; m < 2; ++m)
#pragma unroll
        for (int s = 0; s < 3; ++s) acc[m][s] = f32x16{};
    bf16x8 xf[2][2];
    s16x4 wl[2][3], wh[2][3];
#define K1_WF(f, sec) cat8(wl[f][sec], wh[f][sec])
#define K1_STEP(F, st, t, BAR_HERE) do { \
        const lds_ptr xp_ = (st) + (xoff0 ^ ((t) << 5)); const lds_ptr wp_ = (st) + woff0 + (t) * 2048; \
        acc[0][0] = MFMA32(K1_WF(F, 0), xf[F][0], acc[0][0]); SBAR(); \
        if (BAR_HERE) { LGKM_BAR(); SBAR(); } \
        xf[F ^ 1][0] = lds_rd128(xp_); wl[F ^ 1][0] = lds_tr(wp_); wh[F ^ 1][0] = lds_tr(wp_ + 512); SBAR(); \
        acc[0][1] = MFMA32(K1_WF(F, 1), xf[F][0], acc[0][1]); SBAR(); \
        wl[F ^ 1][1] = lds_tr(wp_ + 8192); wh[F ^ 1][1] = lds_tr(wp_ + 8192 + 512); SBAR(); \
        acc[0][2] = MFMA32(xf[F][0], K1_WF(F, 2), acc[0][2]); SBAR(); \
        wl[F ^ 1][2] = lds_tr(wp_ + 16384); wh[F ^ 1][2] = lds_tr(wp_ + 16384 + 512); xf[F ^ 1][1] = lds_rd128(xp_ + 4096); SBAR(); \
        acc[1][0] = MFMA32(K1_WF(F, 0), xf[F][1], acc[1][0]); SBAR(); \
        acc[1][1] = MFMA32(K1_WF(F, 1), xf[F][1], acc[1][1]); \
        acc[1][2] = MFMA32(xf[F][1], K1_WF(F, 2), acc[1][2]); SBAR(); \
    } while (0)

    LGKM_BAR();
    DG(3);
    {
        const lds_ptr xp_ = lds + xoff0; const lds_ptr wp_ = lds + woff0;
        xf[0][0] = lds_rd128(xp_); xf[0][1] = lds_rd128(xp_ + 4096);
#pragma unroll
        for (int sec = 0; sec < 3; ++sec) { wl[0][sec] = lds_tr(wp_ + sec * 8192); wh[0][sec] = lds_tr(wp_ + sec * 8192 + 512); }
    }
    SBAR();
#pragma unroll
    for (int kt = 0; kt < 15; ++kt) {
        const lds_ptr st = lds + (kt & 3) * K1_STAGE;
        const lds_ptr sn = lds + ((kt + 1) & 3) * K1_STAGE;
        K1_STEP(0, st, 1, false);
        K1_STEP(1, st, 2, false);
        K1_STEP(0, st, 3, false);
        K1_STEP(1, sn, 0, true);
    }
    {
        const lds_ptr st = lds + 3 * K1_STAGE;
        K1_STEP(0, st, 1, false);
        K1_STEP(1, st, 2, false);
        K1_STEP(0, st, 3, false);
#pragma unroll
        for (int m = 0; m < 2; ++m) {
            acc[m][1] = MFMA32(K1_WF(1, 1), xf[1][m], acc[m][1]);
            acc[m][2] = MFMA32(xf[1][m], K1_WF(1, 2), acc[m][2]);
            acc[m][0] = MFMA32(K1_WF(1, 0), xf[1][m], acc[m][0]);
        }
    }
    DG(4);
    {
        const int h = 2 * hp + wn;
        const float qscale = 0.17677669529663687f * 1.4426950408889634f;
#pragma unroll
        for (int m = 0; m < 2; ++m) {
            const int T = tb * 4 + wm * 2 + m;
            char* dq = ws + WS_Q + (size_t)(h * 64 + T) * 2048 + lane * 16;
            char* dk = ws + WS_KV + (size_t)(h * 64 + T) * 4096 + lane * 16;
            *(u32x4*)(dk) = pack8n(acc[m][1], 0);
            *(u32x4*)(dk + 1024) = pack8n(acc[m][1], 1);
            *(u32x4*)(dk + 2048) = pack8n(acc[m][2], 0);
            *(u32x4*)(dk + 3072) = pack8n(acc[m][2], 1);
            *(u32x4*)(dq) = pack8(acc[m][0], 0, qscale);
            *(u32x4*)(dq + 1024) = pack8(acc[m][0], 1, qscale);
        }
    }
    DG_END(5);
}

constexpr int K2_SLOT = 32768;
constexpr int K2_SF = 65536;
constexpr int K2_CV = 98304;
constexpr int CV_HA = 0, CV_HB = 4160, CV_KV = 8320, CV_BYTES = 8320 + 480 + 4096;
constexpr int K2_LX = K2_CV + 4 * CV_BYTES;
constexpr int K2_FL = K2_LX + 2048;
constexpr int K2_LDS = K2_FL + 256;
constexpr int F_X = 0, F_R = 32, C_XD = 64, C_OST = 68, F_GO = 72, C_S1 = 76, C_S2 = 84, C_ZST = 88, F_ZGO = 92;
#define NG_IN 3

__global__ __launch_bounds__(768, 3) void k2_attn(char* __restrict__ ws, const char* __restrict__ Aq,
                                                  const char* __restrict__ Ak, const char* __restrict__ Av,
                                                  const char* __restrict__ shiftk, const char* __restrict__ ssmk,
                                                  const unsigned* __restrict__ ssmD, const char* __restrict__ Wo,
                                                  char* __restrict__ out) {
    __shared__ __attribute__((aligned(16))) char lds_raw[K2_LDS];
    const lds_ptr lds = (lds_ptr)lds_raw;
    const lds_ptr fl = lds + K2_FL;
    const unsigned lds0 = (unsigned)(uintptr_t)lds;
    const int tid = threadIdx.x, lane = tid & 63;
    const int w = __builtin_amdgcn_readfirstlane(tid >> 6);
    const int b = blockIdx.x, xcd = b & 7, idx = b >> 3;
    const int h = xcd * 4 + (idx >> 3), qb = idx & 7;
    const int tt = xcd * 2 + (idx >> 4), nt = idx & 15;
    const char* kvg = ws + WS_KV + (size_t)h * 64 * 4096;
    const __amdgpu_buffer_rsrc_t rs = __builtin_amdgcn_make_buffer_rsrc(ws, 0, 32 << 20, 0x00020000);
    gu32* const cnt = (gu32*)(ws + WS_CNT);
    const unsigned wvoff = (unsigned)(lane >> 3) * 2048u + nt * 128 + (((lane & 7) ^ (((lane >> 4) & 1) << 2)) << 4);
    DG_DECL(1)

    if (w >= 8) {
        const int cw = w - 8;
        const unsigned voff = lane * 16;
        __builtin_amdgcn_s_setprio(3);
        auto issue = [&](int st) {
            const char* src = kvg + (st * 4 + cw) * 4096;
            const unsigned dst = lds0 + (st & 1) * K2_SLOT + cw * 4096;
#pragma unroll
            for (int i = 0; i < 4; ++i) glds16(voff, src + i * 1024, dst + i * 1024);
#pragma unroll
            for (int i = 0; i < 4; ++i) glds16(voff, src + 131072 + i * 1024, dst + 16384 + i * 1024);
        };
        issue(0); issue(1);
        const int ch = xcd * 128 + idx * 4 + cw;
        const lds_ptr cv = lds + K2_CV + cw * CV_BYTES;
        {
            const char* hrow = ssmk + (size_t)ch * 4096;
            const unsigned cv0 = lds0 + K2_CV + cw * CV_BYTES;
#pragma unroll
            for (int i = 0; i < 4; ++i) glds16(voff, hrow + i * 1024, cv0 + CV_HA + 32 + i * 1024);
        }
        const int c0 = xcd * 128 + cw * 32, cq = lane & 7, rg = lane >> 3;
        const int row0 = idx * 64 + rg * 8;
        const unsigned roff = (unsigned)row0 * 2048u + (c0 + cq * 4) * 2;
        const unsigned toffs = (unsigned)(c0 + (lane >> 3)) * 4096u + idx * 128;
        u32x2 kr[11], vr[8];
        u32x4 sk[2];
#pragma unroll
        for (int i = 0; i < 3; ++i) kr[i] = gld64(row0 == 0 ? roff : roff + (i - 3) * 2048, Ak);
#pragma unroll
        for (int i = 3; i < 11; ++i) kr[i] = gld64(roff, Ak + (i - 3) * 2048);
#pragma unroll
        for (int i = 0; i < 8; ++i) vr[i] = gld64(roff, Av + i * 2048);
        sk[0] = gld128((unsigned)(c0 + cq * 4) * 8u, shiftk); sk[1] = gld128((unsigned)(c0 + cq * 4) * 8u, shiftk + 16);
        {
            const u32x4 z4 = {0u, 0u, 0u, 0u};
            if (lane < 2) *(LAS u32x4*)(cv + CV_HA + lane * 16) = z4;
            if (lane < 30) *(LAS u32x4*)(cv + CV_KV + lane * 16) = z4;
            if (cw == 0 && lane < 16) *(LAS u32x4*)(fl + lane * 16) = z4;
        }
        WAIT_BAR(33);
        DG(3);
        WAIT_BAR(25);
        issue(2);
        asm volatile("s_waitcnt vmcnt(8)" ::: "memory");
        SBAR();
#pragma unroll
        for (int i = 0; i < 11; ++i) asm volatile("" : "+v"(kr[i]));
#pragma unroll
        for (int i = 0; i < 8; ++i) asm volatile("" : "+v"(vr[i]));
        asm volatile("" : "+v"(sk[0]), "+v"(sk[1]));
        DG(6);
        {
            if (row0 == 0) { kr[0] = u32x2{0u, 0u}; kr[1] = u32x2{0u, 0u}; kr[2] = u32x2{0u, 0u}; }
#pragma unroll
            for (int c = 0; c < 4; ++c) {
                const unsigned s01 = sk[c >> 1][(c & 1) * 2], s23 = sk[c >> 1][(c & 1) * 2 + 1];
                const float w0 = bflo(s01), w1 = bfhi(s01), w2 = bflo(s23), w3 = bfhi(s23);
                float kf[11], kv[8];
#pragma unroll
                for (int i = 0; i < 11; ++i) kf[i] = (c & 1) ? bfhi(kr[i][c >> 1]) : bflo(kr[i][c >> 1]);
#pragma unroll
                for (int i = 0; i < 8; ++i) {
                    float a = mul_vv(w3, kf[i]);
                    fmac_vv(a, w2, kf[i + 1]); fmac_vv(a, w1, kf[i + 2]); fmac_vv(a, w0, kf[i + 3]);
                    kv[i] = mul_vv(a, (c & 1) ? bfhi(vr[i][c >> 1]) : bflo(vr[i][c >> 1]));
                }
                u32x4 o4;
#pragma unroll
                for (int i = 0; i < 4; ++i) o4[i] = cvtpk(kv[7 - 2 * i], kv[6 - 2 * i]);
                *(LAS u32x4*)(cv + CV_HB + (cq * 4 + c) * 128 + ((rg ^ cq) << 4)) = o4;
            }
#pragma unroll
            for (int i = 0; i < 4; ++i) {
                const u32x4 t = *(const LAS u32x4*)(cv + CV_HB + i * 1024 + lane * 16);
                st_sc1(t, rs, (unsigned)WS_KVR + toffs + i * 32768 + (((lane & 7) ^ (2 * i + (lane >> 5))) << 4));
            }
        }
        CFENCE();
        {
            const u32x4 z4 = {0u, 0u, 0u, 0u};
            if (lane < 2) *(LAS u32x4*)(cv + CV_HB + lane * 16) = z4;
#pragma unroll
            for (int i = 0; i < 4; ++i) {
                const int off = 32 + i * 1024 + lane * 16;
                const u32x4 hv = *(const LAS u32x4*)(cv + CV_HA + off);
                const unsigned pv = *(const LAS unsigned*)(cv + CV_HA + off - 4);
                u32x4 sh;
                sh[0] = __builtin_amdgcn_alignbit(hv[0], pv, 16);
                sh[1] = __builtin_amdgcn_alignbit(hv[1], hv[0], 16);
                sh[2] = __builtin_amdgcn_alignbit(hv[2], hv[1], 16);
                sh[3] = __builtin_amdgcn_alignbit(hv[3], hv[2], 16);
                *(LAS u32x4*)(cv + CV_HB + off) = sh;
            }
        }
        DG(7);
        WAIT_BAR(0);
        if (cw == 0 && lane == 0) (void)__hip_atomic_fetch_add(cnt + (8 + xcd) * 64, 1u, RLX_AGENT);
        issue(3);
#pragma unroll
        for (int i = 0; i < 8; ++i)
            glds16(wvoff, Wo + (size_t)((i >> 2) * 64 + cw * 4 + (i & 3)) * 16384, lds0 + K2_SF + (i >> 2) * 16384 + (cw * 4 + (i & 3)) * 1024);
        if (cw == 0) { __builtin_amdgcn_s_sleep(16); poll_ge(cnt + (8 + xcd) * 64, 32u); }
        WAIT_BAR(0);
        issue(4);
        {
            u32x4 t[4];
#pragma unroll
            for (int i = 0; i < 4; ++i) t[i] = ld_sc1(rs, (unsigned)WS_KVR + ch * 4096 + i * 1024 + voff);
#pragma unroll
            for (int i = 0; i < 4; ++i) *(LAS u32x4*)(cv + CV_KV + 480 + i * 1024 + lane * 16) = t[i];
        }
        const int r1 = lane & 15, q = lane >> 4;
        const int toff = (r1 & 1) ? CV_HA + 2 * (16 + r1 + 9 - 8 * q) : CV_HB + 2 * (16 + r1 + 10 - 8 * q);
        const int boff = CV_KV + 448 + 32 * r1 + 16 * q;
        f32x4 y[8];
#pragma unroll
        for (int t = 0; t < 8; ++t) y[t] = f32x4{};
        auto group = [&](int bb, bool pace) {
            bf16x8 tf[8], bf[8];
#pragma unroll
            for (int a = 0; a < 8; ++a) {
                const LAS unsigned* tp = (const LAS unsigned*)(cv + toff + 64 * bb + 512 * a);
                u32x4 t4 = {tp[0], tp[1], tp[2], tp[3]};
                tf[a] = __builtin_bit_cast(bf16x8, t4);
                bf[a] = lds_rd128(cv + boff - 64 * bb + 512 * a);
            }
#pragma unroll
            for (int a = 0; a < 8; ++a)
#pragma unroll
                for (int ww = 0; ww + a < 8; ++ww) {
                    y[a + ww] = MFMA16(tf[a], bf[ww], y[a + ww]);
                    if (pace) asm volatile("s_nop 7");
                }
        };
#pragma unroll 1
        for (int it = 4; it < 7; ++it) {
            WAIT_BAR(0);
            issue(it + 1);
            if (it - 4 < NG_IN) group(it - 4, true);
        }
        WAIT_BAR(0);
        __builtin_amdgcn_s_setprio(0);
        u32x2 qr[8];
#pragma unroll
        for (int i = 0; i < 8; ++i) qr[i] = gld64(roff, Aq + i * 2048);
#pragma unroll 1
        for (int bb = NG_IN; bb < 8; ++bb) group(bb, false);
        DG(4);
        {
            const unsigned dd = ssmD[ch >> 1];
            const float dch = (ch & 1) ? bfhi(dd) : bflo(dd);
#pragma unroll
            for (int t = 0; t < 8; ++t) {
                const u32x2 kk = *(const LAS u32x2*)(cv + CV_KV + 480 + 512 * t + 32 * r1 + 16 * (q >> 1) + ((q & 1) ? 0 : 8));
                float k0 = mul_sv(dch, bfhi(kk[1])), k1 = mul_sv(dch, bflo(kk[1]));
                float k2 = mul_sv(dch, bfhi(kk[0])), k3 = mul_sv(dch, bflo(kk[0]));
                rnd2(k0, k1); rnd2(k2, k3);
                float y0 = y[t][0], y1 = y[t][1], y2 = y[t][2], y3 = y[t][3];
                rnd2(y0, y1); rnd2(y2, y3);
                u32x2 o = {cvtpk(y0 + k0, y1 + k1), cvtpk(y2 + k2, y3 + k3)};
                *(LAS u32x2*)(cv + CV_HA + 512 * t + 32 * r1 + 8 * q) = o;
            }
#pragma unroll
            for (int i = 0; i < 4; ++i)
                st_sc1(*(const LAS u32x4*)(cv + CV_HA + i * 1024 + lane * 16), rs, (unsigned)WS_ZT + ch * 4096 + i * 1024 + voff);
        }
        DRAIN();
        if (lds_add_rtn(fl + C_ZST, lane) == 3u && lane == 0) (void)__hip_atomic_fetch_add(cnt + (16 + xcd) * 64, 1u, RLX_AGENT);
        lds_wait_ge(fl + C_ZST, 4u);
#pragma unroll
        for (int i = 0; i < 8; ++i)
            glds16(wvoff, Wo + (size_t)((i >> 2) * 64 + 48 + cw * 4 + (i & 3)) * 16384, lds0 + K2_CV + (i >> 2) * 16384 + (cw * 4 + (i & 3)) * 1024);
        DRAIN();
        lds_add(fl + C_S2, lane);
        DG(8);
        if (cw == 0) { poll_ge(cnt + (16 + xcd) * 64, 32u); lds_add(fl + F_ZGO, lane); }
        else lds_wait_ge(fl + F_ZGO, 1u);
        DG(9);
        {
            const lds_ptr tr = lds + K2_CV + 32768 + cw * 4096;
            u32x4 t[4];
#pragma unroll
            for (int i = 0; i < 4; ++i)
                t[i] = ld_sc1(rs, (unsigned)WS_ZT + toffs + i * 32768 + (((lane & 7) ^ (2 * i + (lane >> 5))) << 4));
#pragma unroll
            for (int i = 0; i < 4; ++i) *(LAS u32x4*)(tr + i * 1024 + lane * 16) = t[i];
#pragma unroll
            for (int i = 0; i < 8; ++i) asm volatile("" : "+v"(qr[i]));
            u32x4 zc[4];
#pragma unroll
            for (int c = 0; c < 4; ++c) zc[c] = *(const LAS u32x4*)(tr + (cq * 4 + c) * 128 + ((rg ^ cq) << 4));
            char* dst = out + (size_t)row0 * 4096 + (c0 + cq * 4) * 2;
#pragma unroll
            for (int i = 0; i < 8; ++i) {
                float zz[4];
#pragma unroll
                for (int c = 0; c < 4; ++c) zz[c] = (i & 1) ? bfhi(zc[c][i >> 1]) : bflo(zc[c][i >> 1]);
                u32x2 o = {cvtpk(bflo(qr[i][0]) * zz[0], bfhi(qr[i][0]) * zz[1]),
                           cvtpk(bflo(qr[i][1]) * zz[2], bfhi(qr[i][1]) * zz[3])};
                *(u32x2*)(dst + i * 4096) = o;
            }
        }
        DG_END(5);
        return;
    }

    const int half = w >> 2;
    const int TA = qb * 8 + (w & 3) * 2 + half, TB = TA ^ 1;
    const char* qga = ws + WS_Q + (size_t)(h * 64 + TA) * 2048 + lane * 16;
    const char* qgb = ws + WS_Q + (size_t)(h * 64 + TB) * 2048 + lane * 16;
    const bf16x8 qa0 = *(const bf16x8*)qga, qa1 = *(const bf16x8*)(qga + 1024);
    const bf16x8 qb0 = *(const bf16x8*)qgb, qb1 = *(const bf16x8*)(qgb + 1024);
    bf16x8 sel;
    {
        const short one = (((lane >> 2) & 1) == ((lane >> 4) & 1)) ? (short)0x3f80 : (short)0;
#pragma unroll
        for (int i = 0; i < 8; ++i) sel[i] = one;
    }
    f32x16 oA = f32x16{}, oB = f32x16{};
    f32x4 lA = f32x4{}, lB = f32x4{};
    f32x16 negm, sA, sB;
    const lds_ptr lp = lds + half * 16384 + lane * 16;
    bf16x8 k0[2], k1[2], v0[2], v1[2];
    u32x4 p0, p1 = {0u, 0u, 0u, 0u};
#define EX(X, i) X[i] = __builtin_amdgcn_exp2f(X[i])
#define PB(x) __builtin_bit_cast(bf16x8, x)
    LGKM_BAR();
    DG(3);
    {
        const bf16x8 x0 = lds_rd128(lds + lane * 16), x1 = lds_rd128(lds + lane * 16 + 1024);
        k0[0] = lds_rd128(lp); k1[0] = lds_rd128(lp + 1024);
        v0[0] = lds_rd128(lp + 2048); v1[0] = lds_rd128(lp + 3072);
        sA = MFMA32(x0, qa0, f32x16{});
        sB = MFMA32(x0, qb0, f32x16{});
        sA = MFMA32(x1, qa1, sA);
        sB = MFMA32(x1, qb1, sB);
        float m = __builtin_fmaxf(sA[0], sB[0]);
#pragma unroll
        for (int i = 1; i < 16; ++i) m = __builtin_fmaxf(m, __builtin_fmaxf(sA[i], sB[i]));
        auto rr = __builtin_amdgcn_permlane32_swap(__float_as_uint(m), __float_as_uint(m), false, false);
        m = __builtin_fmaxf(__uint_as_float(rr[0]), __uint_as_float(rr[1]));
#pragma unroll
        for (int i = 0; i < 16; ++i) negm[i] = -m;
        sA = MFMA32(k0[0], qa0, negm);
        sA = MFMA32(k1[0], qa1, sA);
        v1[1] = v1[0];
        EX(sA, 0); EX(sA, 1);
    }
#define SUM16(l, p) asm volatile("v_mfma_f32_16x16x32_bf16 %0, %1, %2, %0" : "+v"(l) : "v"(sel), "v"(p))
#define HALF(C, oC, lC, N, qx, qy, ka, kb, oP, lP, vprev, vcur0, PRE) do { \
        oP = MFMA32(vprev, PB(p1), oP); SBAR(); \
        PRE; EX(C, 2); EX(C, 3); EX(C, 4); SBAR(); \
        N = MFMA32(ka, qx, negm); SBAR(); \
        EX(C, 5); EX(C, 6); EX(C, 7); SBAR(); \
        SUM16(lP, p1); SBAR(); \
        p0[0] = cvtpk(C[0], C[1]); p0[1] = cvtpk(C[2], C[3]); SBAR(); \
        N = MFMA32(kb, qy, N); SBAR(); \
        p0[2] = cvtpk(C[4], C[5]); p0[3] = cvtpk(C[6], C[7]); SBAR(); EX(C, 8); EX(C, 9); SBAR(); \
        oC = MFMA32(vcur0, PB(p0), oC); SBAR(); \
        EX(C, 10); EX(C, 11); EX(C, 12); SBAR(); \
        SUM16(lC, p0); SBAR(); \
        EX(C, 13); EX(C, 14); EX(C, 15); SBAR(); \
        p1[0] = cvtpk(C[8], C[9]); p1[1] = cvtpk(C[10], C[11]); p1[2] = cvtpk(C[12], C[13]); p1[3] = cvtpk(C[14], C[15]); SBAR(); \
        EX(N, 0); EX(N, 1); SBAR(); \
    } while (0)
#define RD4(Y, nx) do { v1[Y] = lds_rd128((nx) + 3072); v0[Y] = lds_rd128((nx) + 2048); \
        k1[Y] = lds_rd128((nx) + 1024); k0[Y] = lds_rd128(nx); } while (0)
#define KT(X, nx, PRE0) do { \
        HALF(sA, oA, lA, sB, qb0, qb1, k0[X], k1[X], oB, lB, v1[X ^ 1], v0[X], PRE0; RD4(X ^ 1, nx)); \
        HALF(sB, oB, lB, sA, qa0, qa1, k0[X ^ 1], k1[X ^ 1], oA, lA, v1[X], v0[X], (void)0); \
    } while (0)
    SBAR();
#pragma unroll 1
    for (int st = 0; st < 8; ++st) {
        const lds_ptr cur = lp + (st & 1) * K2_SLOT;
        const lds_ptr nxt = lp + ((st + 1) & 1) * K2_SLOT;
        PRIO(2);
        KT(0, cur + 4096, (void)0);
        PRIO(1);
        KT(1, cur + 8192, (void)0);
        PRIO(0);
        KT(0, cur + 12288, (void)0);
        PRIO(3);
        KT(1, nxt, if (st < 7) RAW_BAR(); SBAR());
    }
    oB = MFMA32(v1[1], PB(p1), oB);
    SBAR();
    SUM16(lB, p1);
    asm volatile("s_nop 7\n\ts_nop 7" ::: "memory");
    SBAR();
    DG(4);
    {
        const lds_ptr xo = lds + w * 4096 + lane * 16;
#pragma unroll
        for (int i = 0; i < 4; ++i) *(LAS f32x4*)(xo + i * 1024) = f32x4{oB[4 * i], oB[4 * i + 1], oB[4 * i + 2], oB[4 * i + 3]};
        *(LAS float*)(lds + K2_LX + w * 256 + lane * 4) = lB[0];
        lds_add(fl + F_X + w * 4, lane);
        lds_wait_ge(fl + F_X + (w ^ 4) * 4, 1u);
        const lds_ptr xi = lds + (w ^ 4) * 4096 + lane * 16;
#pragma unroll
        for (int i = 0; i < 4; ++i) {
            const f32x4 t = *(const LAS f32x4*)(xi + i * 1024);
#pragma unroll
            for (int j = 0; j < 4; ++j) oA[4 * i + j] += t[j];
        }
        lA[0] += *(const LAS float*)(lds + K2_LX + (w ^ 4) * 256 + lane * 4);
        lds_add(fl + C_XD, lane);
    }
    float l = lA[0];
    {
        float amax = __builtin_fabsf(oA[0]);
#pragma unroll
        for (int i = 1; i < 16; ++i) amax = __builtin_fmaxf(amax, __builtin_fabsf(oA[i]));
        bool nan = false;
#pragma unroll
        for (int i = 0; i < 16; ++i) nan |= (oA[i] != oA[i]);
        const bool ok = (l <= 1.0e30f) && (l >= 1.0e-18f) && (amax <= 3.0e38f) && !nan;
        if (__builtin_expect(__any(!ok), 0)) {
            float m = -INFINITY, ls = 0.f;
            oA = f32x16{};
#pragma unroll 1
            for (int t = 0; t < 64; ++t) {
                const char* g = kvg + t * 4096 + lane * 16;
                const bf16x8 a0 = *(const bf16x8*)g, a1 = *(const bf16x8*)(g + 1024);
                const bf16x8 b0 = *(const bf16x8*)(g + 2048), b1 = *(const bf16x8*)(g + 3072);
                f32x16 s = MFMA32(a0, qa0, f32x16{});
                s = MFMA32(a1, qa1, s);
                float tm = s[0];
#pragma unroll
                for (int i = 1; i < 16; ++i) tm = __builtin_fmaxf(tm, s[i]);
                auto rr = __builtin_amdgcn_permlane32_swap(__float_as_uint(tm), __float_as_uint(tm), false, false);
                tm = __builtin_fmaxf(__uint_as_float(rr[0]), __uint_as_float(rr[1]));
                const float mn = __builtin_fmaxf(m, tm);
                const float alpha = __builtin_amdgcn_exp2f(m - mn);
                m = mn;
                float ps = 0.f;
#pragma unroll
                for (int i = 0; i < 16; ++i) { s[i] = __builtin_amdgcn_exp2f(s[i] - mn); ps += s[i]; oA[i] *= alpha; }
                ls = ls * alpha + ps;
                oA = MFMA32(b0, PB(pack8n(s, 0)), oA);
                oA = MFMA32(b1, PB(pack8n(s, 1)), oA);
            }
            auto rr = __builtin_amdgcn_permlane32_swap(__float_as_uint(ls), __float_as_uint(ls), false, false);
            l = __uint_as_float(rr[0]) + __uint_as_float(rr[1]);
        }
    }
    {
        const float rl = 1.0f / l;
        const unsigned og = (unsigned)WS_O + (unsigned)(TA * 32 + h) * 2048u + lane * 16;
        st_sc1(pack8(oA, 0, rl), rs, og);
        st_sc1(pack8(oA, 1, rl), rs, og + 1024);
        DRAIN();
    }
    DG(5);
    const bool last = lds_add_rtn(fl + C_OST, lane) == 7u;
    if (last && lane == 0) (void)__hip_atomic_fetch_add(cnt + qb * 64, 1u, RLX_AGENT);
    const int j4 = w & 3;
    lds_wait_ge(fl + C_XD, 8u);
#pragma unroll
    for (int i = 0; i < 8; ++i)
        glds16(wvoff, Wo + (size_t)(half * 64 + 16 + j4 * 8 + i) * 16384, lds0 + half * K2_SLOT + (j4 * 8 + i) * 1024);
    if (last) { poll_ge(cnt + xcd * 64, 32u); lds_add(fl + F_GO, lane); }
    else lds_wait_ge(fl + F_GO, 1u);
    DG(6);
    {
        const int T3 = tt * 4 + j4;
        const unsigned og = (unsigned)WS_O + (unsigned)T3 * 65536u + half * 32768 + lane * 16;
        bf16x8 of[24];
#pragma unroll
        for (int i = 0; i < 24; ++i) of[i] = PB(ld_sc1(rs, og + i * 1024));
        SBAR();
        const int r = lane & 31, hh = lane >> 5;
        const int g = lane >> 4, q = (lane & 15) >> 2, p = lane & 3;
        int woff[2];
#pragma unroll
        for (int n = 0; n < 2; ++n) {
            const int c = 4 * (n ^ half) + 2 * (p & 1) + (g & 1);
            woff[n] = (4 * hh + q) * 128 + ((c ^ ((q >> 1) << 2)) << 4) + (p >> 1) * 8;
        }
        f32x16 acc[2] = {f32x16{}, f32x16{}};
#pragma unroll
        for (int c = 0; c < 4; ++c) {
            const lds_ptr cb = lds + (c == 0 ? K2_SF + half * 16384 : c == 3 ? K2_CV + half * 16384 : half * K2_SLOT + (c - 1) * 16384);
            if (c == 1) {
                lds_add(fl + C_S1 + half * 4, lane);
                lds_wait_ge(fl + C_S1 + half * 4, 4u);
            }
            if (c == 3) lds_wait_ge(fl + C_S2, 4u);
#pragma unroll
            for (int i = 0; i < 8; ++i) {
#pragma unroll
                for (int n = 0; n < 2; ++n) {
                    const lds_ptr wp = cb + woff[n] + i * 2048;
                    acc[n] = MFMA32(cat8(lds_tr(wp), lds_tr(wp + 1024)), of[(c * 8 + i) % 24], acc[n]);
                }
            }
            SBAR();
            if (c == 0) {
#pragma unroll
                for (int i = 0; i < 8; ++i) of[i] = PB(ld_sc1(rs, og + (24 + i) * 1024));
            }
            SBAR();
        }
        DG(7);
        const lds_ptr xo = lds + K2_SF + w * 4096 + lane * 16;
#pragma unroll
        for (int i = 0; i < 4; ++i) *(LAS f32x4*)(xo + i * 1024) = f32x4{acc[1][4 * i], acc[1][4 * i + 1], acc[1][4 * i + 2], acc[1][4 * i + 3]};
        lds_add(fl + F_R + w * 4, lane);
        lds_wait_ge(fl + F_R + (w ^ 4) * 4, 1u);
        const lds_ptr xi = lds + K2_SF + (w ^ 4) * 4096 + lane * 16;
#pragma unroll
        for (int i = 0; i < 4; ++i) {
            const f32x4 t = *(const LAS f32x4*)(xi + i * 1024);
#pragma unroll
            for (int j = 0; j < 4; ++j) acc[0][4 * i + j] += t[j];
        }
        char* dst = out + (size_t)(T3 * 32 + r) * 4096 + 2048 + nt * 128 + half * 64 + hh * 32;
        *(u32x4*)(dst) = pack8n(acc[0], 0);
        *(u32x4*)(dst + 16) = pack8n(acc[0], 1);
    }
    DG_END(8);
}

extern "C" void kernel_launch(void* const* d_in, const int* in_sizes, int n_in,
                              void* d_out, int out_size, void* d_ws, size_t ws_size,
                              hipStream_t stream) {
    const void* A_q = d_in[0];
    const void* A_k = d_in[1];
    const void* A_v = d_in[2];
    const void* A_shift = d_in[3];
    const void* A_ssmk = d_in[4];
    const void* A_D = d_in[5];
    const void* B_x = d_in[6];
    const void* B_wqkv = d_in[7];
    const void* B_wo = d_in[8];
    char* ws = (char*)d_ws;
    k1_qkv<<<256, 512, 0, stream>>>((const char*)B_x, (const char*)B_wqkv, ws);
    k2_attn<<<256, 768, 0, stream>>>(ws, (const char*)A_q, (const char*)A_k, (const char*)A_v, (const char*)A_shift,
                                     (const char*)A_ssmk, (const unsigned*)A_D, (const char*)B_wo, (char*)d_out);
}
```

```cpp
#include <hip/hip_runtime.h>
#include <hip/hip_bf16.h>
#include <stdint.h>

typedef short bf16x8 __attribute__((ext_vector_type(8)));
typedef short s16x4 __attribute__((ext_vector_type(4)));
typedef float f32x16 __attribute__((ext_vector_type(16)));
typedef float f32x4 __attribute__((ext_vector_type(4)));
typedef float f32x2 __attribute__((ext_vector_type(2)));
typedef unsigned u32x4 __attribute__((ext_vector_type(4)));
typedef unsigned u32x2 __attribute__((ext_vector_type(2)));
typedef __bf16 bf16x2_t __attribute__((ext_vector_type(2)));

#define LAS __attribute__((address_space(3)))
typedef LAS char* lds_ptr;
#define DI __device__ __forceinline__
#define SBAR() __builtin_amdgcn_sched_barrier(0)
#define MFMA32(a, b, c) __builtin_amdgcn_mfma_f32_32x32x16_bf16((a), (b), (c), 0, 0, 0)
#define MFMA16(a, b, c) __builtin_amdgcn_mfma_f32_16x16x32_bf16((a), (b), (c), 0, 0, 0)
#define WAIT_BAR(N) asm volatile("s_waitcnt vmcnt(" #N ") lgkmcnt(0)\n\ts_barrier" ::: "memory")
#define LGKM_BAR() asm volatile("s_waitcnt lgkmcnt(0)\n\ts_barrier" ::: "memory")

#define DG_DECL(K)
#define DG(i)
#define DG_END(i)

constexpr int S_LEN = 2048, DM = 1024, NH = 32;

constexpr size_t WS_Q = 0;
constexpr size_t WS_KV = 4u << 20;
constexpr size_t WS_O = 12u << 20;
constexpr size_t WS_KVR = 16u << 20;
constexpr size_t WS_ZT = 20u << 20;
constexpr size_t WS_CNT = 24u << 20;

DI unsigned cvtpk(float lo, float hi) {
    f32x2 v = {lo, hi};
    return __builtin_bit_cast(unsigned, __builtin_convertvector(v, bf16x2_t));
}
DI float bflo(unsigned u) { return __uint_as_float(u << 16); }
DI float bfhi(unsigned u) { return __uint_as_float(u & 0xffff0000u); }

DI void glds16(unsigned voff, const void* sbase, unsigned lds_dst) {
    unsigned sv;
    asm volatile("s_mov_b32 %0, m0\n\ts_mov_b32 m0, %3\n\ts_nop 0\n\tglobal_load_lds_dwordx4 %1, %2\n\ts_mov_b32 m0, %0"
                 : "=&s"(sv) : "v"(voff), "s"(sbase), "s"(lds_dst) : "memory");
}
DI bf16x8 lds_rd128(lds_ptr p) { return *(const LAS bf16x8*)p; }
DI s16x4 lds_tr(lds_ptr p) {
    return __builtin_bit_cast(s16x4, __builtin_amdgcn_ds_read_tr16_b64_v4i16((LAS s16x4*)p));
}
DI bf16x8 cat8(s16x4 a, s16x4 b) { return __builtin_shufflevector(a, b, 0, 1, 2, 3, 4, 5, 6, 7); }
DI u32x4 pack8(const f32x16& x, int s, float sc) {
    u32x4 p;
    p[0] = cvtpk(x[8 * s + 0] * sc, x[8 * s + 1] * sc);
    p[1] = cvtpk(x[8 * s + 2] * sc, x[8 * s + 3] * sc);
    p[2] = cvtpk(x[8 * s + 4] * sc, x[8 * s + 5] * sc);
    p[3] = cvtpk(x[8 * s + 6] * sc, x[8 * s + 7] * sc);
    return p;
}
DI u32x4 pack8n(const f32x16& x, int s) {
    u32x4 p;
    p[0] = cvtpk(x[8 * s + 0], x[8 * s + 1]);
    p[1] = cvtpk(x[8 * s + 2], x[8 * s + 3]);
    p[2] = cvtpk(x[8 * s + 4], x[8 * s + 5]);
    p[3] = cvtpk(x[8 * s + 6], x[8 * s + 7]);
    return p;
}
DI void rnd2(float& a, float& b) { unsigned u = cvtpk(a, b); a = bflo(u); b = bfhi(u); }

#define CONV_PACE()
#define RAW_BAR() asm volatile("s_barrier" ::: "memory")
DI unsigned gld32(unsigned voff, const void* sbase) {
    unsigned r;
    asm volatile("global_load_dword %0, %1, %2" : "=v"(r) : "v"(voff), "s"(sbase) : "memory");
    return r;
}
DI u32x2 gld64(unsigned voff, const void* sbase) {
    u32x2 r;
    asm volatile("global_load_dwordx2 %0, %1, %2" : "=v"(r) : "v"(voff), "s"(sbase) : "memory");
    return r;
}
DI float mul_sv(float s, float v) { float r; asm("v_mul_f32_e32 %0, %1, %2" : "=v"(r) : "s"(s), "v"(v)); return r; }
DI float mul_vv(float a, float b) { float r; asm("v_mul_f32_e32 %0, %1, %2" : "=v"(r) : "v"(a), "v"(b)); return r; }
DI void fmac_vv(float& acc, float a, float b) { asm("v_fmac_f32_e32 %0, %1, %2" : "+v"(acc) : "v"(a), "v"(b)); }
DI void fmac_sv(float& acc, float s, float v) { asm("v_fmac_f32_e32 %0, %1, %2" : "+v"(acc) : "s"(s), "v"(v)); }
typedef __attribute__((address_space(1))) unsigned gu32;
#define RLX_AGENT __ATOMIC_RELAXED, __HIP_MEMORY_SCOPE_AGENT
#define CFENCE() asm volatile("" ::: "memory")
#define DRAIN() asm volatile("s_waitcnt vmcnt(0)" ::: "memory")
constexpr int AUX_SC1 = 16;
DI u32x4 ld_sc1(__amdgpu_buffer_rsrc_t rs, unsigned off) { return __builtin_amdgcn_raw_buffer_load_b128(rs, (int)off, 0, AUX_SC1); }
DI void st_sc1(u32x4 v, __amdgpu_buffer_rsrc_t rs, unsigned off) { __builtin_amdgcn_raw_buffer_store_b128(v, rs, (int)off, 0, AUX_SC1); }
DI unsigned lds_add_rtn(lds_ptr p, int lane) {
    unsigned old = 0;
    CFENCE();
    if (lane == 0) old = __hip_atomic_fetch_add((LAS unsigned*)p, 1u, __ATOMIC_RELAXED, __HIP_MEMORY_SCOPE_WORKGROUP);
    old = __builtin_amdgcn_readfirstlane(old);
    CFENCE();
    return old;
}
DI void lds_add(lds_ptr p, int lane) {
    CFENCE();
    if (lane == 0) (void)__hip_atomic_fetch_add((LAS unsigned*)p, 1u, __ATOMIC_RELAXED, __HIP_MEMORY_SCOPE_WORKGROUP);
    CFENCE();
}
DI void lds_wait_ge(lds_ptr p, unsigned v) {
    CFENCE();
    while ((unsigned)__builtin_amdgcn_readfirstlane(*(volatile LAS unsigned*)p) < v) __builtin_amdgcn_s_sleep(1);
    CFENCE();
}
DI void poll_ge(gu32* word, unsigned want) {
    CFENCE();
    for (unsigned spins = 0; spins < (1u << 22); ++spins) {
        if ((unsigned)__builtin_amdgcn_readfirstlane(__hip_atomic_load(word, RLX_AGENT)) >= want) break;
        __builtin_amdgcn_s_sleep(1);
    }
    __builtin_amdgcn_fence(__ATOMIC_ACQUIRE, "wavefront");
    CFENCE();
}
#define PRIO(x) do { __builtin_amdgcn_s_setprio(x); SBAR(); } while (0)
DI u32x4 gld128(unsigned voff, const void* sbase) {
    u32x4 r;
    asm volatile("global_load_dwordx4 %0, %1, %2" : "=v"(r) : "v"(voff), "s"(sbase) : "memory");
    return r;
}

constexpr int K1_STAGE = 40960;
__global__ __launch_bounds__(512, 2) void k1_qkv(const char* __restrict__ X, const char* __restrict__ W,
                                                 char* __restrict__ ws) {
    __shared__ __attribute__((aligned(16))) char lds_raw[4 * K1_STAGE];
    const lds_ptr lds = (lds_ptr)lds_raw;
    const unsigned lds0 = (unsigned)(uintptr_t)lds;
    const int tid = threadIdx.x, lane = tid & 63;
    const int w = __builtin_amdgcn_readfirstlane(tid >> 6);
    const int b = blockIdx.x, xcd = b & 7, idx = b >> 3;
    const int hp = (xcd >> 1) * 4 + (idx & 3), tb = (xcd & 1) * 8 + (idx >> 2);
    DG_DECL(0)

    if (w >= 4) {
        const int lw = w - 4;
        const int rot = (tb & 3) * 4;
        unsigned voffX[4], voffW[2];
#pragma unroll
        for (int pp = 0; pp < 4; ++pp) {
            const int row = 32 * lw + 8 * pp + (lane >> 3);
            const int ch = (lane & 7) ^ ((row >> 1) & 7);
            voffX[pp] = (unsigned)(tb * 128 + row) * 2048u + ch * 16;
        }
#pragma unroll
        for (int pp = 0; pp < 2; ++pp) {
            const int krow = 16 * lw + 8 * pp + (lane >> 3);
            const int ch = (lane & 7) ^ (((lane >> 4) & 1) << 2);
            voffW[pp] = (unsigned)krow * 6144u + hp * 128 + ch * 16;
        }
        auto issue = [&](int kt) {
            const int kr = (kt + rot) & 15;
            const char* xb = X + kr * 128;
            const char* wb = W + (size_t)kr * (64 * 6144);
            const unsigned st = lds0 + (kt & 3) * K1_STAGE;
#pragma unroll
            for (int pp = 0; pp < 4; ++pp) glds16(voffX[pp], xb, st + (4 * lw + pp) * 1024);
#pragma unroll
            for (int sec = 0; sec < 3; ++sec)
#pragma unroll
                for (int pp = 0; pp < 2; ++pp)
                    glds16(voffW[pp], wb + sec * 2048, st + 16384 + sec * 8192 + (2 * lw + pp) * 1024);
        };
        issue(0); issue(1); issue(2);
#pragma unroll 1
        for (int kt = 0; kt < 13; ++kt) {
            WAIT_BAR(20);
            issue(kt + 3);
        }
        WAIT_BAR(20);
        WAIT_BAR(10);
        WAIT_BAR(0);
        if (b == 0 && lw == 0 && lane < 24) *(unsigned*)(ws + WS_CNT + lane * 256) = 0u;
        DG_END(5);
        return;
    }

    const int wm = w >> 1, wn = w & 1;
    const int r = lane & 31, hh = lane >> 5;
    const int g = lane >> 4, q = (lane & 15) >> 2, p = lane & 3;
    const int xoff0 = (64 * wm + r) * 128 + ((hh ^ ((r >> 1) & 7)) << 4);
    const int woff0 = 16384 + (8 * hh + q) * 128 + (((4 * wn + 2 * (g & 1) + (p >> 1)) ^ ((q >> 1) << 2)) << 4) + (p & 1) * 8;

    f32x16 acc[2][3];
#pragma unroll
    for (int m = 0; m < 2; ++m)
#pragma unroll
        for (int s = 0; s < 3; ++s) acc[m][s] = f32x16{};
    bf16x8 xf[2][2];
    s16x4 wl[2][3], wh[2][3];
#define K1_WF(f, sec) cat8(wl[f][sec], wh[f][sec])
#define K1_STEP(F, st, t, BAR_HERE) do { \
        const lds_ptr xp_ = (st) + (xoff0 ^ ((t) << 5)); const lds_ptr wp_ = (st) + woff0 + (t) * 2048; \
        acc[0][0] = MFMA32(K1_WF(F, 0), xf[F][0], acc[0][0]); SBAR(); \
        if (BAR_HERE) { LGKM_BAR(); SBAR(); } \
        xf[F ^ 1][0] = lds_rd128(xp_); wl[F ^ 1][0] = lds_tr(wp_); wh[F ^ 1][0] = lds_tr(wp_ + 512); SBAR(); \
        acc[0][1] = MFMA32(K1_WF(F, 1), xf[F][0], acc[0][1]); SBAR(); \
        wl[F ^ 1][1] = lds_tr(wp_ + 8192); wh[F ^ 1][1] = lds_tr(wp_ + 8192 + 512); SBAR(); \
        acc[0][2] = MFMA32(xf[F][0], K1_WF(F, 2), acc[0][2]); SBAR(); \
        wl[F ^ 1][2] = lds_tr(wp_ + 16384); wh[F ^ 1][2] = lds_tr(wp_ + 16384 + 512); xf[F ^ 1][1] = lds_rd128(xp_ + 4096); SBAR(); \
        acc[1][0] = MFMA32(K1_WF(F, 0), xf[F][1], acc[1][0]); SBAR(); \
        acc[1][1] = MFMA32(K1_WF(F, 1), xf[F][1], acc[1][1]); \
        acc[1][2] = MFMA32(xf[F][1], K1_WF(F, 2), acc[1][2]); SBAR(); \
    } while (0)

    LGKM_BAR();
    DG(3);
    {
        const lds_ptr xp_ = lds + xoff0; const lds_ptr wp_ = lds + woff0;
        xf[0][0] = lds_rd128(xp_); xf[0][1] = lds_rd128(xp_ + 4096);
#pragma unroll
        for (int sec = 0; sec < 3; ++sec) { wl[0][sec] = lds_tr(wp_ + sec * 8192); wh[0][sec] = lds_tr(wp_ + sec * 8192 + 512); }
    }
    SBAR();
#pragma unroll
    for (int kt = 0; kt < 15; ++kt) {
        const lds_ptr st = lds + (kt & 3) * K1_STAGE;
        const lds_ptr sn = lds + ((kt + 1) & 3) * K1_STAGE;
        K1_STEP(0, st, 1, false);
        K1_STEP(1, st, 2, false);
        K1_STEP(0, st, 3, false);
        K1_STEP(1, sn, 0, true);
    }
    {
        const lds_ptr st = lds + 3 * K1_STAGE;
        K1_STEP(0, st, 1, false);
        K1_STEP(1, st, 2, false);
        K1_STEP(0, st, 3, false);
#pragma unroll
        for (int m = 0; m < 2; ++m) {
            acc[m][1] = MFMA32(K1_WF(1, 1), xf[1][m], acc[m][1]);
            acc[m][2] = MFMA32(xf[1][m], K1_WF(1, 2), acc[m][2]);
            acc[m][0] = MFMA32(K1_WF(1, 0), xf[1][m], acc[m][0]);
        }
    }
    DG(4);
    {
        const int h = 2 * hp + wn;
        const float qscale = 0.17677669529663687f * 1.4426950408889634f;
#pragma unroll
        for (int m = 0; m < 2; ++m) {
            const int T = tb * 4 + wm * 2 + m;
            char* dq = ws + WS_Q + (size_t)(h * 64 + T) * 2048 + lane * 16;
            char* dk = ws + WS_KV + (size_t)(h * 64 + T) * 4096 + lane * 16;
            *(u32x4*)(dk) = pack8n(acc[m][1], 0);
            *(u32x4*)(dk + 1024) = pack8n(acc[m][1], 1);
            *(u32x4*)(dk + 2048) = pack8n(acc[m][2], 0);
            *(u32x4*)(dk + 3072) = pack8n(acc[m][2], 1);
            *(u32x4*)(dq) = pack8(acc[m][0], 0, qscale);
            *(u32x4*)(dq + 1024) = pack8(acc[m][0], 1, qscale);
        }
    }
    DG_END(5);
}

constexpr int K2_SLOT = 32768;
constexpr int K2_SF = 65536;
constexpr int K2_CV = 98304;
constexpr int CV_HA = 0, CV_HB = 4160, CV_KV = 8320, CV_BYTES = 8320 + 480 + 4096;
constexpr int K2_LX = K2_CV + 4 * CV_BYTES;
constexpr int K2_FL = K2_LX + 2048;
constexpr int K2_LDS = K2_FL + 256;
constexpr int F_X = 0, F_R = 32, C_XD = 64, C_OST = 68, F_GO = 72, C_S1 = 76, C_S2 = 84, C_ZST = 88, F_ZGO = 92, C_CVF = 96, C_U0 = 100;
#define NG_IN 3

__global__ __launch_bounds__(768, 3) void k2_attn(char* __restrict__ ws, const char* __restrict__ Aq,
                                                  const char* __restrict__ Ak, const char* __restrict__ Av,
                                                  const char* __restrict__ shiftk, const char* __restrict__ ssmk,
                                                  const unsigned* __restrict__ ssmD, const char* __restrict__ Wo,
                                                  char* __restrict__ out) {
    __shared__ __attribute__((aligned(16))) char lds_raw[K2_LDS];
    const lds_ptr lds = (lds_ptr)lds_raw;
    const lds_ptr fl = lds + K2_FL;
    const unsigned lds0 = (unsigned)(uintptr_t)lds;
    const int tid = threadIdx.x, lane = tid & 63;
    const int w = __builtin_amdgcn_readfirstlane(tid >> 6);
    const int b = blockIdx.x, xcd = b & 7, idx = b >> 3;
    const int h = xcd * 4 + (idx >> 3), qb = idx & 7;
    const int tt = xcd * 2 + (idx >> 4), nt = idx & 15;
    const char* kvg = ws + WS_KV + (size_t)h * 64 * 4096;
    const __amdgpu_buffer_rsrc_t rs = __builtin_amdgcn_make_buffer_rsrc(ws, 0, 32 << 20, 0x00020000);
    gu32* const cnt = (gu32*)(ws + WS_CNT);
    const unsigned wvoff = (unsigned)(lane >> 3) * 2048u + nt * 128 + (((lane & 7) ^ (((lane >> 4) & 1) << 2)) << 4);
    DG_DECL(1)

    if (w >= 8) {
        const int cw = w - 8;
        const unsigned voff = lane * 16;
        __builtin_amdgcn_s_setprio(3);
        auto issue = [&](int st) {
            const char* src = kvg + (st * 4 + cw) * 4096;
            const unsigned dst = lds0 + (st & 1) * K2_SLOT + cw * 4096;
#pragma unroll
            for (int i = 0; i < 4; ++i) glds16(voff, src + i * 1024, dst + i * 1024);
#pragma unroll
            for (int i = 0; i < 4; ++i) glds16(voff, src + 131072 + i * 1024, dst + 16384 + i * 1024);
        };
        issue(0);
        const int ch = xcd * 128 + idx * 4 + cw;
        const lds_ptr cv = lds + K2_CV + cw * CV_BYTES;
        {
            const u32x4 z4 = {0u, 0u, 0u, 0u};
            if (lane < 2) *(LAS u32x4*)(cv + CV_HA + lane * 16) = z4;
            if (lane < 30) *(LAS u32x4*)(cv + CV_KV + lane * 16) = z4;
            if (cw == 0 && lane < 16) *(LAS u32x4*)(fl + lane * 16) = z4;
        }
        WAIT_BAR(0);
        DG(3);
        issue(1);
        {
            const char* hrow = ssmk + (size_t)ch * 4096;
            const unsigned cv0 = lds0 + K2_CV + cw * CV_BYTES;
#pragma unroll
            for (int i = 0; i < 4; ++i) glds16(voff, hrow + i * 1024, cv0 + CV_HA + 32 + i * 1024);
        }
        const int c0 = xcd * 128 + cw * 32, cq = lane & 7, rg = lane >> 3;
        const int row0 = idx * 64 + rg * 8;
        const unsigned roff = (unsigned)row0 * 2048u + (c0 + cq * 4) * 2;
        const unsigned toffs = (unsigned)(c0 + (lane >> 3)) * 4096u + idx * 128;
        u32x2 kr[11], vr[8];
        u32x4 sk[2];
#pragma unroll
        for (int i = 0; i < 3; ++i) kr[i] = gld64(row0 == 0 ? roff : roff + (i - 3) * 2048, Ak);
#pragma unroll
        for (int i = 3; i < 11; ++i) kr[i] = gld64(roff, Ak + (i - 3) * 2048);
#pragma unroll
        for (int i = 0; i < 8; ++i) vr[i] = gld64(roff, Av + i * 2048);
        sk[0] = gld128((unsigned)(c0 + cq * 4) * 8u, shiftk); sk[1] = gld128((unsigned)(c0 + cq * 4) * 8u, shiftk + 16);
        WAIT_BAR(25);
        issue(2);
        asm volatile("s_waitcnt vmcnt(8)" ::: "memory");
        SBAR();
#pragma unroll
        for (int i = 0; i < 11; ++i) asm volatile("" : "+v"(kr[i]));
#pragma unroll
        for (int i = 0; i < 8; ++i) asm volatile("" : "+v"(vr[i]));
        asm volatile("" : "+v"(sk[0]), "+v"(sk[1]));
        DG(6);
        {
            if (row0 == 0) { kr[0] = u32x2{0u, 0u}; kr[1] = u32x2{0u, 0u}; kr[2] = u32x2{0u, 0u}; }
#pragma unroll
            for (int c = 0; c < 4; ++c) {
                const unsigned s01 = sk[c >> 1][(c & 1) * 2], s23 = sk[c >> 1][(c & 1) * 2 + 1];
                const float w0 = bflo(s01), w1 = bfhi(s01), w2 = bflo(s23), w3 = bfhi(s23);
                float kf[11], kv[8];
#pragma unroll
                for (int i = 0; i < 11; ++i) kf[i] = (c & 1) ? bfhi(kr[i][c >> 1]) : bflo(kr[i][c >> 1]);
#pragma unroll
                for (int i = 0; i < 8; ++i) {
                    float a = mul_vv(w3, kf[i]);
                    fmac_vv(a, w2, kf[i + 1]); fmac_vv(a, w1, kf[i + 2]); fmac_vv(a, w0, kf[i + 3]);
                    kv[i] = mul_vv(a, (c & 1) ? bfhi(vr[i][c >> 1]) : bflo(vr[i][c >> 1]));
                }
                u32x4 o4;
#pragma unroll
                for (int i = 0; i < 4; ++i) o4[i] = cvtpk(kv[7 - 2 * i], kv[6 - 2 * i]);
                *(LAS u32x4*)(cv + CV_HB + (cq * 4 + c) * 128 + ((rg ^ cq) << 4)) = o4;
            }
#pragma unroll
            for (int i = 0; i < 4; ++i) {
                const u32x4 t = *(const LAS u32x4*)(cv + CV_HB + i * 1024 + lane * 16);
                st_sc1(t, rs, (unsigned)WS_KVR + toffs + i * 32768 + (((lane & 7) ^ (2 * i + (lane >> 5))) << 4));
            }
        }
        CFENCE();
        {
            const u32x4 z4 = {0u, 0u, 0u, 0u};
            if (lane < 2) *(LAS u32x4*)(cv + CV_HB + lane * 16) = z4;
#pragma unroll
            for (int i = 0; i < 4; ++i) {
                const int off = 32 + i * 1024 + lane * 16;
                const u32x4 hv = *(const LAS u32x4*)(cv + CV_HA + off);
                const unsigned pv = *(const LAS unsigned*)(cv + CV_HA + off - 4);
                u32x4 sh;
                sh[0] = __builtin_amdgcn_alignbit(hv[0], pv, 16);
                sh[1] = __builtin_amdgcn_alignbit(hv[1], hv[0], 16);
                sh[2] = __builtin_amdgcn_alignbit(hv[2], hv[1], 16);
                sh[3] = __builtin_amdgcn_alignbit(hv[3], hv[2], 16);
                *(LAS u32x4*)(cv + CV_HB + off) = sh;
            }
        }
        DG(7);
        WAIT_BAR(0);
        if (cw == 0 && lane == 0) (void)__hip_atomic_fetch_add(cnt + (8 + xcd) * 64, 1u, RLX_AGENT);
        issue(3);
#pragma unroll
        for (int i = 0; i < 8; ++i)
            glds16(wvoff, Wo + (size_t)((i >> 2) * 64 + cw * 4 + (i & 3)) * 16384, lds0 + K2_SF + (i >> 2) * 16384 + (cw * 4 + (i & 3)) * 1024);
        if (cw == 0) { __builtin_amdgcn_s_sleep(16); poll_ge(cnt + (8 + xcd) * 64, 32u); }
        WAIT_BAR(0);
        issue(4);
        {
            u32x4 t[4];
#pragma unroll
            for (int i = 0; i < 4; ++i) t[i] = ld_sc1(rs, (unsigned)WS_KVR + ch * 4096 + i * 1024 + voff);
#pragma unroll
            for (int i = 0; i < 4; ++i) *(LAS u32x4*)(cv + CV_KV + 480 + i * 1024 + lane * 16) = t[i];
        }
        const int r1 = lane & 15, q = lane >> 4;
        const int toff = (r1 & 1) ? CV_HA + 2 * (16 + r1 + 9 - 8 * q) : CV_HB + 2 * (16 + r1 + 10 - 8 * q);
        const int boff = CV_KV + 448 + 32 * r1 + 16 * q;
        f32x4 y[8];
#pragma unroll
        for (int t = 0; t < 8; ++t) y[t] = f32x4{};
        auto group = [&](int bb, bool pace) {
            bf16x8 tf[8], bf[8];
#pragma unroll
            for (int a = 0; a < 8; ++a) {
                const LAS unsigned* tp = (const LAS unsigned*)(cv + toff + 64 * bb + 512 * a);
                u32x4 t4 = {tp[0], tp[1], tp[2], tp[3]};
                tf[a] = __builtin_bit_cast(bf16x8, t4);
                bf[a] = lds_rd128(cv + boff - 64 * bb + 512 * a);
            }
#pragma unroll
            for (int a = 0; a < 8; ++a)
#pragma unroll
                for (int ww = 0; ww + a < 8; ++ww) {
                    y[a + ww] = MFMA16(tf[a], bf[ww], y[a + ww]);
                    if (pace) asm volatile("s_nop 7");
                }
        };
#pragma unroll 1
        for (int it = 4; it < 7; ++it) {
            WAIT_BAR(0);
            issue(it + 1);
            if (it - 4 < NG_IN) group(it - 4, true);
        }
        WAIT_BAR(0);
        __builtin_amdgcn_s_setprio(0);
        u32x2 qr[8];
#pragma unroll
        for (int i = 0; i < 8; ++i) qr[i] = gld64(roff, Aq + i * 2048);
#pragma unroll 1
        for (int bb = NG_IN; bb < 8; ++bb) group(bb, false);
        DG(4);
        {
            const unsigned dd = ssmD[ch >> 1];
            const float dch = (ch & 1) ? bfhi(dd) : bflo(dd);
#pragma unroll
            for (int t = 0; t < 8; ++t) {
                const u32x2 kk = *(const LAS u32x2*)(cv + CV_KV + 480 + 512 * t + 32 * r1 + 16 * (q >> 1) + ((q & 1) ? 0 : 8));
                float k0 = mul_sv(dch, bfhi(kk[1])), k1 = mul_sv(dch, bflo(kk[1]));
                float k2 = mul_sv(dch, bfhi(kk[0])), k3 = mul_sv(dch, bflo(kk[0]));
                rnd2(k0, k1); rnd2(k2, k3);
                float y0 = y[t][0], y1 = y[t][1], y2 = y[t][2], y3 = y[t][3];
                rnd2(y0, y1); rnd2(y2, y3);
                u32x2 o = {cvtpk(y0 + k0, y1 + k1), cvtpk(y2 + k2, y3 + k3)};
                *(LAS u32x2*)(cv + CV_HA + 512 * t + 32 * r1 + 8 * q) = o;
            }
#pragma unroll
            for (int i = 0; i < 4; ++i)
                st_sc1(*(const LAS u32x4*)(cv + CV_HA + i * 1024 + lane * 16), rs, (unsigned)WS_ZT + ch * 4096 + i * 1024 + voff);
        }
        lds_add(fl + C_CVF, lane);
        lds_wait_ge(fl + C_CVF, 4u);
#pragma unroll
        for (int i = 0; i < 8; ++i)
            glds16(wvoff, Wo + (size_t)((i >> 2) * 64 + 48 + cw * 4 + (i & 3)) * 16384, lds0 + K2_CV + (i >> 2) * 16384 + (cw * 4 + (i & 3)) * 1024);
        asm volatile("s_waitcnt vmcnt(8)" ::: "memory");
        if (lds_add_rtn(fl + C_ZST, lane) == 3u && lane == 0) (void)__hip_atomic_fetch_add(cnt + (16 + xcd) * 64, 1u, RLX_AGENT);
        DRAIN();
        lds_add(fl + C_S2, lane);
        DG(8);
        if (cw == 0) { poll_ge(cnt + (16 + xcd) * 64, 32u); lds_add(fl + F_ZGO, lane); }
        else lds_wait_ge(fl + F_ZGO, 1u);
        DG(9);
        {
            const lds_ptr tr = lds + K2_CV + 32768 + cw * 4096;
            u32x4 t[4];
#pragma unroll
            for (int i = 0; i < 4; ++i)
                t[i] = ld_sc1(rs, (unsigned)WS_ZT + toffs + i * 32768 + (((lane & 7) ^ (2 * i + (lane >> 5))) << 4));
#pragma unroll
            for (int i = 0; i < 4; ++i) *(LAS u32x4*)(tr + i * 1024 + lane * 16) = t[i];
#pragma unroll
            for (int i = 0; i < 8; ++i) asm volatile("" : "+v"(qr[i]));
            u32x4 zc[4];
#pragma unroll
            for (int c = 0; c < 4; ++c) zc[c] = *(const LAS u32x4*)(tr + (cq * 4 + c) * 128 + ((rg ^ cq) << 4));
            char* dst = out + (size_t)row0 * 4096 + (c0 + cq * 4) * 2;
#pragma unroll
            for (int i = 0; i < 8; ++i) {
                float zz[4];
#pragma unroll
                for (int c = 0; c < 4; ++c) zz[c] = (i & 1) ? bfhi(zc[c][i >> 1]) : bflo(zc[c][i >> 1]);
                u32x2 o = {cvtpk(bflo(qr[i][0]) * zz[0], bfhi(qr[i][0]) * zz[1]),
                           cvtpk(bflo(qr[i][1]) * zz[2], bfhi(qr[i][1]) * zz[3])};
                *(u32x2*)(dst + i * 4096) = o;
            }
        }
        DG_END(5);
        return;
    }

    const int half = w >> 2;
    const int TA = qb * 8 + (w & 3) * 2 + half, TB = TA ^ 1;
    const char* qga = ws + WS_Q + (size_t)(h * 64 + TA) * 2048 + lane * 16;
    const char* qgb = ws + WS_Q + (size_t)(h * 64 + TB) * 2048 + lane * 16;
    const bf16x8 qa0 = *(const bf16x8*)qga, qa1 = *(const bf16x8*)(qga + 1024);
    const bf16x8 qb0 = *(const bf16x8*)qgb, qb1 = *(const bf16x8*)(qgb + 1024);
    bf16x8 sel;
    {
        const short one = (((lane >> 2) & 1) == ((lane >> 4) & 1)) ? (short)0x3f80 : (short)0;
#pragma unroll
        for (int i = 0; i < 8; ++i) sel[i] = one;
    }
    f32x16 oA = f32x16{}, oB = f32x16{};
    f32x4 lA = f32x4{}, lB = f32x4{};
    f32x16 negm, sA, sB;
    const lds_ptr lp = lds + half * 16384 + lane * 16;
    bf16x8 k0[2], k1[2], v0[2], v1[2];
    u32x4 p0, p1 = {0u, 0u, 0u, 0u};
#define EX(X, i) X[i] = __builtin_amdgcn_exp2f(X[i])
#define PB(x) __builtin_bit_cast(bf16x8, x)
    LGKM_BAR();
    DG(3);
    {
        const bf16x8 x0 = lds_rd128(lds + lane * 16), x1 = lds_rd128(lds + lane * 16 + 1024);
        k0[0] = lds_rd128(lp); k1[0] = lds_rd128(lp + 1024);
        v0[0] = lds_rd128(lp + 2048); v1[0] = lds_rd128(lp + 3072);
        sA = MFMA32(x0, qa0, f32x16{});
        sB = MFMA32(x0, qb0, f32x16{});
        sA = MFMA32(x1, qa1, sA);
        sB = MFMA32(x1, qb1, sB);
        float m = __builtin_fmaxf(sA[0], sB[0]);
#pragma unroll
        for (int i = 1; i < 16; ++i) m = __builtin_fmaxf(m, __builtin_fmaxf(sA[i], sB[i]));
        auto rr = __builtin_amdgcn_permlane32_swap(__float_as_uint(m), __float_as_uint(m), false, false);
        m = __builtin_fmaxf(__uint_as_float(rr[0]), __uint_as_float(rr[1]));
#pragma unroll
        for (int i = 0; i < 16; ++i) negm[i] = -m;
        sA = MFMA32(k0[0], qa0, negm);
        sA = MFMA32(k1[0], qa1, sA);
        v1[1] = v1[0];
        EX(sA, 0); EX(sA, 1);
    }
#define SUM16(l, p) asm volatile("v_mfma_f32_16x16x32_bf16 %0, %1, %2, %0" : "+v"(l) : "v"(sel), "v"(p))
#define HALF(C, oC, lC, N, qx, qy, ka, kb, oP, lP, vprev, vcur0, PRE) do { \
        oP = MFMA32(vprev, PB(p1), oP); SBAR(); \
        PRE; EX(C, 2); EX(C, 3); EX(C, 4); SBAR(); \
        N = MFMA32(ka, qx, negm); SBAR(); \
        EX(C, 5); EX(C, 6); EX(C, 7); SBAR(); \
        SUM16(lP, p1); SBAR(); \
        p0[0] = cvtpk(C[0], C[1]); p0[1] = cvtpk(C[2], C[3]); SBAR(); \
        N = MFMA32(kb, qy, N); SBAR(); \
        p0[2] = cvtpk(C[4], C[5]); p0[3] = cvtpk(C[6], C[7]); SBAR(); EX(C, 8); EX(C, 9); SBAR(); \
        oC = MFMA32(vcur0, PB(p0), oC); SBAR(); \
        EX(C, 10); EX(C, 11); EX(C, 12); SBAR(); \
        SUM16(lC, p0); SBAR(); \
        EX(C, 13); EX(C, 14); EX(C, 15); SBAR(); \
        p1[0] = cvtpk(C[8], C[9]); p1[1] = cvtpk(C[10], C[11]); p1[2] = cvtpk(C[12], C[13]); p1[3] = cvtpk(C[14], C[15]); SBAR(); \
        EX(N, 0); EX(N, 1); SBAR(); \
    } while (0)
#define RD4(Y, nx) do { v1[Y] = lds_rd128((nx) + 3072); v0[Y] = lds_rd128((nx) + 2048); \
        k1[Y] = lds_rd128((nx) + 1024); k0[Y] = lds_rd128(nx); } while (0)
#define KT(X, nx, PRE0) do { \
        HALF(sA, oA, lA, sB, qb0, qb1, k0[X], k1[X], oB, lB, v1[X ^ 1], v0[X], PRE0; RD4(X ^ 1, nx)); \
        HALF(sB, oB, lB, sA, qa0, qa1, k0[X ^ 1], k1[X ^ 1], oA, lA, v1[X], v0[X], (void)0); \
    } while (0)
    SBAR();
#pragma unroll 1
    for (int st = 0; st < 8; ++st) {
        const lds_ptr cur = lp + (st & 1) * K2_SLOT;
        const lds_ptr nxt = lp + ((st + 1) & 1) * K2_SLOT;
        PRIO(2);
        KT(0, cur + 4096, (void)0);
        PRIO(1);
        KT(1, cur + 8192, (void)0);
        PRIO(0);
        KT(0, cur + 12288, (void)0);
        PRIO(3);
        KT(1, nxt, if (st < 7) RAW_BAR(); SBAR());
    }
    oB = MFMA32(v1[1], PB(p1), oB);
    SBAR();
    SUM16(lB, p1);
    asm volatile("s_nop 7\n\ts_nop 7" ::: "memory");
    SBAR();
    DG(4);
    {
        const lds_ptr xo = lds + w * 4096 + lane * 16;
#pragma unroll
        for (int i = 0; i < 4; ++i) *(LAS f32x4*)(xo + i * 1024) = f32x4{oB[4 * i], oB[4 * i + 1], oB[4 * i + 2], oB[4 * i + 3]};
        *(LAS float*)(lds + K2_LX + w * 256 + lane * 4) = lB[0];
        lds_add(fl + F_X + w * 4, lane);
        lds_wait_ge(fl + F_X + (w ^ 4) * 4, 1u);
        const lds_ptr xi = lds + (w ^ 4) * 4096 + lane * 16;
#pragma unroll
        for (int i = 0; i < 4; ++i) {
            const f32x4 t = *(const LAS f32x4*)(xi + i * 1024);
#pragma unroll
            for (int j = 0; j < 4; ++j) oA[4 * i + j] += t[j];
        }
        lA[0] += *(const LAS float*)(lds + K2_LX + (w ^ 4) * 256 + lane * 4);
        lds_add(fl + C_XD, lane);
    }
    float l = lA[0];
    {
        float amax = __builtin_fabsf(oA[0]);
#pragma unroll
        for (int i = 1; i < 16; ++i) amax = __builtin_fmaxf(amax, __builtin_fabsf(oA[i]));
        bool nan = false;
#pragma unroll
        for (int i = 0; i < 16; ++i) nan |= (oA[i] != oA[i]);
        const bool ok = (l <= 1.0e30f) && (l >= 1.0e-18f) && (amax <= 3.0e38f) && !nan;
        if (__builtin_expect(__any(!ok), 0)) {
            float m = -INFINITY, ls = 0.f;
            oA = f32x16{};
#pragma unroll 1
            for (int t = 0; t < 64; ++t) {
                const char* g = kvg + t * 4096 + lane * 16;
                const bf16x8 a0 = *(const bf16x8*)g, a1 = *(const bf16x8*)(g + 1024);
                const bf16x8 b0 = *(const bf16x8*)(g + 2048), b1 = *(const bf16x8*)(g + 3072);
                f32x16 s = MFMA32(a0, qa0, f32x16{});
                s = MFMA32(a1, qa1, s);
                float tm = s[0];
#pragma unroll
                for (int i = 1; i < 16; ++i) tm = __builtin_fmaxf(tm, s[i]);
                auto rr = __builtin_amdgcn_permlane32_swap(__float_as_uint(tm), __float_as_uint(tm), false, false);
                tm = __builtin_fmaxf(__uint_as_float(rr[0]), __uint_as_float(rr[1]));
                const float mn = __builtin_fmaxf(m, tm);
                const float alpha = __builtin_amdgcn_exp2f(m - mn);
                m = mn;
                float ps = 0.f;
#pragma unroll
                for (int i = 0; i < 16; ++i) { s[i] = __builtin_amdgcn_exp2f(s[i] - mn); ps += s[i]; oA[i] *= alpha; }
                ls = ls * alpha + ps;
                oA = MFMA32(b0, PB(pack8n(s, 0)), oA);
                oA = MFMA32(b1, PB(pack8n(s, 1)), oA);
            }
            auto rr = __builtin_amdgcn_permlane32_swap(__float_as_uint(ls), __float_as_uint(ls), false, false);
            l = __uint_as_float(rr[0]) + __uint_as_float(rr[1]);
        }
    }
    {
        const float rl = 1.0f / l;
        const unsigned og = (unsigned)WS_O + (unsigned)(TA * 32 + h) * 2048u + lane * 16;
        st_sc1(pack8(oA, 0, rl), rs, og);
        st_sc1(pack8(oA, 1, rl), rs, og + 1024);
        DRAIN();
    }
    DG(5);
    const unsigned arr = lds_add_rtn(fl + C_OST, lane);
    const int j4 = w & 3;
    if (arr == 7u) {
        if (lane == 0) (void)__hip_atomic_fetch_add(cnt + qb * 64, 1u, RLX_AGENT);
        poll_ge(cnt + xcd * 64, 32u);
        lds_add(fl + F_GO, lane);
    } else {
        if (arr < 4u) {
            lds_wait_ge(fl + C_XD, 8u);
            const char* src = Wo + (size_t)((arr >> 1) * 64 + 16 + (arr & 1) * 16) * 16384;
#pragma unroll
            for (int i = 0; i < 16; ++i) glds16(wvoff, src + (size_t)i * 16384, lds0 + arr * 16384 + i * 1024);
            DRAIN();
            lds_add(fl + C_S1, lane);
        }
        lds_wait_ge(fl + F_GO, 1u);
    }
    DG(6);
    {
        const int T3 = tt * 4 + j4;
        const unsigned og = (unsigned)WS_O + (unsigned)T3 * 65536u + half * 32768 + lane * 16;
        bf16x8 of[24];
#pragma unroll
        for (int i = 0; i < 24; ++i) of[i] = PB(ld_sc1(rs, og + i * 1024));
        SBAR();
        const int r = lane & 31, hh = lane >> 5;
        const int g = lane >> 4, q = (lane & 15) >> 2, p = lane & 3;
        int woff[2];
#pragma unroll
        for (int n = 0; n < 2; ++n) {
            const int c = 4 * (n ^ half) + 2 * (p & 1) + (g & 1);
            woff[n] = (4 * hh + q) * 128 + ((c ^ ((q >> 1) << 2)) << 4) + (p >> 1) * 8;
        }
        f32x16 acc[2] = {f32x16{}, f32x16{}};
#pragma unroll
        for (int c = 0; c < 4; ++c) {
            const lds_ptr cb = lds + (c == 0 ? K2_SF + half * 16384 : c == 3 ? K2_CV + half * 16384 : half * K2_SLOT + (c - 1) * 16384);
            if (c == 1) {
                lds_add(fl + C_U0 + half * 4, lane);
                lds_wait_ge(fl + C_S1, 4u);
            }
            if (c == 3) lds_wait_ge(fl + C_S2, 4u);
#pragma unroll
            for (int i = 0; i < 8; ++i) {
#pragma unroll
                for (int n = 0; n < 2; ++n) {
                    const lds_ptr wp = cb + woff[n] + i * 2048;
                    acc[n] = MFMA32(cat8(lds_tr(wp), lds_tr(wp + 1024)), of[(c * 8 + i) % 24], acc[n]);
                }
            }
            SBAR();
            if (c == 0) {
#pragma unroll
                for (int i = 0; i < 8; ++i) of[i] = PB(ld_sc1(rs, og + (24 + i) * 1024));
            }
            SBAR();
        }
        DG(7);
        lds_wait_ge(fl + C_U0 + half * 4, 4u);
        const lds_ptr xo = lds + K2_SF + w * 4096 + lane * 16;
#pragma unroll
        for (int i = 0; i < 4; ++i) *(LAS f32x4*)(xo + i * 1024) = f32x4{acc[1][4 * i], acc[1][4 * i + 1], acc[1][4 * i + 2], acc[1][4 * i + 3]};
        lds_add(fl + F_R + w * 4, lane);
        lds_wait_ge(fl + F_R + (w ^ 4) * 4, 1u);
        const lds_ptr xi = lds + K2_SF + (w ^ 4) * 4096 + lane * 16;
#pragma unroll
        for (int i = 0; i < 4; ++i) {
            const f32x4 t = *(const LAS f32x4*)(xi + i * 1024);
#pragma unroll
            for (int j = 0; j < 4; ++j) acc[0][4 * i + j] += t[j];
        }
        char* dst = out + (size_t)(T3 * 32 + r) * 4096 + 2048 + nt * 128 + half * 64 + hh * 32;
        *(u32x4*)(dst) = pack8n(acc[0], 0);
        *(u32x4*)(dst + 16) = pack8n(acc[0], 1);
    }
    DG_END(8);
}

extern "C" void kernel_launch(void* const* d_in, const int* in_sizes, int n_in,
                              void* d_out, int out_size, void* d_ws, size_t ws_size,
                              hipStream_t stream) {
    const void* A_q = d_in[0];
    const void* A_k = d_in[1];
    const void* A_v = d_in[2];
    const void* A_shift = d_in[3];
    const void* A_ssmk = d_in[4];
    const void* A_D = d_in[5];
    const void* B_x = d_in[6];
    const void* B_wqkv = d_in[7];
    const void* B_wo = d_in[8];
    char* ws = (char*)d_ws;
    k1_qkv<<<256, 512, 0, stream>>>((const char*)B_x, (const char*)B_wqkv, ws);
    k2_attn<<<256, 768, 0, stream>>>(ws, (const char*)A_q, (const char*)A_k, (const char*)A_v, (const char*)A_shift,
                                     (const char*)A_ssmk, (const unsigned*)A_D, (const char*)B_wo, (char*)d_out);
}
```

```cpp
#include <hip/hip_runtime.h>
#include <hip/hip_bf16.h>
#include <stdint.h>

typedef short bf16x8 __attribute__((ext_vector_type(8)));
typedef short s16x4 __attribute__((ext_vector_type(4)));
typedef float f32x16 __attribute__((ext_vector_type(16)));
typedef float f32x4 __attribute__((ext_vector_type(4)));
typedef float f32x2 __attribute__((ext_vector_type(2)));
typedef unsigned u32x4 __attribute__((ext_vector_type(4)));
typedef unsigned u32x2 __attribute__((ext_vector_type(2)));
typedef __bf16 bf16x2_t __attribute__((ext_vector_type(2)));

#define LAS __attribute__((address_space(3)))
typedef LAS char* lds_ptr;
#define DI __device__ __forceinline__
#define SBAR() __builtin_amdgcn_sched_barrier(0)
#define MFMA32(a, b, c) __builtin_amdgcn_mfma_f32_32x32x16_bf16((a), (b), (c), 0, 0, 0)
#define MFMA16(a, b, c) __builtin_amdgcn_mfma_f32_16x16x32_bf16((a), (b), (c), 0, 0, 0)
#define WAIT_BAR(N) asm volatile("s_waitcnt vmcnt(" #N ") lgkmcnt(0)\n\ts_barrier" ::: "memory")
#define LGKM_BAR() asm volatile("s_waitcnt lgkmcnt(0)\n\ts_barrier" ::: "memory")

#define DG_DECL(K)
#define DG(i)
#define DG_END(i)

constexpr int S_LEN = 2048, DM = 1024, NH = 32;

constexpr size_t WS_Q = 0;
constexpr size_t WS_KV = 4u << 20;
constexpr size_t WS_O = 12u << 20;
constexpr size_t WS_KVR = 16u << 20;
constexpr size_t WS_ZT = 20u << 20;
constexpr size_t WS_CNT = 24u << 20;

DI unsigned cvtpk(float lo, float hi) {
    f32x2 v = {lo, hi};
    return __builtin_bit_cast(unsigned, __builtin_convertvector(v, bf16x2_t));
}
DI float bflo(unsigned u) { return __uint_as_float(u << 16); }
DI float bfhi(unsigned u) { return __uint_as_float(u & 0xffff0000u); }

DI void glds16(unsigned voff, const void* sbase, unsigned lds_dst) {
    unsigned sv;
    asm volatile("s_mov_b32 %0, m0\n\ts_mov_b32 m0, %3\n\ts_nop 0\n\tglobal_load_lds_dwordx4 %1, %2\n\ts_mov_b32 m0, %0"
                 : "=&s"(sv) : "v"(voff), "s"(sbase), "s"(lds_dst) : "memory");
}
DI bf16x8 lds_rd128(lds_ptr p) { return *(const LAS bf16x8*)p; }
DI s16x4 lds_tr(lds_ptr p) {
    return __builtin_bit_cast(s16x4, __builtin_amdgcn_ds_read_tr16_b64_v4i16((LAS s16x4*)p));
}
DI bf16x8 cat8(s16x4 a, s16x4 b) { return __builtin_shufflevector(a, b, 0, 1, 2, 3, 4, 5, 6, 7); }
DI u32x4 pack8(const f32x16& x, int s, float sc) {
    u32x4 p;
    p[0] = cvtpk(x[8 * s + 0] * sc, x[8 * s + 1] * sc);
    p[1] = cvtpk(x[8 * s + 2] * sc, x[8 * s + 3] * sc);
    p[2] = cvtpk(x[8 * s + 4] * sc, x[8 * s + 5] * sc);
    p[3] = cvtpk(x[8 * s + 6] * sc, x[8 * s + 7] * sc);
    return p;
}
DI u32x4 pack8n(const f32x16& x, int s) {
    u32x4 p;
    p[0] = cvtpk(x[8 * s + 0], x[8 * s + 1]);
    p[1] = cvtpk(x[8 * s + 2], x[8 * s + 3]);
    p[2] = cvtpk(x[8 * s + 4], x[8 * s + 5]);
    p[3] = cvtpk(x[8 * s + 6], x[8 * s + 7]);
    return p;
}
DI void rnd2(float& a, float& b) { unsigned u = cvtpk(a, b); a = bflo(u); b = bfhi(u); }

#define CONV_PACE()
#define RAW_BAR() asm volatile("s_barrier" ::: "memory")
DI unsigned gld32(unsigned voff, const void* sbase) {
    unsigned r;
    asm volatile("global_load_dword %0, %1, %2" : "=v"(r) : "v"(voff), "s"(sbase) : "memory");
    return r;
}
DI u32x2 gld64(unsigned voff, const void* sbase) {
    u32x2 r;
    asm volatile("global_load_dwordx2 %0, %1, %2" : "=v"(r) : "v"(voff), "s"(sbase) : "memory");
    return r;
}
DI float mul_sv(float s, float v) { float r; asm("v_mul_f32_e32 %0, %1, %2" : "=v"(r) : "s"(s), "v"(v)); return r; }
DI float mul_vv(float a, float b) { float r; asm("v_mul_f32_e32 %0, %1, %2" : "=v"(r) : "v"(a), "v"(b)); return r; }
DI float add_vv(float a, float b) { float r; asm("v_add_f32_e32 %0, %1, %2" : "=v"(r) : "v"(a), "v"(b)); return r; }
DI void fmac_vv(float& acc, float a, float b) { asm("v_fmac_f32_e32 %0, %1, %2" : "+v"(acc) : "v"(a), "v"(b)); }
DI void fmac_sv(float& acc, float s, float v) { asm("v_fmac_f32_e32 %0, %1, %2" : "+v"(acc) : "s"(s), "v"(v)); }
typedef __attribute__((address_space(1))) unsigned gu32;
#define RLX_AGENT __ATOMIC_RELAXED, __HIP_MEMORY_SCOPE_AGENT
#define CFENCE() asm volatile("" ::: "memory")
#define DRAIN() asm volatile("s_waitcnt vmcnt(0)" ::: "memory")
constexpr int AUX_SC1 = 16;
DI u32x4 ld_sc1(__amdgpu_buffer_rsrc_t rs, unsigned off) { return __builtin_amdgcn_raw_buffer_load_b128(rs, (int)off, 0, AUX_SC1); }
DI u32x4 ld_sc1s(__amdgpu_buffer_rsrc_t rs, unsigned off, int soff) { return __builtin_amdgcn_raw_buffer_load_b128(rs, (int)off, soff, AUX_SC1); }
DI void st_sc1(u32x4 v, __amdgpu_buffer_rsrc_t rs, unsigned off) { __builtin_amdgcn_raw_buffer_store_b128(v, rs, (int)off, 0, AUX_SC1); }
DI unsigned lds_add_rtn(lds_ptr p, int lane) {
    unsigned old = 0;
    CFENCE();
    if (lane == 0) old = __hip_atomic_fetch_add((LAS unsigned*)p, 1u, __ATOMIC_RELAXED, __HIP_MEMORY_SCOPE_WORKGROUP);
    old = __builtin_amdgcn_readfirstlane(old);
    CFENCE();
    return old;
}
DI void lds_add(lds_ptr p, int lane) {
    CFENCE();
    if (lane == 0) (void)__hip_atomic_fetch_add((LAS unsigned*)p, 1u, __ATOMIC_RELAXED, __HIP_MEMORY_SCOPE_WORKGROUP);
    CFENCE();
}
DI void lds_wait_ge(lds_ptr p, unsigned v) {
    CFENCE();
    while ((unsigned)__builtin_amdgcn_readfirstlane(*(volatile LAS unsigned*)p) < v) __builtin_amdgcn_s_sleep(1);
    CFENCE();
}
DI void poll_ge(gu32* word, unsigned want) {
    CFENCE();
    for (unsigned spins = 0; spins < (1u << 22); ++spins) {
        if ((unsigned)__builtin_amdgcn_readfirstlane(__hip_atomic_load(word, RLX_AGENT)) >= want) break;
        __builtin_amdgcn_s_sleep(1);
    }
    __builtin_amdgcn_fence(__ATOMIC_ACQUIRE, "wavefront");
    CFENCE();
}
#define PRIO(x) do { __builtin_amdgcn_s_setprio(x); SBAR(); } while (0)
DI u32x4 gld128(unsigned voff, const void* sbase) {
    u32x4 r;
    asm volatile("global_load_dwordx4 %0, %1, %2" : "=v"(r) : "v"(voff), "s"(sbase) : "memory");
    return r;
}

constexpr int K1_STAGE = 40960;
__global__ __launch_bounds__(512, 2) void k1_qkv(const char* __restrict__ X, const char* __restrict__ W,
                                                 char* __restrict__ ws) {
    __shared__ __attribute__((aligned(16))) char lds_raw[4 * K1_STAGE];
    const lds_ptr lds = (lds_ptr)lds_raw;
    const unsigned lds0 = (unsigned)(uintptr_t)lds;
    const int tid = threadIdx.x, lane = tid & 63;
    const int w = __builtin_amdgcn_readfirstlane(tid >> 6);
    const int b = blockIdx.x, xcd = b & 7, idx = b >> 3;
    const int hp = (xcd >> 1) * 4 + (idx & 3), tb = (xcd & 1) * 8 + (idx >> 2);
    DG_DECL(0)

    if (w >= 4) {
        const int lw = w - 4;
        const int rot = (tb & 3) * 4;
        unsigned voffX[4], voffW[2];
#pragma unroll
        for (int pp = 0; pp < 4; ++pp) {
            const int row = 32 * lw + 8 * pp + (lane >> 3);
            const int ch = (lane & 7) ^ ((row >> 1) & 7);
            voffX[pp] = (unsigned)(tb * 128 + row) * 2048u + ch * 16;
        }
#pragma unroll
        for (int pp = 0; pp < 2; ++pp) {
            const int krow = 16 * lw + 8 * pp + (lane >> 3);
            const int ch = (lane & 7) ^ (((lane >> 4) & 1) << 2);
            voffW[pp] = (unsigned)krow * 6144u + hp * 128 + ch * 16;
        }
        auto issue = [&](int kt) {
            const int kr = (kt + rot) & 15;
            const char* xb = X + kr * 128;
            const char* wb = W + (size_t)kr * (64 * 6144);
            const unsigned st = lds0 + (kt & 3) * K1_STAGE;
#pragma unroll
            for (int pp = 0; pp < 4; ++pp) glds16(voffX[pp], xb, st + (4 * lw + pp) * 1024);
#pragma unroll
            for (int sec = 0; sec < 3; ++sec)
#pragma unroll
                for (int pp = 0; pp < 2; ++pp)
                    glds16(voffW[pp], wb + sec * 2048, st + 16384 + sec * 8192 + (2 * lw + pp) * 1024);
        };
        issue(0); issue(1); issue(2);
#pragma unroll 1
        for (int kt = 0; kt < 13; ++kt) {
            WAIT_BAR(20);
            issue(kt + 3);
        }
        WAIT_BAR(20);
        WAIT_BAR(10);
        WAIT_BAR(0);
        if (lw == 0 && lane < 3) *(unsigned*)(ws + WS_CNT + (lane * 256 + b) * 256) = 0u;
        DG_END(5);
        return;
    }

    const int wm = w >> 1, wn = w & 1;
    const int r = lane & 31, hh = lane >> 5;
    const int g = lane >> 4, q = (lane & 15) >> 2, p = lane & 3;
    const int xoff0 = (64 * wm + r) * 128 + ((hh ^ ((r >> 1) & 7)) << 4);
    const int woff0 = 16384 + (8 * hh + q) * 128 + (((4 * wn + 2 * (g & 1) + (p >> 1)) ^ ((q >> 1) << 2)) << 4) + (p & 1) * 8;

    f32x16 acc[2][3];
#pragma unroll
    for (int m = 0; m < 2; ++m)
#pragma unroll
        for (int s = 0; s < 3; ++s) acc[m][s] = f32x16{};
    bf16x8 xf[2][2];
    s16x4 wl[2][3], wh[2][3];
#define K1_WF(f, sec) cat8(wl[f][sec], wh[f][sec])
#define K1_STEP(F, st, t, BAR_HERE) do { \
        const lds_ptr xp_ = (st) + (xoff0 ^ ((t) << 5)); const lds_ptr wp_ = (st) + woff0 + (t) * 2048; \
        acc[0][0] = MFMA32(K1_WF(F, 0), xf[F][0], acc[0][0]); SBAR(); \
        if (BAR_HERE) { LGKM_BAR(); SBAR(); } \
        xf[F ^ 1][0] = lds_rd128(xp_); wl[F ^ 1][0] = lds_tr(wp_); wh[F ^ 1][0] = lds_tr(wp_ + 512); SBAR(); \
        acc[0][1] = MFMA32(K1_WF(F, 1), xf[F][0], acc[0][1]); SBAR(); \
        wl[F ^ 1][1] = lds_tr(wp_ + 8192); wh[F ^ 1][1] = lds_tr(wp_ + 8192 + 512); SBAR(); \
        acc[0][2] = MFMA32(xf[F][0], K1_WF(F, 2), acc[0][2]); SBAR(); \
        wl[F ^ 1][2] = lds_tr(wp_ + 16384); wh[F ^ 1][2] = lds_tr(wp_ + 16384 + 512); xf[F ^ 1][1] = lds_rd128(xp_ + 4096); SBAR(); \
        acc[1][0] = MFMA32(K1_WF(F, 0), xf[F][1], acc[1][0]); SBAR(); \
        acc[1][1] = MFMA32(K1_WF(F, 1), xf[F][1], acc[1][1]); \
        acc[1][2] = MFMA32(xf[F][1], K1_WF(F, 2), acc[1][2]); SBAR(); \
    } while (0)

    LGKM_BAR();
    DG(3);
    {
        const lds_ptr xp_ = lds + xoff0; const lds_ptr wp_ = lds + woff0;
        xf[0][0] = lds_rd128(xp_); xf[0][1] = lds_rd128(xp_ + 4096);
#pragma unroll
        for (int sec = 0; sec < 3; ++sec) { wl[0][sec] = lds_tr(wp_ + sec * 8192); wh[0][sec] = lds_tr(wp_ + sec * 8192 + 512); }
    }
    SBAR();
#pragma unroll
    for (int kt = 0; kt < 15; ++kt) {
        const lds_ptr st = lds + (kt & 3) * K1_STAGE;
        const lds_ptr sn = lds + ((kt + 1) & 3) * K1_STAGE;
        K1_STEP(0, st, 1, false);
        K1_STEP(1, st, 2, false);
        K1_STEP(0, st, 3, false);
        K1_STEP(1, sn, 0, true);
    }
    {
        const lds_ptr st = lds + 3 * K1_STAGE;
        K1_STEP(0, st, 1, false);
        K1_STEP(1, st, 2, false);
        K1_STEP(0, st, 3, false);
#pragma unroll
        for (int m = 0; m < 2; ++m) {
            acc[m][1] = MFMA32(K1_WF(1, 1), xf[1][m], acc[m][1]);
            acc[m][2] = MFMA32(xf[1][m], K1_WF(1, 2), acc[m][2]);
            acc[m][0] = MFMA32(K1_WF(1, 0), xf[1][m], acc[m][0]);
        }
    }
    DG(4);
    {
        const int h = 2 * hp + wn;
        const float qscale = 0.17677669529663687f * 1.4426950408889634f;
#pragma unroll
        for (int m = 0; m < 2; ++m) {
            const int T = tb * 4 + wm * 2 + m;
            char* dq = ws + WS_Q + (size_t)(h * 64 + T) * 2048 + lane * 16;
            char* dk = ws + WS_KV + (size_t)(h * 64 + T) * 4096 + lane * 16;
            *(u32x4*)(dk) = pack8n(acc[m][1], 0);
            *(u32x4*)(dk + 1024) = pack8n(acc[m][1], 1);
            *(u32x4*)(dk + 2048) = pack8n(acc[m][2], 0);
            *(u32x4*)(dk + 3072) = pack8n(acc[m][2], 1);
            *(u32x4*)(dq) = pack8(acc[m][0], 0, qscale);
            *(u32x4*)(dq + 1024) = pack8(acc[m][0], 1, qscale);
        }
    }
    DG_END(5);
}

constexpr int K2_SLOT = 32768;
constexpr int K2_SF = 65536;
constexpr int K2_CV = 98304;
constexpr int CV_HA = 0, CV_HB = 4160, CV_KV = 8320, CV_BYTES = 8320 + 480 + 4096;
constexpr int K2_LX = K2_CV + 4 * CV_BYTES;
constexpr int K2_FL = K2_LX + 2048;
constexpr int K2_SCR = K2_FL + 256;
constexpr int K2_LDS = K2_SCR + 4096;
constexpr int F_X = 0, F_R = 32, F_W2 = 64, C_OST = 68, F_GO = 72, C_S1 = 76, C_S2 = 80, C_S3 = 128, C_ZST = 88, F_ZGO = 92,
              C_KST = 96, C_U0 = 100, C_LD = 108, F_KGO = 116, C_OL = 120;
#define NG_IN 2
#define GATE_WORD C_LD
#define GATE_VAL 4u
#define X_NZ 4
#define X_NW3 8
#define ZP_GATE 4u
#define ZL_GATE 8u
#define OWIN 8
#define KROT 2
#define NG_LAST 0
#define PACE_IN "s_nop 7"
#define PACE_OUT "s_nop 1"
#define P3_PRIO 1
#define AUX_PRIO 0

__global__ __launch_bounds__(768, 3) void k2_attn(char* __restrict__ ws, const char* __restrict__ Aq,
                                                  const char* __restrict__ Ak, const char* __restrict__ Av,
                                                  const char* __restrict__ shiftk, const char* __restrict__ ssmk,
                                                  const unsigned* __restrict__ ssmD, const char* __restrict__ Wo,
                                                  char* __restrict__ out) {
    __shared__ __attribute__((aligned(16))) char lds_raw[K2_LDS];
    const lds_ptr lds = (lds_ptr)lds_raw;
    const lds_ptr fl = lds + K2_FL;
    const unsigned lds0 = (unsigned)(uintptr_t)lds;
    const int tid = threadIdx.x, lane = tid & 63;
    const int w = __builtin_amdgcn_readfirstlane(tid >> 6);
    const int b = blockIdx.x, xcd = b & 7, idx = b >> 3;
    const int h = xcd * 4 + (idx >> 3), qb = idx & 7;
    const int tt = xcd * 2 + (idx >> 4), nt = idx & 15;
    const char* kvg = ws + WS_KV + (size_t)h * 64 * 4096;
    const __amdgpu_buffer_rsrc_t rs = __builtin_amdgcn_make_buffer_rsrc(ws, 0, 32 << 20, 0x00020000);
    gu32* const cnt = (gu32*)(ws + WS_CNT);
    auto signal = [&](int ty, int g) {
        if (lane < 32) (void)__hip_atomic_fetch_add(cnt + (ty * 256 + lane * 8 + g) * 64, 1u, RLX_AGENT);
    };
    const unsigned wvoff = (unsigned)(lane >> 3) * 2048u + nt * 128 + (((lane & 7) ^ (((lane >> 4) & 1) << 2)) << 4);
    auto wsrc = [&](int hf, int lp) { return Wo + (size_t)(hf * 64 + ((lp + nt * KROT * 2) & 63)) * 16384; };
    DG_DECL(1)

    if (w >= 8) {
        const int cw = w - 8;
        const unsigned voff = lane * 16;
        __builtin_amdgcn_s_setprio(3);
        auto issue = [&](int st) {
            const char* src = kvg + (st * 4 + cw) * 4096;
            const unsigned dst = lds0 + (st & 1) * K2_SLOT + cw * 4096;
#pragma unroll
            for (int i = 0; i < 4; ++i) glds16(voff, src + i * 1024, dst + i * 1024);
#pragma unroll
            for (int i = 0; i < 4; ++i) glds16(voff, src + 131072 + i * 1024, dst + 16384 + i * 1024);
        };
        issue(0);
        const int ch = xcd * 128 + idx * 4 + cw;
        const lds_ptr cv = lds + K2_CV + cw * CV_BYTES;
        {
            const u32x4 z4 = {0u, 0u, 0u, 0u};
            if (lane < 2) *(LAS u32x4*)(cv + CV_HA + lane * 16) = z4;
            if (lane < 30) *(LAS u32x4*)(cv + CV_KV + lane * 16) = z4;
            if (cw == 0 && lane < 16) *(LAS u32x4*)(fl + lane * 16) = z4;
        }
        WAIT_BAR(0);
        issue(1);
        {
            const char* hrow = ssmk + (size_t)ch * 4096;
            const unsigned cv0 = lds0 + K2_CV + cw * CV_BYTES;
#pragma unroll
            for (int i = 0; i < 4; ++i) glds16(voff, hrow + i * 1024, cv0 + CV_HA + 32 + i * 1024);
        }
        const int c0 = xcd * 128 + cw * 32, cq = lane & 7, rg = lane >> 3;
        const int row0 = idx * 64 + rg * 8;
        const unsigned roff = (unsigned)row0 * 2048u + (c0 + cq * 4) * 2;
        const unsigned toffs = (unsigned)(c0 + (lane >> 3)) * 4096u + idx * 128;
        {
            const unsigned sbase = (unsigned)(idx * 64 + cw * 16 + (lane >> 4)) * 2048u + xcd * 256;
#pragma unroll
            for (int i = 0; i < 4; ++i) {
                const unsigned so = sbase + (((lane & 15) ^ (((2 * cw + (i >> 1)) & 3) << 2)) << 4);
                glds16(so, Ak + i * 8192, lds0 + K2_SF + cw * 4096 + i * 1024);
                glds16(so, Av + i * 8192, lds0 + K2_SF + 16384 + cw * 4096 + i * 1024);
            }
            const unsigned ho = (unsigned)(idx * 64 - (idx ? 4 : 0) + (lane >> 4)) * 2048u + xcd * 256 + (((lane & 15) ^ 12) << 4);
            glds16(ho, Ak, lds0 + K2_SCR + cw * 1024);
        }
        u32x4 sk[2];
        sk[0] = gld128((unsigned)(c0 + cq * 4) * 8u, shiftk); sk[1] = gld128((unsigned)(c0 + cq * 4) * 8u, shiftk + 16);
        const unsigned dd = ssmD[ch >> 1];
        DG(11);
        WAIT_BAR(15);
        DG(12);
        issue(2);
        asm volatile("s_waitcnt vmcnt(19)" ::: "memory");
        {
            const u32x4 z4 = {0u, 0u, 0u, 0u};
            if (lane < 2) *(LAS u32x4*)(cv + CV_HB + lane * 16) = z4;
#pragma unroll
            for (int i = 0; i < 4; ++i) {
                const int off = 32 + i * 1024 + lane * 16;
                const u32x4 hv = *(const LAS u32x4*)(cv + CV_HA + off);
                const unsigned pv = *(const LAS unsigned*)(cv + CV_HA + off - 4);
                u32x4 sh;
                sh[0] = __builtin_amdgcn_alignbit(hv[0], pv, 16);
                sh[1] = __builtin_amdgcn_alignbit(hv[1], hv[0], 16);
                sh[2] = __builtin_amdgcn_alignbit(hv[2], hv[1], 16);
                sh[3] = __builtin_amdgcn_alignbit(hv[3], hv[2], 16);
                *(LAS u32x4*)(cv + CV_HB + off) = sh;
            }
        }
        DRAIN(); DG(9);
        WAIT_BAR(0);
        issue(3);
        SBAR();
        asm volatile("" : "+v"(sk[0]), "+v"(sk[1]));
        DG(6);
        {
            u32x2 kr[11], vr[8];
            const lds_ptr kb = lds + K2_SF + rg * 2048 + ((cw ^ (rg & 3)) << 6) + cq * 8;
            const lds_ptr hb = lds + (rg ? K2_SF + rg * 2048 - 768 : K2_SCR + cw * 1024 + 256) + ((cw ^ ((rg + 3) & 3)) << 6) + cq * 8;
#pragma unroll
            for (int i = 0; i < 3; ++i) kr[i] = *(const LAS u32x2*)(hb + i * 256);
#pragma unroll
            for (int i = 0; i < 8; ++i) kr[3 + i] = *(const LAS u32x2*)(kb + i * 256);
#pragma unroll
            for (int i = 0; i < 8; ++i) vr[i] = *(const LAS u32x2*)(kb + 16384 + i * 256);
            if (row0 == 0) { kr[0] = u32x2{0u, 0u}; kr[1] = u32x2{0u, 0u}; kr[2] = u32x2{0u, 0u}; }
            const lds_ptr tb = cv + CV_KV + 480;
#pragma unroll
            for (int c = 0; c < 4; ++c) {
                const unsigned s01 = sk[c >> 1][(c & 1) * 2], s23 = sk[c >> 1][(c & 1) * 2 + 1];
                const float w0 = bflo(s01), w1 = bfhi(s01), w2 = bflo(s23), w3 = bfhi(s23);
                float kf[11], kv[8];
#pragma unroll
                for (int i = 0; i < 11; ++i) kf[i] = (c & 1) ? bfhi(kr[i][c >> 1]) : bflo(kr[i][c >> 1]);
#pragma unroll
                for (int i = 0; i < 8; ++i) {
                    float a = mul_vv(w3, kf[i]);
                    fmac_vv(a, w2, kf[i + 1]); fmac_vv(a, w1, kf[i + 2]); fmac_vv(a, w0, kf[i + 3]);
                    kv[i] = mul_vv(a, (c & 1) ? bfhi(vr[i][c >> 1]) : bflo(vr[i][c >> 1]));
                }
                u32x4 o4;
#pragma unroll
                for (int i = 0; i < 4; ++i) o4[i] = cvtpk(kv[7 - 2 * i], kv[6 - 2 * i]);
                *(LAS u32x4*)(tb + (cq * 4 + c) * 128 + ((rg ^ cq) << 4)) = o4;
            }
#pragma unroll
            for (int i = 0; i < 4; ++i) {
                const u32x4 t = *(const LAS u32x4*)(tb + i * 1024 + lane * 16);
                st_sc1(t, rs, (unsigned)WS_KVR + toffs + i * 32768 + (((lane & 7) ^ (2 * i + (lane >> 5))) << 4));
            }
        }
        CFENCE();
        DG(7);
        WAIT_BAR(4);
        DG(3);
        issue(4);
        asm volatile("s_waitcnt vmcnt(8)" ::: "memory");
        if (lds_add_rtn(fl + C_KST, lane) == 3u) signal(1, xcd);
        WAIT_BAR(0);
        DG(5);
        if (cw == 0) { poll_ge(cnt + (256 + b) * 64, 32u); lds_add(fl + F_KGO, lane); }
        else { issue(5); lds_wait_ge(fl + F_KGO, 1u); }
        {
            u32x4 t[4];
#pragma unroll
            for (int i = 0; i < 4; ++i) t[i] = ld_sc1(rs, (unsigned)WS_KVR + ch * 4096 + i * 1024 + voff);
            if (cw == 0) issue(5);
#pragma unroll
            for (int i = 0; i < 4; ++i) *(LAS u32x4*)(cv + CV_KV + 480 + i * 1024 + lane * 16) = t[i];
        }
        const int r1 = lane & 15, q = lane >> 4;
        const int toff = (r1 & 1) ? CV_HA + 2 * (16 + r1 + 9 - 8 * q) : CV_HB + 2 * (16 + r1 + 10 - 8 * q);
        const int boff = CV_KV + 448 + 32 * r1 + 16 * q;
        f32x4 y[8];
#pragma unroll
        for (int t = 0; t < 8; ++t) y[t] = f32x4{};
        bf16x8 tf[8], bf[8];
        auto ldt = [&](int bb, int a) {
            const LAS unsigned* tp = (const LAS unsigned*)(cv + toff + 64 * bb + 512 * a);
            u32x4 t4 = {tp[0], tp[1], tp[2], tp[3]};
            tf[a] = __builtin_bit_cast(bf16x8, t4);
        };
        auto ldb = [&](int bb, int a) { bf[a] = lds_rd128(cv + boff - 64 * bb + 512 * a); };
#define GROUP(bb, PACE) do { \
            _Pragma("unroll") for (int a = 0; a < 8; ++a) { \
                _Pragma("unroll") for (int ww = 7 - a; ww >= 0; --ww) { \
                    y[a + ww] = MFMA16(tf[a], bf[ww], y[a + ww]); SBAR(); \
                    asm volatile(PACE); SBAR(); \
                } \
                ldt(((bb) + 1) & 7, a); ldb(((bb) + 1) & 7, 7 - a); SBAR(); \
            } } while (0)
#pragma unroll
        for (int a = 0; a < 8; ++a) { ldt(0, a); ldb(0, 7 - a); }
        SBAR();
#pragma unroll 1
        for (int bb = NG_IN - 3; bb < NG_IN; ++bb) {
            if (bb >= 0) GROUP(bb, PACE_IN);
            if (bb == NG_IN - 3) {
                WAIT_BAR(0);
                issue(6);
#pragma unroll
                for (int i = 0; i < 8; ++i)
                    glds16(wvoff, wsrc(i >> 2, cw * 4 + (i & 3)), lds0 + K2_SF + (i >> 2) * 16384 + (cw * 4 + (i & 3)) * 1024);
            } else if (bb == NG_IN - 2) {
                WAIT_BAR(8);
                issue(7);
#pragma unroll
                for (int i = 0; i < 8; ++i)
                    glds16(wvoff, wsrc(idx >> 4, 32 + cw * 8 + i), lds0 + K2_SCR + cw * 1024);
            } else {
                WAIT_BAR(8);
            }
        }
        u32x2 qr[8];
#pragma unroll
        for (int i = 0; i < 8; ++i)
            glds16(wvoff, wsrc(i >> 2, 16 + cw * 4 + (i & 3)), lds0 + (i >> 2) * 16384 + (cw * 4 + (i & 3)) * 1024);
#pragma unroll
        for (int i = 0; i < 8; ++i) qr[i] = gld64(roff, Aq + i * 2048);
#pragma unroll 1
        for (int bb = NG_IN; bb < NG_IN + NG_LAST; ++bb) GROUP(bb, PACE_IN);
        DRAIN();
        lds_add(fl + C_S1, lane);
        CFENCE();
        while ((unsigned)__builtin_amdgcn_readfirstlane(*(volatile LAS unsigned*)(fl + GATE_WORD)) < GATE_VAL) __builtin_amdgcn_s_sleep(2);
        CFENCE();
        __builtin_amdgcn_s_setprio(AUX_PRIO);
#pragma unroll 1
        for (int bb = NG_IN + NG_LAST; bb < 8; ++bb) GROUP(bb, PACE_OUT);
        DG(4);
        __builtin_amdgcn_s_setprio(3);
        {
            const float dch = (ch & 1) ? bfhi(dd) : bflo(dd);
#pragma unroll
            for (int t = 0; t < 8; ++t) {
                const u32x2 kk = *(const LAS u32x2*)(cv + CV_KV + 480 + 512 * t + 32 * r1 + 16 * (q >> 1) + ((q & 1) ? 0 : 8));
                float k0 = mul_sv(dch, bfhi(kk[1])), k1 = mul_sv(dch, bflo(kk[1]));
                float k2 = mul_sv(dch, bfhi(kk[0])), k3 = mul_sv(dch, bflo(kk[0]));
                rnd2(k0, k1); rnd2(k2, k3);
                float y0 = y[t][0], y1 = y[t][1], y2 = y[t][2], y3 = y[t][3];
                rnd2(y0, y1); rnd2(y2, y3);
                u32x2 o = {cvtpk(y0 + k0, y1 + k1), cvtpk(y2 + k2, y3 + k3)};
                *(LAS u32x2*)(cv + CV_HA + 512 * t + 32 * r1 + 8 * q) = o;
            }
#pragma unroll
            for (int i = 0; i < X_NZ; ++i)
                st_sc1(*(const LAS u32x4*)(cv + CV_HA + i * 1024 + lane * 16), rs, (unsigned)WS_ZT + ch * 4096 + i * 1024 + voff);
        }
        DG(10);
        DRAIN();
        if (lds_add_rtn(fl + C_ZST, lane) == 3u) {
            signal(2, xcd);
            lds_wait_ge(fl + C_OL, ZP_GATE);
            poll_ge(cnt + (512 + b) * 64, 32u);
            lds_add(fl + F_ZGO, lane);
        } else lds_wait_ge(fl + F_ZGO, 1u);
        lds_wait_ge(fl + C_OL, ZL_GATE);
        DG(13);
        {
            const lds_ptr tr = cv;
            u32x4 t[4];
#pragma unroll
            for (int i = 0; i < 4; ++i)
                t[i] = ld_sc1(rs, (unsigned)WS_ZT + toffs + i * 32768 + (((lane & 7) ^ (2 * i + (lane >> 5))) << 4));
#pragma unroll
            for (int i = 0; i < 4; ++i) *(LAS u32x4*)(tr + i * 1024 + lane * 16) = t[i];
#pragma unroll
            for (int i = 0; i < 8; ++i) asm volatile("" : "+v"(qr[i]));
            u32x4 zc[4];
#pragma unroll
            for (int c = 0; c < 4; ++c) zc[c] = *(const LAS u32x4*)(tr + (cq * 4 + c) * 128 + ((rg ^ cq) << 4));
            char* dst = out + (size_t)row0 * 4096 + (c0 + cq * 4) * 2;
#pragma unroll
            for (int i = 0; i < 8; ++i) {
                float zz[4];
#pragma unroll
                for (int c = 0; c < 4; ++c) zz[c] = (i & 1) ? bfhi(zc[c][i >> 1]) : bflo(zc[c][i >> 1]);
                u32x2 o = {cvtpk(bflo(qr[i][0]) * zz[0], bfhi(qr[i][0]) * zz[1]),
                           cvtpk(bflo(qr[i][1]) * zz[2], bfhi(qr[i][1]) * zz[3])};
                *(u32x2*)(dst + i * 4096) = o;
            }
        }
        DG_END(14);
        return;
    }

    const int half = w >> 2;
    const int TA = qb * 8 + (w & 3) * 2 + half, TB = TA ^ 1;
    const char* qga = ws + WS_Q + (size_t)(h * 64 + TA) * 2048 + lane * 16;
    const char* qgb = ws + WS_Q + (size_t)(h * 64 + TB) * 2048 + lane * 16;
    const bf16x8 qa0 = *(const bf16x8*)qga, qa1 = *(const bf16x8*)(qga + 1024);
    const bf16x8 qb0 = *(const bf16x8*)qgb, qb1 = *(const bf16x8*)(qgb + 1024);
    bf16x8 sel;
    {
        const short one = (((lane >> 2) & 1) == ((lane >> 4) & 1)) ? (short)0x3f80 : (short)0;
#pragma unroll
        for (int i = 0; i < 8; ++i) sel[i] = one;
    }
    f32x16 oA = f32x16{}, oB = f32x16{};
    f32x4 lA = f32x4{}, lB = f32x4{};
    f32x16 negm, sA, sB;
    const lds_ptr lp = lds + half * 16384 + lane * 16;
    bf16x8 k0[2], k1[2], v0[2], v1[2];
    u32x4 p0, p1 = {0u, 0u, 0u, 0u};
#define EX(X, i) X[i] = __builtin_amdgcn_exp2f(X[i])
#define PB(x) __builtin_bit_cast(bf16x8, x)
    LGKM_BAR();
    DG(3);
    {
        const bf16x8 x0 = lds_rd128(lds + lane * 16), x1 = lds_rd128(lds + lane * 16 + 1024);
        k0[0] = lds_rd128(lp); k1[0] = lds_rd128(lp + 1024);
        v0[0] = lds_rd128(lp + 2048); v1[0] = lds_rd128(lp + 3072);
        sA = MFMA32(x0, qa0, f32x16{});
        sB = MFMA32(x0, qb0, f32x16{});
        sA = MFMA32(x1, qa1, sA);
        sB = MFMA32(x1, qb1, sB);
        float m = __builtin_fmaxf(sA[0], sB[0]);
#pragma unroll
        for (int i = 1; i < 16; ++i) m = __builtin_fmaxf(m, __builtin_fmaxf(sA[i], sB[i]));
        auto rr = __builtin_amdgcn_permlane32_swap(__float_as_uint(m), __float_as_uint(m), false, false);
        m = __builtin_fmaxf(__uint_as_float(rr[0]), __uint_as_float(rr[1]));
#pragma unroll
        for (int i = 0; i < 16; ++i) negm[i] = -m;
        sA = MFMA32(k0[0], qa0, negm);
        sA = MFMA32(k1[0], qa1, sA);
        v1[1] = v1[0];
        EX(sA, 0); EX(sA, 1);
    }
#define SUM16(l, p) asm volatile("v_mfma_f32_16x16x32_bf16 %0, %1, %2, %0" : "+v"(l) : "v"(sel), "v"(p))
#define HALF(C, oC, lC, N, qx, qy, ka, kb, oP, lP, vprev, vcur0, PRE) do { \
        oP = MFMA32(vprev, PB(p1), oP); SBAR(); \
        PRE; EX(C, 2); EX(C, 3); EX(C, 4); SBAR(); \
        N = MFMA32(ka, qx, negm); SBAR(); \
        EX(C, 5); EX(C, 6); EX(C, 7); SBAR(); \
        SUM16(lP, p1); SBAR(); \
        p0[0] = cvtpk(C[0], C[1]); p0[1] = cvtpk(C[2], C[3]); SBAR(); \
        N = MFMA32(kb, qy, N); SBAR(); \
        p0[2] = cvtpk(C[4], C[5]); p0[3] = cvtpk(C[6], C[7]); SBAR(); EX(C, 8); EX(C, 9); SBAR(); \
        oC = MFMA32(vcur0, PB(p0), oC); SBAR(); \
        EX(C, 10); EX(C, 11); EX(C, 12); SBAR(); \
        SUM16(lC, p0); SBAR(); \
        EX(C, 13); EX(C, 14); EX(C, 15); SBAR(); \
        p1[0] = cvtpk(C[8], C[9]); p1[1] = cvtpk(C[10], C[11]); p1[2] = cvtpk(C[12], C[13]); p1[3] = cvtpk(C[14], C[15]); SBAR(); \
        EX(N, 0); EX(N, 1); SBAR(); \
    } while (0)
#define RD4(Y, nx) do { v1[Y] = lds_rd128((nx) + 3072); v0[Y] = lds_rd128((nx) + 2048); \
        k1[Y] = lds_rd128((nx) + 1024); k0[Y] = lds_rd128(nx); } while (0)
#define KT(X, nx, PRE0) do { \
        HALF(sA, oA, lA, sB, qb0, qb1, k0[X], k1[X], oB, lB, v1[X ^ 1], v0[X], PRE0; RD4(X ^ 1, nx)); \
        HALF(sB, oB, lB, sA, qa0, qa1, k0[X ^ 1], k1[X ^ 1], oA, lA, v1[X], v0[X], (void)0); \
    } while (0)
    SBAR();
#pragma unroll 1
    for (int st = 0; st < 8; ++st) {
        const lds_ptr cur = lp + (st & 1) * K2_SLOT;
        const lds_ptr nxt = lp + ((st + 1) & 1) * K2_SLOT;
        PRIO(2);
        KT(0, cur + 4096, (void)0);
        PRIO(1);
        KT(1, cur + 8192, (void)0);
        PRIO(0);
        KT(0, cur + 12288, (void)0);
        PRIO(3);
        KT(1, nxt, if (st < 7) RAW_BAR(); SBAR());
    }
    oB = MFMA32(v1[1], PB(p1), oB);
    SBAR();
    SUM16(lB, p1);
    asm volatile("s_nop 7\n\ts_nop 7" ::: "memory");
    SBAR();
    DG(4);
    {
        lds_add(fl + C_LD + half * 4, lane);
        const lds_ptr xo = lds + K2_SLOT + w * 4096 + lane * 16;
        *(LAS float*)(lds + K2_LX + w * 256 + lane * 4) = lB[0];
        lds_wait_ge(fl + C_LD + half * 4, 4u);
#pragma unroll
        for (int i = 0; i < 4; ++i) *(LAS f32x4*)(xo + i * 1024) = f32x4{oB[4 * i], oB[4 * i + 1], oB[4 * i + 2], oB[4 * i + 3]};
        lds_add(fl + F_X + w * 4, lane);
        lds_wait_ge(fl + F_X + (w ^ 4) * 4, 1u);
        const lds_ptr xi = lds + K2_SLOT + (w ^ 4) * 4096 + lane * 16;
#pragma unroll
        for (int i = 0; i < 4; ++i) {
            const f32x4 t = *(const LAS f32x4*)(xi + i * 1024);
#pragma unroll
            for (int j = 0; j < 4; ++j) oA[4 * i + j] = add_vv(oA[4 * i + j], t[j]);
        }
        lA[0] += *(const LAS float*)(lds + K2_LX + (w ^ 4) * 256 + lane * 4);
    }
    DG(9);
    float l = lA[0];
    {
        float t0 = 0.f, t1 = 0.f;
#pragma unroll
        for (int i = 0; i < 8; ++i) { fmac_sv(t0, 0.f, oA[i]); fmac_sv(t1, 0.f, oA[8 + i]); }
        const bool ok = (l <= 1.0e30f) && (l >= 1.0e-18f) && (t0 + t1 == 0.f);
        if (__builtin_expect(__any(!ok), 0)) {
            float m = -INFINITY, ls = 0.f;
            oA = f32x16{};
#pragma unroll 1
            for (int t = 0; t < 64; ++t) {
                const char* g = kvg + t * 4096 + lane * 16;
                const bf16x8 a0 = *(const bf16x8*)g, a1 = *(const bf16x8*)(g + 1024);
                const bf16x8 b0 = *(const bf16x8*)(g + 2048), b1 = *(const bf16x8*)(g + 3072);
                f32x16 s = MFMA32(a0, qa0, f32x16{});
                s = MFMA32(a1, qa1, s);
                float tm = s[0];
#pragma unroll
                for (int i = 1; i < 16; ++i) tm = __builtin_fmaxf(tm, s[i]);
                auto rr = __builtin_amdgcn_permlane32_swap(__float_as_uint(tm), __float_as_uint(tm), false, false);
                tm = __builtin_fmaxf(__uint_as_float(rr[0]), __uint_as_float(rr[1]));
                const float mn = __builtin_fmaxf(m, tm);
                const float alpha = __builtin_amdgcn_exp2f(m - mn);
                m = mn;
                float ps = 0.f;
#pragma unroll
                for (int i = 0; i < 16; ++i) { s[i] = __builtin_amdgcn_exp2f(s[i] - mn); ps += s[i]; oA[i] *= alpha; }
                ls = ls * alpha + ps;
                oA = MFMA32(b0, PB(pack8n(s, 0)), oA);
                oA = MFMA32(b1, PB(pack8n(s, 1)), oA);
            }
            auto rr = __builtin_amdgcn_permlane32_swap(__float_as_uint(ls), __float_as_uint(ls), false, false);
            l = __uint_as_float(rr[0]) + __uint_as_float(rr[1]);
        }
    }
    u32x4 w3[4];
    {
        const float rl = __builtin_amdgcn_rcpf(l);
        const unsigned og = (unsigned)WS_O + (unsigned)(TA * 32 + h) * 2048u + lane * 16;
        st_sc1(pack8(oA, 0, rl), rs, og);
        st_sc1(pack8(oA, 1, rl), rs, og + 1024);
        CFENCE();
#pragma unroll
        for (int i = 0; i < 4; ++i)
            glds16(wvoff, wsrc(half ^ 1, 32 + (w & 3) * 4 + i), lds0 + K2_SLOT + ((w ^ 4) * 4 + i) * 1024);
#pragma unroll
        for (int i = 0; i < 4; ++i) w3[i] = gld128(wvoff, wsrc(half, 48 + (w & 3) * 4 + i));
        asm volatile("s_waitcnt vmcnt(8)" ::: "memory");
    }
    DG(5);
    const unsigned arr = lds_add_rtn(fl + C_OST, lane);
    const int j4 = w & 3;
    if (arr == 7u) {
        signal(0, qb);
        poll_ge(cnt + b * 64, 32u);
        lds_add(fl + F_GO, lane);
    } else {
        lds_wait_ge(fl + F_GO, 1u);
    }
    DG(6);
    {
        const int T3 = tt * 4 + j4;
        const unsigned og = (unsigned)WS_O + (unsigned)T3 * 65536u + half * 32768 + lane * 16;
        bf16x8 of[OWIN];
#define LDO(i) do { of[(i) % OWIN] = PB(ld_sc1s(rs, og, (((i) + nt * KROT) & 31) * 1024)); SBAR(); } while (0)
#pragma unroll
        for (int i = 0; i < OWIN; ++i) {
            if (i == OWIN / 2) PRIO(2);
            LDO(i);
        }
        asm volatile("s_waitcnt vmcnt(%0)" :: "n"(OWIN) : "memory");
        lds_add(fl + C_S2, lane);
        __builtin_amdgcn_s_setprio(P3_PRIO);
        SBAR();
        const int r = lane & 31, hh = lane >> 5;
        const int g = lane >> 4, q = (lane & 15) >> 2, p = lane & 3;
        int woff[2];
#pragma unroll
        for (int n = 0; n < 2; ++n) {
            const int c = 4 * (n ^ half) + 2 * (p & 1) + (g & 1);
            woff[n] = (4 * hh + q) * 128 + ((c ^ ((q >> 1) << 2)) << 4) + (p >> 1) * 8;
        }
        f32x16 acc[2] = {f32x16{}, f32x16{}};
        const lds_ptr wl0 = lds + half * 16384 + woff[0], wl1 = lds + half * 16384 + woff[1];
        s16x4 wa[2][2], wb[2][2];
#define LDW(s) do { const int o_ = ((s) < 8 ? K2_SF : (s) >= 24 ? K2_SF : (((s) >> 3) - 1) * K2_SLOT) + ((s) & 7) * 2048; \
            wa[(s) & 1][0] = lds_tr(wl0 + o_); wb[(s) & 1][0] = lds_tr(wl0 + o_ + 1024); \
            wa[(s) & 1][1] = lds_tr(wl1 + o_); wb[(s) & 1][1] = lds_tr(wl1 + o_ + 1024); SBAR(); } while (0)
        lds_wait_ge(fl + C_S1, 4u);
        lds_wait_ge(fl + C_S2, 8u);
        LDW(0);
#pragma unroll
        for (int s = 0; s < 32; ++s) {
            if (s == 16) {
                lds_wait_ge(fl + C_U0 + half * 4, 4u);
#pragma unroll
                for (int i = 0; i < 4; ++i) {
                    asm volatile("" : "+v"(w3[i]));
                    *(LAS u32x4*)(lds + K2_SF + (w * 4 + i) * 1024 + lane * 16) = w3[i];
                }
                lds_add(fl + C_S3 + half * 4, lane);
            }
            if (s == 23) lds_wait_ge(fl + C_S3 + half * 4, 4u);
            if (s < 31) LDW(s + 1);
            if (s == 7) lds_add(fl + C_U0 + half * 4, lane);
            SBAR();
#pragma unroll
            for (int n = 0; n < 2; ++n) acc[n] = MFMA32(cat8(wa[s & 1][n], wb[s & 1][n]), of[s % OWIN], acc[n]);
            SBAR();
            if (s + OWIN < 32) LDO(s + OWIN);
            if (s + OWIN == 31) lds_add(fl + C_OL, lane);
            if (s == 0) DG(10);
            if (s == 15) DG(11);
        }
        DG(7);
        const lds_ptr xo = lds + w * 4096 + lane * 16;
#pragma unroll
        for (int i = 0; i < 4; ++i) *(LAS f32x4*)(xo + i * 1024) = f32x4{acc[1][4 * i], acc[1][4 * i + 1], acc[1][4 * i + 2], acc[1][4 * i + 3]};
        lds_add(fl + F_R + w * 4, lane);
        lds_wait_ge(fl + F_R + (w ^ 4) * 4, 1u);
        const lds_ptr xi = lds + (w ^ 4) * 4096 + lane * 16;
#pragma unroll
        for (int i = 0; i < 4; ++i) {
            const f32x4 t = *(const LAS f32x4*)(xi + i * 1024);
#pragma unroll
            for (int j = 0; j < 4; ++j) acc[0][4 * i + j] += t[j];
        }
        char* dst = out + (size_t)(T3 * 32 + r) * 4096 + 2048 + nt * 128 + half * 64 + hh * 32;
        *(u32x4*)(dst) = pack8n(acc[0], 0);
        *(u32x4*)(dst + 16) = pack8n(acc[0], 1);
    }
    DG_END(8);
}

extern "C" void kernel_launch(void* const* d_in, const int* in_sizes, int n_in,
                              void* d_out, int out_size, void* d_ws, size_t ws_size,
                              hipStream_t stream) {
    const void* A_q = d_in[0];
    const void* A_k = d_in[1];
    const void* A_v = d_in[2];
    const void* A_shift = d_in[3];
    const void* A_ssmk = d_in[4];
    const void* A_D = d_in[5];
    const void* B_x = d_in[6];
    const void* B_wqkv = d_in[7];
    const void* B_wo = d_in[8];
    char* ws = (char*)d_ws;
    k1_qkv<<<256, 512, 0, stream>>>((const char*)B_x, (const char*)B_wqkv, ws);
    k2_attn<<<256, 768, 0, stream>>>(ws, (const char*)A_q, (const char*)A_k, (const char*)A_v, (const char*)A_shift,
                                     (const char*)A_ssmk, (const unsigned*)A_D, (const char*)B_wo, (char*)d_out);
}
```

```cpp
#include <hip/hip_runtime.h>
#include <hip/hip_bf16.h>
#include <stdint.h>

typedef short bf16x8 __attribute__((ext_vector_type(8)));
typedef short s16x4 __attribute__((ext_vector_type(4)));
typedef float f32x16 __attribute__((ext_vector_type(16)));
typedef float f32x4 __attribute__((ext_vector_type(4)));
typedef float f32x2 __attribute__((ext_vector_type(2)));
typedef unsigned u32x4 __attribute__((ext_vector_type(4)));
typedef unsigned u32x2 __attribute__((ext_vector_type(2)));
typedef __bf16 bf16x2_t __attribute__((ext_vector_type(2)));

#define LAS __attribute__((address_space(3)))
typedef LAS char* lds_ptr;
#define DI __device__ __forceinline__
#define SBAR() __builtin_amdgcn_sched_barrier(0)
#define MFMA32(a, b, c) __builtin_amdgcn_mfma_f32_32x32x16_bf16((a), (b), (c), 0, 0, 0)
#define MFMA16(a, b, c) __builtin_amdgcn_mfma_f32_16x16x32_bf16((a), (b), (c), 0, 0, 0)
#define WAIT_BAR(N) asm volatile("s_waitcnt vmcnt(" #N ") lgkmcnt(0)\n\ts_barrier" ::: "memory")
#define LGKM_BAR() asm volatile("s_waitcnt lgkmcnt(0)\n\ts_barrier" ::: "memory")

#define DG_DECL(K)
#define DG(i)
#define DG_END(i)

constexpr int S_LEN = 2048, DM = 1024, NH = 32;

constexpr size_t WS_Q = 0;
constexpr size_t WS_KV = 4u << 20;
constexpr size_t WS_O = 12u << 20;
constexpr size_t WS_KVR = 16u << 20;
constexpr size_t WS_ZT = 20u << 20;
constexpr size_t WS_CNT = 24u << 20;

DI unsigned cvtpk(float lo, float hi) {
    f32x2 v = {lo, hi};
    return __builtin_bit_cast(unsigned, __builtin_convertvector(v, bf16x2_t));
}
DI float bflo(unsigned u) { return __uint_as_float(u << 16); }
DI float bfhi(unsigned u) { return __uint_as_float(u & 0xffff0000u); }

DI void glds16(unsigned voff, const void* sbase, unsigned lds_dst) {
    unsigned sv;
    asm volatile("s_mov_b32 %0, m0\n\ts_mov_b32 m0, %3\n\ts_nop 0\n\tglobal_load_lds_dwordx4 %1, %2\n\ts_mov_b32 m0, %0"
                 : "=&s"(sv) : "v"(voff), "s"(sbase), "s"(lds_dst) : "memory");
}
DI bf16x8 lds_rd128(lds_ptr p) { return *(const LAS bf16x8*)p; }
DI s16x4 lds_tr(lds_ptr p) {
    return __builtin_bit_cast(s16x4, __builtin_amdgcn_ds_read_tr16_b64_v4i16((LAS s16x4*)p));
}
DI bf16x8 cat8(s16x4 a, s16x4 b) { return __builtin_shufflevector(a, b, 0, 1, 2, 3, 4, 5, 6, 7); }
DI u32x4 pack8(const f32x16& x, int s, float sc) {
    u32x4 p;
    p[0] = cvtpk(x[8 * s + 0] * sc, x[8 * s + 1] * sc);
    p[1] = cvtpk(x[8 * s + 2] * sc, x[8 * s + 3] * sc);
    p[2] = cvtpk(x[8 * s + 4] * sc, x[8 * s + 5] * sc);
    p[3] = cvtpk(x[8 * s + 6] * sc, x[8 * s + 7] * sc);
    return p;
}
DI u32x4 pack8n(const f32x16& x, int s) {
    u32x4 p;
    p[0] = cvtpk(x[8 * s + 0], x[8 * s + 1]);
    p[1] = cvtpk(x[8 * s + 2], x[8 * s + 3]);
    p[2] = cvtpk(x[8 * s + 4], x[8 * s + 5]);
    p[3] = cvtpk(x[8 * s + 6], x[8 * s + 7]);
    return p;
}
DI void rnd2(float& a, float& b) { unsigned u = cvtpk(a, b); a = bflo(u); b = bfhi(u); }

#define CONV_PACE()
#define RAW_BAR() asm volatile("s_barrier" ::: "memory")
DI unsigned gld32(unsigned voff, const void* sbase) {
    unsigned r;
    asm volatile("global_load_dword %0, %1, %2" : "=v"(r) : "v"(voff), "s"(sbase) : "memory");
    return r;
}
DI u32x2 gld64(unsigned voff, const void* sbase) {
    u32x2 r;
    asm volatile("global_load_dwordx2 %0, %1, %2" : "=v"(r) : "v"(voff), "s"(sbase) : "memory");
    return r;
}
DI float mul_sv(float s, float v) { float r; asm("v_mul_f32_e32 %0, %1, %2" : "=v"(r) : "s"(s), "v"(v)); return r; }
DI float mul_vv(float a, float b) { float r; asm("v_mul_f32_e32 %0, %1, %2" : "=v"(r) : "v"(a), "v"(b)); return r; }
DI float add_vv(float a, float b) { float r; asm("v_add_f32_e32 %0, %1, %2" : "=v"(r) : "v"(a), "v"(b)); return r; }
DI void fmac_vv(float& acc, float a, float b) { asm("v_fmac_f32_e32 %0, %1, %2" : "+v"(acc) : "v"(a), "v"(b)); }
DI void fmac_sv(float& acc, float s, float v) { asm("v_fmac_f32_e32 %0, %1, %2" : "+v"(acc) : "s"(s), "v"(v)); }
typedef __attribute__((address_space(1))) unsigned gu32;
#define RLX_AGENT __ATOMIC_RELAXED, __HIP_MEMORY_SCOPE_AGENT
#define CFENCE() asm volatile("" ::: "memory")
#define DRAIN() asm volatile("s_waitcnt vmcnt(0)" ::: "memory")
constexpr int AUX_SC1 = 16;
DI u32x4 ld_sc1(__amdgpu_buffer_rsrc_t rs, unsigned off) { return __builtin_amdgcn_raw_buffer_load_b128(rs, (int)off, 0, AUX_SC1); }
DI u32x4 ld_sc1s(__amdgpu_buffer_rsrc_t rs, unsigned off, int soff) { return __builtin_amdgcn_raw_buffer_load_b128(rs, (int)off, soff, AUX_SC1); }
DI void st_sc1(u32x4 v, __amdgpu_buffer_rsrc_t rs, unsigned off) { __builtin_amdgcn_raw_buffer_store_b128(v, rs, (int)off, 0, AUX_SC1); }
DI unsigned lds_add_rtn(lds_ptr p, int lane) {
    unsigned old = 0;
    CFENCE();
    if (lane == 0) old = __hip_atomic_fetch_add((LAS unsigned*)p, 1u, __ATOMIC_RELAXED, __HIP_MEMORY_SCOPE_WORKGROUP);
    old = __builtin_amdgcn_readfirstlane(old);
    CFENCE();
    return old;
}
DI void lds_add(lds_ptr p, int lane) {
    CFENCE();
    if (lane == 0) (void)__hip_atomic_fetch_add((LAS unsigned*)p, 1u, __ATOMIC_RELAXED, __HIP_MEMORY_SCOPE_WORKGROUP);
    CFENCE();
}
DI void lds_wait_ge(lds_ptr p, unsigned v) {
    CFENCE();
    while ((unsigned)__builtin_amdgcn_readfirstlane(*(volatile LAS unsigned*)p) < v) __builtin_amdgcn_s_sleep(1);
    CFENCE();
}
DI void poll_ge(gu32* word, unsigned want) {
    CFENCE();
    for (unsigned spins = 0; spins < (1u << 22); ++spins) {
        if ((unsigned)__builtin_amdgcn_readfirstlane(__hip_atomic_load(word, RLX_AGENT)) >= want) break;
        __builtin_amdgcn_s_sleep(1);
    }
    __builtin_amdgcn_fence(__ATOMIC_ACQUIRE, "wavefront");
    CFENCE();
}
#define PRIO(x) do { __builtin_amdgcn_s_setprio(x); SBAR(); } while (0)
DI u32x4 gld128(unsigned voff, const void* sbase) {
    u32x4 r;
    asm volatile("global_load_dwordx4 %0, %1, %2" : "=v"(r) : "v"(voff), "s"(sbase) : "memory");
    return r;
}

constexpr int K1_STAGE = 40960;
__global__ __launch_bounds__(512, 2) void k1_qkv(const char* __restrict__ X, const char* __restrict__ W,
                                                 char* __restrict__ ws) {
    __shared__ __attribute__((aligned(16))) char lds_raw[4 * K1_STAGE];
    const lds_ptr lds = (lds_ptr)lds_raw;
    const unsigned lds0 = (unsigned)(uintptr_t)lds;
    const int tid = threadIdx.x, lane = tid & 63;
    const int w = __builtin_amdgcn_readfirstlane(tid >> 6);
    const int b = blockIdx.x, xcd = b & 7, idx = b >> 3;
    const int hp = (xcd >> 1) * 4 + (idx & 3), tb = (xcd & 1) * 8 + (idx >> 2);
    DG_DECL(0)

    if (w >= 4) {
        const int lw = w - 4;
        const int rot = (tb & 3) * 4;
        unsigned voffX[4], voffW[2];
#pragma unroll
        for (int pp = 0; pp < 4; ++pp) {
            const int row = 32 * lw + 8 * pp + (lane >> 3);
            const int ch = (lane & 7) ^ ((row >> 1) & 7);
            voffX[pp] = (unsigned)(tb * 128 + row) * 2048u + ch * 16;
        }
#pragma unroll
        for (int pp = 0; pp < 2; ++pp) {
            const int krow = 16 * lw + 8 * pp + (lane >> 3);
            const int ch = (lane & 7) ^ (((lane >> 4) & 1) << 2);
            voffW[pp] = (unsigned)krow * 6144u + hp * 128 + ch * 16;
        }
        auto issue = [&](int kt) {
            const int kr = (kt + rot) & 15;
            const char* xb = X + kr * 128;
            const char* wb = W + (size_t)kr * (64 * 6144);
            const unsigned st = lds0 + (kt & 3) * K1_STAGE;
#pragma unroll
            for (int pp = 0; pp < 4; ++pp) glds16(voffX[pp], xb, st + (4 * lw + pp) * 1024);
#pragma unroll
            for (int sec = 0; sec < 3; ++sec)
#pragma unroll
                for (int pp = 0; pp < 2; ++pp)
                    glds16(voffW[pp], wb + sec * 2048, st + 16384 + sec * 8192 + (2 * lw + pp) * 1024);
        };
        issue(0); issue(1); issue(2);
#pragma unroll 1
        for (int kt = 0; kt < 13; ++kt) {
            WAIT_BAR(20);
            issue(kt + 3);
        }
        WAIT_BAR(20);
        WAIT_BAR(10);
        WAIT_BAR(0);
        if (lw == 0 && lane < 3) *(unsigned*)(ws + WS_CNT + (lane * 256 + b) * 256) = 0u;
        DG_END(5);
        return;
    }

    const int wm = w >> 1, wn = w & 1;
    const int r = lane & 31, hh = lane >> 5;
    const int g = lane >> 4, q = (lane & 15) >> 2, p = lane & 3;
    const int xoff0 = (64 * wm + r) * 128 + ((hh ^ ((r >> 1) & 7)) << 4);
    const int woff0 = 16384 + (8 * hh + q) * 128 + (((4 * wn + 2 * (g & 1) + (p >> 1)) ^ ((q >> 1) << 2)) << 4) + (p & 1) * 8;

    f32x16 acc[2][3];
#pragma unroll
    for (int m = 0; m < 2; ++m)
#pragma unroll
        for (int s = 0; s < 3; ++s) acc[m][s] = f32x16{};
    bf16x8 xf[2][2];
    s16x4 wl[2][3], wh[2][3];
#define K1_WF(f, sec) cat8(wl[f][sec], wh[f][sec])
#define K1_STEP(F, st, t, BAR_HERE) do { \
        const lds_ptr xp_ = (st) + (xoff0 ^ ((t) << 5)); const lds_ptr wp_ = (st) + woff0 + (t) * 2048; \
        acc[0][0] = MFMA32(K1_WF(F, 0), xf[F][0], acc[0][0]); SBAR(); \
        if (BAR_HERE) { LGKM_BAR(); SBAR(); } \
        xf[F ^ 1][0] = lds_rd128(xp_); wl[F ^ 1][0] = lds_tr(wp_); wh[F ^ 1][0] = lds_tr(wp_ + 512); SBAR(); \
        acc[0][1] = MFMA32(K1_WF(F, 1), xf[F][0], acc[0][1]); SBAR(); \
        wl[F ^ 1][1] = lds_tr(wp_ + 8192); wh[F ^ 1][1] = lds_tr(wp_ + 8192 + 512); SBAR(); \
        acc[0][2] = MFMA32(xf[F][0], K1_WF(F, 2), acc[0][2]); SBAR(); \
        wl[F ^ 1][2] = lds_tr(wp_ + 16384); wh[F ^ 1][2] = lds_tr(wp_ + 16384 + 512); xf[F ^ 1][1] = lds_rd128(xp_ + 4096); SBAR(); \
        acc[1][0] = MFMA32(K1_WF(F, 0), xf[F][1], acc[1][0]); SBAR(); \
        acc[1][1] = MFMA32(K1_WF(F, 1), xf[F][1], acc[1][1]); \
        acc[1][2] = MFMA32(xf[F][1], K1_WF(F, 2), acc[1][2]); SBAR(); \
    } while (0)

    LGKM_BAR();
    DG(3);
    {
        const lds_ptr xp_ = lds + xoff0; const lds_ptr wp_ = lds + woff0;
        xf[0][0] = lds_rd128(xp_); xf[0][1] = lds_rd128(xp_ + 4096);
#pragma unroll
        for (int sec = 0; sec < 3; ++sec) { wl[0][sec] = lds_tr(wp_ + sec * 8192); wh[0][sec] = lds_tr(wp_ + sec * 8192 + 512); }
    }
    SBAR();
#pragma unroll
    for (int kt = 0; kt < 15; ++kt) {
        const lds_ptr st = lds + (kt & 3) * K1_STAGE;
        const lds_ptr sn = lds + ((kt + 1) & 3) * K1_STAGE;
        K1_STEP(0, st, 1, false);
        K1_STEP(1, st, 2, false);
        K1_STEP(0, st, 3, false);
        K1_STEP(1, sn, 0, true);
    }
    {
        const lds_ptr st = lds + 3 * K1_STAGE;
        K1_STEP(0, st, 1, false);
        K1_STEP(1, st, 2, false);
        K1_STEP(0, st, 3, false);
#pragma unroll
        for (int m = 0; m < 2; ++m) {
            acc[m][1] = MFMA32(K1_WF(1, 1), xf[1][m], acc[m][1]);
            acc[m][2] = MFMA32(xf[1][m], K1_WF(1, 2), acc[m][2]);
            acc[m][0] = MFMA32(K1_WF(1, 0), xf[1][m], acc[m][0]);
        }
    }
    DG(4);
    {
        const int h = 2 * hp + wn;
        const float qscale = 0.17677669529663687f * 1.4426950408889634f;
        const __amdgpu_buffer_rsrc_t rs = __builtin_amdgcn_make_buffer_rsrc(ws, 0, 32 << 20, 0x00020000);
#pragma unroll
        for (int m = 0; m < 2; ++m) {
            const int T = tb * 4 + wm * 2 + m;
            const unsigned dq = (unsigned)WS_Q + (h * 64 + T) * 2048 + lane * 16;
            const unsigned dk = (unsigned)WS_KV + (h * 64 + T) * 4096 + lane * 16;
            st_sc1(pack8n(acc[m][1], 0), rs, dk);
            st_sc1(pack8n(acc[m][1], 1), rs, dk + 1024);
            st_sc1(pack8n(acc[m][2], 0), rs, dk + 2048);
            st_sc1(pack8n(acc[m][2], 1), rs, dk + 3072);
            st_sc1(pack8(acc[m][0], 0, qscale), rs, dq);
            st_sc1(pack8(acc[m][0], 1, qscale), rs, dq + 1024);
        }
    }
    DG_END(5);
}

constexpr int K2_SLOT = 32768;
constexpr int K2_SF = 65536;
constexpr int K2_CV = 98304;
constexpr int CV_HA = 0, CV_HB = 4160, CV_KV = 8320, CV_BYTES = 8320 + 480 + 4096;
constexpr int K2_LX = K2_CV + 4 * CV_BYTES;
constexpr int K2_FL = K2_LX + 2048;
constexpr int K2_SCR = K2_FL + 256;
constexpr int K2_LDS = K2_SCR + 4096;
constexpr int F_X = 0, F_R = 32, F_W2 = 64, C_OST = 68, F_GO = 72, C_S1 = 76, C_S2 = 80, C_S3 = 128, C_ZST = 88, F_ZGO = 92,
              C_KST = 96, C_U0 = 100, C_LD = 108, F_KGO = 116, C_OL = 120;
#define NG_IN 0
#define GATE_WORD C_LD
#define GATE_VAL 4u
#define X_NZ 4
#define X_NW3 8
#define ZP_GATE 4u
#define ZL_GATE 8u
#define OWIN 8
#define KROT 2
#define NG_LAST 0
#define PACE_IN "s_nop 7"
#define PACE_OUT "s_nop 1"
#define P3_PRIO 1
#define AUX_PRIO 0

__global__ __launch_bounds__(768, 3) void k2_attn(char* __restrict__ ws, const char* __restrict__ Aq,
                                                  const char* __restrict__ Ak, const char* __restrict__ Av,
                                                  const char* __restrict__ shiftk, const char* __restrict__ ssmk,
                                                  const unsigned* __restrict__ ssmD, const char* __restrict__ Wo,
                                                  char* __restrict__ out) {
    __shared__ __attribute__((aligned(16))) char lds_raw[K2_LDS];
    const lds_ptr lds = (lds_ptr)lds_raw;
    const lds_ptr fl = lds + K2_FL;
    const unsigned lds0 = (unsigned)(uintptr_t)lds;
    const int tid = threadIdx.x, lane = tid & 63;
    const int w = __builtin_amdgcn_readfirstlane(tid >> 6);
    const int b = blockIdx.x, xcd = b & 7, idx = b >> 3;
    const int h = xcd * 4 + (idx >> 3), qb = idx & 7;
    const int tt = xcd * 2 + (idx >> 4), nt = idx & 15;
    const char* kvg = ws + WS_KV + (size_t)h * 64 * 4096;
    const __amdgpu_buffer_rsrc_t rs = __builtin_amdgcn_make_buffer_rsrc(ws, 0, 32 << 20, 0x00020000);
    gu32* const cnt = (gu32*)(ws + WS_CNT);
    auto signal = [&](int ty, int g) {
        if (lane < 32) (void)__hip_atomic_fetch_add(cnt + (ty * 256 + lane * 8 + g) * 64, 1u, RLX_AGENT);
    };
    const unsigned wvoff = (unsigned)(lane >> 3) * 2048u + nt * 128 + (((lane & 7) ^ (((lane >> 4) & 1) << 2)) << 4);
    auto wsrc = [&](int hf, int lp) { return Wo + (size_t)(hf * 64 + ((lp + nt * KROT * 2) & 63)) * 16384; };
    DG_DECL(1)

    if (w >= 8) {
        const int cw = w - 8;
        const unsigned voff = lane * 16;
        __builtin_amdgcn_s_setprio(3);
        auto issue = [&](int st) {
            const char* src = kvg + (st * 4 + cw) * 4096;
            const unsigned dst = lds0 + (st & 1) * K2_SLOT + cw * 4096;
#pragma unroll
            for (int i = 0; i < 4; ++i) glds16(voff, src + i * 1024, dst + i * 1024);
#pragma unroll
            for (int i = 0; i < 4; ++i) glds16(voff, src + 131072 + i * 1024, dst + 16384 + i * 1024);
        };
        issue(0);
        const int ch = xcd * 128 + idx * 4 + cw;
        const lds_ptr cv = lds + K2_CV + cw * CV_BYTES;
        {
            const u32x4 z4 = {0u, 0u, 0u, 0u};
            if (lane < 2) *(LAS u32x4*)(cv + CV_HA + lane * 16) = z4;
            if (lane < 30) *(LAS u32x4*)(cv + CV_KV + lane * 16) = z4;
            if (cw == 0 && lane < 16) *(LAS u32x4*)(fl + lane * 16) = z4;
        }
        WAIT_BAR(0);
        issue(1);
        const int c0 = xcd * 128 + cw * 32, cq = lane & 7, rg = lane >> 3;
        const int row0 = idx * 64 + rg * 8;
        const unsigned roff = (unsigned)row0 * 2048u + (c0 + cq * 4) * 2;
        const unsigned toffs = (unsigned)(c0 + (lane >> 3)) * 4096u + idx * 128;
        u32x4 sk[2];
        sk[0] = gld128((unsigned)(c0 + cq * 4) * 8u, shiftk); sk[1] = gld128((unsigned)(c0 + cq * 4) * 8u, shiftk + 16);
        unsigned dd = gld32((unsigned)(ch >> 1) * 4u, ssmD);
        auto stage = [&](const char* src, int dst) {
            const unsigned sbase = (unsigned)(idx * 64 + cw * 16 + (lane >> 4)) * 2048u + xcd * 256;
#pragma unroll
            for (int i = 0; i < 4; ++i)
                glds16(sbase + (((lane & 15) ^ (((2 * cw + (i >> 1)) & 3) << 2)) << 4), src + i * 8192, lds0 + dst + cw * 4096 + i * 1024);
        };
        WAIT_BAR(3);
        DG(12);
        issue(2);
        stage(Ak, K2_SF);
        glds16((unsigned)(idx * 64 - (idx ? 4 : 0) + (lane >> 4)) * 2048u + xcd * 256 + (((lane & 15) ^ 12) << 4), Ak, lds0 + K2_SCR + cw * 1024);
        WAIT_BAR(5);
        DG(9);
        issue(3);
        stage(Av, K2_SF + 16384);
        WAIT_BAR(4);
        DG(3);
        issue(4);
#pragma unroll
        for (int i = 0; i < 4; ++i) glds16(voff, ssmk + (size_t)ch * 4096 + i * 1024, lds0 + K2_CV + cw * CV_BYTES + CV_HA + 32 + i * 1024);
        WAIT_BAR(4);
        DG(5);
        issue(5);
        SBAR();
        asm volatile("" : "+v"(sk[0]), "+v"(sk[1]), "+v"(dd));
        dd = __builtin_amdgcn_readfirstlane(dd);
        {
            u32x2 kr[11], vr[8];
            const lds_ptr kb = lds + K2_SF + rg * 2048 + ((cw ^ (rg & 3)) << 6) + cq * 8;
            const lds_ptr hb = lds + (rg ? K2_SF + rg * 2048 - 768 : K2_SCR + cw * 1024 + 256) + ((cw ^ ((rg + 3) & 3)) << 6) + cq * 8;
#pragma unroll
            for (int i = 0; i < 3; ++i) kr[i] = *(const LAS u32x2*)(hb + i * 256);
#pragma unroll
            for (int i = 0; i < 8; ++i) kr[3 + i] = *(const LAS u32x2*)(kb + i * 256);
#pragma unroll
            for (int i = 0; i < 8; ++i) vr[i] = *(const LAS u32x2*)(kb + 16384 + i * 256);
            if (row0 == 0) { kr[0] = u32x2{0u, 0u}; kr[1] = u32x2{0u, 0u}; kr[2] = u32x2{0u, 0u}; }
            const lds_ptr tb = cv + CV_KV + 480;
#pragma unroll
            for (int c = 0; c < 4; ++c) {
                const unsigned s01 = sk[c >> 1][(c & 1) * 2], s23 = sk[c >> 1][(c & 1) * 2 + 1];
                const float w0 = bflo(s01), w1 = bfhi(s01), w2 = bflo(s23), w3 = bfhi(s23);
                float kf[11], kv[8];
#pragma unroll
                for (int i = 0; i < 11; ++i) kf[i] = (c & 1) ? bfhi(kr[i][c >> 1]) : bflo(kr[i][c >> 1]);
#pragma unroll
                for (int i = 0; i < 8; ++i) {
                    float a = mul_vv(w3, kf[i]);
                    fmac_vv(a, w2, kf[i + 1]); fmac_vv(a, w1, kf[i + 2]); fmac_vv(a, w0, kf[i + 3]);
                    kv[i] = mul_vv(a, (c & 1) ? bfhi(vr[i][c >> 1]) : bflo(vr[i][c >> 1]));
                }
                u32x4 o4;
#pragma unroll
                for (int i = 0; i < 4; ++i) o4[i] = cvtpk(kv[7 - 2 * i], kv[6 - 2 * i]);
                *(LAS u32x4*)(tb + (cq * 4 + c) * 128 + ((rg ^ cq) << 4)) = o4;
            }
#pragma unroll
            for (int i = 0; i < 4; ++i) {
                const u32x4 t = *(const LAS u32x4*)(tb + i * 1024 + lane * 16);
                st_sc1(t, rs, (unsigned)WS_KVR + toffs + i * 32768 + (((lane & 7) ^ (2 * i + (lane >> 5))) << 4));
            }
        }
        CFENCE();
        DG(7);
        WAIT_BAR(4);
        DG(6);
        issue(6);
        asm volatile("s_waitcnt vmcnt(8)" ::: "memory");
        if (lds_add_rtn(fl + C_KST, lane) == 3u) signal(1, xcd);
#pragma unroll
        for (int i = 0; i < 8; ++i)
            glds16(wvoff, wsrc(i >> 2, cw * 4 + (i & 3)), lds0 + K2_SF + (i >> 2) * 16384 + (cw * 4 + (i & 3)) * 1024);
        {
            const u32x4 z4 = {0u, 0u, 0u, 0u};
            if (lane < 2) *(LAS u32x4*)(cv + CV_HB + lane * 16) = z4;
#pragma unroll
            for (int i = 0; i < 4; ++i) {
                const int off = 32 + i * 1024 + lane * 16;
                const u32x4 hv = *(const LAS u32x4*)(cv + CV_HA + off);
                const unsigned pv = *(const LAS unsigned*)(cv + CV_HA + off - 4);
                u32x4 sh;
                sh[0] = __builtin_amdgcn_alignbit(hv[0], pv, 16);
                sh[1] = __builtin_amdgcn_alignbit(hv[1], hv[0], 16);
                sh[2] = __builtin_amdgcn_alignbit(hv[2], hv[1], 16);
                sh[3] = __builtin_amdgcn_alignbit(hv[3], hv[2], 16);
                *(LAS u32x4*)(cv + CV_HB + off) = sh;
            }
        }
        WAIT_BAR(8);
        DG(8);
        issue(7);
#pragma unroll
        for (int i = 0; i < 8; ++i)
            glds16(wvoff, wsrc(idx >> 4, 32 + cw * 8 + i), lds0 + K2_SCR + cw * 1024);
        WAIT_BAR(8);
        DG(11);
        if (cw == 0) { poll_ge(cnt + (256 + b) * 64, 32u); lds_add(fl + F_KGO, lane); }
        else lds_wait_ge(fl + F_KGO, 1u);
        {
            u32x4 t[4];
#pragma unroll
            for (int i = 0; i < 4; ++i) t[i] = ld_sc1(rs, (unsigned)WS_KVR + ch * 4096 + i * 1024 + voff);
#pragma unroll
            for (int i = 0; i < 4; ++i) *(LAS u32x4*)(cv + CV_KV + 480 + i * 1024 + lane * 16) = t[i];
        }
        const int r1 = lane & 15, q = lane >> 4;
        const int toff = (r1 & 1) ? CV_HA + 2 * (16 + r1 + 9 - 8 * q) : CV_HB + 2 * (16 + r1 + 10 - 8 * q);
        const int boff = CV_KV + 448 + 32 * r1 + 16 * q;
        f32x4 y[8];
#pragma unroll
        for (int t = 0; t < 8; ++t) y[t] = f32x4{};
        bf16x8 tf[8], bf[8];
        auto ldt = [&](int bb, int a) {
            const LAS unsigned* tp = (const LAS unsigned*)(cv + toff + 64 * bb + 512 * a);
            u32x4 t4 = {tp[0], tp[1], tp[2], tp[3]};
            tf[a] = __builtin_bit_cast(bf16x8, t4);
        };
        auto ldb = [&](int bb, int a) { bf[a] = lds_rd128(cv + boff - 64 * bb + 512 * a); };
#define GROUP(bb, PACE) do { \
            _Pragma("unroll") for (int a = 0; a < 8; ++a) { \
                _Pragma("unroll") for (int ww = 7 - a; ww >= 0; --ww) { \
                    y[a + ww] = MFMA16(tf[a], bf[ww], y[a + ww]); SBAR(); \
                    asm volatile(PACE); SBAR(); \
                } \
                ldt(((bb) + 1) & 7, a); ldb(((bb) + 1) & 7, 7 - a); SBAR(); \
            } } while (0)
#pragma unroll
        for (int a = 0; a < 8; ++a) { ldt(0, a); ldb(0, 7 - a); }
        SBAR();
        u32x2 qr[8];
#pragma unroll
        for (int i = 0; i < 8; ++i)
            glds16(wvoff, wsrc(i >> 2, 16 + cw * 4 + (i & 3)), lds0 + (i >> 2) * 16384 + (cw * 4 + (i & 3)) * 1024);
#pragma unroll
        for (int i = 0; i < 8; ++i) qr[i] = gld64(roff, Aq + i * 2048);
#pragma unroll 1
        for (int bb = NG_IN; bb < NG_IN + NG_LAST; ++bb) GROUP(bb, PACE_IN);
        asm volatile("s_waitcnt vmcnt(8)" ::: "memory");
        lds_add(fl + C_S1, lane);
        CFENCE();
        while ((unsigned)__builtin_amdgcn_readfirstlane(*(volatile LAS unsigned*)(fl + GATE_WORD)) < GATE_VAL) __builtin_amdgcn_s_sleep(2);
        CFENCE();
        __builtin_amdgcn_s_setprio(AUX_PRIO);
#pragma unroll 1
        for (int bb = NG_IN + NG_LAST; bb < 8; ++bb) GROUP(bb, PACE_OUT);
        DG(4);
        __builtin_amdgcn_s_setprio(3);
        {
            const float dch = (ch & 1) ? bfhi(dd) : bflo(dd);
#pragma unroll
            for (int t = 0; t < 8; ++t) {
                const u32x2 kk = *(const LAS u32x2*)(cv + CV_KV + 480 + 512 * t + 32 * r1 + 16 * (q >> 1) + ((q & 1) ? 0 : 8));
                float k0 = mul_sv(dch, bfhi(kk[1])), k1 = mul_sv(dch, bflo(kk[1]));
                float k2 = mul_sv(dch, bfhi(kk[0])), k3 = mul_sv(dch, bflo(kk[0]));
                rnd2(k0, k1); rnd2(k2, k3);
                float y0 = y[t][0], y1 = y[t][1], y2 = y[t][2], y3 = y[t][3];
                rnd2(y0, y1); rnd2(y2, y3);
                u32x2 o = {cvtpk(y0 + k0, y1 + k1), cvtpk(y2 + k2, y3 + k3)};
                *(LAS u32x2*)(cv + CV_HA + 512 * t + 32 * r1 + 8 * q) = o;
            }
#pragma unroll
            for (int i = 0; i < X_NZ; ++i)
                st_sc1(*(const LAS u32x4*)(cv + CV_HA + i * 1024 + lane * 16), rs, (unsigned)WS_ZT + ch * 4096 + i * 1024 + voff);
        }
        DG(10);
        DRAIN();
        if (lds_add_rtn(fl + C_ZST, lane) == 3u) {
            signal(2, xcd);
            lds_wait_ge(fl + C_OL, ZP_GATE);
            poll_ge(cnt + (512 + b) * 64, 32u);
            lds_add(fl + F_ZGO, lane);
        } else lds_wait_ge(fl + F_ZGO, 1u);
        lds_wait_ge(fl + C_OL, ZL_GATE);
        DG(13);
        {
            const lds_ptr tr = cv;
            u32x4 t[4];
#pragma unroll
            for (int i = 0; i < 4; ++i)
                t[i] = ld_sc1(rs, (unsigned)WS_ZT + toffs + i * 32768 + (((lane & 7) ^ (2 * i + (lane >> 5))) << 4));
#pragma unroll
            for (int i = 0; i < 4; ++i) *(LAS u32x4*)(tr + i * 1024 + lane * 16) = t[i];
#pragma unroll
            for (int i = 0; i < 8; ++i) asm volatile("" : "+v"(qr[i]));
            u32x4 zc[4];
#pragma unroll
            for (int c = 0; c < 4; ++c) zc[c] = *(const LAS u32x4*)(tr + (cq * 4 + c) * 128 + ((rg ^ cq) << 4));
            char* dst = out + (size_t)row0 * 4096 + (c0 + cq * 4) * 2;
#pragma unroll
            for (int i = 0; i < 8; ++i) {
                float zz[4];
#pragma unroll
                for (int c = 0; c < 4; ++c) zz[c] = (i & 1) ? bfhi(zc[c][i >> 1]) : bflo(zc[c][i >> 1]);
                u32x2 o = {cvtpk(bflo(qr[i][0]) * zz[0], bfhi(qr[i][0]) * zz[1]),
                           cvtpk(bflo(qr[i][1]) * zz[2], bfhi(qr[i][1]) * zz[3])};
                *(u32x2*)(dst + i * 4096) = o;
            }
        }
        DG_END(14);
        return;
    }

    const int half = w >> 2;
    const int TA = qb * 8 + (w & 3) * 2 + half, TB = TA ^ 1;
    const char* qga = ws + WS_Q + (size_t)(h * 64 + TA) * 2048 + lane * 16;
    const char* qgb = ws + WS_Q + (size_t)(h * 64 + TB) * 2048 + lane * 16;
    const bf16x8 qa0 = *(const bf16x8*)qga, qa1 = *(const bf16x8*)(qga + 1024);
    const bf16x8 qb0 = *(const bf16x8*)qgb, qb1 = *(const bf16x8*)(qgb + 1024);
    bf16x8 sel;
    {
        const short one = (((lane >> 2) & 1) == ((lane >> 4) & 1)) ? (short)0x3f80 : (short)0;
#pragma unroll
        for (int i = 0; i < 8; ++i) sel[i] = one;
    }
    f32x16 oA = f32x16{}, oB = f32x16{};
    f32x4 lA = f32x4{}, lB = f32x4{};
    f32x16 negm, sA, sB;
    const lds_ptr lp = lds + half * 16384 + lane * 16;
    bf16x8 k0[2], k1[2], v0[2], v1[2];
    u32x4 p0, p1 = {0u, 0u, 0u, 0u};
#define EX(X, i) X[i] = __builtin_amdgcn_exp2f(X[i])
#define PB(x) __builtin_bit_cast(bf16x8, x)
    LGKM_BAR();
    DG(3);
    {
        const bf16x8 x0 = lds_rd128(lds + lane * 16), x1 = lds_rd128(lds + lane * 16 + 1024);
        k0[0] = lds_rd128(lp); k1[0] = lds_rd128(lp + 1024);
        v0[0] = lds_rd128(lp + 2048); v1[0] = lds_rd128(lp + 3072);
        sA = MFMA32(x0, qa0, f32x16{});
        sB = MFMA32(x0, qb0, f32x16{});
        sA = MFMA32(x1, qa1, sA);
        sB = MFMA32(x1, qb1, sB);
        float m = __builtin_fmaxf(sA[0], sB[0]);
#pragma unroll
        for (int i = 1; i < 16; ++i) m = __builtin_fmaxf(m, __builtin_fmaxf(sA[i], sB[i]));
        auto rr = __builtin_amdgcn_permlane32_swap(__float_as_uint(m), __float_as_uint(m), false, false);
        m = __builtin_fmaxf(__uint_as_float(rr[0]), __uint_as_float(rr[1]));
#pragma unroll
        for (int i = 0; i < 16; ++i) negm[i] = -m;
        sA = MFMA32(k0[0], qa0, negm);
        sA = MFMA32(k1[0], qa1, sA);
        v1[1] = v1[0];
        EX(sA, 0); EX(sA, 1);
    }
#define SUM16(l, p) asm volatile("v_mfma_f32_16x16x32_bf16 %0, %1, %2, %0" : "+v"(l) : "v"(sel), "v"(p))
#define HALF(C, oC, lC, N, qx, qy, ka, kb, oP, lP, vprev, vcur0, PRE) do { \
        oP = MFMA32(vprev, PB(p1), oP); SBAR(); \
        PRE; EX(C, 2); EX(C, 3); EX(C, 4); SBAR(); \
        N = MFMA32(ka, qx, negm); SBAR(); \
        EX(C, 5); EX(C, 6); EX(C, 7); SBAR(); \
        SUM16(lP, p1); SBAR(); \
        p0[0] = cvtpk(C[0], C[1]); p0[1] = cvtpk(C[2], C[3]); SBAR(); \
        N = MFMA32(kb, qy, N); SBAR(); \
        p0[2] = cvtpk(C[4], C[5]); p0[3] = cvtpk(C[6], C[7]); SBAR(); EX(C, 8); EX(C, 9); SBAR(); \
        oC = MFMA32(vcur0, PB(p0), oC); SBAR(); \
        EX(C, 10); EX(C, 11); EX(C, 12); SBAR(); \
        SUM16(lC, p0); SBAR(); \
        EX(C, 13); EX(C, 14); EX(C, 15); SBAR(); \
        p1[0] = cvtpk(C[8], C[9]); p1[1] = cvtpk(C[10], C[11]); p1[2] = cvtpk(C[12], C[13]); p1[3] = cvtpk(C[14], C[15]); SBAR(); \
        EX(N, 0); EX(N, 1); SBAR(); \
    } while (0)
#define RD4(Y, nx) do { v1[Y] = lds_rd128((nx) + 3072); v0[Y] = lds_rd128((nx) + 2048); \
        k1[Y] = lds_rd128((nx) + 1024); k0[Y] = lds_rd128(nx); } while (0)
#define KT(X, nx, PRE0) do { \
        HALF(sA, oA, lA, sB, qb0, qb1, k0[X], k1[X], oB, lB, v1[X ^ 1], v0[X], PRE0; RD4(X ^ 1, nx)); \
        HALF(sB, oB, lB, sA, qa0, qa1, k0[X ^ 1], k1[X ^ 1], oA, lA, v1[X], v0[X], (void)0); \
    } while (0)
    SBAR();
#pragma unroll 1
    for (int st = 0; st < 8; ++st) {
        const lds_ptr cur = lp + (st & 1) * K2_SLOT;
        const lds_ptr nxt = lp + ((st + 1) & 1) * K2_SLOT;
        PRIO(2);
        KT(0, cur + 4096, (void)0);
        PRIO(1);
        KT(1, cur + 8192, (void)0);
        PRIO(0);
        KT(0, cur + 12288, (void)0);
        PRIO(3);
        KT(1, nxt, if (st < 7) RAW_BAR(); SBAR());
    }
    oB = MFMA32(v1[1], PB(p1), oB);
    SBAR();
    SUM16(lB, p1);
    asm volatile("s_nop 7\n\ts_nop 7" ::: "memory");
    SBAR();
    DG(4);
    {
        lds_add(fl + C_LD + half * 4, lane);
        const lds_ptr xo = lds + K2_SLOT + w * 4096 + lane * 16;
        *(LAS float*)(lds + K2_LX + w * 256 + lane * 4) = lB[0];
        lds_wait_ge(fl + C_LD + half * 4, 4u);
#pragma unroll
        for (int i = 0; i < 4; ++i) *(LAS f32x4*)(xo + i * 1024) = f32x4{oB[4 * i], oB[4 * i + 1], oB[4 * i + 2], oB[4 * i + 3]};
        lds_add(fl + F_X + w * 4, lane);
        lds_wait_ge(fl + F_X + (w ^ 4) * 4, 1u);
        const lds_ptr xi = lds + K2_SLOT + (w ^ 4) * 4096 + lane * 16;
#pragma unroll
        for (int i = 0; i < 4; ++i) {
            const f32x4 t = *(const LAS f32x4*)(xi + i * 1024);
#pragma unroll
            for (int j = 0; j < 4; ++j) oA[4 * i + j] = add_vv(oA[4 * i + j], t[j]);
        }
        lA[0] += *(const LAS float*)(lds + K2_LX + (w ^ 4) * 256 + lane * 4);
    }
    DG(9);
    float l = lA[0];
    {
        float t0 = 0.f, t1 = 0.f;
#pragma unroll
        for (int i = 0; i < 8; ++i) { fmac_sv(t0, 0.f, oA[i]); fmac_sv(t1, 0.f, oA[8 + i]); }
        const bool ok = (l <= 1.0e30f) && (l >= 1.0e-18f) && (t0 + t1 == 0.f);
        if (__builtin_expect(__any(!ok), 0)) {
            float m = -INFINITY, ls = 0.f;
            oA = f32x16{};
#pragma unroll 1
            for (int t = 0; t < 64; ++t) {
                const char* g = kvg + t * 4096 + lane * 16;
                const bf16x8 a0 = *(const bf16x8*)g, a1 = *(const bf16x8*)(g + 1024);
                const bf16x8 b0 = *(const bf16x8*)(g + 2048), b1 = *(const bf16x8*)(g + 3072);
                f32x16 s = MFMA32(a0, qa0, f32x16{});
                s = MFMA32(a1, qa1, s);
                float tm = s[0];
#pragma unroll
                for (int i = 1; i < 16; ++i) tm = __builtin_fmaxf(tm, s[i]);
                auto rr = __builtin_amdgcn_permlane32_swap(__float_as_uint(tm), __float_as_uint(tm), false, false);
                tm = __builtin_fmaxf(__uint_as_float(rr[0]), __uint_as_float(rr[1]));
                const float mn = __builtin_fmaxf(m, tm);
                const float alpha = __builtin_amdgcn_exp2f(m - mn);
                m = mn;
                float ps = 0.f;
#pragma unroll
                for (int i = 0; i < 16; ++i) { s[i] = __builtin_amdgcn_exp2f(s[i] - mn); ps += s[i]; oA[i] *= alpha; }
                ls = ls * alpha + ps;
                oA = MFMA32(b0, PB(pack8n(s, 0)), oA);
                oA = MFMA32(b1, PB(pack8n(s, 1)), oA);
            }
            auto rr = __builtin_amdgcn_permlane32_swap(__float_as_uint(ls), __float_as_uint(ls), false, false);
            l = __uint_as_float(rr[0]) + __uint_as_float(rr[1]);
        }
    }
    u32x4 w3[4];
    {
        const float rl = __builtin_amdgcn_rcpf(l);
        const unsigned og = (unsigned)WS_O + (unsigned)(TA * 32 + h) * 2048u + lane * 16;
        st_sc1(pack8(oA, 0, rl), rs, og);
        st_sc1(pack8(oA, 1, rl), rs, og + 1024);
        CFENCE();
#pragma unroll
        for (int i = 0; i < 4; ++i)
            glds16(wvoff, wsrc(half ^ 1, 32 + (w & 3) * 4 + i), lds0 + K2_SLOT + ((w ^ 4) * 4 + i) * 1024);
#pragma unroll
        for (int i = 0; i < 4; ++i) w3[i] = gld128(wvoff, wsrc(half, 48 + (w & 3) * 4 + i));
        asm volatile("s_waitcnt vmcnt(8)" ::: "memory");
    }
    DG(5);
    const unsigned arr = lds_add_rtn(fl + C_OST, lane);
    const int j4 = w & 3;
    if (arr == 7u) {
        signal(0, qb);
        poll_ge(cnt + b * 64, 32u);
        lds_add(fl + F_GO, lane);
    } else {
        lds_wait_ge(fl + F_GO, 1u);
    }
    DG(6);
    {
        const int T3 = tt * 4 + j4;
        const unsigned og = (unsigned)WS_O + (unsigned)T3 * 65536u + half * 32768 + lane * 16;
        bf16x8 of[OWIN];
#define LDO(i) do { of[(i) % OWIN] = PB(ld_sc1s(rs, og, (((i) + nt * KROT) & 31) * 1024)); SBAR(); } while (0)
#pragma unroll
        for (int i = 0; i < OWIN; ++i) {
            if (i == OWIN / 2) PRIO(2);
            LDO(i);
        }
        asm volatile("s_waitcnt vmcnt(%0)" :: "n"(OWIN) : "memory");
        lds_add(fl + C_S2, lane);
        __builtin_amdgcn_s_setprio(P3_PRIO);
        SBAR();
        const int r = lane & 31, hh = lane >> 5;
        const int g = lane >> 4, q = (lane & 15) >> 2, p = lane & 3;
        int woff[2];
#pragma unroll
        for (int n = 0; n < 2; ++n) {
            const int c = 4 * (n ^ half) + 2 * (p & 1) + (g & 1);
            woff[n] = (4 * hh + q) * 128 + ((c ^ ((q >> 1) << 2)) << 4) + (p >> 1) * 8;
        }
        f32x16 acc[2] = {f32x16{}, f32x16{}};
        const lds_ptr wl0 = lds + half * 16384 + woff[0], wl1 = lds + half * 16384 + woff[1];
        s16x4 wa[2][2], wb[2][2];
#define LDW(s) do { const int o_ = ((s) < 8 ? K2_SF : (s) >= 24 ? K2_SF : (((s) >> 3) - 1) * K2_SLOT) + ((s) & 7) * 2048; \
            wa[(s) & 1][0] = lds_tr(wl0 + o_); wb[(s) & 1][0] = lds_tr(wl0 + o_ + 1024); \
            wa[(s) & 1][1] = lds_tr(wl1 + o_); wb[(s) & 1][1] = lds_tr(wl1 + o_ + 1024); SBAR(); } while (0)
        lds_wait_ge(fl + C_S1, 4u);
        lds_wait_ge(fl + C_S2, 8u);
        LDW(0);
#pragma unroll
        for (int s = 0; s < 32; ++s) {
            if (s == 16) {
                lds_wait_ge(fl + C_U0 + half * 4, 4u);
#pragma unroll
                for (int i = 0; i < 4; ++i) {
                    asm volatile("" : "+v"(w3[i]));
                    *(LAS u32x4*)(lds + K2_SF + (w * 4 + i) * 1024 + lane * 16) = w3[i];
                }
                lds_add(fl + C_S3 + half * 4, lane);
            }
            if (s == 23) lds_wait_ge(fl + C_S3 + half * 4, 4u);
            if (s < 31) LDW(s + 1);
            if (s == 7) lds_add(fl + C_U0 + half * 4, lane);
            SBAR();
#pragma unroll
            for (int n = 0; n < 2; ++n) acc[n] = MFMA32(cat8(wa[s & 1][n], wb[s & 1][n]), of[s % OWIN], acc[n]);
            SBAR();
            if (s + OWIN < 32) LDO(s + OWIN);
            if (s + OWIN == 31) lds_add(fl + C_OL, lane);
            if (s == 0) DG(10);
            if (s == 15) DG(11);
        }
        DG(7);
        const lds_ptr xo = lds + w * 4096 + lane * 16;
#pragma unroll
        for (int i = 0; i < 4; ++i) *(LAS f32x4*)(xo + i * 1024) = f32x4{acc[1][4 * i], acc[1][4 * i + 1], acc[1][4 * i + 2], acc[1][4 * i + 3]};
        lds_add(fl + F_R + w * 4, lane);
        lds_wait_ge(fl + F_R + (w ^ 4) * 4, 1u);
        const lds_ptr xi = lds + (w ^ 4) * 4096 + lane * 16;
#pragma unroll
        for (int i = 0; i < 4; ++i) {
            const f32x4 t = *(const LAS f32x4*)(xi + i * 1024);
#pragma unroll
            for (int j = 0; j < 4; ++j) acc[0][4 * i + j] += t[j];
        }
        char* dst = out + (size_t)(T3 * 32 + r) * 4096 + 2048 + nt * 128 + half * 64 + hh * 32;
        *(u32x4*)(dst) = pack8n(acc[0], 0);
        *(u32x4*)(dst + 16) = pack8n(acc[0], 1);
    }
    DG_END(8);
}

extern "C" void kernel_launch(void* const* d_in, const int* in_sizes, int n_in,
                              void* d_out, int out_size, void* d_ws, size_t ws_size,
                              hipStream_t stream) {
    const void* A_q = d_in[0];
    const void* A_k = d_in[1];
    const void* A_v = d_in[2];
    const void* A_shift = d_in[3];
    const void* A_ssmk = d_in[4];
    const void* A_D = d_in[5];
    const void* B_x = d_in[6];
    const void* B_wqkv = d_in[7];
    const void* B_wo = d_in[8];
    char* ws = (char*)d_ws;
    k1_qkv<<<256, 512, 0, stream>>>((const char*)B_x, (const char*)B_wqkv, ws);
    k2_attn<<<256, 768, 0, stream>>>(ws, (const char*)A_q, (const char*)A_k, (const char*)A_v, (const char*)A_shift,
                                     (const char*)A_ssmk, (const unsigned*)A_D, (const char*)B_wo, (char*)d_out);
}
```

```cpp
#include <hip/hip_runtime.h>
#include <hip/hip_bf16.h>
#include <stdint.h>

typedef short bf16x8 __attribute__((ext_vector_type(8)));
typedef short s16x4 __attribute__((ext_vector_type(4)));
typedef float f32x16 __attribute__((ext_vector_type(16)));
typedef float f32x4 __attribute__((ext_vector_type(4)));
typedef float f32x2 __attribute__((ext_vector_type(2)));
typedef unsigned u32x4 __attribute__((ext_vector_type(4)));
typedef unsigned u32x2 __attribute__((ext_vector_type(2)));
typedef __bf16 bf16x2_t __attribute__((ext_vector_type(2)));

#define LAS __attribute__((address_space(3)))
typedef LAS char* lds_ptr;
#define DI __device__ __forceinline__
#define SBAR() __builtin_amdgcn_sched_barrier(0)
#define MFMA32(a, b, c) __builtin_amdgcn_mfma_f32_32x32x16_bf16((a), (b), (c), 0, 0, 0)
#define MFMA16(a, b, c) __builtin_amdgcn_mfma_f32_16x16x32_bf16((a), (b), (c), 0, 0, 0)
#define WAIT_BAR(N) asm volatile("s_waitcnt vmcnt(" #N ") lgkmcnt(0)\n\ts_barrier" ::: "memory")
#define LGKM_BAR() asm volatile("s_waitcnt lgkmcnt(0)\n\ts_barrier" ::: "memory")

#define DG_DECL(K)
#define DG(i)
#define DG_END(i)

constexpr int S_LEN = 2048, DM = 1024, NH = 32;

constexpr size_t WS_Q = 0;
constexpr size_t WS_KV = 4u << 20;
constexpr size_t WS_O = 12u << 20;
constexpr size_t WS_KVR = 16u << 20;
constexpr size_t WS_ZT = 20u << 20;
constexpr size_t WS_CNT = 24u << 20;

DI unsigned cvtpk(float lo, float hi) {
    f32x2 v = {lo, hi};
    return __builtin_bit_cast(unsigned, __builtin_convertvector(v, bf16x2_t));
}
DI float bflo(unsigned u) { return __uint_as_float(u << 16); }
DI float bfhi(unsigned u) { return __uint_as_float(u & 0xffff0000u); }

DI void glds16(unsigned voff, const void* sbase, unsigned lds_dst) {
    unsigned sv;
    asm volatile("s_mov_b32 %0, m0\n\ts_mov_b32 m0, %3\n\ts_nop 0\n\tglobal_load_lds_dwordx4 %1, %2\n\ts_mov_b32 m0, %0"
                 : "=&s"(sv) : "v"(voff), "s"(sbase), "s"(lds_dst) : "memory");
}
DI bf16x8 lds_rd128(lds_ptr p) { return *(const LAS bf16x8*)p; }
DI s16x4 lds_tr(lds_ptr p) {
    return __builtin_bit_cast(s16x4, __builtin_amdgcn_ds_read_tr16_b64_v4i16((LAS s16x4*)p));
}
DI bf16x8 cat8(s16x4 a, s16x4 b) { return __builtin_shufflevector(a, b, 0, 1, 2, 3, 4, 5, 6, 7); }
DI u32x4 pack8(const f32x16& x, int s, float sc) {
    u32x4 p;
    p[0] = cvtpk(x[8 * s + 0] * sc, x[8 * s + 1] * sc);
    p[1] = cvtpk(x[8 * s + 2] * sc, x[8 * s + 3] * sc);
    p[2] = cvtpk(x[8 * s + 4] * sc, x[8 * s + 5] * sc);
    p[3] = cvtpk(x[8 * s + 6] * sc, x[8 * s + 7] * sc);
    return p;
}
DI u32x4 pack8n(const f32x16& x, int s) {
    u32x4 p;
    p[0] = cvtpk(x[8 * s + 0], x[8 * s + 1]);
    p[1] = cvtpk(x[8 * s + 2], x[8 * s + 3]);
    p[2] = cvtpk(x[8 * s + 4], x[8 * s + 5]);
    p[3] = cvtpk(x[8 * s + 6], x[8 * s + 7]);
    return p;
}
DI void rnd2(float& a, float& b) { unsigned u = cvtpk(a, b); a = bflo(u); b = bfhi(u); }

#define CONV_PACE()
#define RAW_BAR() asm volatile("s_barrier" ::: "memory")
DI unsigned gld32(unsigned voff, const void* sbase) {
    unsigned r;
    asm volatile("global_load_dword %0, %1, %2" : "=v"(r) : "v"(voff), "s"(sbase) : "memory");
    return r;
}
DI u32x2 gld64(unsigned voff, const void* sbase) {
    u32x2 r;
    asm volatile("global_load_dwordx2 %0, %1, %2" : "=v"(r) : "v"(voff), "s"(sbase) : "memory");
    return r;
}
DI float mul_sv(float s, float v) { float r; asm("v_mul_f32_e32 %0, %1, %2" : "=v"(r) : "s"(s), "v"(v)); return r; }
DI float mul_vv(float a, float b) { float r; asm("v_mul_f32_e32 %0, %1, %2" : "=v"(r) : "v"(a), "v"(b)); return r; }
DI float add_vv(float a, float b) { float r; asm("v_add_f32_e32 %0, %1, %2" : "=v"(r) : "v"(a), "v"(b)); return r; }
DI void fmac_vv(float& acc, float a, float b) { asm("v_fmac_f32_e32 %0, %1, %2" : "+v"(acc) : "v"(a), "v"(b)); }
DI void fmac_sv(float& acc, float s, float v) { asm("v_fmac_f32_e32 %0, %1, %2" : "+v"(acc) : "s"(s), "v"(v)); }
typedef __attribute__((address_space(1))) unsigned gu32;
#define RLX_AGENT __ATOMIC_RELAXED, __HIP_MEMORY_SCOPE_AGENT
#define CFENCE() asm volatile("" ::: "memory")
#define DRAIN() asm volatile("s_waitcnt vmcnt(0)" ::: "memory")
constexpr int AUX_SC1 = 16;
DI u32x4 ld_sc1(__amdgpu_buffer_rsrc_t rs, unsigned off) { return __builtin_amdgcn_raw_buffer_load_b128(rs, (int)off, 0, AUX_SC1); }
DI u32x4 ld_sc1s(__amdgpu_buffer_rsrc_t rs, unsigned off, int soff) { return __builtin_amdgcn_raw_buffer_load_b128(rs, (int)off, soff, AUX_SC1); }
DI void st_sc1(u32x4 v, __amdgpu_buffer_rsrc_t rs, unsigned off) { __builtin_amdgcn_raw_buffer_store_b128(v, rs, (int)off, 0, AUX_SC1); }
DI unsigned lds_add_rtn(lds_ptr p, int lane) {
    unsigned old = 0;
    CFENCE();
    if (lane == 0) old = __hip_atomic_fetch_add((LAS unsigned*)p, 1u, __ATOMIC_RELAXED, __HIP_MEMORY_SCOPE_WORKGROUP);
    old = __builtin_amdgcn_readfirstlane(old);
    CFENCE();
    return old;
}
DI void lds_add(lds_ptr p, int lane) {
    CFENCE();
    if (lane == 0) (void)__hip_atomic_fetch_add((LAS unsigned*)p, 1u, __ATOMIC_RELAXED, __HIP_MEMORY_SCOPE_WORKGROUP);
    CFENCE();
}
DI void lds_wait_ge(lds_ptr p, unsigned v) {
    CFENCE();
    while ((unsigned)__builtin_amdgcn_readfirstlane(*(volatile LAS unsigned*)p) < v) __builtin_amdgcn_s_sleep(1);
    CFENCE();
}
DI void poll_ge(gu32* word, unsigned want) {
    CFENCE();
    for (unsigned spins = 0; spins < (1u << 22); ++spins) {
        if ((unsigned)__builtin_amdgcn_readfirstlane(__hip_atomic_load(word, RLX_AGENT)) >= want) break;
        __builtin_amdgcn_s_sleep(1);
    }
    __builtin_amdgcn_fence(__ATOMIC_ACQUIRE, "wavefront");
    CFENCE();
}
#define PRIO(x) do { __builtin_amdgcn_s_setprio(x); SBAR(); } while (0)
DI u32x4 gld128(unsigned voff, const void* sbase) {
    u32x4 r;
    asm volatile("global_load_dwordx4 %0, %1, %2" : "=v"(r) : "v"(voff), "s"(sbase) : "memory");
    return r;
}

constexpr int K1_STAGE = 40960;
__global__ __launch_bounds__(512, 2) void k1_qkv(const char* __restrict__ X, const char* __restrict__ W,
                                                 char* __restrict__ ws) {
    __shared__ __attribute__((aligned(16))) char lds_raw[4 * K1_STAGE];
    const lds_ptr lds = (lds_ptr)lds_raw;
    const unsigned lds0 = (unsigned)(uintptr_t)lds;
    const int tid = threadIdx.x, lane = tid & 63;
    const int w = __builtin_amdgcn_readfirstlane(tid >> 6);
    const int b = blockIdx.x, xcd = b & 7, idx = b >> 3;
    const int hp = (xcd >> 1) * 4 + (idx & 3), tb = (xcd & 1) * 8 + (idx >> 2);
    DG_DECL(0)
    const unsigned long long xr0 = __builtin_amdgcn_s_memrealtime(); const unsigned long long xc0 = __builtin_readcyclecounter();

    if (w >= 4) {
        const int lw = w - 4;
        const int rot = (tb & 3) * 4;
        unsigned voffX[4], voffW[2];
#pragma unroll
        for (int pp = 0; pp < 4; ++pp) {
            const int row = 32 * lw + 8 * pp + (lane >> 3);
            const int ch = (lane & 7) ^ ((row >> 1) & 7);
            voffX[pp] = (unsigned)(tb * 128 + row) * 2048u + ch * 16;
        }
#pragma unroll
        for (int pp = 0; pp < 2; ++pp) {
            const int krow = 16 * lw + 8 * pp + (lane >> 3);
            const int ch = (lane & 7) ^ (((lane >> 4) & 1) << 2);
            voffW[pp] = (unsigned)krow * 6144u + hp * 128 + ch * 16;
        }
        auto issue = [&](int kt) {
            const int kr = (kt + rot) & 15;
            const char* xb = X + kr * 128;
            const char* wb = W + (size_t)kr * (64 * 6144);
            const unsigned st = lds0 + (kt & 3) * K1_STAGE;
#pragma unroll
            for (int pp = 0; pp < 4; ++pp) glds16(voffX[pp], xb, st + (4 * lw + pp) * 1024);
#pragma unroll
            for (int sec = 0; sec < 3; ++sec)
#pragma unroll
                for (int pp = 0; pp < 2; ++pp)
                    glds16(voffW[pp], wb + sec * 2048, st + 16384 + sec * 8192 + (2 * lw + pp) * 1024);
        };
        issue(0); issue(1); issue(2);
#pragma unroll 1
        for (int kt = 0; kt < 13; ++kt) {
            WAIT_BAR(20);
            issue(kt + 3);
        }
        WAIT_BAR(20);
        WAIT_BAR(10);
        WAIT_BAR(0);
        if (lw == 0 && lane < 3) *(unsigned*)(ws + WS_CNT + (lane * 256 + b) * 256) = 0u;
        DG_END(5);
        return;
    }

    const int wm = w >> 1, wn = w & 1;
    const int r = lane & 31, hh = lane >> 5;
    const int g = lane >> 4, q = (lane & 15) >> 2, p = lane & 3;
    const int xoff0 = (64 * wm + r) * 128 + ((hh ^ ((r >> 1) & 7)) << 4);
    const int woff0 = 16384 + (8 * hh + q) * 128 + (((4 * wn + 2 * (g & 1) + (p >> 1)) ^ ((q >> 1) << 2)) << 4) + (p & 1) * 8;

    f32x16 acc[2][3];
#pragma unroll
    for (int m = 0; m < 2; ++m)
#pragma unroll
        for (int s = 0; s < 3; ++s) acc[m][s] = f32x16{};
    bf16x8 xf[2][2];
    s16x4 wl[2][3], wh[2][3];
#define K1_WF(f, sec) cat8(wl[f][sec], wh[f][sec])
#define K1_STEP(F, st, t, BAR_HERE) do { \
        const lds_ptr xp_ = (st) + (xoff0 ^ ((t) << 5)); const lds_ptr wp_ = (st) + woff0 + (t) * 2048; \
        acc[0][0] = MFMA32(K1_WF(F, 0), xf[F][0], acc[0][0]); SBAR(); \
        if (BAR_HERE) { LGKM_BAR(); SBAR(); } \
        xf[F ^ 1][0] = lds_rd128(xp_); wl[F ^ 1][0] = lds_tr(wp_); wh[F ^ 1][0] = lds_tr(wp_ + 512); SBAR(); \
        acc[0][1] = MFMA32(K1_WF(F, 1), xf[F][0], acc[0][1]); SBAR(); \
        wl[F ^ 1][1] = lds_tr(wp_ + 8192); wh[F ^ 1][1] = lds_tr(wp_ + 8192 + 512); SBAR(); \
        acc[0][2] = MFMA32(xf[F][0], K1_WF(F, 2), acc[0][2]); SBAR(); \
        wl[F ^ 1][2] = lds_tr(wp_ + 16384); wh[F ^ 1][2] = lds_tr(wp_ + 16384 + 512); xf[F ^ 1][1] = lds_rd128(xp_ + 4096); SBAR(); \
        acc[1][0] = MFMA32(K1_WF(F, 0), xf[F][1], acc[1][0]); SBAR(); \
        acc[1][1] = MFMA32(K1_WF(F, 1), xf[F][1], acc[1][1]); \
        acc[1][2] = MFMA32(xf[F][1], K1_WF(F, 2), acc[1][2]); SBAR(); \
    } while (0)

    LGKM_BAR();
    DG(3);
    {
        const lds_ptr xp_ = lds + xoff0; const lds_ptr wp_ = lds + woff0;
        xf[0][0] = lds_rd128(xp_); xf[0][1] = lds_rd128(xp_ + 4096);
#pragma unroll
        for (int sec = 0; sec < 3; ++sec) { wl[0][sec] = lds_tr(wp_ + sec * 8192); wh[0][sec] = lds_tr(wp_ + sec * 8192 + 512); }
    }
    SBAR();
#pragma unroll
    for (int kt = 0; kt < 15; ++kt) {
        const lds_ptr st = lds + (kt & 3) * K1_STAGE;
        const lds_ptr sn = lds + ((kt + 1) & 3) * K1_STAGE;
        K1_STEP(0, st, 1, false);
        K1_STEP(1, st, 2, false);
        K1_STEP(0, st, 3, false);
        K1_STEP(1, sn, 0, true);
    }
    {
        const lds_ptr st = lds + 3 * K1_STAGE;
        K1_STEP(0, st, 1, false);
        K1_STEP(1, st, 2, false);
        K1_STEP(0, st, 3, false);
#pragma unroll
        for (int m = 0; m < 2; ++m) {
            acc[m][1] = MFMA32(K1_WF(1, 1), xf[1][m], acc[m][1]);
            acc[m][2] = MFMA32(xf[1][m], K1_WF(1, 2), acc[m][2]);
            acc[m][0] = MFMA32(K1_WF(1, 0), xf[1][m], acc[m][0]);
        }
    }
    DG(4);
    {
        const int h = 2 * hp + wn;
        const float qscale = 0.17677669529663687f * 1.4426950408889634f;
        const __amdgpu_buffer_rsrc_t rs = __builtin_amdgcn_make_buffer_rsrc(ws, 0, 32 << 20, 0x00020000);
#pragma unroll
        for (int m = 0; m < 2; ++m) {
            const int T = tb * 4 + wm * 2 + m;
            const unsigned dq = (unsigned)WS_Q + (h * 64 + T) * 2048 + lane * 16;
            const unsigned dk = (unsigned)WS_KV + (h * 64 + T) * 4096 + lane * 16;
            st_sc1(pack8n(acc[m][1], 0), rs, dk);
            st_sc1(pack8n(acc[m][1], 1), rs, dk + 1024);
            st_sc1(pack8n(acc[m][2], 0), rs, dk + 2048);
            st_sc1(pack8n(acc[m][2], 1), rs, dk + 3072);
            st_sc1(pack8(acc[m][0], 0, qscale), rs, dq);
            st_sc1(pack8(acc[m][0], 1, qscale), rs, dq + 1024);
        }
    }
    if (b == 0 && w == 0) { DRAIN(); const unsigned long long xr1 = __builtin_amdgcn_s_memrealtime(); const unsigned long long xc1 = __builtin_readcyclecounter();
        if (lane == 0) { unsigned long long* p = (unsigned long long*)(ws + (30u << 20)); p[0] = xr0; p[1] = xr1; p[2] = xc1 - xc0; } }
    DG_END(5);
}

constexpr int K2_SLOT = 32768;
constexpr int K2_SF = 65536;
constexpr int K2_CV = 98304;
constexpr int CV_HA = 0, CV_HB = 4160, CV_KV = 8320, CV_BYTES = 8320 + 480 + 4096;
constexpr int K2_LX = K2_CV + 4 * CV_BYTES;
constexpr int K2_FL = K2_LX + 2048;
constexpr int K2_SCR = K2_FL + 256;
constexpr int K2_LDS = K2_SCR + 4096;
constexpr int F_X = 0, F_R = 32, F_W2 = 64, C_OST = 68, F_GO = 72, C_S1 = 76, C_S2 = 80, C_S3 = 128, C_ZST = 88, F_ZGO = 92,
              C_KST = 96, C_U0 = 100, C_LD = 108, F_KGO = 116, C_OL = 120;
#define NG_IN 0
#define GATE_WORD C_LD
#define GATE_VAL 4u
#define X_NZ 4
#define X_NW3 8
#define ZP_GATE 4u
#define ZL_GATE 8u
#define OWIN 8
#define KROT 2
#define NG_LAST 0
#define PACE_IN "s_nop 7"
#define PACE_OUT "s_nop 1"
#define P3_PRIO 1
#define AUX_PRIO 0

__global__ __launch_bounds__(768, 3) void k2_attn(char* __restrict__ ws, const char* __restrict__ Aq,
                                                  const char* __restrict__ Ak, const char* __restrict__ Av,
                                                  const char* __restrict__ shiftk, const char* __restrict__ ssmk,
                                                  const unsigned* __restrict__ ssmD, const char* __restrict__ Wo,
                                                  char* __restrict__ out) {
    __shared__ __attribute__((aligned(16))) char lds_raw[K2_LDS];
    const lds_ptr lds = (lds_ptr)lds_raw;
    const lds_ptr fl = lds + K2_FL;
    const unsigned lds0 = (unsigned)(uintptr_t)lds;
    const int tid = threadIdx.x, lane = tid & 63;
    const int w = __builtin_amdgcn_readfirstlane(tid >> 6);
    const int b = blockIdx.x, xcd = b & 7, idx = b >> 3;
    const int h = xcd * 4 + (idx >> 3), qb = idx & 7;
    const int tt = xcd * 2 + (idx >> 4), nt = idx & 15;
    const char* kvg = ws + WS_KV + (size_t)h * 64 * 4096;
    const __amdgpu_buffer_rsrc_t rs = __builtin_amdgcn_make_buffer_rsrc(ws, 0, 32 << 20, 0x00020000);
    gu32* const cnt = (gu32*)(ws + WS_CNT);
    auto signal = [&](int ty, int g) {
        if (lane < 32) (void)__hip_atomic_fetch_add(cnt + (ty * 256 + lane * 8 + g) * 64, 1u, RLX_AGENT);
    };
    const unsigned wvoff = (unsigned)(lane >> 3) * 2048u + nt * 128 + (((lane & 7) ^ (((lane >> 4) & 1) << 2)) << 4);
    auto wsrc = [&](int hf, int lp) { return Wo + (size_t)(hf * 64 + ((lp + nt * KROT * 2) & 63)) * 16384; };
    DG_DECL(1)
    const unsigned long long xr0 = __builtin_amdgcn_s_memrealtime(); const unsigned long long xc0 = __builtin_readcyclecounter();

    if (w >= 8) {
        const int cw = w - 8;
        const unsigned voff = lane * 16;
        __builtin_amdgcn_s_setprio(3);
        auto issue = [&](int st) {
            const char* src = kvg + (st * 4 + cw) * 4096;
            const unsigned dst = lds0 + (st & 1) * K2_SLOT + cw * 4096;
#pragma unroll
            for (int i = 0; i < 4; ++i) glds16(voff, src + i * 1024, dst + i * 1024);
#pragma unroll
            for (int i = 0; i < 4; ++i) glds16(voff, src + 131072 + i * 1024, dst + 16384 + i * 1024);
        };
        issue(0);
        const int ch = xcd * 128 + idx * 4 + cw;
        const lds_ptr cv = lds + K2_CV + cw * CV_BYTES;
        {
            const u32x4 z4 = {0u, 0u, 0u, 0u};
            if (lane < 2) *(LAS u32x4*)(cv + CV_HA + lane * 16) = z4;
            if (lane < 30) *(LAS u32x4*)(cv + CV_KV + lane * 16) = z4;
            if (cw == 0 && lane < 16) *(LAS u32x4*)(fl + lane * 16) = z4;
        }
        WAIT_BAR(0);
        issue(1);
        const int c0 = xcd * 128 + cw * 32, cq = lane & 7, rg = lane >> 3;
        const int row0 = idx * 64 + rg * 8;
        const unsigned roff = (unsigned)row0 * 2048u + (c0 + cq * 4) * 2;
        const unsigned toffs = (unsigned)(c0 + (lane >> 3)) * 4096u + idx * 128;
        u32x4 sk[2];
        sk[0] = gld128((unsigned)(c0 + cq * 4) * 8u, shiftk); sk[1] = gld128((unsigned)(c0 + cq * 4) * 8u, shiftk + 16);
        unsigned dd = gld32((unsigned)(ch >> 1) * 4u, ssmD);
        auto stage = [&](const char* src, int dst) {
            const unsigned sbase = (unsigned)(idx * 64 + cw * 16 + (lane >> 4)) * 2048u + xcd * 256;
#pragma unroll
            for (int i = 0; i < 4; ++i)
                glds16(sbase + (((lane & 15) ^ (((2 * cw + (i >> 1)) & 3) << 2)) << 4), src + i * 8192, lds0 + dst + cw * 4096 + i * 1024);
        };
        WAIT_BAR(3);
        DG(12);
        issue(2);
        stage(Ak, K2_SF);
        glds16((unsigned)(idx * 64 - (idx ? 4 : 0) + (lane >> 4)) * 2048u + xcd * 256 + (((lane & 15) ^ 12) << 4), Ak, lds0 + K2_SCR + cw * 1024);
        WAIT_BAR(5);
        DG(9);
        issue(3);
        stage(Av, K2_SF + 16384);
        WAIT_BAR(4);
        DG(3);
        issue(4);
#pragma unroll
        for (int i = 0; i < 4; ++i) glds16(voff, ssmk + (size_t)ch * 4096 + i * 1024, lds0 + K2_CV + cw * CV_BYTES + CV_HA + 32 + i * 1024);
        WAIT_BAR(4);
        DG(5);
        issue(5);
        SBAR();
        asm volatile("" : "+v"(sk[0]), "+v"(sk[1]), "+v"(dd));
        dd = __builtin_amdgcn_readfirstlane(dd);
        {
            u32x2 kr[11], vr[8];
            const lds_ptr kb = lds + K2_SF + rg * 2048 + ((cw ^ (rg & 3)) << 6) + cq * 8;
            const lds_ptr hb = lds + (rg ? K2_SF + rg * 2048 - 768 : K2_SCR + cw * 1024 + 256) + ((cw ^ ((rg + 3) & 3)) << 6) + cq * 8;
#pragma unroll
            for (int i = 0; i < 3; ++i) kr[i] = *(const LAS u32x2*)(hb + i * 256);
#pragma unroll
            for (int i = 0; i < 8; ++i) kr[3 + i] = *(const LAS u32x2*)(kb + i * 256);
#pragma unroll
            for (int i = 0; i < 8; ++i) vr[i] = *(const LAS u32x2*)(kb + 16384 + i * 256);
            if (row0 == 0) { kr[0] = u32x2{0u, 0u}; kr[1] = u32x2{0u, 0u}; kr[2] = u32x2{0u, 0u}; }
            const lds_ptr tb = cv + CV_KV + 480;
#pragma unroll
            for (int c = 0; c < 4; ++c) {
                const unsigned s01 = sk[c >> 1][(c & 1) * 2], s23 = sk[c >> 1][(c & 1) * 2 + 1];
                const float w0 = bflo(s01), w1 = bfhi(s01), w2 = bflo(s23), w3 = bfhi(s23);
                float kf[11], kv[8];
#pragma unroll
                for (int i = 0; i < 11; ++i) kf[i] = (c & 1) ? bfhi(kr[i][c >> 1]) : bflo(kr[i][c >> 1]);
#pragma unroll
                for (int i = 0; i < 8; ++i) {
                    float a = mul_vv(w3, kf[i]);
                    fmac_vv(a, w2, kf[i + 1]); fmac_vv(a, w1, kf[i + 2]); fmac_vv(a, w0, kf[i + 3]);
                    kv[i] = mul_vv(a, (c & 1) ? bfhi(vr[i][c >> 1]) : bflo(vr[i][c >> 1]));
                }
                u32x4 o4;
#pragma unroll
                for (int i = 0; i < 4; ++i) o4[i] = cvtpk(kv[7 - 2 * i], kv[6 - 2 * i]);
                *(LAS u32x4*)(tb + (cq * 4 + c) * 128 + ((rg ^ cq) << 4)) = o4;
            }
#pragma unroll
            for (int i = 0; i < 4; ++i) {
                const u32x4 t = *(const LAS u32x4*)(tb + i * 1024 + lane * 16);
                st_sc1(t, rs, (unsigned)WS_KVR + toffs + i * 32768 + (((lane & 7) ^ (2 * i + (lane >> 5))) << 4));
            }
        }
        CFENCE();
        DG(7);
        WAIT_BAR(4);
        DG(6);
        issue(6);
        asm volatile("s_waitcnt vmcnt(8)" ::: "memory");
        if (lds_add_rtn(fl + C_KST, lane) == 3u) signal(1, xcd);
#pragma unroll
        for (int i = 0; i < 8; ++i)
            glds16(wvoff, wsrc(i >> 2, cw * 4 + (i & 3)), lds0 + K2_SF + (i >> 2) * 16384 + (cw * 4 + (i & 3)) * 1024);
        {
            const u32x4 z4 = {0u, 0u, 0u, 0u};
            if (lane < 2) *(LAS u32x4*)(cv + CV_HB + lane * 16) = z4;
#pragma unroll
            for (int i = 0; i < 4; ++i) {
                const int off = 32 + i * 1024 + lane * 16;
                const u32x4 hv = *(const LAS u32x4*)(cv + CV_HA + off);
                const unsigned pv = *(const LAS unsigned*)(cv + CV_HA + off - 4);
                u32x4 sh;
                sh[0] = __builtin_amdgcn_alignbit(hv[0], pv, 16);
                sh[1] = __builtin_amdgcn_alignbit(hv[1], hv[0], 16);
                sh[2] = __builtin_amdgcn_alignbit(hv[2], hv[1], 16);
                sh[3] = __builtin_amdgcn_alignbit(hv[3], hv[2], 16);
                *(LAS u32x4*)(cv + CV_HB + off) = sh;
            }
        }
        WAIT_BAR(8);
        DG(8);
        issue(7);
#pragma unroll
        for (int i = 0; i < 8; ++i)
            glds16(wvoff, wsrc(idx >> 4, 32 + cw * 8 + i), lds0 + K2_SCR + cw * 1024);
        WAIT_BAR(8);
        DG(11);
        if (cw == 0) { poll_ge(cnt + (256 + b) * 64, 32u); lds_add(fl + F_KGO, lane); }
        else lds_wait_ge(fl + F_KGO, 1u);
        {
            u32x4 t[4];
#pragma unroll
            for (int i = 0; i < 4; ++i) t[i] = ld_sc1(rs, (unsigned)WS_KVR + ch * 4096 + i * 1024 + voff);
#pragma unroll
            for (int i = 0; i < 4; ++i) *(LAS u32x4*)(cv + CV_KV + 480 + i * 1024 + lane * 16) = t[i];
        }
        const int r1 = lane & 15, q = lane >> 4;
        const int toff = (r1 & 1) ? CV_HA + 2 * (16 + r1 + 9 - 8 * q) : CV_HB + 2 * (16 + r1 + 10 - 8 * q);
        const int boff = CV_KV + 448 + 32 * r1 + 16 * q;
        f32x4 y[8];
#pragma unroll
        for (int t = 0; t < 8; ++t) y[t] = f32x4{};
        bf16x8 tf[8], bf[8];
        auto ldt = [&](int bb, int a) {
            const LAS unsigned* tp = (const LAS unsigned*)(cv + toff + 64 * bb + 512 * a);
            u32x4 t4 = {tp[0], tp[1], tp[2], tp[3]};
            tf[a] = __builtin_bit_cast(bf16x8, t4);
        };
        auto ldb = [&](int bb, int a) { bf[a] = lds_rd128(cv + boff - 64 * bb + 512 * a); };
#define GROUP(bb, PACE) do { \
            _Pragma("unroll") for (int a = 0; a < 8; ++a) { \
                _Pragma("unroll") for (int ww = 7 - a; ww >= 0; --ww) { \
                    y[a + ww] = MFMA16(tf[a], bf[ww], y[a + ww]); SBAR(); \
                    asm volatile(PACE); SBAR(); \
                } \
                ldt(((bb) + 1) & 7, a); ldb(((bb) + 1) & 7, 7 - a); SBAR(); \
            } } while (0)
#pragma unroll
        for (int a = 0; a < 8; ++a) { ldt(0, a); ldb(0, 7 - a); }
        SBAR();
        u32x2 qr[8];
#pragma unroll
        for (int i = 0; i < 8; ++i)
            glds16(wvoff, wsrc(i >> 2, 16 + cw * 4 + (i & 3)), lds0 + (i >> 2) * 16384 + (cw * 4 + (i & 3)) * 1024);
#pragma unroll
        for (int i = 0; i < 8; ++i) qr[i] = gld64(roff, Aq + i * 2048);
#pragma unroll 1
        for (int bb = NG_IN; bb < NG_IN + NG_LAST; ++bb) GROUP(bb, PACE_IN);
        asm volatile("s_waitcnt vmcnt(8)" ::: "memory");
        lds_add(fl + C_S1, lane);
        CFENCE();
        while ((unsigned)__builtin_amdgcn_readfirstlane(*(volatile LAS unsigned*)(fl + GATE_WORD)) < GATE_VAL) __builtin_amdgcn_s_sleep(2);
        CFENCE();
        __builtin_amdgcn_s_setprio(AUX_PRIO);
#pragma unroll 1
        for (int bb = NG_IN + NG_LAST; bb < 8; ++bb) GROUP(bb, PACE_OUT);
        DG(4);
        __builtin_amdgcn_s_setprio(3);
        {
            const float dch = (ch & 1) ? bfhi(dd) : bflo(dd);
#pragma unroll
            for (int t = 0; t < 8; ++t) {
                const u32x2 kk = *(const LAS u32x2*)(cv + CV_KV + 480 + 512 * t + 32 * r1 + 16 * (q >> 1) + ((q & 1) ? 0 : 8));
                float k0 = mul_sv(dch, bfhi(kk[1])), k1 = mul_sv(dch, bflo(kk[1]));
                float k2 = mul_sv(dch, bfhi(kk[0])), k3 = mul_sv(dch, bflo(kk[0]));
                rnd2(k0, k1); rnd2(k2, k3);
                float y0 = y[t][0], y1 = y[t][1], y2 = y[t][2], y3 = y[t][3];
                rnd2(y0, y1); rnd2(y2, y3);
                u32x2 o = {cvtpk(y0 + k0, y1 + k1), cvtpk(y2 + k2, y3 + k3)};
                *(LAS u32x2*)(cv + CV_HA + 512 * t + 32 * r1 + 8 * q) = o;
            }
#pragma unroll
            for (int i = 0; i < X_NZ; ++i)
                st_sc1(*(const LAS u32x4*)(cv + CV_HA + i * 1024 + lane * 16), rs, (unsigned)WS_ZT + ch * 4096 + i * 1024 + voff);
        }
        DG(10);
        DRAIN();
        if (lds_add_rtn(fl + C_ZST, lane) == 3u) {
            signal(2, xcd);
            lds_wait_ge(fl + C_OL, ZP_GATE);
            poll_ge(cnt + (512 + b) * 64, 32u);
            lds_add(fl + F_ZGO, lane);
        } else lds_wait_ge(fl + F_ZGO, 1u);
        lds_wait_ge(fl + C_OL, ZL_GATE);
        DG(13);
        {
            const lds_ptr tr = cv;
            u32x4 t[4];
#pragma unroll
            for (int i = 0; i < 4; ++i)
                t[i] = ld_sc1(rs, (unsigned)WS_ZT + toffs + i * 32768 + (((lane & 7) ^ (2 * i + (lane >> 5))) << 4));
#pragma unroll
            for (int i = 0; i < 4; ++i) *(LAS u32x4*)(tr + i * 1024 + lane * 16) = t[i];
#pragma unroll
            for (int i = 0; i < 8; ++i) asm volatile("" : "+v"(qr[i]));
            u32x4 zc[4];
#pragma unroll
            for (int c = 0; c < 4; ++c) zc[c] = *(const LAS u32x4*)(tr + (cq * 4 + c) * 128 + ((rg ^ cq) << 4));
            char* dst = out + (size_t)row0 * 4096 + (c0 + cq * 4) * 2;
#pragma unroll
            for (int i = 0; i < 8; ++i) {
                float zz[4];
#pragma unroll
                for (int c = 0; c < 4; ++c) zz[c] = (i & 1) ? bfhi(zc[c][i >> 1]) : bflo(zc[c][i >> 1]);
                u32x2 o = {cvtpk(bflo(qr[i][0]) * zz[0], bfhi(qr[i][0]) * zz[1]),
                           cvtpk(bflo(qr[i][1]) * zz[2], bfhi(qr[i][1]) * zz[3])};
                *(u32x2*)(dst + i * 4096) = o;
            }
        }
        DG_END(14);
        return;
    }

    const int half = w >> 2;
    const int TA = qb * 8 + (w & 3) * 2 + half, TB = TA ^ 1;
    const char* qga = ws + WS_Q + (size_t)(h * 64 + TA) * 2048 + lane * 16;
    const char* qgb = ws + WS_Q + (size_t)(h * 64 + TB) * 2048 + lane * 16;
    const bf16x8 qa0 = *(const bf16x8*)qga, qa1 = *(const bf16x8*)(qga + 1024);
    const bf16x8 qb0 = *(const bf16x8*)qgb, qb1 = *(const bf16x8*)(qgb + 1024);
    bf16x8 sel;
    {
        const short one = (((lane >> 2) & 1) == ((lane >> 4) & 1)) ? (short)0x3f80 : (short)0;
#pragma unroll
        for (int i = 0; i < 8; ++i) sel[i] = one;
    }
    f32x16 oA = f32x16{}, oB = f32x16{};
    f32x4 lA = f32x4{}, lB = f32x4{};
    f32x16 negm, sA, sB;
    const lds_ptr lp = lds + half * 16384 + lane * 16;
    bf16x8 k0[2], k1[2], v0[2], v1[2];
    u32x4 p0, p1 = {0u, 0u, 0u, 0u};
#define EX(X, i) X[i] = __builtin_amdgcn_exp2f(X[i])
#define PB(x) __builtin_bit_cast(bf16x8, x)
    LGKM_BAR();
    DG(3);
    {
        const bf16x8 x0 = lds_rd128(lds + lane * 16), x1 = lds_rd128(lds + lane * 16 + 1024);
        k0[0] = lds_rd128(lp); k1[0] = lds_rd128(lp + 1024);
        v0[0] = lds_rd128(lp + 2048); v1[0] = lds_rd128(lp + 3072);
        sA = MFMA32(x0, qa0, f32x16{});
        sB = MFMA32(x0, qb0, f32x16{});
        sA = MFMA32(x1, qa1, sA);
        sB = MFMA32(x1, qb1, sB);
        float m = __builtin_fmaxf(sA[0], sB[0]);
#pragma unroll
        for (int i = 1; i < 16; ++i) m = __builtin_fmaxf(m, __builtin_fmaxf(sA[i], sB[i]));
        auto rr = __builtin_amdgcn_permlane32_swap(__float_as_uint(m), __float_as_uint(m), false, false);
        m = __builtin_fmaxf(__uint_as_float(rr[0]), __uint_as_float(rr[1]));
#pragma unroll
        for (int i = 0; i < 16; ++i) negm[i] = -m;
        sA = MFMA32(k0[0], qa0, negm);
        sA = MFMA32(k1[0], qa1, sA);
        v1[1] = v1[0];
        EX(sA, 0); EX(sA, 1);
    }
#define SUM16(l, p) asm volatile("v_mfma_f32_16x16x32_bf16 %0, %1, %2, %0" : "+v"(l) : "v"(sel), "v"(p))
#define HALF(C, oC, lC, N, qx, qy, ka, kb, oP, lP, vprev, vcur0, PRE) do { \
        oP = MFMA32(vprev, PB(p1), oP); SBAR(); \
        PRE; EX(C, 2); EX(C, 3); EX(C, 4); SBAR(); \
        N = MFMA32(ka, qx, negm); SBAR(); \
        EX(C, 5); EX(C, 6); EX(C, 7); SBAR(); \
        SUM16(lP, p1); SBAR(); \
        p0[0] = cvtpk(C[0], C[1]); p0[1] = cvtpk(C[2], C[3]); SBAR(); \
        N = MFMA32(kb, qy, N); SBAR(); \
        p0[2] = cvtpk(C[4], C[5]); p0[3] = cvtpk(C[6], C[7]); SBAR(); EX(C, 8); EX(C, 9); SBAR(); \
        oC = MFMA32(vcur0, PB(p0), oC); SBAR(); \
        EX(C, 10); EX(C, 11); EX(C, 12); SBAR(); \
        SUM16(lC, p0); SBAR(); \
        EX(C, 13); EX(C, 14); EX(C, 15); SBAR(); \
        p1[0] = cvtpk(C[8], C[9]); p1[1] = cvtpk(C[10], C[11]); p1[2] = cvtpk(C[12], C[13]); p1[3] = cvtpk(C[14], C[15]); SBAR(); \
        EX(N, 0); EX(N, 1); SBAR(); \
    } while (0)
#define RD4(Y, nx) do { v1[Y] = lds_rd128((nx) + 3072); v0[Y] = lds_rd128((nx) + 2048); \
        k1[Y] = lds_rd128((nx) + 1024); k0[Y] = lds_rd128(nx); } while (0)
#define KT(X, nx, PRE0) do { \
        HALF(sA, oA, lA, sB, qb0, qb1, k0[X], k1[X], oB, lB, v1[X ^ 1], v0[X], PRE0; RD4(X ^ 1, nx)); \
        HALF(sB, oB, lB, sA, qa0, qa1, k0[X ^ 1], k1[X ^ 1], oA, lA, v1[X], v0[X], (void)0); \
    } while (0)
    SBAR();
#pragma unroll 1
    for (int st = 0; st < 8; ++st) {
        const lds_ptr cur = lp + (st & 1) * K2_SLOT;
        const lds_ptr nxt = lp + ((st + 1) & 1) * K2_SLOT;
        PRIO(2);
        KT(0, cur + 4096, (void)0);
        PRIO(1);
        KT(1, cur + 8192, (void)0);
        PRIO(0);
        KT(0, cur + 12288, (void)0);
        PRIO(3);
        KT(1, nxt, if (st < 7) RAW_BAR(); SBAR());
    }
    oB = MFMA32(v1[1], PB(p1), oB);
    SBAR();
    SUM16(lB, p1);
    asm volatile("s_nop 7\n\ts_nop 7" ::: "memory");
    SBAR();
    DG(4);
    {
        lds_add(fl + C_LD + half * 4, lane);
        const lds_ptr xo = lds + K2_SLOT + w * 4096 + lane * 16;
        *(LAS float*)(lds + K2_LX + w * 256 + lane * 4) = lB[0];
        lds_wait_ge(fl + C_LD + half * 4, 4u);
#pragma unroll
        for (int i = 0; i < 4; ++i) *(LAS f32x4*)(xo + i * 1024) = f32x4{oB[4 * i], oB[4 * i + 1], oB[4 * i + 2], oB[4 * i + 3]};
        lds_add(fl + F_X + w * 4, lane);
        lds_wait_ge(fl + F_X + (w ^ 4) * 4, 1u);
        const lds_ptr xi = lds + K2_SLOT + (w ^ 4) * 4096 + lane * 16;
#pragma unroll
        for (int i = 0; i < 4; ++i) {
            const f32x4 t = *(const LAS f32x4*)(xi + i * 1024);
#pragma unroll
            for (int j = 0; j < 4; ++j) oA[4 * i + j] = add_vv(oA[4 * i + j], t[j]);
        }
        lA[0] += *(const LAS float*)(lds + K2_LX + (w ^ 4) * 256 + lane * 4);
    }
    DG(9);
    float l = lA[0];
    {
        float t0 = 0.f, t1 = 0.f;
#pragma unroll
        for (int i = 0; i < 8; ++i) { fmac_sv(t0, 0.f, oA[i]); fmac_sv(t1, 0.f, oA[8 + i]); }
        const bool ok = (l <= 1.0e30f) && (l >= 1.0e-18f) && (t0 + t1 == 0.f);
        if (__builtin_expect(__any(!ok), 0)) {
            float m = -INFINITY, ls = 0.f;
            oA = f32x16{};
#pragma unroll 1
            for (int t = 0; t < 64; ++t) {
                const char* g = kvg + t * 4096 + lane * 16;
                const bf16x8 a0 = *(const bf16x8*)g, a1 = *(const bf16x8*)(g + 1024);
                const bf16x8 b0 = *(const bf16x8*)(g + 2048), b1 = *(const bf16x8*)(g + 3072);
                f32x16 s = MFMA32(a0, qa0, f32x16{});
                s = MFMA32(a1, qa1, s);
                float tm = s[0];
#pragma unroll
                for (int i = 1; i < 16; ++i) tm = __builtin_fmaxf(tm, s[i]);
                auto rr = __builtin_amdgcn_permlane32_swap(__float_as_uint(tm), __float_as_uint(tm), false, false);
                tm = __builtin_fmaxf(__uint_as_float(rr[0]), __uint_as_float(rr[1]));
                const float mn = __builtin_fmaxf(m, tm);
                const float alpha = __builtin_amdgcn_exp2f(m - mn);
                m = mn;
                float ps = 0.f;
#pragma unroll
                for (int i = 0; i < 16; ++i) { s[i] = __builtin_amdgcn_exp2f(s[i] - mn); ps += s[i]; oA[i] *= alpha; }
                ls = ls * alpha + ps;
                oA = MFMA32(b0, PB(pack8n(s, 0)), oA);
                oA = MFMA32(b1, PB(pack8n(s, 1)), oA);
            }
            auto rr = __builtin_amdgcn_permlane32_swap(__float_as_uint(ls), __float_as_uint(ls), false, false);
            l = __uint_as_float(rr[0]) + __uint_as_float(rr[1]);
        }
    }
    u32x4 w3[4];
    {
        const float rl = __builtin_amdgcn_rcpf(l);
        const unsigned og = (unsigned)WS_O + (unsigned)(TA * 32 + h) * 2048u + lane * 16;
        st_sc1(pack8(oA, 0, rl), rs, og);
        st_sc1(pack8(oA, 1, rl), rs, og + 1024);
        CFENCE();
#pragma unroll
        for (int i = 0; i < 4; ++i)
            glds16(wvoff, wsrc(half ^ 1, 32 + (w & 3) * 4 + i), lds0 + K2_SLOT + ((w ^ 4) * 4 + i) * 1024);
#pragma unroll
        for (int i = 0; i < 4; ++i) w3[i] = gld128(wvoff, wsrc(half, 48 + (w & 3) * 4 + i));
        asm volatile("s_waitcnt vmcnt(8)" ::: "memory");
    }
    DG(5);
    const unsigned arr = lds_add_rtn(fl + C_OST, lane);
    const int j4 = w & 3;
    if (arr == 7u) {
        signal(0, qb);
        poll_ge(cnt + b * 64, 32u);
        lds_add(fl + F_GO, lane);
    } else {
        lds_wait_ge(fl + F_GO, 1u);
    }
    DG(6);
    {
        const int T3 = tt * 4 + j4;
        const unsigned og = (unsigned)WS_O + (unsigned)T3 * 65536u + half * 32768 + lane * 16;
        bf16x8 of[OWIN];
#define LDO(i) do { of[(i) % OWIN] = PB(ld_sc1s(rs, og, (((i) + nt * KROT) & 31) * 1024)); SBAR(); } while (0)
#pragma unroll
        for (int i = 0; i < OWIN; ++i) {
            if (i == OWIN / 2) PRIO(2);
            LDO(i);
        }
        asm volatile("s_waitcnt vmcnt(%0)" :: "n"(OWIN) : "memory");
        lds_add(fl + C_S2, lane);
        __builtin_amdgcn_s_setprio(P3_PRIO);
        SBAR();
        const int r = lane & 31, hh = lane >> 5;
        const int g = lane >> 4, q = (lane & 15) >> 2, p = lane & 3;
        int woff[2];
#pragma unroll
        for (int n = 0; n < 2; ++n) {
            const int c = 4 * (n ^ half) + 2 * (p & 1) + (g & 1);
            woff[n] = (4 * hh + q) * 128 + ((c ^ ((q >> 1) << 2)) << 4) + (p >> 1) * 8;
        }
        f32x16 acc[2] = {f32x16{}, f32x16{}};
        const lds_ptr wl0 = lds + half * 16384 + woff[0], wl1 = lds + half * 16384 + woff[1];
        s16x4 wa[2][2], wb[2][2];
#define LDW(s) do { const int o_ = ((s) < 8 ? K2_SF : (s) >= 24 ? K2_SF : (((s) >> 3) - 1) * K2_SLOT) + ((s) & 7) * 2048; \
            wa[(s) & 1][0] = lds_tr(wl0 + o_); wb[(s) & 1][0] = lds_tr(wl0 + o_ + 1024); \
            wa[(s) & 1][1] = lds_tr(wl1 + o_); wb[(s) & 1][1] = lds_tr(wl1 + o_ + 1024); SBAR(); } while (0)
        lds_wait_ge(fl + C_S1, 4u);
        lds_wait_ge(fl + C_S2, 8u);
        LDW(0);
#pragma unroll
        for (int s = 0; s < 32; ++s) {
            if (s == 16) {
                lds_wait_ge(fl + C_U0 + half * 4, 4u);
#pragma unroll
                for (int i = 0; i < 4; ++i) {
                    asm volatile("" : "+v"(w3[i]));
                    *(LAS u32x4*)(lds + K2_SF + (w * 4 + i) * 1024 + lane * 16) = w3[i];
                }
                lds_add(fl + C_S3 + half * 4, lane);
            }
            if (s == 23) lds_wait_ge(fl + C_S3 + half * 4, 4u);
            if (s < 31) LDW(s + 1);
            if (s == 7) lds_add(fl + C_U0 + half * 4, lane);
            SBAR();
#pragma unroll
            for (int n = 0; n < 2; ++n) acc[n] = MFMA32(cat8(wa[s & 1][n], wb[s & 1][n]), of[s % OWIN], acc[n]);
            SBAR();
            if (s + OWIN < 32) LDO(s + OWIN);
            if (s + OWIN == 31) lds_add(fl + C_OL, lane);
            if (s == 0) DG(10);
            if (s == 15) DG(11);
        }
        DG(7);
        const lds_ptr xo = lds + w * 4096 + lane * 16;
#pragma unroll
        for (int i = 0; i < 4; ++i) *(LAS f32x4*)(xo + i * 1024) = f32x4{acc[1][4 * i], acc[1][4 * i + 1], acc[1][4 * i + 2], acc[1][4 * i + 3]};
        lds_add(fl + F_R + w * 4, lane);
        lds_wait_ge(fl + F_R + (w ^ 4) * 4, 1u);
        const lds_ptr xi = lds + (w ^ 4) * 4096 + lane * 16;
#pragma unroll
        for (int i = 0; i < 4; ++i) {
            const f32x4 t = *(const LAS f32x4*)(xi + i * 1024);
#pragma unroll
            for (int j = 0; j < 4; ++j) acc[0][4 * i + j] += t[j];
        }
        char* dst = out + (size_t)(T3 * 32 + r) * 4096 + 2048 + nt * 128 + half * 64 + hh * 32;
        *(u32x4*)(dst) = pack8n(acc[0], 0);
        *(u32x4*)(dst + 16) = pack8n(acc[0], 1);
    }
    if (b == 0 && w == 0) { DRAIN(); const unsigned long long xr1 = __builtin_amdgcn_s_memrealtime(); const unsigned long long xc1 = __builtin_readcyclecounter();
        const volatile unsigned long long* p = (const volatile unsigned long long*)(ws + (30u << 20));
        unsigned long long k1s = p[0], k1e = p[1], k1c = p[2];
        unsigned long long Q = 0;
        if (Q > 20000) Q = 20000;
        const unsigned long long tend = xr1 + 3 * Q;
        while (__builtin_amdgcn_s_memrealtime() < tend) __builtin_amdgcn_s_sleep(1);
    }
    DG_END(8);
}

extern "C" void kernel_launch(void* const* d_in, const int* in_sizes, int n_in,
                              void* d_out, int out_size, void* d_ws, size_t ws_size,
                              hipStream_t stream) {
    const void* A_q = d_in[0];
    const void* A_k = d_in[1];
    const void* A_v = d_in[2];
    const void* A_shift = d_in[3];
    const void* A_ssmk = d_in[4];
    const void* A_D = d_in[5];
    const void* B_x = d_in[6];
    const void* B_wqkv = d_in[7];
    const void* B_wo = d_in[8];
    char* ws = (char*)d_ws;
    k1_qkv<<<256, 512, 0, stream>>>((const char*)B_x, (const char*)B_wqkv, ws);
    k2_attn<<<256, 768, 0, stream>>>(ws, (const char*)A_q, (const char*)A_k, (const char*)A_v, (const char*)A_shift,
                                     (const char*)A_ssmk, (const unsigned*)A_D, (const char*)B_wo, (char*)d_out);
}
```

```cpp
#include <hip/hip_runtime.h>
#include <hip/hip_bf16.h>
#include <stdint.h>

typedef short bf16x8 __attribute__((ext_vector_type(8)));
typedef short s16x4 __attribute__((ext_vector_type(4)));
typedef float f32x16 __attribute__((ext_vector_type(16)));
typedef float f32x4 __attribute__((ext_vector_type(4)));
typedef float f32x2 __attribute__((ext_vector_type(2)));
typedef unsigned u32x4 __attribute__((ext_vector_type(4)));
typedef unsigned u32x2 __attribute__((ext_vector_type(2)));
typedef __bf16 bf16x2_t __attribute__((ext_vector_type(2)));

#define LAS __attribute__((address_space(3)))
typedef LAS char* lds_ptr;
#define DI __device__ __forceinline__
#define SBAR() __builtin_amdgcn_sched_barrier(0)
#define MFMA32(a, b, c) __builtin_amdgcn_mfma_f32_32x32x16_bf16((a), (b), (c), 0, 0, 0)
#define MFMA16(a, b, c) __builtin_amdgcn_mfma_f32_16x16x32_bf16((a), (b), (c), 0, 0, 0)
#define WAIT_BAR(N) asm volatile("s_waitcnt vmcnt(" #N ") lgkmcnt(0)\n\ts_barrier" ::: "memory")
#define LGKM_BAR() asm volatile("s_waitcnt lgkmcnt(0)\n\ts_barrier" ::: "memory")

#define DG_DECL(K)
#define DG(i)
#define DG_END(i)

constexpr int S_LEN = 2048, DM = 1024, NH = 32;

constexpr size_t WS_Q = 0;
constexpr size_t WS_KV = 4u << 20;
constexpr size_t WS_O = 12u << 20;
constexpr size_t WS_KVR = 16u << 20;
constexpr size_t WS_ZT = 20u << 20;
constexpr size_t WS_CNT = 24u << 20;

DI unsigned cvtpk(float lo, float hi) {
    f32x2 v = {lo, hi};
    return __builtin_bit_cast(unsigned, __builtin_convertvector(v, bf16x2_t));
}
DI float bflo(unsigned u) { return __uint_as_float(u << 16); }
DI float bfhi(unsigned u) { return __uint_as_float(u & 0xffff0000u); }

DI void glds16(unsigned voff, const void* sbase, unsigned lds_dst) {
    unsigned sv;
    asm volatile("s_mov_b32 %0, m0\n\ts_mov_b32 m0, %3\n\ts_nop 0\n\tglobal_load_lds_dwordx4 %1, %2\n\ts_mov_b32 m0, %0"
                 : "=&s"(sv) : "v"(voff), "s"(sbase), "s"(lds_dst) : "memory");
}
DI bf16x8 lds_rd128(lds_ptr p) { return *(const LAS bf16x8*)p; }
DI s16x4 lds_tr(lds_ptr p) {
    return __builtin_bit_cast(s16x4, __builtin_amdgcn_ds_read_tr16_b64_v4i16((LAS s16x4*)p));
}
DI bf16x8 cat8(s16x4 a, s16x4 b) { return __builtin_shufflevector(a, b, 0, 1, 2, 3, 4, 5, 6, 7); }
DI u32x4 pack8(const f32x16& x, int s, float sc) {
    u32x4 p;
    p[0] = cvtpk(x[8 * s + 0] * sc, x[8 * s + 1] * sc);
    p[1] = cvtpk(x[8 * s + 2] * sc, x[8 * s + 3] * sc);
    p[2] = cvtpk(x[8 * s + 4] * sc, x[8 * s + 5] * sc);
    p[3] = cvtpk(x[8 * s + 6] * sc, x[8 * s + 7] * sc);
    return p;
}
DI u32x4 pack8n(const f32x16& x, int s) {
    u32x4 p;
    p[0] = cvtpk(x[8 * s + 0], x[8 * s + 1]);
    p[1] = cvtpk(x[8 * s + 2], x[8 * s + 3]);
    p[2] = cvtpk(x[8 * s + 4], x[8 * s + 5]);
    p[3] = cvtpk(x[8 * s + 6], x[8 * s + 7]);
    return p;
}
DI void rnd2(float& a, float& b) { unsigned u = cvtpk(a, b); a = bflo(u); b = bfhi(u); }

#define CONV_PACE()
#define RAW_BAR() asm volatile("s_barrier" ::: "memory")
DI unsigned gld32(unsigned voff, const void* sbase) {
    unsigned r;
    asm volatile("global_load_dword %0, %1, %2" : "=v"(r) : "v"(voff), "s"(sbase) : "memory");
    return r;
}
DI u32x2 gld64(unsigned voff, const void* sbase) {
    u32x2 r;
    asm volatile("global_load_dwordx2 %0, %1, %2" : "=v"(r) : "v"(voff), "s"(sbase) : "memory");
    return r;
}
DI float mul_sv(float s, float v) { float r; asm("v_mul_f32_e32 %0, %1, %2" : "=v"(r) : "s"(s), "v"(v)); return r; }
DI float mul_vv(float a, float b) { float r; asm("v_mul_f32_e32 %0, %1, %2" : "=v"(r) : "v"(a), "v"(b)); return r; }
DI float add_vv(float a, float b) { float r; asm("v_add_f32_e32 %0, %1, %2" : "=v"(r) : "v"(a), "v"(b)); return r; }
DI void fmac_vv(float& acc, float a, float b) { asm("v_fmac_f32_e32 %0, %1, %2" : "+v"(acc) : "v"(a), "v"(b)); }
DI void fmac_sv(float& acc, float s, float v) { asm("v_fmac_f32_e32 %0, %1, %2" : "+v"(acc) : "s"(s), "v"(v)); }
typedef __attribute__((address_space(1))) unsigned gu32;
#define RLX_AGENT __ATOMIC_RELAXED, __HIP_MEMORY_SCOPE_AGENT
#define CFENCE() asm volatile("" ::: "memory")
#define DRAIN() asm volatile("s_waitcnt vmcnt(0)" ::: "memory")
constexpr int AUX_SC1 = 16;
DI u32x4 ld_sc1(__amdgpu_buffer_rsrc_t rs, unsigned off) { return __builtin_amdgcn_raw_buffer_load_b128(rs, (int)off, 0, AUX_SC1); }
DI u32x4 ld_sc1s(__amdgpu_buffer_rsrc_t rs, unsigned off, int soff) { return __builtin_amdgcn_raw_buffer_load_b128(rs, (int)off, soff, AUX_SC1); }
DI void st_sc1(u32x4 v, __amdgpu_buffer_rsrc_t rs, unsigned off) { __builtin_amdgcn_raw_buffer_store_b128(v, rs, (int)off, 0, AUX_SC1); }
DI unsigned lds_add_rtn(lds_ptr p, int lane) {
    unsigned old = 0;
    CFENCE();
    if (lane == 0) old = __hip_atomic_fetch_add((LAS unsigned*)p, 1u, __ATOMIC_RELAXED, __HIP_MEMORY_SCOPE_WORKGROUP);
    old = __builtin_amdgcn_readfirstlane(old);
    CFENCE();
    return old;
}
DI void lds_add(lds_ptr p, int lane) {
    CFENCE();
    if (lane == 0) (void)__hip_atomic_fetch_add((LAS unsigned*)p, 1u, __ATOMIC_RELAXED, __HIP_MEMORY_SCOPE_WORKGROUP);
    CFENCE();
}
DI void lds_wait_ge(lds_ptr p, unsigned v) {
    CFENCE();
    while ((unsigned)__builtin_amdgcn_readfirstlane(*(volatile LAS unsigned*)p) < v) __builtin_amdgcn_s_sleep(1);
    CFENCE();
}
DI void poll_ge(gu32* word, unsigned want) {
    CFENCE();
    for (unsigned spins = 0; spins < (1u << 22); ++spins) {
        if ((unsigned)__builtin_amdgcn_readfirstlane(__hip_atomic_load(word, RLX_AGENT)) >= want) break;
        __builtin_amdgcn_s_sleep(1);
    }
    __builtin_amdgcn_fence(__ATOMIC_ACQUIRE, "wavefront");
    CFENCE();
}
#define PRIO(x) do { __builtin_amdgcn_s_setprio(x); SBAR(); } while (0)
DI u32x4 gld128(unsigned voff, const void* sbase) {
    u32x4 r;
    asm volatile("global_load_dwordx4 %0, %1, %2" : "=v"(r) : "v"(voff), "s"(sbase) : "memory");
    return r;
}

constexpr int K1_STAGE = 40960;
__global__ __launch_bounds__(512, 2) void k1_qkv(const char* __restrict__ X, const char* __restrict__ W,
                                                 char* __restrict__ ws) {
    __shared__ __attribute__((aligned(16))) char lds_raw[4 * K1_STAGE];
    const lds_ptr lds = (lds_ptr)lds_raw;
    const unsigned lds0 = (unsigned)(uintptr_t)lds;
    const int tid = threadIdx.x, lane = tid & 63;
    const int w = __builtin_amdgcn_readfirstlane(tid >> 6);
    const int b = blockIdx.x, xcd = b & 7, idx = b >> 3;
    const int hp = (xcd >> 1) * 4 + (idx & 3), tb = (xcd & 1) * 8 + (idx >> 2);
    DG_DECL(0)
    const unsigned long long xr0 = __builtin_amdgcn_s_memrealtime(); const unsigned long long xc0 = __builtin_readcyclecounter();

    if (w >= 4) {
        const int lw = w - 4;
        const int rot = (tb & 3) * 4;
        unsigned voffX[4], voffW[2];
#pragma unroll
        for (int pp = 0; pp < 4; ++pp) {
            const int row = 32 * lw + 8 * pp + (lane >> 3);
            const int ch = (lane & 7) ^ ((row >> 1) & 7);
            voffX[pp] = (unsigned)(tb * 128 + row) * 2048u + ch * 16;
        }
#pragma unroll
        for (int pp = 0; pp < 2; ++pp) {
            const int krow = 16 * lw + 8 * pp + (lane >> 3);
            const int ch = (lane & 7) ^ (((lane >> 4) & 1) << 2);
            voffW[pp] = (unsigned)krow * 6144u + hp * 128 + ch * 16;
        }
        auto issue = [&](int kt) {
            const int kr = (kt + rot) & 15;
            const char* xb = X + kr * 128;
            const char* wb = W + (size_t)kr * (64 * 6144);
            const unsigned st = lds0 + (kt & 3) * K1_STAGE;
#pragma unroll
            for (int pp = 0; pp < 4; ++pp) glds16(voffX[pp], xb, st + (4 * lw + pp) * 1024);
#pragma unroll
            for (int sec = 0; sec < 3; ++sec)
#pragma unroll
                for (int pp = 0; pp < 2; ++pp)
                    glds16(voffW[pp], wb + sec * 2048, st + 16384 + sec * 8192 + (2 * lw + pp) * 1024);
        };
        issue(0); issue(1); issue(2);
#pragma unroll 1
        for (int kt = 0; kt < 13; ++kt) {
            WAIT_BAR(20);
            issue(kt + 3);
        }
        WAIT_BAR(20);
        WAIT_BAR(10);
        WAIT_BAR(0);
        if (lw == 0 && lane < 3) *(unsigned*)(ws + WS_CNT + (lane * 256 + b) * 256) = 0u;
        DG_END(5);
        return;
    }

    const int wm = w >> 1, wn = w & 1;
    const int r = lane & 31, hh = lane >> 5;
    const int g = lane >> 4, q = (lane & 15) >> 2, p = lane & 3;
    const int xoff0 = (64 * wm + r) * 128 + ((hh ^ ((r >> 1) & 7)) << 4);
    const int woff0 = 16384 + (8 * hh + q) * 128 + (((4 * wn + 2 * (g & 1) + (p >> 1)) ^ ((q >> 1) << 2)) << 4) + (p & 1) * 8;

    f32x16 acc[2][3];
#pragma unroll
    for (int m = 0; m < 2; ++m)
#pragma unroll
        for (int s = 0; s < 3; ++s) acc[m][s] = f32x16{};
    bf16x8 xf[2][2];
    s16x4 wl[2][3], wh[2][3];
#define K1_WF(f, sec) cat8(wl[f][sec], wh[f][sec])
#define K1_STEP(F, st, t, BAR_HERE) do { \
        const lds_ptr xp_ = (st) + (xoff0 ^ ((t) << 5)); const lds_ptr wp_ = (st) + woff0 + (t) * 2048; \
        acc[0][0] = MFMA32(K1_WF(F, 0), xf[F][0], acc[0][0]); SBAR(); \
        if (BAR_HERE) { LGKM_BAR(); SBAR(); } \
        xf[F ^ 1][0] = lds_rd128(xp_); wl[F ^ 1][0] = lds_tr(wp_); wh[F ^ 1][0] = lds_tr(wp_ + 512); SBAR(); \
        acc[0][1] = MFMA32(K1_WF(F, 1), xf[F][0], acc[0][1]); SBAR(); \
        wl[F ^ 1][1] = lds_tr(wp_ + 8192); wh[F ^ 1][1] = lds_tr(wp_ + 8192 + 512); SBAR(); \
        acc[0][2] = MFMA32(xf[F][0], K1_WF(F, 2), acc[0][2]); SBAR(); \
        wl[F ^ 1][2] = lds_tr(wp_ + 16384); wh[F ^ 1][2] = lds_tr(wp_ + 16384 + 512); xf[F ^ 1][1] = lds_rd128(xp_ + 4096); SBAR(); \
        acc[1][0] = MFMA32(K1_WF(F, 0), xf[F][1], acc[1][0]); SBAR(); \
        acc[1][1] = MFMA32(K1_WF(F, 1), xf[F][1], acc[1][1]); \
        acc[1][2] = MFMA32(xf[F][1], K1_WF(F, 2), acc[1][2]); SBAR(); \
    } while (0)

    LGKM_BAR();
    DG(3);
    {
        const lds_ptr xp_ = lds + xoff0; const lds_ptr wp_ = lds + woff0;
        xf[0][0] = lds_rd128(xp_); xf[0][1] = lds_rd128(xp_ + 4096);
#pragma unroll
        for (int sec = 0; sec < 3; ++sec) { wl[0][sec] = lds_tr(wp_ + sec * 8192); wh[0][sec] = lds_tr(wp_ + sec * 8192 + 512); }
    }
    SBAR();
#pragma unroll
    for (int kt = 0; kt < 15; ++kt) {
        const lds_ptr st = lds + (kt & 3) * K1_STAGE;
        const lds_ptr sn = lds + ((kt + 1) & 3) * K1_STAGE;
        K1_STEP(0, st, 1, false);
        K1_STEP(1, st, 2, false);
        K1_STEP(0, st, 3, false);
        K1_STEP(1, sn, 0, true);
    }
    {
        const lds_ptr st = lds + 3 * K1_STAGE;
        K1_STEP(0, st, 1, false);
        K1_STEP(1, st, 2, false);
        K1_STEP(0, st, 3, false);
#pragma unroll
        for (int m = 0; m < 2; ++m) {
            acc[m][1] = MFMA32(K1_WF(1, 1), xf[1][m], acc[m][1]);
            acc[m][2] = MFMA32(xf[1][m], K1_WF(1, 2), acc[m][2]);
            acc[m][0] = MFMA32(K1_WF(1, 0), xf[1][m], acc[m][0]);
        }
    }
    DG(4);
    {
        const int h = 2 * hp + wn;
        const float qscale = 0.17677669529663687f * 1.4426950408889634f;
        const __amdgpu_buffer_rsrc_t rs = __builtin_amdgcn_make_buffer_rsrc(ws, 0, 32 << 20, 0x00020000);
#pragma unroll
        for (int m = 0; m < 2; ++m) {
            const int T = tb * 4 + wm * 2 + m;
            const unsigned dq = (unsigned)WS_Q + (h * 64 + T) * 2048 + lane * 16;
            const unsigned dk = (unsigned)WS_KV + (h * 64 + T) * 4096 + lane * 16;
            st_sc1(pack8n(acc[m][1], 0), rs, dk);
            st_sc1(pack8n(acc[m][1], 1), rs, dk + 1024);
            st_sc1(pack8n(acc[m][2], 0), rs, dk + 2048);
            st_sc1(pack8n(acc[m][2], 1), rs, dk + 3072);
            st_sc1(pack8(acc[m][0], 0, qscale), rs, dq);
            st_sc1(pack8(acc[m][0], 1, qscale), rs, dq + 1024);
        }
    }
    if (b == 0 && w == 0) { DRAIN(); const unsigned long long xr1 = __builtin_amdgcn_s_memrealtime(); const unsigned long long xc1 = __builtin_readcyclecounter();
        if (lane == 0) { unsigned long long* p = (unsigned long long*)(ws + (30u << 20)); p[0] = xr0; p[1] = xr1; p[2] = xc1 - xc0; } }
    DG_END(5);
}

constexpr int K2_SLOT = 32768;
constexpr int K2_SF = 65536;
constexpr int K2_CV = 98304;
constexpr int CV_HA = 0, CV_HB = 4160, CV_KV = 8320, CV_BYTES = 8320 + 480 + 4096;
constexpr int K2_LX = K2_CV + 4 * CV_BYTES;
constexpr int K2_FL = K2_LX + 2048;
constexpr int K2_SCR = K2_FL + 256;
constexpr int K2_LDS = K2_SCR + 4096;
constexpr int F_X = 0, F_R = 32, F_W2 = 64, C_OST = 68, F_GO = 72, C_S1 = 76, C_S2 = 80, C_S3 = 128, C_ZST = 88, F_ZGO = 92,
              C_KST = 96, C_U0 = 100, C_LD = 108, F_KGO = 116, C_OL = 120;
#define NG_IN 0
#define GATE_WORD C_LD
#define GATE_VAL 4u
#define X_NZ 4
#define X_NW3 8
#define ZP_GATE 4u
#define ZL_GATE 8u
#define OWIN 8
#define KROT 2
#define NG_LAST 0
#define PACE_IN "s_nop 7"
#define PACE_OUT "s_nop 1"
#define P3_PRIO 1
#define AUX_PRIO 0

__global__ __launch_bounds__(768, 3) void k2_attn(char* __restrict__ ws, const char* __restrict__ Aq,
                                                  const char* __restrict__ Ak, const char* __restrict__ Av,
                                                  const char* __restrict__ shiftk, const char* __restrict__ ssmk,
                                                  const unsigned* __restrict__ ssmD, const char* __restrict__ Wo,
                                                  char* __restrict__ out) {
    __shared__ __attribute__((aligned(16))) char lds_raw[K2_LDS];
    const lds_ptr lds = (lds_ptr)lds_raw;
    const lds_ptr fl = lds + K2_FL;
    const unsigned lds0 = (unsigned)(uintptr_t)lds;
    const int tid = threadIdx.x, lane = tid & 63;
    const int w = __builtin_amdgcn_readfirstlane(tid >> 6);
    const int b = blockIdx.x, xcd = b & 7, idx = b >> 3;
    const int h = xcd * 4 + (idx >> 3), qb = idx & 7;
    const int tt = xcd * 2 + (idx >> 4), nt = idx & 15;
    const char* kvg = ws + WS_KV + (size_t)h * 64 * 4096;
    const __amdgpu_buffer_rsrc_t rs = __builtin_amdgcn_make_buffer_rsrc(ws, 0, 32 << 20, 0x00020000);
    gu32* const cnt = (gu32*)(ws + WS_CNT);
    auto signal = [&](int ty, int g) {
        if (lane < 32) (void)__hip_atomic_fetch_add(cnt + (ty * 256 + lane * 8 + g) * 64, 1u, RLX_AGENT);
    };
    const unsigned wvoff = (unsigned)(lane >> 3) * 2048u + nt * 128 + (((lane & 7) ^ (((lane >> 4) & 1) << 2)) << 4);
    auto wsrc = [&](int hf, int lp) { return Wo + (size_t)(hf * 64 + ((lp + nt * KROT * 2) & 63)) * 16384; };
    DG_DECL(1)
    const unsigned long long xr0 = __builtin_amdgcn_s_memrealtime(); const unsigned long long xc0 = __builtin_readcyclecounter();

    if (w >= 8) {
        const int cw = w - 8;
        const unsigned voff = lane * 16;
        __builtin_amdgcn_s_setprio(3);
        auto issue = [&](int st) {
            const char* src = kvg + (st * 4 + cw) * 4096;
            const unsigned dst = lds0 + (st & 1) * K2_SLOT + cw * 4096;
#pragma unroll
            for (int i = 0; i < 4; ++i) glds16(voff, src + i * 1024, dst + i * 1024);
#pragma unroll
            for (int i = 0; i < 4; ++i) glds16(voff, src + 131072 + i * 1024, dst + 16384 + i * 1024);
        };
        issue(0);
        const int ch = xcd * 128 + idx * 4 + cw;
        const lds_ptr cv = lds + K2_CV + cw * CV_BYTES;
        {
            const u32x4 z4 = {0u, 0u, 0u, 0u};
            if (lane < 2) *(LAS u32x4*)(cv + CV_HA + lane * 16) = z4;
            if (lane < 30) *(LAS u32x4*)(cv + CV_KV + lane * 16) = z4;
            if (cw == 0 && lane < 16) *(LAS u32x4*)(fl + lane * 16) = z4;
        }
        WAIT_BAR(0);
        issue(1);
        const int c0 = xcd * 128 + cw * 32, cq = lane & 7, rg = lane >> 3;
        const int row0 = idx * 64 + rg * 8;
        const unsigned roff = (unsigned)row0 * 2048u + (c0 + cq * 4) * 2;
        const unsigned toffs = (unsigned)(c0 + (lane >> 3)) * 4096u + idx * 128;
        u32x4 sk[2];
        sk[0] = gld128((unsigned)(c0 + cq * 4) * 8u, shiftk); sk[1] = gld128((unsigned)(c0 + cq * 4) * 8u, shiftk + 16);
        unsigned dd = gld32((unsigned)(ch >> 1) * 4u, ssmD);
        auto stage = [&](const char* src, int dst) {
            const unsigned sbase = (unsigned)(idx * 64 + cw * 16 + (lane >> 4)) * 2048u + xcd * 256;
#pragma unroll
            for (int i = 0; i < 4; ++i)
                glds16(sbase + (((lane & 15) ^ (((2 * cw + (i >> 1)) & 3) << 2)) << 4), src + i * 8192, lds0 + dst + cw * 4096 + i * 1024);
        };
        WAIT_BAR(3);
        DG(12);
        issue(2);
        stage(Ak, K2_SF);
        glds16((unsigned)(idx * 64 - (idx ? 4 : 0) + (lane >> 4)) * 2048u + xcd * 256 + (((lane & 15) ^ 12) << 4), Ak, lds0 + K2_SCR + cw * 1024);
        WAIT_BAR(5);
        DG(9);
        issue(3);
        stage(Av, K2_SF + 16384);
        WAIT_BAR(4);
        DG(3);
        issue(4);
#pragma unroll
        for (int i = 0; i < 4; ++i) glds16(voff, ssmk + (size_t)ch * 4096 + i * 1024, lds0 + K2_CV + cw * CV_BYTES + CV_HA + 32 + i * 1024);
        WAIT_BAR(4);
        DG(5);
        issue(5);
        SBAR();
        asm volatile("" : "+v"(sk[0]), "+v"(sk[1]), "+v"(dd));
        dd = __builtin_amdgcn_readfirstlane(dd);
        {
            u32x2 kr[11], vr[8];
            const lds_ptr kb = lds + K2_SF + rg * 2048 + ((cw ^ (rg & 3)) << 6) + cq * 8;
            const lds_ptr hb = lds + (rg ? K2_SF + rg * 2048 - 768 : K2_SCR + cw * 1024 + 256) + ((cw ^ ((rg + 3) & 3)) << 6) + cq * 8;
#pragma unroll
            for (int i = 0; i < 3; ++i) kr[i] = *(const LAS u32x2*)(hb + i * 256);
#pragma unroll
            for (int i = 0; i < 8; ++i) kr[3 + i] = *(const LAS u32x2*)(kb + i * 256);
#pragma unroll
            for (int i = 0; i < 8; ++i) vr[i] = *(const LAS u32x2*)(kb + 16384 + i * 256);
            if (row0 == 0) { kr[0] = u32x2{0u, 0u}; kr[1] = u32x2{0u, 0u}; kr[2] = u32x2{0u, 0u}; }
            const lds_ptr tb = cv + CV_KV + 480;
#pragma unroll
            for (int c = 0; c < 4; ++c) {
                const unsigned s01 = sk[c >> 1][(c & 1) * 2], s23 = sk[c >> 1][(c & 1) * 2 + 1];
                const float w0 = bflo(s01), w1 = bfhi(s01), w2 = bflo(s23), w3 = bfhi(s23);
                float kf[11], kv[8];
#pragma unroll
                for (int i = 0; i < 11; ++i) kf[i] = (c & 1) ? bfhi(kr[i][c >> 1]) : bflo(kr[i][c >> 1]);
#pragma unroll
                for (int i = 0; i < 8; ++i) {
                    float a = mul_vv(w3, kf[i]);
                    fmac_vv(a, w2, kf[i + 1]); fmac_vv(a, w1, kf[i + 2]); fmac_vv(a, w0, kf[i + 3]);
                    kv[i] = mul_vv(a, (c & 1) ? bfhi(vr[i][c >> 1]) : bflo(vr[i][c >> 1]));
                }
                u32x4 o4;
#pragma unroll
                for (int i = 0; i < 4; ++i) o4[i] = cvtpk(kv[7 - 2 * i], kv[6 - 2 * i]);
                *(LAS u32x4*)(tb + (cq * 4 + c) * 128 + ((rg ^ cq) << 4)) = o4;
            }
#pragma unroll
            for (int i = 0; i < 4; ++i) {
                const u32x4 t = *(const LAS u32x4*)(tb + i * 1024 + lane * 16);
                st_sc1(t, rs, (unsigned)WS_KVR + toffs + i * 32768 + (((lane & 7) ^ (2 * i + (lane >> 5))) << 4));
            }
        }
        CFENCE();
        DG(7);
        WAIT_BAR(4);
        DG(6);
        issue(6);
        asm volatile("s_waitcnt vmcnt(8)" ::: "memory");
        if (lds_add_rtn(fl + C_KST, lane) == 3u) signal(1, xcd);
#pragma unroll
        for (int i = 0; i < 8; ++i)
            glds16(wvoff, wsrc(i >> 2, cw * 4 + (i & 3)), lds0 + K2_SF + (i >> 2) * 16384 + (cw * 4 + (i & 3)) * 1024);
        {
            const u32x4 z4 = {0u, 0u, 0u, 0u};
            if (lane < 2) *(LAS u32x4*)(cv + CV_HB + lane * 16) = z4;
#pragma unroll
            for (int i = 0; i < 4; ++i) {
                const int off = 32 + i * 1024 + lane * 16;
                const u32x4 hv = *(const LAS u32x4*)(cv + CV_HA + off);
                const unsigned pv = *(const LAS unsigned*)(cv + CV_HA + off - 4);
                u32x4 sh;
                sh[0] = __builtin_amdgcn_alignbit(hv[0], pv, 16);
                sh[1] = __builtin_amdgcn_alignbit(hv[1], hv[0], 16);
                sh[2] = __builtin_amdgcn_alignbit(hv[2], hv[1], 16);
                sh[3] = __builtin_amdgcn_alignbit(hv[3], hv[2], 16);
                *(LAS u32x4*)(cv + CV_HB + off) = sh;
            }
        }
        WAIT_BAR(8);
        DG(8);
        issue(7);
#pragma unroll
        for (int i = 0; i < 8; ++i)
            glds16(wvoff, wsrc(idx >> 4, 32 + cw * 8 + i), lds0 + K2_SCR + cw * 1024);
        WAIT_BAR(8);
        DG(11);
        if (cw == 0) { poll_ge(cnt + (256 + b) * 64, 32u); lds_add(fl + F_KGO, lane); }
        else lds_wait_ge(fl + F_KGO, 1u);
        {
            u32x4 t[4];
#pragma unroll
            for (int i = 0; i < 4; ++i) t[i] = ld_sc1(rs, (unsigned)WS_KVR + ch * 4096 + i * 1024 + voff);
#pragma unroll
            for (int i = 0; i < 4; ++i) *(LAS u32x4*)(cv + CV_KV + 480 + i * 1024 + lane * 16) = t[i];
        }
        const int r1 = lane & 15, q = lane >> 4;
        const int toff = (r1 & 1) ? CV_HA + 2 * (16 + r1 + 9 - 8 * q) : CV_HB + 2 * (16 + r1 + 10 - 8 * q);
        const int boff = CV_KV + 448 + 32 * r1 + 16 * q;
        f32x4 y[8];
#pragma unroll
        for (int t = 0; t < 8; ++t) y[t] = f32x4{};
        bf16x8 tf[8], bf[8];
        auto ldt = [&](int bb, int a) {
            const LAS unsigned* tp = (const LAS unsigned*)(cv + toff + 64 * bb + 512 * a);
            u32x4 t4 = {tp[0], tp[1], tp[2], tp[3]};
            tf[a] = __builtin_bit_cast(bf16x8, t4);
        };
        auto ldb = [&](int bb, int a) { bf[a] = lds_rd128(cv + boff - 64 * bb + 512 * a); };
#define GROUP(bb, PACE) do { \
            _Pragma("unroll") for (int a = 0; a < 8; ++a) { \
                _Pragma("unroll") for (int ww = 7 - a; ww >= 0; --ww) { \
                    y[a + ww] = MFMA16(tf[a], bf[ww], y[a + ww]); SBAR(); \
                    asm volatile(PACE); SBAR(); \
                } \
                ldt(((bb) + 1) & 7, a); ldb(((bb) + 1) & 7, 7 - a); SBAR(); \
            } } while (0)
#pragma unroll
        for (int a = 0; a < 8; ++a) { ldt(0, a); ldb(0, 7 - a); }
        SBAR();
        u32x2 qr[8];
#pragma unroll
        for (int i = 0; i < 8; ++i)
            glds16(wvoff, wsrc(i >> 2, 16 + cw * 4 + (i & 3)), lds0 + (i >> 2) * 16384 + (cw * 4 + (i & 3)) * 1024);
#pragma unroll
        for (int i = 0; i < 8; ++i) qr[i] = gld64(roff, Aq + i * 2048);
#pragma unroll 1
        for (int bb = NG_IN; bb < NG_IN + NG_LAST; ++bb) GROUP(bb, PACE_IN);
        asm volatile("s_waitcnt vmcnt(8)" ::: "memory");
        lds_add(fl + C_S1, lane);
        CFENCE();
        while ((unsigned)__builtin_amdgcn_readfirstlane(*(volatile LAS unsigned*)(fl + GATE_WORD)) < GATE_VAL) __builtin_amdgcn_s_sleep(2);
        CFENCE();
        __builtin_amdgcn_s_setprio(AUX_PRIO);
#pragma unroll 1
        for (int bb = NG_IN + NG_LAST; bb < 8; ++bb) GROUP(bb, PACE_OUT);
        DG(4);
        __builtin_amdgcn_s_setprio(3);
        {
            const float dch = (ch & 1) ? bfhi(dd) : bflo(dd);
#pragma unroll
            for (int t = 0; t < 8; ++t) {
                const u32x2 kk = *(const LAS u32x2*)(cv + CV_KV + 480 + 512 * t + 32 * r1 + 16 * (q >> 1) + ((q & 1) ? 0 : 8));
                float k0 = mul_sv(dch, bfhi(kk[1])), k1 = mul_sv(dch, bflo(kk[1]));
                float k2 = mul_sv(dch, bfhi(kk[0])), k3 = mul_sv(dch, bflo(kk[0]));
                rnd2(k0, k1); rnd2(k2, k3);
                float y0 = y[t][0], y1 = y[t][1], y2 = y[t][2], y3 = y[t][3];
                rnd2(y0, y1); rnd2(y2, y3);
                u32x2 o = {cvtpk(y0 + k0, y1 + k1), cvtpk(y2 + k2, y3 + k3)};
                *(LAS u32x2*)(cv + CV_HA + 512 * t + 32 * r1 + 8 * q) = o;
            }
#pragma unroll
            for (int i = 0; i < X_NZ; ++i)
                st_sc1(*(const LAS u32x4*)(cv + CV_HA + i * 1024 + lane * 16), rs, (unsigned)WS_ZT + ch * 4096 + i * 1024 + voff);
        }
        DG(10);
        DRAIN();
        if (lds_add_rtn(fl + C_ZST, lane) == 3u) {
            signal(2, xcd);
            lds_wait_ge(fl + C_OL, ZP_GATE);
            poll_ge(cnt + (512 + b) * 64, 32u);
            lds_add(fl + F_ZGO, lane);
        } else lds_wait_ge(fl + F_ZGO, 1u);
        lds_wait_ge(fl + C_OL, ZL_GATE);
        DG(13);
        {
            const lds_ptr tr = cv;
            u32x4 t[4];
#pragma unroll
            for (int i = 0; i < 4; ++i)
                t[i] = ld_sc1(rs, (unsigned)WS_ZT + toffs + i * 32768 + (((lane & 7) ^ (2 * i + (lane >> 5))) << 4));
#pragma unroll
            for (int i = 0; i < 4; ++i) *(LAS u32x4*)(tr + i * 1024 + lane * 16) = t[i];
#pragma unroll
            for (int i = 0; i < 8; ++i) asm volatile("" : "+v"(qr[i]));
            u32x4 zc[4];
#pragma unroll
            for (int c = 0; c < 4; ++c) zc[c] = *(const LAS u32x4*)(tr + (cq * 4 + c) * 128 + ((rg ^ cq) << 4));
            char* dst = out + (size_t)row0 * 4096 + (c0 + cq * 4) * 2;
#pragma unroll
            for (int i = 0; i < 8; ++i) {
                float zz[4];
#pragma unroll
                for (int c = 0; c < 4; ++c) zz[c] = (i & 1) ? bfhi(zc[c][i >> 1]) : bflo(zc[c][i >> 1]);
                u32x2 o = {cvtpk(bflo(qr[i][0]) * zz[0], bfhi(qr[i][0]) * zz[1]),
                           cvtpk(bflo(qr[i][1]) * zz[2], bfhi(qr[i][1]) * zz[3])};
                *(u32x2*)(dst + i * 4096) = o;
            }
        }
        DG_END(14);
        return;
    }

    const int half = w >> 2;
    const int TA = qb * 8 + (w & 3) * 2 + half, TB = TA ^ 1;
    const char* qga = ws + WS_Q + (size_t)(h * 64 + TA) * 2048 + lane * 16;
    const char* qgb = ws + WS_Q + (size_t)(h * 64 + TB) * 2048 + lane * 16;
    const bf16x8 qa0 = *(const bf16x8*)qga, qa1 = *(const bf16x8*)(qga + 1024);
    const bf16x8 qb0 = *(const bf16x8*)qgb, qb1 = *(const bf16x8*)(qgb + 1024);
    bf16x8 sel;
    {
        const short one = (((lane >> 2) & 1) == ((lane >> 4) & 1)) ? (short)0x3f80 : (short)0;
#pragma unroll
        for (int i = 0; i < 8; ++i) sel[i] = one;
    }
    f32x16 oA = f32x16{}, oB = f32x16{};
    f32x4 lA = f32x4{}, lB = f32x4{};
    f32x16 negm, sA, sB;
    const lds_ptr lp = lds + half * 16384 + lane * 16;
    bf16x8 k0[2], k1[2], v0[2], v1[2];
    u32x4 p0, p1 = {0u, 0u, 0u, 0u};
#define EX(X, i) X[i] = __builtin_amdgcn_exp2f(X[i])
#define PB(x) __builtin_bit_cast(bf16x8, x)
    LGKM_BAR();
    DG(3);
    {
        const bf16x8 x0 = lds_rd128(lds + lane * 16), x1 = lds_rd128(lds + lane * 16 + 1024);
        k0[0] = lds_rd128(lp); k1[0] = lds_rd128(lp + 1024);
        v0[0] = lds_rd128(lp + 2048); v1[0] = lds_rd128(lp + 3072);
        sA = MFMA32(x0, qa0, f32x16{});
        sB = MFMA32(x0, qb0, f32x16{});
        sA = MFMA32(x1, qa1, sA);
        sB = MFMA32(x1, qb1, sB);
        float m = __builtin_fmaxf(sA[0], sB[0]);
#pragma unroll
        for (int i = 1; i < 16; ++i) m = __builtin_fmaxf(m, __builtin_fmaxf(sA[i], sB[i]));
        auto rr = __builtin_amdgcn_permlane32_swap(__float_as_uint(m), __float_as_uint(m), false, false);
        m = __builtin_fmaxf(__uint_as_float(rr[0]), __uint_as_float(rr[1]));
#pragma unroll
        for (int i = 0; i < 16; ++i) negm[i] = -m;
        sA = MFMA32(k0[0], qa0, negm);
        sA = MFMA32(k1[0], qa1, sA);
        v1[1] = v1[0];
        EX(sA, 0); EX(sA, 1);
    }
#define SUM16(l, p) asm volatile("v_mfma_f32_16x16x32_bf16 %0, %1, %2, %0" : "+v"(l) : "v"(sel), "v"(p))
#define HALF(C, oC, lC, N, qx, qy, ka, kb, oP, lP, vprev, vcur0, PRE) do { \
        oP = MFMA32(vprev, PB(p1), oP); SBAR(); \
        PRE; EX(C, 2); EX(C, 3); EX(C, 4); SBAR(); \
        N = MFMA32(ka, qx, negm); SBAR(); \
        EX(C, 5); EX(C, 6); EX(C, 7); SBAR(); \
        SUM16(lP, p1); SBAR(); \
        p0[0] = cvtpk(C[0], C[1]); p0[1] = cvtpk(C[2], C[3]); SBAR(); \
        N = MFMA32(kb, qy, N); SBAR(); \
        p0[2] = cvtpk(C[4], C[5]); p0[3] = cvtpk(C[6], C[7]); SBAR(); EX(C, 8); EX(C, 9); SBAR(); \
        oC = MFMA32(vcur0, PB(p0), oC); SBAR(); \
        EX(C, 10); EX(C, 11); EX(C, 12); SBAR(); \
        SUM16(lC, p0); SBAR(); \
        EX(C, 13); EX(C, 14); EX(C, 15); SBAR(); \
        p1[0] = cvtpk(C[8], C[9]); p1[1] = cvtpk(C[10], C[11]); p1[2] = cvtpk(C[12], C[13]); p1[3] = cvtpk(C[14], C[15]); SBAR(); \
        EX(N, 0); EX(N, 1); SBAR(); \
    } while (0)
#define RD4(Y, nx) do { v1[Y] = lds_rd128((nx) + 3072); v0[Y] = lds_rd128((nx) + 2048); \
        k1[Y] = lds_rd128((nx) + 1024); k0[Y] = lds_rd128(nx); } while (0)
#define KT(X, nx, PRE0) do { \
        HALF(sA, oA, lA, sB, qb0, qb1, k0[X], k1[X], oB, lB, v1[X ^ 1], v0[X], PRE0; RD4(X ^ 1, nx)); \
        HALF(sB, oB, lB, sA, qa0, qa1, k0[X ^ 1], k1[X ^ 1], oA, lA, v1[X], v0[X], (void)0); \
    } while (0)
    SBAR();
#pragma unroll 1
    for (int st = 0; st < 8; ++st) {
        const lds_ptr cur = lp + (st & 1) * K2_SLOT;
        const lds_ptr nxt = lp + ((st + 1) & 1) * K2_SLOT;
        PRIO(2);
        KT(0, cur + 4096, (void)0);
        PRIO(1);
        KT(1, cur + 8192, (void)0);
        PRIO(0);
        KT(0, cur + 12288, (void)0);
        PRIO(3);
        KT(1, nxt, if (st < 7) RAW_BAR(); SBAR());
    }
    oB = MFMA32(v1[1], PB(p1), oB);
    SBAR();
    SUM16(lB, p1);
    asm volatile("s_nop 7\n\ts_nop 7" ::: "memory");
    SBAR();
    DG(4);
    {
        lds_add(fl + C_LD + half * 4, lane);
        const lds_ptr xo = lds + K2_SLOT + w * 4096 + lane * 16;
        *(LAS float*)(lds + K2_LX + w * 256 + lane * 4) = lB[0];
        lds_wait_ge(fl + C_LD + half * 4, 4u);
#pragma unroll
        for (int i = 0; i < 4; ++i) *(LAS f32x4*)(xo + i * 1024) = f32x4{oB[4 * i], oB[4 * i + 1], oB[4 * i + 2], oB[4 * i + 3]};
        lds_add(fl + F_X + w * 4, lane);
        lds_wait_ge(fl + F_X + (w ^ 4) * 4, 1u);
        const lds_ptr xi = lds + K2_SLOT + (w ^ 4) * 4096 + lane * 16;
#pragma unroll
        for (int i = 0; i < 4; ++i) {
            const f32x4 t = *(const LAS f32x4*)(xi + i * 1024);
#pragma unroll
            for (int j = 0; j < 4; ++j) oA[4 * i + j] = add_vv(oA[4 * i + j], t[j]);
        }
        lA[0] += *(const LAS float*)(lds + K2_LX + (w ^ 4) * 256 + lane * 4);
    }
    DG(9);
    float l = lA[0];
    {
        float t0 = 0.f, t1 = 0.f;
#pragma unroll
        for (int i = 0; i < 8; ++i) { fmac_sv(t0, 0.f, oA[i]); fmac_sv(t1, 0.f, oA[8 + i]); }
        const bool ok = (l <= 1.0e30f) && (l >= 1.0e-18f) && (t0 + t1 == 0.f);
        if (__builtin_expect(__any(!ok), 0)) {
            float m = -INFINITY, ls = 0.f;
            oA = f32x16{};
#pragma unroll 1
            for (int t = 0; t < 64; ++t) {
                const char* g = kvg + t * 4096 + lane * 16;
                const bf16x8 a0 = *(const bf16x8*)g, a1 = *(const bf16x8*)(g + 1024);
                const bf16x8 b0 = *(const bf16x8*)(g + 2048), b1 = *(const bf16x8*)(g + 3072);
                f32x16 s = MFMA32(a0, qa0, f32x16{});
                s = MFMA32(a1, qa1, s);
                float tm = s[0];
#pragma unroll
                for (int i = 1; i < 16; ++i) tm = __builtin_fmaxf(tm, s[i]);
                auto rr = __builtin_amdgcn_permlane32_swap(__float_as_uint(tm), __float_as_uint(tm), false, false);
                tm = __builtin_fmaxf(__uint_as_float(rr[0]), __uint_as_float(rr[1]));
                const float mn = __builtin_fmaxf(m, tm);
                const float alpha = __builtin_amdgcn_exp2f(m - mn);
                m = mn;
                float ps = 0.f;
#pragma unroll
                for (int i = 0; i < 16; ++i) { s[i] = __builtin_amdgcn_exp2f(s[i] - mn); ps += s[i]; oA[i] *= alpha; }
                ls = ls * alpha + ps;
                oA = MFMA32(b0, PB(pack8n(s, 0)), oA);
                oA = MFMA32(b1, PB(pack8n(s, 1)), oA);
            }
            auto rr = __builtin_amdgcn_permlane32_swap(__float_as_uint(ls), __float_as_uint(ls), false, false);
            l = __uint_as_float(rr[0]) + __uint_as_float(rr[1]);
        }
    }
    u32x4 w3[4];
    {
        const float rl = __builtin_amdgcn_rcpf(l);
        const unsigned og = (unsigned)WS_O + (unsigned)(TA * 32 + h) * 2048u + lane * 16;
        st_sc1(pack8(oA, 0, rl), rs, og);
        st_sc1(pack8(oA, 1, rl), rs, og + 1024);
        CFENCE();
#pragma unroll
        for (int i = 0; i < 4; ++i)
            glds16(wvoff, wsrc(half ^ 1, 32 + (w & 3) * 4 + i), lds0 + K2_SLOT + ((w ^ 4) * 4 + i) * 1024);
#pragma unroll
        for (int i = 0; i < 4; ++i) w3[i] = gld128(wvoff, wsrc(half, 48 + (w & 3) * 4 + i));
        asm volatile("s_waitcnt vmcnt(8)" ::: "memory");
    }
    DG(5);
    const unsigned arr = lds_add_rtn(fl + C_OST, lane);
    const int j4 = w & 3;
    if (arr == 7u) {
        signal(0, qb);
        poll_ge(cnt + b * 64, 32u);
        lds_add(fl + F_GO, lane);
    } else {
        lds_wait_ge(fl + F_GO, 1u);
    }
    DG(6);
    {
        const int T3 = tt * 4 + j4;
        const unsigned og = (unsigned)WS_O + (unsigned)T3 * 65536u + half * 32768 + lane * 16;
        bf16x8 of[OWIN];
#define LDO(i) do { of[(i) % OWIN] = PB(ld_sc1s(rs, og, (((i) + nt * KROT) & 31) * 1024)); SBAR(); } while (0)
#pragma unroll
        for (int i = 0; i < OWIN; ++i) {
            if (i == OWIN / 2) PRIO(2);
            LDO(i);
        }
        asm volatile("s_waitcnt vmcnt(%0)" :: "n"(OWIN) : "memory");
        lds_add(fl + C_S2, lane);
        __builtin_amdgcn_s_setprio(P3_PRIO);
        SBAR();
        const int r = lane & 31, hh = lane >> 5;
        const int g = lane >> 4, q = (lane & 15) >> 2, p = lane & 3;
        int woff[2];
#pragma unroll
        for (int n = 0; n < 2; ++n) {
            const int c = 4 * (n ^ half) + 2 * (p & 1) + (g & 1);
            woff[n] = (4 * hh + q) * 128 + ((c ^ ((q >> 1) << 2)) << 4) + (p >> 1) * 8;
        }
        f32x16 acc[2] = {f32x16{}, f32x16{}};
        const lds_ptr wl0 = lds + half * 16384 + woff[0], wl1 = lds + half * 16384 + woff[1];
        s16x4 wa[2][2], wb[2][2];
#define LDW(s) do { const int o_ = ((s) < 8 ? K2_SF : (s) >= 24 ? K2_SF : (((s) >> 3) - 1) * K2_SLOT) + ((s) & 7) * 2048; \
            wa[(s) & 1][0] = lds_tr(wl0 + o_); wb[(s) & 1][0] = lds_tr(wl0 + o_ + 1024); \
            wa[(s) & 1][1] = lds_tr(wl1 + o_); wb[(s) & 1][1] = lds_tr(wl1 + o_ + 1024); SBAR(); } while (0)
        lds_wait_ge(fl + C_S1, 4u);
        lds_wait_ge(fl + C_S2, 8u);
        LDW(0);
#pragma unroll
        for (int s = 0; s < 32; ++s) {
            if (s == 16) {
                lds_wait_ge(fl + C_U0 + half * 4, 4u);
#pragma unroll
                for (int i = 0; i < 4; ++i) {
                    asm volatile("" : "+v"(w3[i]));
                    *(LAS u32x4*)(lds + K2_SF + (w * 4 + i) * 1024 + lane * 16) = w3[i];
                }
                lds_add(fl + C_S3 + half * 4, lane);
            }
            if (s == 23) lds_wait_ge(fl + C_S3 + half * 4, 4u);
            if (s < 31) LDW(s + 1);
            if (s == 7) lds_add(fl + C_U0 + half * 4, lane);
            SBAR();
#pragma unroll
            for (int n = 0; n < 2; ++n) acc[n] = MFMA32(cat8(wa[s & 1][n], wb[s & 1][n]), of[s % OWIN], acc[n]);
            SBAR();
            if (s + OWIN < 32) LDO(s + OWIN);
            if (s + OWIN == 31) lds_add(fl + C_OL, lane);
            if (s == 0) DG(10);
            if (s == 15) DG(11);
        }
        DG(7);
        const lds_ptr xo = lds + w * 4096 + lane * 16;
#pragma unroll
        for (int i = 0; i < 4; ++i) *(LAS f32x4*)(xo + i * 1024) = f32x4{acc[1][4 * i], acc[1][4 * i + 1], acc[1][4 * i + 2], acc[1][4 * i + 3]};
        lds_add(fl + F_R + w * 4, lane);
        lds_wait_ge(fl + F_R + (w ^ 4) * 4, 1u);
        const lds_ptr xi = lds + (w ^ 4) * 4096 + lane * 16;
#pragma unroll
        for (int i = 0; i < 4; ++i) {
            const f32x4 t = *(const LAS f32x4*)(xi + i * 1024);
#pragma unroll
            for (int j = 0; j < 4; ++j) acc[0][4 * i + j] += t[j];
        }
        char* dst = out + (size_t)(T3 * 32 + r) * 4096 + 2048 + nt * 128 + half * 64 + hh * 32;
        *(u32x4*)(dst) = pack8n(acc[0], 0);
        *(u32x4*)(dst + 16) = pack8n(acc[0], 1);
    }
    if (w == 0) { DRAIN(); if (lane == 0) *(unsigned long long*)(ws + (30u << 20) + 4096 + b * 8) = __builtin_amdgcn_s_memrealtime(); }
    if (b == 0 && w == 0) { DRAIN(); const unsigned long long xr1 = __builtin_amdgcn_s_memrealtime(); const unsigned long long xc1 = __builtin_readcyclecounter();
        const volatile unsigned long long* p = (const volatile unsigned long long*)(ws + (30u << 20));
        unsigned long long k1s = p[0], k1e = p[1], k1c = p[2];
        unsigned long long k0s = p[8]; unsigned long long Q = 0;
        if (Q > 20000) Q = 20000;
        const unsigned long long tend = xr1 + 3 * Q;
        while (__builtin_amdgcn_s_memrealtime() < tend) __builtin_amdgcn_s_sleep(1);
    }
    DG_END(8);
}

__global__ void k0_null(char* ws) {
    const unsigned long long t = __builtin_amdgcn_s_memrealtime();
    if (threadIdx.x == 0 && blockIdx.x == 0) *(unsigned long long*)(ws + (30u << 20) + 64) = t;
}
__global__ void k3_tail(char* ws) {
    const unsigned long long t = __builtin_amdgcn_s_memrealtime();
    const unsigned long long* p = (const unsigned long long*)(ws + (30u << 20) + 4096);
    unsigned long long m = 0;
    for (int i = 0; i < 256; ++i) m = p[i] > m ? p[i] : m;
    unsigned long long Q = 0;
    if (Q > 20000) Q = 20000;
    const unsigned long long tend = t + 3 * Q;
    while (__builtin_amdgcn_s_memrealtime() < tend) __builtin_amdgcn_s_sleep(1);
}
extern "C" void kernel_launch(void* const* d_in, const int* in_sizes, int n_in,
                              void* d_out, int out_size, void* d_ws, size_t ws_size,
                              hipStream_t stream) {
    const void* A_q = d_in[0];
    const void* A_k = d_in[1];
    const void* A_v = d_in[2];
    const void* A_shift = d_in[3];
    const void* A_ssmk = d_in[4];
    const void* A_D = d_in[5];
    const void* B_x = d_in[6];
    const void* B_wqkv = d_in[7];
    const void* B_wo = d_in[8];
    char* ws = (char*)d_ws;
    k1_qkv<<<256, 512, 0, stream>>>((const char*)B_x, (const char*)B_wqkv, ws);
    k2_attn<<<256, 768, 0, stream>>>(ws, (const char*)A_q, (const char*)A_k, (const char*)A_v, (const char*)A_shift,
                                     (const char*)A_ssmk, (const unsigned*)A_D, (const char*)B_wo, (char*)d_out);
}
```

```cpp
#include <hip/hip_runtime.h>
#include <hip/hip_bf16.h>
#include <stdint.h>

typedef short bf16x8 __attribute__((ext_vector_type(8)));
typedef short s16x4 __attribute__((ext_vector_type(4)));
typedef float f32x16 __attribute__((ext_vector_type(16)));
typedef float f32x4 __attribute__((ext_vector_type(4)));
typedef float f32x2 __attribute__((ext_vector_type(2)));
typedef unsigned u32x4 __attribute__((ext_vector_type(4)));
typedef unsigned u32x2 __attribute__((ext_vector_type(2)));
typedef __bf16 bf16x2_t __attribute__((ext_vector_type(2)));

#define LAS __attribute__((address_space(3)))
typedef LAS char* lds_ptr;
#define DI __device__ __forceinline__
#define SBAR() __builtin_amdgcn_sched_barrier(0)
#define MFMA32(a, b, c) __builtin_amdgcn_mfma_f32_32x32x16_bf16((a), (b), (c), 0, 0, 0)
#define MFMA16(a, b, c) __builtin_amdgcn_mfma_f32_16x16x32_bf16((a), (b), (c), 0, 0, 0)
#define WAIT_BAR(N) asm volatile("s_waitcnt vmcnt(" #N ") lgkmcnt(0)\n\ts_barrier" ::: "memory")
#define LGKM_BAR() asm volatile("s_waitcnt lgkmcnt(0)\n\ts_barrier" ::: "memory")

#define DG_DECL(K)
#define DG(i)
#define DG_END(i)
#define DGC(i)

constexpr int S_LEN = 2048, DM = 1024, NH = 32;

constexpr size_t WS_Q = 0;
constexpr size_t WS_KV = 4u << 20;
constexpr size_t WS_O = 12u << 20;
constexpr size_t WS_KVR = 16u << 20;
constexpr size_t WS_ZT = 20u << 20;
constexpr size_t WS_CNT = 24u << 20;

DI unsigned cvtpk(float lo, float hi) {
    f32x2 v = {lo, hi};
    return __builtin_bit_cast(unsigned, __builtin_convertvector(v, bf16x2_t));
}
DI float bflo(unsigned u) { return __uint_as_float(u << 16); }
DI float bfhi(unsigned u) { return __uint_as_float(u & 0xffff0000u); }

DI void glds16(unsigned voff, const void* sbase, unsigned lds_dst) {
    unsigned sv;
    asm volatile("s_mov_b32 %0, m0\n\ts_mov_b32 m0, %3\n\ts_nop 0\n\tglobal_load_lds_dwordx4 %1, %2\n\ts_mov_b32 m0, %0"
                 : "=&s"(sv) : "v"(voff), "s"(sbase), "s"(lds_dst) : "memory");
}
DI bf16x8 lds_rd128(lds_ptr p) { return *(const LAS bf16x8*)p; }
DI s16x4 lds_tr(lds_ptr p) {
    return __builtin_bit_cast(s16x4, __builtin_amdgcn_ds_read_tr16_b64_v4i16((LAS s16x4*)p));
}
DI bf16x8 cat8(s16x4 a, s16x4 b) { return __builtin_shufflevector(a, b, 0, 1, 2, 3, 4, 5, 6, 7); }
DI u32x4 pack8(const f32x16& x, int s, float sc) {
    u32x4 p;
    p[0] = cvtpk(x[8 * s + 0] * sc, x[8 * s + 1] * sc);
    p[1] = cvtpk(x[8 * s + 2] * sc, x[8 * s + 3] * sc);
    p[2] = cvtpk(x[8 * s + 4] * sc, x[8 * s + 5] * sc);
    p[3] = cvtpk(x[8 * s + 6] * sc, x[8 * s + 7] * sc);
    return p;
}
DI u32x4 pack8n(const f32x16& x, int s) {
    u32x4 p;
    p[0] = cvtpk(x[8 * s + 0], x[8 * s + 1]);
    p[1] = cvtpk(x[8 * s + 2], x[8 * s + 3]);
    p[2] = cvtpk(x[8 * s + 4], x[8 * s + 5]);
    p[3] = cvtpk(x[8 * s + 6], x[8 * s + 7]);
    return p;
}
DI void rnd2(float& a, float& b) { unsigned u = cvtpk(a, b); a = bflo(u); b = bfhi(u); }

#define CONV_PACE()
#define RAW_BAR() asm volatile("s_barrier" ::: "memory")
DI unsigned gld32(unsigned voff, const void* sbase) {
    unsigned r;
    asm volatile("global_load_dword %0, %1, %2" : "=v"(r) : "v"(voff), "s"(sbase) : "memory");
    return r;
}
DI u32x2 gld64(unsigned voff, const void* sbase) {
    u32x2 r;
    asm volatile("global_load_dwordx2 %0, %1, %2" : "=v"(r) : "v"(voff), "s"(sbase) : "memory");
    return r;
}
DI float mul_sv(float s, float v) { float r; asm("v_mul_f32_e32 %0, %1, %2" : "=v"(r) : "s"(s), "v"(v)); return r; }
DI float mul_vv(float a, float b) { float r; asm("v_mul_f32_e32 %0, %1, %2" : "=v"(r) : "v"(a), "v"(b)); return r; }
DI float add_vv(float a, float b) { float r; asm("v_add_f32_e32 %0, %1, %2" : "=v"(r) : "v"(a), "v"(b)); return r; }
DI void acc_vv(float& a, float b) { asm("v_add_f32_e32 %0, %0, %1" : "+v"(a) : "v"(b)); }
DI void fmac_vv(float& acc, float a, float b) { asm("v_fmac_f32_e32 %0, %1, %2" : "+v"(acc) : "v"(a), "v"(b)); }
DI void fmac_sv(float& acc, float s, float v) { asm("v_fmac_f32_e32 %0, %1, %2" : "+v"(acc) : "s"(s), "v"(v)); }
typedef __attribute__((address_space(1))) unsigned gu32;
#define RLX_AGENT __ATOMIC_RELAXED, __HIP_MEMORY_SCOPE_AGENT
#define CFENCE() asm volatile("" ::: "memory")
#define DRAIN() asm volatile("s_waitcnt vmcnt(0)" ::: "memory")
constexpr int AUX_SC1 = 16;
DI u32x4 ld_sc1(__amdgpu_buffer_rsrc_t rs, unsigned off) { return __builtin_amdgcn_raw_buffer_load_b128(rs, (int)off, 0, AUX_SC1); }
DI u32x4 ld_sc1s(__amdgpu_buffer_rsrc_t rs, unsigned off, int soff) { return __builtin_amdgcn_raw_buffer_load_b128(rs, (int)off, soff, AUX_SC1); }
DI void st_sc1(u32x4 v, __amdgpu_buffer_rsrc_t rs, unsigned off) { __builtin_amdgcn_raw_buffer_store_b128(v, rs, (int)off, 0, AUX_SC1); }
constexpr unsigned EMPTY = 0xffffffffu;
DI unsigned nz(unsigned x) { return x < EMPTY - 1u ? x : EMPTY - 1u; }
DI u32x4 nz4(u32x4 v) { return u32x4{nz(v[0]), nz(v[1]), nz(v[2]), nz(v[3])}; }
DI unsigned umax(unsigned a, unsigned b) { return a > b ? a : b; }
DI unsigned umax3(unsigned a, unsigned b, unsigned c) { return umax(umax(a, b), c); }
DI unsigned mx4(unsigned m, u32x4 v) { return umax3(umax3(m, v[0], v[1]), v[2], v[3]); }
DI unsigned mx8(u32x4 a, u32x4 b) { return umax(umax3(umax3(umax3(a[0], a[1], a[2]), a[3], b[0]), b[1], b[2]), b[3]); }
DI bool missing(unsigned m) { return __builtin_amdgcn_ballot_w64(m == EMPTY) != 0; }
DI unsigned lds_add_rtn(lds_ptr p, int lane) {
    unsigned old = 0;
    CFENCE();
    if (lane == 0) old = __hip_atomic_fetch_add((LAS unsigned*)p, 1u, __ATOMIC_RELAXED, __HIP_MEMORY_SCOPE_WORKGROUP);
    old = __builtin_amdgcn_readfirstlane(old);
    CFENCE();
    return old;
}
DI void lds_add(lds_ptr p, int lane) {
    unsigned long long sv;
    asm volatile("s_mov_b64 %0, exec\n\ts_mov_b64 exec, 1\n\tds_add_u32 %1, %2\n\ts_mov_b64 exec, %0"
                 : "=&s"(sv) : "v"(p), "v"(1u) : "memory");
}
DI void lds_wait_ge(lds_ptr p, unsigned v) {
    CFENCE();
    while ((unsigned)__builtin_amdgcn_readfirstlane(*(volatile LAS unsigned*)p) < v) __builtin_amdgcn_s_sleep(1);
    CFENCE();
}
DI void poll_ge(gu32* word, unsigned want) {
    CFENCE();
    for (unsigned spins = 0; spins < (1u << 22); ++spins) {
        if ((unsigned)__builtin_amdgcn_readfirstlane(__hip_atomic_load(word, RLX_AGENT)) >= want) break;
        __builtin_amdgcn_s_sleep(1);
    }
    __builtin_amdgcn_fence(__ATOMIC_ACQUIRE, "wavefront");
    CFENCE();
}
#define PB(x) __builtin_bit_cast(bf16x8, x)
#define PRIO(x) do { __builtin_amdgcn_s_setprio(x); SBAR(); } while (0)
DI u32x4 gld128(unsigned voff, const void* sbase) {
    u32x4 r;
    asm volatile("global_load_dwordx4 %0, %1, %2" : "=v"(r) : "v"(voff), "s"(sbase) : "memory");
    return r;
}

#define K1_ROT ((tb & 1) * 8)
constexpr int K1_STAGE = 40960;
__global__ __launch_bounds__(512, 2) void k1_qkv(const char* __restrict__ X, const char* __restrict__ W,
                                                 char* __restrict__ ws) {
    __shared__ __attribute__((aligned(16))) char lds_raw[4 * K1_STAGE];
    const lds_ptr lds = (lds_ptr)lds_raw;
    const unsigned lds0 = (unsigned)(uintptr_t)lds;
    const int tid = threadIdx.x, lane = tid & 63;
    const int w = __builtin_amdgcn_readfirstlane(tid >> 6);
    const int b = blockIdx.x, xcd = b & 7, idx = b >> 3;
    const int hp = (xcd >> 1) * 4 + (idx & 3), tb = (xcd & 1) * 8 + (idx >> 2);
    DG_DECL(0)

    if (w >= 4) {
        const int lw = w - 4;
        const int rot = K1_ROT;
        unsigned voffX[4], voffW[2];
#pragma unroll
        for (int pp = 0; pp < 4; ++pp) {
            const int row = 32 * lw + 8 * pp + (lane >> 3);
            const int ch = (lane & 7) ^ ((row >> 1) & 7);
            voffX[pp] = (unsigned)(tb * 128 + row) * 2048u + ch * 16;
        }
#pragma unroll
        for (int pp = 0; pp < 2; ++pp) {
            const int krow = 16 * lw + 8 * pp + (lane >> 3);
            const int ch = (lane & 7) ^ (((lane >> 4) & 1) << 2);
            voffW[pp] = (unsigned)krow * 6144u + hp * 128 + ch * 16;
        }
        auto issue = [&](int kt) {
            const int kr = (kt + rot) & 15;
            const char* xb = X + kr * 128;
            const char* wb = W + (size_t)kr * (64 * 6144);
            const unsigned st = lds0 + (kt & 3) * K1_STAGE;
#pragma unroll
            for (int pp = 0; pp < 4; ++pp) glds16(voffX[pp], xb, st + (4 * lw + pp) * 1024);
#pragma unroll
            for (int sec = 0; sec < 3; ++sec)
#pragma unroll
                for (int pp = 0; pp < 2; ++pp)
                    glds16(voffW[pp], wb + sec * 2048, st + 16384 + sec * 8192 + (2 * lw + pp) * 1024);
        };
        issue(0); issue(1); issue(2);
#pragma unroll 1
        for (int kt = 0; kt < 13; ++kt) {
            WAIT_BAR(20);
            issue(kt + 3);
        }
        {
            const __amdgpu_buffer_rsrc_t rs = __builtin_amdgcn_make_buffer_rsrc(ws, 0, 32 << 20, 0x00020000);
            const u32x4 e4 = {EMPTY, EMPTY, EMPTY, EMPTY};
            const unsigned eo = (unsigned)(b * 4 + lw) * 4096u + lane * 16;
            CFENCE();
#pragma unroll
            for (int i = 0; i < 4; ++i) { st_sc1(e4, rs, (unsigned)WS_O + eo + i * 1024); st_sc1(e4, rs, (unsigned)WS_ZT + eo + i * 1024); }
            CFENCE();
        }
        WAIT_BAR(28);
        WAIT_BAR(18);
        WAIT_BAR(8);
        if (lw == 0 && lane == 0) *(unsigned*)(ws + WS_CNT + b * 256) = 0u;
        DG_END(5);
        return;
    }

    const int wm = w >> 1, wn = w & 1;
    const int r = lane & 31, hh = lane >> 5;
    const int g = lane >> 4, q = (lane & 15) >> 2, p = lane & 3;
    const int xoff0 = (64 * wm + r) * 128 + ((hh ^ ((r >> 1) & 7)) << 4);
    const int woff0 = 16384 + (8 * hh + q) * 128 + (((4 * wn + 2 * (g & 1) + (p >> 1)) ^ ((q >> 1) << 2)) << 4) + (p & 1) * 8;

    f32x16 acc[2][3];
#pragma unroll
    for (int m = 0; m < 2; ++m)
#pragma unroll
        for (int s = 0; s < 3; ++s) acc[m][s] = f32x16{};
    bf16x8 xf[2][2];
    s16x4 wl[2][3], wh[2][3];
#define K1_WF(f, sec) cat8(wl[f][sec], wh[f][sec])
#define K1_STEP(F, st, t, BAR_HERE) do { \
        const lds_ptr xp_ = (st) + (xoff0 ^ ((t) << 5)); const lds_ptr wp_ = (st) + woff0 + (t) * 2048; \
        acc[0][0] = MFMA32(K1_WF(F, 0), xf[F][0], acc[0][0]); SBAR(); \
        if (BAR_HERE) { LGKM_BAR(); SBAR(); } \
        xf[F ^ 1][0] = lds_rd128(xp_); wl[F ^ 1][0] = lds_tr(wp_); wh[F ^ 1][0] = lds_tr(wp_ + 512); SBAR(); \
        acc[0][1] = MFMA32(K1_WF(F, 1), xf[F][0], acc[0][1]); SBAR(); \
        wl[F ^ 1][1] = lds_tr(wp_ + 8192); wh[F ^ 1][1] = lds_tr(wp_ + 8192 + 512); SBAR(); \
        acc[0][2] = MFMA32(xf[F][0], K1_WF(F, 2), acc[0][2]); SBAR(); \
        wl[F ^ 1][2] = lds_tr(wp_ + 16384); wh[F ^ 1][2] = lds_tr(wp_ + 16384 + 512); xf[F ^ 1][1] = lds_rd128(xp_ + 4096); SBAR(); \
        acc[1][0] = MFMA32(K1_WF(F, 0), xf[F][1], acc[1][0]); SBAR(); \
        acc[1][1] = MFMA32(K1_WF(F, 1), xf[F][1], acc[1][1]); \
        acc[1][2] = MFMA32(xf[F][1], K1_WF(F, 2), acc[1][2]); SBAR(); \
    } while (0)

    LGKM_BAR();
    DG(3);
    {
        const lds_ptr xp_ = lds + xoff0; const lds_ptr wp_ = lds + woff0;
        xf[0][0] = lds_rd128(xp_); xf[0][1] = lds_rd128(xp_ + 4096);
#pragma unroll
        for (int sec = 0; sec < 3; ++sec) { wl[0][sec] = lds_tr(wp_ + sec * 8192); wh[0][sec] = lds_tr(wp_ + sec * 8192 + 512); }
    }
    SBAR();
#pragma unroll
    for (int kt = 0; kt < 15; ++kt) {
        const lds_ptr st = lds + (kt & 3) * K1_STAGE;
        const lds_ptr sn = lds + ((kt + 1) & 3) * K1_STAGE;
        K1_STEP(0, st, 1, false);
        K1_STEP(1, st, 2, false);
        K1_STEP(0, st, 3, false);
        K1_STEP(1, sn, 0, true);
    }
    {
        const lds_ptr st = lds + 3 * K1_STAGE;
        K1_STEP(0, st, 1, false);
        K1_STEP(1, st, 2, false);
        K1_STEP(0, st, 3, false);
#pragma unroll
        for (int m = 0; m < 2; ++m) {
            acc[m][1] = MFMA32(K1_WF(1, 1), xf[1][m], acc[m][1]);
            acc[m][2] = MFMA32(xf[1][m], K1_WF(1, 2), acc[m][2]);
            acc[m][0] = MFMA32(K1_WF(1, 0), xf[1][m], acc[m][0]);
        }
    }
    DG(4);
    {
        const int h = 2 * hp + wn;
        const float qscale = 0.17677669529663687f * 1.4426950408889634f;
        const __amdgpu_buffer_rsrc_t rs = __builtin_amdgcn_make_buffer_rsrc(ws, 0, 32 << 20, 0x00020000);
#pragma unroll
        for (int m = 0; m < 2; ++m) {
            const int T = tb * 4 + wm * 2 + m;
            const unsigned dq = (unsigned)WS_Q + (h * 64 + T) * 2048 + lane * 16;
            const unsigned dk = (unsigned)WS_KV + (h * 64 + T) * 4096 + lane * 16;
            st_sc1(pack8n(acc[m][1], 0), rs, dk);
            st_sc1(pack8n(acc[m][1], 1), rs, dk + 1024);
            st_sc1(pack8n(acc[m][2], 0), rs, dk + 2048);
            st_sc1(pack8n(acc[m][2], 1), rs, dk + 3072);
            st_sc1(pack8(acc[m][0], 0, qscale), rs, dq);
            st_sc1(pack8(acc[m][0], 1, qscale), rs, dq + 1024);
        }
    }
    DG_END(5);
}

constexpr int K2_SLOT = 32768;
constexpr int K2_SF = 65536;
constexpr int K2_CV = 98304;
constexpr int CV_HA = 0, CV_HB = 4160, CV_KV = 8320, CV_BYTES = 8320 + 480 + 4096;
constexpr int K2_LX = K2_CV + 4 * CV_BYTES;
constexpr int K2_FL = K2_LX + 2048;
constexpr int K2_SCR = K2_FL + 256;
constexpr int K2_LDS = K2_SCR + 4096;
constexpr int F_X = 0, F_R = 32, F_W2 = 64, C_OST = 68, C_S1 = 76, C_S2 = 80, C_S3 = 128, C_ZST = 88, F_ZGO = 92,
              C_KST = 96, C_U0 = 100, C_LD = 108, F_KGO = 116, C_END = 120, C_OL = 72;
#define NG_IN 0
#define GATE_WORD C_END
#define GATE_VAL 0
#define X_NZ 4
#define X_NW3 8
#define ZP_GATE 4u
#define ZL_GATE 4
#define ZL_AT 31
#define Z_SLEEP0 0
#define O_SLEEP 0
#define Z_SLEEP 8
#define OWIN 16
#define OW0 8
#define WD 2
#define W3_AT 10
#define X_NOLDW 0
#define X_NOLDO 0
#define X_NOCHK 0
#define KROT 2
#define NG_LAST 0
#define PACE_IN "s_nop 7"
#define PACE_OUT "s_nop 1"
#define P3_PRIO 1
#define AUX_PRIO 2

__global__ __launch_bounds__(768, 3) void k2_attn(char* __restrict__ ws, const char* __restrict__ Aq,
                                                  const char* __restrict__ Ak, const char* __restrict__ Av,
                                                  const char* __restrict__ shiftk, const char* __restrict__ ssmk,
                                                  const unsigned* __restrict__ ssmD, const char* __restrict__ Wo,
                                                  char* __restrict__ out) {
    __shared__ __attribute__((aligned(16))) char lds_raw[K2_LDS];
    const lds_ptr lds = (lds_ptr)lds_raw;
    const lds_ptr fl = lds + K2_FL;
    const unsigned lds0 = (unsigned)(uintptr_t)lds;
    const int tid = threadIdx.x, lane = tid & 63;
    const int w = __builtin_amdgcn_readfirstlane(tid >> 6);
    const int b = blockIdx.x, xcd = b & 7, idx = b >> 3;
    const int h = xcd * 4 + (idx >> 3), qb = idx & 7;
    const int tt = xcd * 2 + (idx >> 4), nt = idx & 15;
    const char* kvg = ws + WS_KV + (size_t)h * 64 * 4096;
    const __amdgpu_buffer_rsrc_t rs = __builtin_amdgcn_make_buffer_rsrc(ws, 0, 32 << 20, 0x00020000);
    gu32* const cnt = (gu32*)(ws + WS_CNT);
    auto signal = [&](int g) {
        if (lane < 32) (void)__hip_atomic_fetch_add(cnt + (lane * 8 + g) * 64, 1u, RLX_AGENT);
    };
    const unsigned wvoff = (unsigned)(lane >> 3) * 2048u + nt * 128 + (((lane & 7) ^ (((lane >> 4) & 1) << 2)) << 4);
    auto wsrc = [&](int hf, int lp) { return Wo + (size_t)(hf * 64 + ((lp + nt * KROT * 2) & 63)) * 16384; };
    DG_DECL(1)

    if (w >= 8) {
        const int cw = w - 8;
        const unsigned voff = lane * 16;
        __builtin_amdgcn_s_setprio(3);
        auto issue = [&](int st) {
            const char* src = kvg + (st * 4 + cw) * 4096;
            const unsigned dst = lds0 + (st & 1) * K2_SLOT + cw * 4096;
#pragma unroll
            for (int i = 0; i < 4; ++i) glds16(voff, src + i * 1024, dst + i * 1024);
#pragma unroll
            for (int i = 0; i < 4; ++i) glds16(voff, src + 131072 + i * 1024, dst + 16384 + i * 1024);
        };
        issue(0);
        const int ch = xcd * 128 + idx * 4 + cw;
        const lds_ptr cv = lds + K2_CV + cw * CV_BYTES;
        {
            const u32x4 z4 = {0u, 0u, 0u, 0u};
            if (lane < 2) *(LAS u32x4*)(cv + CV_HA + lane * 16) = z4;
            if (lane < 30) *(LAS u32x4*)(cv + CV_KV + lane * 16) = z4;
            if (cw == 0 && lane < 16) *(LAS u32x4*)(fl + lane * 16) = z4;
        }
        WAIT_BAR(0);
        issue(1);
        const int c0 = xcd * 128 + cw * 32, cq = lane & 7, rg = lane >> 3;
        const int row0 = idx * 64 + rg * 8;
        const unsigned roff = (unsigned)row0 * 2048u + (c0 + cq * 4) * 2;
        const unsigned toffs = (unsigned)(c0 + (lane >> 3)) * 4096u + idx * 128;
        u32x4 sk[2];
        sk[0] = gld128((unsigned)(c0 + cq * 4) * 8u, shiftk); sk[1] = gld128((unsigned)(c0 + cq * 4) * 8u, shiftk + 16);
        unsigned dd = gld32((unsigned)(ch >> 1) * 4u, ssmD);
        auto stage = [&](const char* src, int dst) {
            const unsigned sbase = (unsigned)(idx * 64 + cw * 16 + (lane >> 4)) * 2048u + xcd * 256;
#pragma unroll
            for (int i = 0; i < 4; ++i)
                glds16(sbase + (((lane & 15) ^ (((2 * cw + (i >> 1)) & 3) << 2)) << 4), src + i * 8192, lds0 + dst + cw * 4096 + i * 1024);
        };
        WAIT_BAR(3);
        DG(12);
        issue(2);
        stage(Ak, K2_SF);
        glds16((unsigned)(idx * 64 - (idx ? 4 : 0) + (lane >> 4)) * 2048u + xcd * 256 + (((lane & 15) ^ 12) << 4), Ak, lds0 + K2_SCR + cw * 1024);
        WAIT_BAR(5);
        DG(9);
        issue(3);
        stage(Av, K2_SF + 16384);
        WAIT_BAR(4);
        DG(3);
        issue(4);
#pragma unroll
        for (int i = 0; i < 4; ++i) glds16(voff, ssmk + (size_t)ch * 4096 + i * 1024, lds0 + K2_CV + cw * CV_BYTES + CV_HA + 32 + i * 1024);
        WAIT_BAR(4);
        DG(5);
        issue(5);
        SBAR();
        asm volatile("" : "+v"(sk[0]), "+v"(sk[1]), "+v"(dd));
        dd = __builtin_amdgcn_readfirstlane(dd);
        {
            u32x2 kr[11], vr[8];
            const lds_ptr kb = lds + K2_SF + rg * 2048 + ((cw ^ (rg & 3)) << 6) + cq * 8;
            const lds_ptr hb = lds + (rg ? K2_SF + rg * 2048 - 768 : K2_SCR + cw * 1024 + 256) + ((cw ^ ((rg + 3) & 3)) << 6) + cq * 8;
#pragma unroll
            for (int i = 0; i < 3; ++i) kr[i] = *(const LAS u32x2*)(hb + i * 256);
#pragma unroll
            for (int i = 0; i < 8; ++i) kr[3 + i] = *(const LAS u32x2*)(kb + i * 256);
#pragma unroll
            for (int i = 0; i < 8; ++i) vr[i] = *(const LAS u32x2*)(kb + 16384 + i * 256);
            if (row0 == 0) { kr[0] = u32x2{0u, 0u}; kr[1] = u32x2{0u, 0u}; kr[2] = u32x2{0u, 0u}; }
            const lds_ptr tb = cv + CV_KV + 480;
#pragma unroll
            for (int c = 0; c < 4; ++c) {
                const unsigned s01 = sk[c >> 1][(c & 1) * 2], s23 = sk[c >> 1][(c & 1) * 2 + 1];
                const float w0 = bflo(s01), w1 = bfhi(s01), w2 = bflo(s23), w3 = bfhi(s23);
                float kf[11], kv[8];
#pragma unroll
                for (int i = 0; i < 11; ++i) kf[i] = (c & 1) ? bfhi(kr[i][c >> 1]) : bflo(kr[i][c >> 1]);
#pragma unroll
                for (int i = 0; i < 8; ++i) {
                    float a = mul_vv(w3, kf[i]);
                    fmac_vv(a, w2, kf[i + 1]); fmac_vv(a, w1, kf[i + 2]); fmac_vv(a, w0, kf[i + 3]);
                    kv[i] = mul_vv(a, (c & 1) ? bfhi(vr[i][c >> 1]) : bflo(vr[i][c >> 1]));
                }
                u32x4 o4;
#pragma unroll
                for (int i = 0; i < 4; ++i) o4[i] = cvtpk(kv[7 - 2 * i], kv[6 - 2 * i]);
                *(LAS u32x4*)(tb + (cq * 4 + c) * 128 + ((rg ^ cq) << 4)) = o4;
            }
#pragma unroll
            for (int i = 0; i < 4; ++i) {
                const u32x4 t = *(const LAS u32x4*)(tb + i * 1024 + lane * 16);
                st_sc1(t, rs, (unsigned)WS_KVR + toffs + i * 32768 + (((lane & 7) ^ (2 * i + (lane >> 5))) << 4));
            }
        }
        CFENCE();
        DG(7);
        WAIT_BAR(4);
        DG(6);
        issue(6);
        asm volatile("s_waitcnt vmcnt(8)" ::: "memory");
        if (lds_add_rtn(fl + C_KST, lane) == 3u) signal(xcd);
#pragma unroll
        for (int i = 0; i < 8; ++i)
            glds16(wvoff, wsrc(i >> 2, cw * 4 + (i & 3)), lds0 + K2_SF + (i >> 2) * 16384 + (cw * 4 + (i & 3)) * 1024);
        {
            const u32x4 z4 = {0u, 0u, 0u, 0u};
            if (lane < 2) *(LAS u32x4*)(cv + CV_HB + lane * 16) = z4;
#pragma unroll
            for (int i = 0; i < 4; ++i) {
                const int off = 32 + i * 1024 + lane * 16;
                const u32x4 hv = *(const LAS u32x4*)(cv + CV_HA + off);
                const unsigned pv = *(const LAS unsigned*)(cv + CV_HA + off - 4);
                u32x4 sh;
                sh[0] = __builtin_amdgcn_alignbit(hv[0], pv, 16);
                sh[1] = __builtin_amdgcn_alignbit(hv[1], hv[0], 16);
                sh[2] = __builtin_amdgcn_alignbit(hv[2], hv[1], 16);
                sh[3] = __builtin_amdgcn_alignbit(hv[3], hv[2], 16);
                *(LAS u32x4*)(cv + CV_HB + off) = sh;
            }
        }
        WAIT_BAR(8);
        DG(8);
        issue(7);
#pragma unroll
        for (int i = 0; i < 8; ++i)
            glds16(wvoff, wsrc(idx >> 4, 32 + cw * 8 + i), lds0 + K2_SCR + cw * 1024);
        WAIT_BAR(8);
        DG(11);
        if (cw == 0) { poll_ge(cnt + b * 64, 32u); lds_add(fl + F_KGO, lane); }
        else lds_wait_ge(fl + F_KGO, 1u);
        {
            u32x4 t[4];
#pragma unroll
            for (int i = 0; i < 4; ++i) t[i] = ld_sc1(rs, (unsigned)WS_KVR + ch * 4096 + i * 1024 + voff);
#pragma unroll
            for (int i = 0; i < 4; ++i) *(LAS u32x4*)(cv + CV_KV + 480 + i * 1024 + lane * 16) = t[i];
        }
        const int r1 = lane & 15, q = lane >> 4;
        const int toff = (r1 & 1) ? CV_HA + 2 * (16 + r1 + 9 - 8 * q) : CV_HB + 2 * (16 + r1 + 10 - 8 * q);
        const int boff = CV_KV + 448 + 32 * r1 + 16 * q;
        f32x4 y[8];
        u32x4 tf[8];
        bf16x8 bf[8];
        const lds_ptr bp = cv + boff - 448;
        lds_ptr tp[4];
#pragma unroll
        for (int i = 0; i < 4; ++i) { tp[i] = cv + toff + 1024 * i; asm volatile("" : "+v"(tp[i])); }
#define LDT(bb, a, hf) do { const LAS unsigned* t_ = (const LAS unsigned*)(tp[(a) >> 1] + 64 * (bb) + 512 * ((a) & 1) + 8 * (hf)); \
            tf[a][2 * (hf)] = t_[0]; tf[a][2 * (hf) + 1] = t_[1]; SBAR(); } while (0)
#define LDB(bb, a) do { bf[a] = lds_rd128(bp + 448 - 64 * (bb) + 512 * (a)); SBAR(); } while (0)
#define CM(a, ww) do { asm volatile("v_mfma_f32_16x16x32_bf16 %0, %1, %2, %0" : "+v"(y[(a) + (ww)]) : "v"(tf[a]), "v"(bf[ww])); SBAR(); } while (0)
#define CM0(a, ww) do { asm volatile("v_mfma_f32_16x16x32_bf16 %0, %1, %2, 0" : "=&v"(y[(a) + (ww)]) : "v"(tf[a]), "v"(bf[ww])); SBAR(); } while (0)
#define PC() do { asm volatile(PACE_OUT); SBAR(); } while (0)
#pragma unroll
        for (int a = 0; a < 8; ++a) { if (a < 5) { LDT(0, a, 0); LDT(0, a, 1); } LDB(0, 7 - a); }
#pragma unroll
        for (int i = 0; i < 8; ++i)
            glds16(wvoff, wsrc(i >> 2, 16 + cw * 4 + (i & 3)), lds0 + (i >> 2) * 16384 + (cw * 4 + (i & 3)) * 1024);
#pragma unroll
        for (int i = 0; i < 4; ++i)
            glds16((unsigned)(idx * 64 + cw * 16 + (lane >> 4)) * 2048u + xcd * 256 + (lane & 15) * 16, Aq + i * 8192, lds0 + K2_SCR + cw * 1024);
        RAW_BAR();
        asm volatile("s_waitcnt vmcnt(4)" ::: "memory");
        lds_add(fl + C_S1, lane);
        __builtin_amdgcn_s_setprio(AUX_PRIO);
        const float dch = (ch & 1) ? bfhi(dd) : bflo(dd);
        const lds_ptr kdp = cv + CV_KV + 480 + 32 * r1 + 16 * (q >> 1) + ((q & 1) ? 0 : 8);
        float kd[8][4];
        u32x2 kk;
        unsigned kc0, kc1, zo[8][2];
#define KD_RD(t) do { kk = *(const LAS u32x2*)(kdp + 512 * (t)); SBAR(); } while (0)
#define KD_UP0(t) do { kd[t][0] = bfhi(kk[1]); kd[t][1] = bflo(kk[1]); SBAR(); } while (0)
#define KD_UP1(t) do { kd[t][2] = bfhi(kk[0]); kd[t][3] = bflo(kk[0]); SBAR(); } while (0)
#define KD_MU0(t) do { kd[t][0] = mul_sv(dch, kd[t][0]); kd[t][1] = mul_sv(dch, kd[t][1]); SBAR(); } while (0)
#define KD_MU1(t) do { kd[t][2] = mul_sv(dch, kd[t][2]); kd[t][3] = mul_sv(dch, kd[t][3]); SBAR(); } while (0)
#define KD_CV(t) do { kc0 = cvtpk(kd[t][0], kd[t][1]); kc1 = cvtpk(kd[t][2], kd[t][3]); SBAR(); } while (0)
#define KD_X0(t) do { kd[t][0] = bflo(kc0); kd[t][1] = bfhi(kc0); SBAR(); } while (0)
#define KD_X1(t) do { kd[t][2] = bflo(kc1); kd[t][3] = bfhi(kc1); SBAR(); } while (0)
#define ZF0(t) do { kc0 = cvtpk(y[t][0], y[t][1]); kc1 = cvtpk(y[t][2], y[t][3]); SBAR(); } while (0)
#define ZF1(t) do { y[t][0] = bflo(kc0); y[t][1] = bfhi(kc0); SBAR(); } while (0)
#define ZF2(t) do { y[t][2] = bflo(kc1); y[t][3] = bfhi(kc1); SBAR(); } while (0)
#define ZF3(t) do { y[t][0] = add_vv(y[t][0], kd[t][0]); y[t][1] = add_vv(y[t][1], kd[t][1]); SBAR(); } while (0)
#define ZF4(t) do { y[t][2] = add_vv(y[t][2], kd[t][2]); y[t][3] = add_vv(y[t][3], kd[t][3]); SBAR(); } while (0)
#define ZF5(t) do { zo[t][0] = nz(cvtpk(y[t][0], y[t][1])); zo[t][1] = nz(cvtpk(y[t][2], y[t][3])); SBAR(); } while (0)
#define ZST(pr) do { auto s0 = __builtin_amdgcn_permlane16_swap(zo[2 * (pr)][0], zo[2 * (pr) + 1][0], false, false); \
            auto s1 = __builtin_amdgcn_permlane16_swap(zo[2 * (pr)][1], zo[2 * (pr) + 1][1], false, false); \
            st_sc1(u32x4{s0[0], s1[0], s0[1], s1[1]}, rs, (unsigned)WS_ZT + ch * 4096 + (pr) * 1024 + voff); SBAR(); } while (0)
        CM0(0, 7); LDB(1, 7); CM0(0, 6); LDT(0, 5, 0); CM0(0, 5); LDT(0, 5, 1); CM0(0, 4); LDT(0, 6, 0); CM0(0, 3); LDT(0, 6, 1); CM0(0, 2); LDT(0, 7, 0); CM0(0, 1); LDT(0, 7, 1); CM0(0, 0); KD_RD(0);
        CM(1, 6); LDB(1, 6); CM(1, 5); LDT(1, 0, 0); CM(1, 4); LDT(1, 0, 1); CM(1, 3); PC(); CM(1, 2); PC(); CM(1, 1); KD_UP0(0); CM(1, 0); KD_UP1(0);
        CM(2, 5); LDB(1, 5); CM(2, 4); LDT(1, 1, 0); CM(2, 3); LDT(1, 1, 1); CM(2, 2); KD_MU0(0); CM(2, 1); KD_MU1(0); CM(2, 0); KD_CV(0);
        CM(3, 4); LDB(1, 4); CM(3, 3); LDT(1, 2, 0); CM(3, 2); LDT(1, 2, 1); CM(3, 1); KD_X0(0); CM(3, 0); KD_X1(0);
        CM(4, 3); LDB(1, 3); CM(4, 2); LDT(1, 3, 0); CM(4, 1); LDT(1, 3, 1); CM(4, 0); KD_RD(1);
        CM(5, 2); LDB(1, 2); CM(5, 1); LDT(1, 4, 0); CM(5, 0); LDT(1, 4, 1);
        CM(6, 1); LDB(1, 1); CM(6, 0); PC();
        CM(7, 0); LDB(1, 0);
        CM(0, 7); LDB(2, 7); CM(0, 6); LDT(1, 5, 0); CM(0, 5); LDT(1, 5, 1); CM(0, 4); LDT(1, 6, 0); CM(0, 3); LDT(1, 6, 1); CM(0, 2); LDT(1, 7, 0); CM(0, 1); LDT(1, 7, 1); CM(0, 0); PC();
        CM(1, 6); LDB(2, 6); CM(1, 5); LDT(2, 0, 0); CM(1, 4); LDT(2, 0, 1); CM(1, 3); KD_UP0(1); CM(1, 2); KD_UP1(1); CM(1, 1); KD_MU0(1); CM(1, 0); KD_MU1(1);
        CM(2, 5); LDB(2, 5); CM(2, 4); LDT(2, 1, 0); CM(2, 3); LDT(2, 1, 1); CM(2, 2); KD_CV(1); CM(2, 1); KD_X0(1); CM(2, 0); KD_X1(1);
        CM(3, 4); LDB(2, 4); CM(3, 3); LDT(2, 2, 0); CM(3, 2); LDT(2, 2, 1); CM(3, 1); KD_RD(2); CM(3, 0); PC();
        CM(4, 3); LDB(2, 3); CM(4, 2); LDT(2, 3, 0); CM(4, 1); LDT(2, 3, 1); CM(4, 0); PC();
        CM(5, 2); LDB(2, 2); CM(5, 1); LDT(2, 4, 0); CM(5, 0); LDT(2, 4, 1);
        CM(6, 1); LDB(2, 1); CM(6, 0); KD_UP0(2);
        CM(7, 0); LDB(2, 0);
        CM(0, 7); LDB(3, 7); CM(0, 6); LDT(2, 5, 0); CM(0, 5); LDT(2, 5, 1); CM(0, 4); LDT(2, 6, 0); CM(0, 3); LDT(2, 6, 1); CM(0, 2); LDT(2, 7, 0); CM(0, 1); LDT(2, 7, 1); CM(0, 0); KD_UP1(2);
        CM(1, 6); LDB(3, 6); CM(1, 5); LDT(3, 0, 0); CM(1, 4); LDT(3, 0, 1); CM(1, 3); KD_MU0(2); CM(1, 2); KD_MU1(2); CM(1, 1); KD_CV(2); CM(1, 0); KD_X0(2);
        CM(2, 5); LDB(3, 5); CM(2, 4); LDT(3, 1, 0); CM(2, 3); LDT(3, 1, 1); CM(2, 2); KD_X1(2); CM(2, 1); KD_RD(3); CM(2, 0); PC();
        CM(3, 4); LDB(3, 4); CM(3, 3); LDT(3, 2, 0); CM(3, 2); LDT(3, 2, 1); CM(3, 1); PC(); CM(3, 0); KD_UP0(3);
        CM(4, 3); LDB(3, 3); CM(4, 2); LDT(3, 3, 0); CM(4, 1); LDT(3, 3, 1); CM(4, 0); KD_UP1(3);
        CM(5, 2); LDB(3, 2); CM(5, 1); LDT(3, 4, 0); CM(5, 0); LDT(3, 4, 1);
        CM(6, 1); LDB(3, 1); CM(6, 0); KD_MU0(3);
        CM(7, 0); LDB(3, 0);
        CM(0, 7); LDB(4, 7); CM(0, 6); LDT(3, 5, 0); CM(0, 5); LDT(3, 5, 1); CM(0, 4); LDT(3, 6, 0); CM(0, 3); LDT(3, 6, 1); CM(0, 2); LDT(3, 7, 0); CM(0, 1); LDT(3, 7, 1); CM(0, 0); KD_MU1(3);
        CM(1, 6); LDB(4, 6); CM(1, 5); LDT(4, 0, 0); CM(1, 4); LDT(4, 0, 1); CM(1, 3); KD_CV(3); CM(1, 2); KD_X0(3); CM(1, 1); KD_X1(3); CM(1, 0); KD_RD(4);
        CM(2, 5); LDB(4, 5); CM(2, 4); LDT(4, 1, 0); CM(2, 3); LDT(4, 1, 1); CM(2, 2); PC(); CM(2, 1); PC(); CM(2, 0); KD_UP0(4);
        CM(3, 4); LDB(4, 4); CM(3, 3); LDT(4, 2, 0); CM(3, 2); LDT(4, 2, 1); CM(3, 1); KD_UP1(4); CM(3, 0); KD_MU0(4);
        CM(4, 3); LDB(4, 3); CM(4, 2); LDT(4, 3, 0); CM(4, 1); LDT(4, 3, 1); CM(4, 0); KD_MU1(4);
        CM(5, 2); LDB(4, 2); CM(5, 1); LDT(4, 4, 0); CM(5, 0); LDT(4, 4, 1);
        CM(6, 1); LDB(4, 1); CM(6, 0); KD_CV(4);
        CM(7, 0); LDB(4, 0);
        CM(0, 7); LDB(5, 7); CM(0, 6); LDT(4, 5, 0); CM(0, 5); LDT(4, 5, 1); CM(0, 4); LDT(4, 6, 0); CM(0, 3); LDT(4, 6, 1); CM(0, 2); LDT(4, 7, 0); CM(0, 1); LDT(4, 7, 1); CM(0, 0); KD_X0(4);
        CM(1, 6); LDB(5, 6); CM(1, 5); LDT(5, 0, 0); CM(1, 4); LDT(5, 0, 1); CM(1, 3); KD_X1(4); CM(1, 2); KD_RD(5); CM(1, 1); PC(); CM(1, 0); PC();
        CM(2, 5); LDB(5, 5); CM(2, 4); LDT(5, 1, 0); CM(2, 3); LDT(5, 1, 1); CM(2, 2); KD_UP0(5); CM(2, 1); KD_UP1(5); CM(2, 0); KD_MU0(5);
        CM(3, 4); LDB(5, 4); CM(3, 3); LDT(5, 2, 0); CM(3, 2); LDT(5, 2, 1); CM(3, 1); KD_MU1(5); CM(3, 0); KD_CV(5);
        CM(4, 3); LDB(5, 3); CM(4, 2); LDT(5, 3, 0); CM(4, 1); LDT(5, 3, 1); CM(4, 0); KD_X0(5);
        CM(5, 2); LDB(5, 2); CM(5, 1); LDT(5, 4, 0); CM(5, 0); LDT(5, 4, 1);
        CM(6, 1); LDB(5, 1); CM(6, 0); KD_X1(5);
        CM(7, 0); LDB(5, 0);
        CM(0, 7); LDB(6, 7); CM(0, 6); LDT(5, 5, 0); CM(0, 5); LDT(5, 5, 1); CM(0, 4); LDT(5, 6, 0); CM(0, 3); LDT(5, 6, 1); CM(0, 2); LDT(5, 7, 0); CM(0, 1); LDT(5, 7, 1); CM(0, 0); KD_RD(6);
        CM(1, 6); LDB(6, 6); CM(1, 5); LDT(6, 0, 0); CM(1, 4); LDT(6, 0, 1); CM(1, 3); PC(); CM(1, 2); PC(); CM(1, 1); KD_UP0(6); CM(1, 0); KD_UP1(6);
        CM(2, 5); LDB(6, 5); CM(2, 4); LDT(6, 1, 0); CM(2, 3); LDT(6, 1, 1); CM(2, 2); KD_MU0(6); CM(2, 1); KD_MU1(6); CM(2, 0); KD_CV(6);
        CM(3, 4); LDB(6, 4); CM(3, 3); LDT(6, 2, 0); CM(3, 2); LDT(6, 2, 1); CM(3, 1); KD_X0(6); CM(3, 0); KD_X1(6);
        CM(4, 3); LDB(6, 3); CM(4, 2); LDT(6, 3, 0); CM(4, 1); LDT(6, 3, 1); CM(4, 0); KD_RD(7);
        CM(5, 2); LDB(6, 2); CM(5, 1); LDT(6, 4, 0); CM(5, 0); LDT(6, 4, 1);
        CM(6, 1); LDB(6, 1); CM(6, 0); PC();
        CM(7, 0); LDB(6, 0);
        CM(0, 7); LDB(7, 7); CM(0, 6); LDT(6, 5, 0); CM(0, 5); LDT(6, 5, 1); CM(0, 4); LDT(6, 6, 0); CM(0, 3); LDT(6, 6, 1); CM(0, 2); LDT(6, 7, 0); CM(0, 1); LDT(6, 7, 1); CM(0, 0); PC();
        CM(1, 6); LDB(7, 6); CM(1, 5); LDT(7, 0, 0); CM(1, 4); LDT(7, 0, 1); CM(1, 3); KD_UP0(7); CM(1, 2); KD_UP1(7); CM(1, 1); KD_MU0(7); CM(1, 0); KD_MU1(7);
        CM(2, 5); LDB(7, 5); CM(2, 4); LDT(7, 1, 0); CM(2, 3); LDT(7, 1, 1); CM(2, 2); KD_CV(7); CM(2, 1); KD_X0(7); CM(2, 0); KD_X1(7);
        CM(3, 4); LDB(7, 4); CM(3, 3); LDT(7, 2, 0); CM(3, 2); LDT(7, 2, 1); CM(3, 1); PC(); CM(3, 0); PC();
        CM(4, 3); LDB(7, 3); CM(4, 2); LDT(7, 3, 0); CM(4, 1); LDT(7, 3, 1); CM(4, 0); PC();
        CM(5, 2); LDB(7, 2); CM(5, 1); LDT(7, 4, 0); CM(5, 0); LDT(7, 4, 1);
        CM(6, 1); LDB(7, 1); CM(6, 0); PC();
        CM(7, 0); LDB(7, 0);
        CM(0, 3); LDT(7, 5, 0); CM(0, 2); LDT(7, 5, 1); CM(1, 2); LDT(7, 6, 0); CM(1, 1); LDT(7, 6, 1); CM(2, 1); LDT(7, 7, 0); CM(2, 0); LDT(7, 7, 1);
        CM(3, 0); PC(); CM(0, 1); PC(); CM(0, 0); PC(); CM(1, 0); ZF0(2);  CM(0, 5); ZF1(2);  CM(0, 4); ZF2(2);
        CM(1, 4); ZF3(2);  CM(1, 3); ZF4(2);  CM(2, 3); ZF5(2);  CM(2, 2); ZF0(3);  CM(3, 2); ZF1(3);  CM(3, 1); ZF2(3);
        CM(4, 1); ZF3(3);  CM(4, 0); ZF4(3);  CM(5, 0); ZF5(3);  CM(0, 7); ZST(1);  CM(0, 6); ZF0(0);  CM(1, 6); ZF1(0);
        CM(1, 5); ZF2(0);  CM(2, 5); ZF3(0);  CM(2, 4); ZF4(0);  CM(3, 4); ZF5(0);  CM(3, 3); ZF0(1);  CM(4, 3); ZF1(1);
        CM(4, 2); ZF2(1);  CM(5, 2); ZF3(1);  CM(5, 1); ZF4(1);  CM(6, 1); ZF5(1);  CM(6, 0); ZST(0);  CM(7, 0); ZF0(4);
        DG(4);
        ZF1(4); ZF2(4); ZF3(4); ZF4(4); ZF5(4); ZF0(5); ZF1(5); ZF2(5); ZF3(5); ZF4(5); ZF5(5); ZST(2); ZF0(6); ZF1(6); ZF2(6); ZF3(6); ZF4(6); ZF5(6); ZF0(7); ZF1(7); ZF2(7); ZF3(7); ZF4(7); ZF5(7); ZST(3);
        DG(10);
        __builtin_amdgcn_s_setprio(3);
        while ((unsigned)__builtin_amdgcn_readfirstlane(*(volatile LAS unsigned*)(fl + C_OL)) < ZL_GATE) __builtin_amdgcn_s_sleep(4);
        CFENCE();
        u32x2 qr[8];
#pragma unroll
        for (int i = 0; i < 8; ++i) qr[i] = gld64(roff, Aq + i * 2048);
        {
            const lds_ptr tr = cv;
            u32x4 t[4];
            const unsigned zb = (unsigned)WS_ZT + (unsigned)(c0 + (lane >> 3)) * 4096u + (idx >> 3) * 1024 + ((idx & 3) * 4 + ((idx >> 2) & 1) * 16) * 16;
            for (unsigned spins = 0; spins < (1u << 20); ++spins) {
#pragma unroll
                for (int i = 0; i < 4; ++i) {
                    const int ck = (lane & 7) ^ (2 * i + (lane >> 5));
                    t[i] = ld_sc1(rs, zb + i * 32768 + ((ck >> 1) + (ck & 1) * 32) * 16);
                }
                if (!missing(mx4(mx4(mx8(t[0], t[1]), t[2]), t[3]))) break;
                __builtin_amdgcn_s_sleep(Z_SLEEP);
            }
            DG(13);
#pragma unroll
            for (int i = 0; i < 4; ++i) *(LAS u32x4*)(tr + i * 1024 + lane * 16) = t[i];
#pragma unroll
            for (int i = 0; i < 8; ++i) asm volatile("" : "+v"(qr[i]));
            u32x4 zc[4];
#pragma unroll
            for (int c = 0; c < 4; ++c) zc[c] = *(const LAS u32x4*)(tr + (cq * 4 + c) * 128 + ((rg ^ cq) << 4));
            char* dst = out + (size_t)row0 * 4096 + (c0 + cq * 4) * 2;
#pragma unroll
            for (int i = 0; i < 8; ++i) {
                float zz[4];
#pragma unroll
                for (int c = 0; c < 4; ++c) zz[c] = (i & 1) ? bfhi(zc[c][i >> 1]) : bflo(zc[c][i >> 1]);
                u32x2 o = {cvtpk(mul_vv(bflo(qr[i][0]), zz[0]), mul_vv(bfhi(qr[i][0]), zz[1])),
                           cvtpk(mul_vv(bflo(qr[i][1]), zz[2]), mul_vv(bfhi(qr[i][1]), zz[3]))};
                *(u32x2*)(dst + i * 4096) = o;
            }
        }
        DG_END(14);
        return;
    }

    const int half = w >> 2;
    const int TA = qb * 8 + (w & 3) * 2 + half, TB = TA ^ 1;
    const char* qga = ws + WS_Q + (size_t)(h * 64 + TA) * 2048 + lane * 16;
    const char* qgb = ws + WS_Q + (size_t)(h * 64 + TB) * 2048 + lane * 16;
    const bf16x8 qa0 = *(const bf16x8*)qga, qa1 = *(const bf16x8*)(qga + 1024);
    const bf16x8 qb0 = *(const bf16x8*)qgb, qb1 = *(const bf16x8*)(qgb + 1024);
    bf16x8 sel;
    {
        const short one = (((lane >> 2) & 1) == ((lane >> 4) & 1)) ? (short)0x3f80 : (short)0;
#pragma unroll
        for (int i = 0; i < 8; ++i) sel[i] = one;
    }
    f32x16 oA = f32x16{}, oB = f32x16{};
    f32x4 lA = f32x4{}, lB = f32x4{};
    f32x16 negm, sA, sB;
    const lds_ptr lp = lds + half * 16384 + lane * 16;
    bf16x8 k0[2], k1[2], v0[2], v1[2];
    u32x4 p0, p1 = {0u, 0u, 0u, 0u};
#define EX(X, i) X[i] = __builtin_amdgcn_exp2f(X[i])
    LGKM_BAR();
    DG(3);
    {
        const bf16x8 x0 = lds_rd128(lds + lane * 16), x1 = lds_rd128(lds + lane * 16 + 1024);
        k0[0] = lds_rd128(lp); k1[0] = lds_rd128(lp + 1024);
        v0[0] = lds_rd128(lp + 2048); v1[0] = lds_rd128(lp + 3072);
        sA = MFMA32(x0, qa0, f32x16{});
        sB = MFMA32(x0, qb0, f32x16{});
        sA = MFMA32(x1, qa1, sA);
        sB = MFMA32(x1, qb1, sB);
        float m = __builtin_fmaxf(sA[0], sB[0]);
#pragma unroll
        for (int i = 1; i < 16; ++i) m = __builtin_fmaxf(m, __builtin_fmaxf(sA[i], sB[i]));
        auto rr = __builtin_amdgcn_permlane32_swap(__float_as_uint(m), __float_as_uint(m), false, false);
        m = __builtin_fmaxf(__uint_as_float(rr[0]), __uint_as_float(rr[1]));
#pragma unroll
        for (int i = 0; i < 16; ++i) negm[i] = -m;
        sA = MFMA32(k0[0], qa0, negm);
        sA = MFMA32(k1[0], qa1, sA);
        v1[1] = v1[0];
        EX(sA, 0); EX(sA, 1);
    }
#define SUM16(l, p) asm volatile("v_mfma_f32_16x16x32_bf16 %0, %1, %2, %0" : "+v"(l) : "v"(sel), "v"(p))
#define HALF(C, oC, lC, N, qx, qy, ka, kb, oP, lP, vprev, vcur0, PRE) do { \
        oP = MFMA32(vprev, PB(p1), oP); SBAR(); \
        PRE; EX(C, 2); EX(C, 3); EX(C, 4); SBAR(); \
        N = MFMA32(ka, qx, negm); SBAR(); \
        EX(C, 5); EX(C, 6); EX(C, 7); SBAR(); \
        SUM16(lP, p1); SBAR(); \
        p0[0] = cvtpk(C[0], C[1]); p0[1] = cvtpk(C[2], C[3]); SBAR(); \
        N = MFMA32(kb, qy, N); SBAR(); \
        p0[2] = cvtpk(C[4], C[5]); p0[3] = cvtpk(C[6], C[7]); SBAR(); EX(C, 8); EX(C, 9); SBAR(); \
        oC = MFMA32(vcur0, PB(p0), oC); SBAR(); \
        EX(C, 10); EX(C, 11); EX(C, 12); SBAR(); \
        SUM16(lC, p0); SBAR(); \
        EX(C, 13); EX(C, 14); EX(C, 15); SBAR(); \
        p1[0] = cvtpk(C[8], C[9]); p1[1] = cvtpk(C[10], C[11]); p1[2] = cvtpk(C[12], C[13]); p1[3] = cvtpk(C[14], C[15]); SBAR(); \
        EX(N, 0); EX(N, 1); SBAR(); \
    } while (0)
#define RD4(Y, nx) do { v1[Y] = lds_rd128((nx) + 3072); v0[Y] = lds_rd128((nx) + 2048); \
        k1[Y] = lds_rd128((nx) + 1024); k0[Y] = lds_rd128(nx); } while (0)
#define KT(X, nx, PRE0) do { \
        HALF(sA, oA, lA, sB, qb0, qb1, k0[X], k1[X], oB, lB, v1[X ^ 1], v0[X], PRE0; RD4(X ^ 1, nx)); \
        HALF(sB, oB, lB, sA, qa0, qa1, k0[X ^ 1], k1[X ^ 1], oA, lA, v1[X], v0[X], (void)0); \
    } while (0)
    SBAR();
#pragma unroll 1
    for (int st = 0; st < 8; ++st) {
        const lds_ptr cur = lp + (st & 1) * K2_SLOT;
        const lds_ptr nxt = lp + ((st + 1) & 1) * K2_SLOT;
        PRIO(2);
        KT(0, cur + 4096, (void)0);
        PRIO(1);
        KT(1, cur + 8192, (void)0);
        PRIO(0);
        KT(0, cur + 12288, (void)0);
        PRIO(3);
        KT(1, nxt, RAW_BAR(); SBAR());
    }
    oB = MFMA32(v1[1], PB(p1), oB);
    SBAR();
    SUM16(lB, p1);
    asm volatile("s_nop 7" : "+v"(oB) :: "memory");
    SBAR();
    DG(4);
    {
        const lds_ptr xo = lds + K2_SLOT + w * 4096 + lane * 16;
        *(LAS float*)(lds + K2_LX + w * 256 + lane * 4) = lB[0];
#pragma unroll
        for (int i = 0; i < 4; ++i) *(LAS f32x4*)(xo + i * 1024) = f32x4{oB[4 * i], oB[4 * i + 1], oB[4 * i + 2], oB[4 * i + 3]};
        lds_add(fl + F_X + w * 4, lane);
        lds_add(fl + C_END + half * 4, lane);
        lds_wait_ge(fl + F_X + (w ^ 4) * 4, 1u);
        const lds_ptr xi = lds + K2_SLOT + (w ^ 4) * 4096 + lane * 16;
        f32x4 t[4];
        const float lx = *(const LAS float*)(lds + K2_LX + (w ^ 4) * 256 + lane * 4);
#pragma unroll
        for (int i = 0; i < 4; ++i) t[i] = *(const LAS f32x4*)(xi + i * 1024);
        SBAR();
        lA[0] += lx;
#pragma unroll
        for (int i = 0; i < 16; ++i) { float o = oA[i]; acc_vv(o, t[i >> 2][i & 3]); oA[i] = o; }
    }
    DG(9);
    const float l = lA[0];
    u32x4 on0, on1;
    auto finish = [&](float ls) {
        float rl = __builtin_amdgcn_rcpf(ls);
        asm volatile("s_nop 1" : "+v"(rl));
        f32x16 o;
#pragma unroll
        for (int i = 0; i < 16; ++i) o[i] = mul_vv(oA[i], rl);
        on0 = nz4(pack8n(o, 0)); on1 = nz4(pack8n(o, 1));
    };
    finish(l);
    {
        float t0, t1;
        asm("v_mul_f32_e32 %0, 0, %2\n\tv_mul_f32_e32 %1, 0, %3\n\tv_fmac_f32_e32 %0, 0, %4\n\tv_fmac_f32_e32 %1, 0, %5\n\tv_fmac_f32_e32 %0, 0, %6\n\tv_fmac_f32_e32 %1, 0, %7\n\tv_fmac_f32_e32 %0, 0, %8\n\tv_fmac_f32_e32 %1, 0, %9\n\t"
            "v_fmac_f32_e32 %0, 0, %10\n\tv_fmac_f32_e32 %1, 0, %11\n\tv_fmac_f32_e32 %0, 0, %12\n\tv_fmac_f32_e32 %1, 0, %13\n\tv_fmac_f32_e32 %0, 0, %14\n\tv_fmac_f32_e32 %1, 0, %15\n\tv_fmac_f32_e32 %0, 0, %16\n\tv_fmac_f32_e32 %1, 0, %17"
            : "=&v"(t0), "=&v"(t1) : "v"(oA[0]), "v"(oA[1]), "v"(oA[2]), "v"(oA[3]), "v"(oA[4]), "v"(oA[5]), "v"(oA[6]), "v"(oA[7]), "v"(oA[8]), "v"(oA[9]), "v"(oA[10]), "v"(oA[11]), "v"(oA[12]), "v"(oA[13]), "v"(oA[14]), "v"(oA[15]));
        const bool ok = (l <= 1.0e30f) && (l >= 1.0e-18f) && (t0 + t1 == 0.f);
        if (__builtin_expect(__any(!ok), 0)) {
            float m = -INFINITY, ls = 0.f;
            oA = f32x16{};
#pragma unroll 1
            for (int t = 0; t < 64; ++t) {
                const char* g = kvg + t * 4096 + lane * 16;
                const bf16x8 a0 = *(const bf16x8*)g, a1 = *(const bf16x8*)(g + 1024);
                const bf16x8 b0 = *(const bf16x8*)(g + 2048), b1 = *(const bf16x8*)(g + 3072);
                f32x16 s = MFMA32(a0, qa0, f32x16{});
                s = MFMA32(a1, qa1, s);
                float tm = s[0];
#pragma unroll
                for (int i = 1; i < 16; ++i) tm = __builtin_fmaxf(tm, s[i]);
                auto rr = __builtin_amdgcn_permlane32_swap(__float_as_uint(tm), __float_as_uint(tm), false, false);
                tm = __builtin_fmaxf(__uint_as_float(rr[0]), __uint_as_float(rr[1]));
                const float mn = __builtin_fmaxf(m, tm);
                const float alpha = __builtin_amdgcn_exp2f(m - mn);
                m = mn;
                float ps = 0.f;
#pragma unroll
                for (int i = 0; i < 16; ++i) { s[i] = __builtin_amdgcn_exp2f(s[i] - mn); ps += s[i]; oA[i] *= alpha; }
                ls = ls * alpha + ps;
                oA = MFMA32(b0, PB(pack8n(s, 0)), oA);
                oA = MFMA32(b1, PB(pack8n(s, 1)), oA);
            }
            auto rr = __builtin_amdgcn_permlane32_swap(__float_as_uint(ls), __float_as_uint(ls), false, false);
            asm volatile("s_nop 7\n\ts_nop 7" : "+v"(oA));
            finish(__uint_as_float(rr[0]) + __uint_as_float(rr[1]));
        }
    }
    u32x4 w3[4];
    {
        const unsigned og = (unsigned)WS_O + (unsigned)(TA * 32 + h) * 2048u + lane * 16;
        st_sc1(on0, rs, og);
        st_sc1(on1, rs, og + 1024);
        lds_add(fl + C_S1, lane);
        const char* s2 = wsrc(half ^ 1, 32 + (w & 3) * 4);
        const char* s3 = wsrc(half, 48 + (w & 3) * 4);
        lds_wait_ge(fl + C_S1, 12u);
        DG(5);
#pragma unroll
        for (int i = 0; i < 4; ++i)
            glds16(wvoff, s2 + i * 16384, lds0 + K2_SLOT + ((w ^ 4) * 4 + i) * 1024);
#pragma unroll
        for (int i = 0; i < 4; ++i) w3[i] = gld128(wvoff, s3 + i * 16384);
    }
    const int j4 = w & 3;
    {
        const int T3 = tt * 4 + j4;
        const unsigned og = (unsigned)WS_O + (unsigned)T3 * 65536u + half * 32768 + lane * 16;
        const int r = lane & 31, hh = lane >> 5;
        const int g = lane >> 4, q = (lane & 15) >> 2, p = lane & 3;
        int woff[2];
#pragma unroll
        for (int n = 0; n < 2; ++n) {
            const int c = 4 * (n ^ half) + 2 * (p & 1) + (g & 1);
            woff[n] = (4 * hh + q) * 128 + ((c ^ ((q >> 1) << 2)) << 4) + (p >> 1) * 8;
        }
        f32x16 acc[2] = {f32x16{}, f32x16{}};
        lds_ptr wl[2][2] = {{lds + half * 16384 + woff[0], lds + half * 16384 + woff[1]},
                            {lds + K2_SF + half * 16384 + woff[0], lds + K2_SF + half * 16384 + woff[1]}};
        lds_ptr w3d = lds + K2_SF + w * 4096 + lane * 16;
        asm volatile("" : "+v"(wl[0][0]), "+v"(wl[0][1]), "+v"(wl[1][0]), "+v"(wl[1][1]), "+v"(w3d));
        s16x4 wa[WD + 1][2], wb[WD + 1][2];
#define LDW(s, n) do { const int f_ = (s) < 8 || (s) >= 24, o_ = (f_ ? 0 : (((s) >> 3) - 1) * K2_SLOT) + ((s) & 7) * 2048; \
            wa[(s) % (WD + 1)][n] = lds_tr(wl[f_][n] + o_); wb[(s) % (WD + 1)][n] = lds_tr(wl[f_][n] + o_ + 1024); SBAR(); } while (0)
#pragma unroll
        for (int s = 0; s < WD; ++s) { LDW(s, 0); LDW(s, 1); }
        u32x4 of[OWIN];
#define LDO(i) do { of[(i) % OWIN] = ld_sc1s(rs, og, (((i) + nt * KROT) & 31) * 1024); SBAR(); } while (0)
        for (unsigned spins = 0; spins < (1u << 20); ++spins) {
#pragma unroll
            for (int i = 0; i < OW0; ++i) LDO(i);
            unsigned m = mx8(of[0], of[1]);
#pragma unroll
            for (int i = 2; i < OW0; ++i) m = mx4(m, of[i]);
            if (!missing(m)) break;
            DGC(12);
        }
        lds_add(fl + C_S2, lane);
#pragma unroll
        for (int i = OW0; i < OWIN; ++i) LDO(i);
        DG(6);
        __builtin_amdgcn_s_setprio(P3_PRIO);
        SBAR();
        unsigned fv = 0;
#define FLAG_RD(a) do { fv = *(volatile LAS unsigned*)(a); SBAR(); } while (0)
#define FLAG_CHK(a, n) do { if (__builtin_expect((unsigned)__builtin_amdgcn_readfirstlane(fv) < (n), 0)) lds_wait_ge(a, n); CFENCE(); SBAR(); } while (0)
#pragma unroll
        for (int s = 0; s < 32; ++s) {
            acc[0] = MFMA32(cat8(wa[s % (WD + 1)][0], wb[s % (WD + 1)][0]), PB(of[s % OWIN]), acc[0]);
            SBAR();
            if (s == 16 - WD) FLAG_CHK(fl + C_S2, 8u);
            if (s == 24 - WD) FLAG_CHK(fl + C_S3 + half * 4, 4u);
            if (s + WD < 32) LDW(s + WD, 0);
            if ((s & 1) && s + 1 >= OW0 && s + 1 < 32 && __builtin_expect(missing(mx8(of[(s + 1) % OWIN], of[(s + 2) % OWIN])), 0)) {
                for (unsigned spins = 0; spins < (1u << 20); ++spins) {
                    __builtin_amdgcn_s_sleep(2);
                    LDO(s + 1); LDO(s + 2);
                    DGC(13);
                    if (!missing(mx8(of[(s + 1) % OWIN], of[(s + 2) % OWIN]))) break;
                }
            }
            SBAR();
            acc[1] = MFMA32(cat8(wa[s % (WD + 1)][1], wb[s % (WD + 1)][1]), PB(of[s % OWIN]), acc[1]);
            SBAR();
            if (s + WD < 32) LDW(s + WD, 1);
            if (s + OWIN < 32) LDO(s + OWIN);
            if (s == 7 - WD) lds_add(fl + C_U0 + half * 4, lane);
            if (s == W3_AT - 2) FLAG_RD(fl + C_U0 + half * 4);
            if (s == 14 - WD) FLAG_RD(fl + C_S2);
            if (s == 22 - WD) FLAG_RD(fl + C_S3 + half * 4);
            if (s == W3_AT) FLAG_CHK(fl + C_U0 + half * 4, 4u);
            if (s >= W3_AT && s < W3_AT + 4) {
                asm volatile("" : "+v"(w3[(s - W3_AT) & 3]));
                *(LAS u32x4*)(w3d + ((s - W3_AT) & 3) * 1024) = w3[(s - W3_AT) & 3];
            }
            if (s == W3_AT + 3) lds_add(fl + C_S3 + half * 4, lane);
            if (ZL_GATE && s + OWIN == ZL_AT) lds_add(fl + C_OL, lane);
            SBAR();
            if (s == 0) DG(10);
            if (s == 15) DG(11);
            if (s == 23) DG(14);
        }
        DG(7);
        const lds_ptr xo = lds + w * 4096 + lane * 16;
#pragma unroll
        for (int i = 0; i < 4; ++i) *(LAS f32x4*)(xo + i * 1024) = f32x4{acc[1][4 * i], acc[1][4 * i + 1], acc[1][4 * i + 2], acc[1][4 * i + 3]};
        lds_add(fl + F_R + w * 4, lane);
        lds_wait_ge(fl + F_R + (w ^ 4) * 4, 1u);
        const lds_ptr xi = lds + (w ^ 4) * 4096 + lane * 16;
#pragma unroll
        for (int i = 0; i < 4; ++i) {
            const f32x4 t = *(const LAS f32x4*)(xi + i * 1024);
#pragma unroll
            for (int j = 0; j < 4; ++j) acc[0][4 * i + j] = add_vv(acc[0][4 * i + j], t[j]);
        }
        char* dst = out + (size_t)(T3 * 32 + r) * 4096 + 2048 + nt * 128 + half * 64 + hh * 32;
        *(u32x4*)(dst) = pack8n(acc[0], 0);
        *(u32x4*)(dst + 16) = pack8n(acc[0], 1);
    }
    DG_END(8);
}

extern "C" void kernel_launch(void* const* d_in, const int* in_sizes, int n_in,
                              void* d_out, int out_size, void* d_ws, size_t ws_size,
                              hipStream_t stream) {
    const void* A_q = d_in[0];
    const void* A_k = d_in[1];
    const void* A_v = d_in[2];
    const void* A_shift = d_in[3];
    const void* A_ssmk = d_in[4];
    const void* A_D = d_in[5];
    const void* B_x = d_in[6];
    const void* B_wqkv = d_in[7];
    const void* B_wo = d_in[8];
    char* ws = (char*)d_ws;
    k1_qkv<<<256, 512, 0, stream>>>((const char*)B_x, (const char*)B_wqkv, ws);
    k2_attn<<<256, 768, 0, stream>>>(ws, (const char*)A_q, (const char*)A_k, (const char*)A_v, (const char*)A_shift,
                                     (const char*)A_ssmk, (const unsigned*)A_D, (const char*)B_wo, (char*)d_out);
}
```

```cpp
#include <hip/hip_runtime.h>
#include <hip/hip_bf16.h>
#include <stdint.h>

typedef short bf16x8 __attribute__((ext_vector_type(8)));
typedef short s16x4 __attribute__((ext_vector_type(4)));
typedef float f32x16 __attribute__((ext_vector_type(16)));
typedef float f32x4 __attribute__((ext_vector_type(4)));
typedef float f32x2 __attribute__((ext_vector_type(2)));
typedef unsigned u32x4 __attribute__((ext_vector_type(4)));
typedef unsigned u32x2 __attribute__((ext_vector_type(2)));
typedef __bf16 bf16x2_t __attribute__((ext_vector_type(2)));

#define LAS __attribute__((address_space(3)))
typedef LAS char* lds_ptr;
#define DI __device__ __forceinline__
#define SBAR() __builtin_amdgcn_sched_barrier(0)
#define MFMA32(a, b, c) __builtin_amdgcn_mfma_f32_32x32x16_bf16((a), (b), (c), 0, 0, 0)
#define MFMA16(a, b, c) __builtin_amdgcn_mfma_f32_16x16x32_bf16((a), (b), (c), 0, 0, 0)
#define WAIT_BAR(N) asm volatile("s_waitcnt vmcnt(" #N ") lgkmcnt(0)\n\ts_barrier" ::: "memory")
#define LGKM_BAR() asm volatile("s_waitcnt lgkmcnt(0)\n\ts_barrier" ::: "memory")

#define DG_DECL(K)
#define DG(i)
#define DG_END(i)
#define DGC(i)

constexpr int S_LEN = 2048, DM = 1024, NH = 32;

constexpr size_t WS_Q = 0;
constexpr size_t WS_KV = 4u << 20;
constexpr size_t WS_O = 12u << 20;
constexpr size_t WS_KVR = 16u << 20;
constexpr size_t WS_ZT = 20u << 20;
constexpr size_t WS_CNT = 24u << 20;

DI unsigned cvtpk(float lo, float hi) {
    f32x2 v = {lo, hi};
    return __builtin_bit_cast(unsigned, __builtin_convertvector(v, bf16x2_t));
}
DI float bflo(unsigned u) { return __uint_as_float(u << 16); }
DI float bfhi(unsigned u) { return __uint_as_float(u & 0xffff0000u); }

DI void glds16(unsigned voff, const void* sbase, unsigned lds_dst) {
    unsigned sv;
    asm volatile("s_mov_b32 %0, m0\n\ts_mov_b32 m0, %3\n\ts_nop 0\n\tglobal_load_lds_dwordx4 %1, %2\n\ts_mov_b32 m0, %0"
                 : "=&s"(sv) : "v"(voff), "s"(sbase), "s"(lds_dst) : "memory");
}
DI bf16x8 lds_rd128(lds_ptr p) { return *(const LAS bf16x8*)p; }
DI s16x4 lds_tr(lds_ptr p) {
    return __builtin_bit_cast(s16x4, __builtin_amdgcn_ds_read_tr16_b64_v4i16((LAS s16x4*)p));
}
DI bf16x8 cat8(s16x4 a, s16x4 b) { return __builtin_shufflevector(a, b, 0, 1, 2, 3, 4, 5, 6, 7); }
DI u32x4 pack8(const f32x16& x, int s, float sc) {
    u32x4 p;
    p[0] = cvtpk(x[8 * s + 0] * sc, x[8 * s + 1] * sc);
    p[1] = cvtpk(x[8 * s + 2] * sc, x[8 * s + 3] * sc);
    p[2] = cvtpk(x[8 * s + 4] * sc, x[8 * s + 5] * sc);
    p[3] = cvtpk(x[8 * s + 6] * sc, x[8 * s + 7] * sc);
    return p;
}
DI u32x4 pack8n(const f32x16& x, int s) {
    u32x4 p;
    p[0] = cvtpk(x[8 * s + 0], x[8 * s + 1]);
    p[1] = cvtpk(x[8 * s + 2], x[8 * s + 3]);
    p[2] = cvtpk(x[8 * s + 4], x[8 * s + 5]);
    p[3] = cvtpk(x[8 * s + 6], x[8 * s + 7]);
    return p;
}
DI void rnd2(float& a, float& b) { unsigned u = cvtpk(a, b); a = bflo(u); b = bfhi(u); }

#define CONV_PACE()
#define RAW_BAR() asm volatile("s_barrier" ::: "memory")
DI unsigned gld32(unsigned voff, const void* sbase) {
    unsigned r;
    asm volatile("global_load_dword %0, %1, %2" : "=v"(r) : "v"(voff), "s"(sbase) : "memory");
    return r;
}
DI u32x2 gld64(unsigned voff, const void* sbase) {
    u32x2 r;
    asm volatile("global_load_dwordx2 %0, %1, %2" : "=v"(r) : "v"(voff), "s"(sbase) : "memory");
    return r;
}
DI float mul_sv(float s, float v) { float r; asm("v_mul_f32_e32 %0, %1, %2" : "=v"(r) : "s"(s), "v"(v)); return r; }
DI float mul_vv(float a, float b) { float r; asm("v_mul_f32_e32 %0, %1, %2" : "=v"(r) : "v"(a), "v"(b)); return r; }
DI float add_vv(float a, float b) { float r; asm("v_add_f32_e32 %0, %1, %2" : "=v"(r) : "v"(a), "v"(b)); return r; }
DI void acc_vv(float& a, float b) { asm("v_add_f32_e32 %0, %0, %1" : "+v"(a) : "v"(b)); }
DI void fmac_vv(float& acc, float a, float b) { asm("v_fmac_f32_e32 %0, %1, %2" : "+v"(acc) : "v"(a), "v"(b)); }
DI void fmac_sv(float& acc, float s, float v) { asm("v_fmac_f32_e32 %0, %1, %2" : "+v"(acc) : "s"(s), "v"(v)); }
typedef __attribute__((address_space(1))) unsigned gu32;
#define RLX_AGENT __ATOMIC_RELAXED, __HIP_MEMORY_SCOPE_AGENT
#define CFENCE() asm volatile("" ::: "memory")
#define DRAIN() asm volatile("s_waitcnt vmcnt(0)" ::: "memory")
constexpr int AUX_SC1 = 16;
DI u32x4 ld_sc1(__amdgpu_buffer_rsrc_t rs, unsigned off) { return __builtin_amdgcn_raw_buffer_load_b128(rs, (int)off, 0, AUX_SC1); }
DI u32x4 ld_sc1s(__amdgpu_buffer_rsrc_t rs, unsigned off, int soff) { return __builtin_amdgcn_raw_buffer_load_b128(rs, (int)off, soff, AUX_SC1); }
DI void st_sc1(u32x4 v, __amdgpu_buffer_rsrc_t rs, unsigned off) { __builtin_amdgcn_raw_buffer_store_b128(v, rs, (int)off, 0, AUX_SC1); }
constexpr unsigned EMPTY = 0xffffffffu;
DI unsigned nz(unsigned x) { return x < EMPTY - 1u ? x : EMPTY - 1u; }
DI u32x4 nz4(u32x4 v) { return u32x4{nz(v[0]), nz(v[1]), nz(v[2]), nz(v[3])}; }
DI unsigned umax(unsigned a, unsigned b) { return a > b ? a : b; }
DI unsigned umax3(unsigned a, unsigned b, unsigned c) { return umax(umax(a, b), c); }
DI unsigned mx4(unsigned m, u32x4 v) { return umax3(umax3(m, v[0], v[1]), v[2], v[3]); }
DI unsigned mx8(u32x4 a, u32x4 b) { return umax(umax3(umax3(umax3(a[0], a[1], a[2]), a[3], b[0]), b[1], b[2]), b[3]); }
DI bool missing(unsigned m) { return __builtin_amdgcn_ballot_w64(m == EMPTY) != 0; }
DI unsigned lds_add_rtn(lds_ptr p, int lane) {
    unsigned old = 0;
    CFENCE();
    if (lane == 0) old = __hip_atomic_fetch_add((LAS unsigned*)p, 1u, __ATOMIC_RELAXED, __HIP_MEMORY_SCOPE_WORKGROUP);
    old = __builtin_amdgcn_readfirstlane(old);
    CFENCE();
    return old;
}
DI void lds_add(lds_ptr p, int lane) {
    unsigned long long sv;
    asm volatile("s_mov_b64 %0, exec\n\ts_mov_b64 exec, 1\n\tds_add_u32 %1, %2\n\ts_mov_b64 exec, %0"
                 : "=&s"(sv) : "v"(p), "v"(1u) : "memory");
}
DI void lds_wait_ge(lds_ptr p, unsigned v) {
    CFENCE();
    while ((unsigned)__builtin_amdgcn_readfirstlane(*(volatile LAS unsigned*)p) < v) __builtin_amdgcn_s_sleep(1);
    CFENCE();
}
DI void poll_ge(gu32* word, unsigned want) {
    CFENCE();
    for (unsigned spins = 0; spins < (1u << 22); ++spins) {
        if ((unsigned)__builtin_amdgcn_readfirstlane(__hip_atomic_load(word, RLX_AGENT)) >= want) break;
        __builtin_amdgcn_s_sleep(1);
    }
    __builtin_amdgcn_fence(__ATOMIC_ACQUIRE, "wavefront");
    CFENCE();
}
#define PB(x) __builtin_bit_cast(bf16x8, x)
#define PRIO(x) do { __builtin_amdgcn_s_setprio(x); SBAR(); } while (0)
DI u32x4 gld128(unsigned voff, const void* sbase) {
    u32x4 r;
    asm volatile("global_load_dwordx4 %0, %1, %2" : "=v"(r) : "v"(voff), "s"(sbase) : "memory");
    return r;
}

#define K1_UNR 15
#define K1_LA 5
#define K1_ROT ((tb & 3) * 4)
constexpr int K1_STAGE = 40960, K1U = K1_UNR;
__global__ __launch_bounds__(512, 2) void k1_qkv(const char* __restrict__ X, const char* __restrict__ W,
                                                 char* __restrict__ ws) {
    __shared__ __attribute__((aligned(16))) char lds_raw[4 * K1_STAGE];
    const lds_ptr lds = (lds_ptr)lds_raw;
    const unsigned lds0 = (unsigned)(uintptr_t)lds;
    const int tid = threadIdx.x, lane = tid & 63;
    const int w = __builtin_amdgcn_readfirstlane(tid >> 6);
    const int b = blockIdx.x, xcd = b & 7, idx = b >> 3;
    const int hp = (xcd >> 1) * 4 + (idx & 3), tb = (xcd & 1) * 8 + (idx >> 2);
    DG_DECL(0)

    if (w >= 4) {
        const int lw = w - 4;
        const int rot = K1_ROT;
        unsigned voffX[4], voffW[2];
#pragma unroll
        for (int pp = 0; pp < 4; ++pp) {
            const int row = 32 * lw + 8 * pp + (lane >> 3);
            const int ch = (lane & 7) ^ ((row >> 1) & 7);
            voffX[pp] = (unsigned)(tb * 128 + row) * 2048u + ch * 16;
        }
#pragma unroll
        for (int pp = 0; pp < 2; ++pp) {
            const int krow = 16 * lw + 8 * pp + (lane >> 3);
            const int ch = (lane & 7) ^ (((lane >> 4) & 1) << 2);
            voffW[pp] = (unsigned)krow * 6144u + ch * 16;
        }
        auto issue = [&](int kt, int lo, int hi) {
            const int kr = (kt + rot) & 15;
            const char* xb = X + kr * 128;
            const char* wb = W + (size_t)kr * (64 * 6144) + hp * 128;
            const unsigned st = lds0 + (kt & 3) * K1_STAGE;
#pragma unroll
            for (int pp = 0; pp < 4; ++pp)
                if (pp >= lo && pp < hi) glds16(voffX[pp], xb, st + (4 * lw + pp) * 1024);
#pragma unroll
            for (int sec = 0; sec < 3; ++sec)
#pragma unroll
                for (int pp = 0; pp < 2; ++pp)
                    if (4 + 2 * sec + pp >= lo && 4 + 2 * sec + pp < hi)
                        glds16(voffW[pp], wb + sec * 2048, st + 16384 + sec * 8192 + (2 * lw + pp) * 1024);
        };
#define WAITN_BAR(N) asm volatile("s_waitcnt vmcnt(%0) lgkmcnt(0)\n\ts_barrier" :: "n"(N) : "memory")
        DG(13);
        issue(0, 0, 10); issue(1, 0, 10); issue(2, 0, K1_LA);
        DG(14);
#pragma unroll 1
        for (int kt = 0; kt < 13; ++kt) {
            WAITN_BAR(10 + K1_LA);
            issue(kt + 2, K1_LA, 10);
            issue(kt + 3, 0, K1_LA);
        }
        WAITN_BAR(10 + K1_LA);
        issue(15, K1_LA, 10);
        WAIT_BAR(10);
        WAIT_BAR(0);
        if (lw == 0 && lane == 0) *(unsigned*)(ws + WS_CNT + b * 256) = 0u;
        DG_END(5);
        return;
    }

    const int wm = w >> 1, wn = w & 1;
    const int r = lane & 31, hh = lane >> 5;
    const int g = lane >> 4, q = (lane & 15) >> 2, p = lane & 3;
    const int xoff0 = (64 * wm + r) * 128 + ((hh ^ ((r >> 1) & 7)) << 4);
    const int woff0 = 16384 + (8 * hh + q) * 128 + (((4 * wn + 2 * (g & 1) + (p >> 1)) ^ ((q >> 1) << 2)) << 4) + (p & 1) * 8;

    f32x16 acc[2][3];
#pragma unroll
    for (int m = 0; m < 2; ++m)
#pragma unroll
        for (int s = 0; s < 3; ++s) acc[m][s] = f32x16{};
    bf16x8 xf[2][2];
    s16x4 wl[2][3], wh[2][3];
#define K1_WF(f, sec) cat8(wl[f][sec], wh[f][sec])
#define K1_STEP(F, st, t, BAR_HERE) do { \
        const lds_ptr xp_ = (st) + (xoff0 ^ ((t) << 5)); const lds_ptr wp_ = (st) + woff0 + (t) * 2048; \
        acc[0][0] = MFMA32(K1_WF(F, 0), xf[F][0], acc[0][0]); SBAR(); \
        if (BAR_HERE) { LGKM_BAR(); SBAR(); } \
        xf[F ^ 1][0] = lds_rd128(xp_); wl[F ^ 1][0] = lds_tr(wp_); wh[F ^ 1][0] = lds_tr(wp_ + 512); SBAR(); \
        acc[0][1] = MFMA32(K1_WF(F, 1), xf[F][0], acc[0][1]); SBAR(); \
        wl[F ^ 1][1] = lds_tr(wp_ + 8192); wh[F ^ 1][1] = lds_tr(wp_ + 8192 + 512); SBAR(); \
        acc[0][2] = MFMA32(xf[F][0], K1_WF(F, 2), acc[0][2]); SBAR(); \
        wl[F ^ 1][2] = lds_tr(wp_ + 16384); wh[F ^ 1][2] = lds_tr(wp_ + 16384 + 512); xf[F ^ 1][1] = lds_rd128(xp_ + 4096); SBAR(); \
        acc[1][0] = MFMA32(K1_WF(F, 0), xf[F][1], acc[1][0]); SBAR(); \
        acc[1][1] = MFMA32(K1_WF(F, 1), xf[F][1], acc[1][1]); \
        acc[1][2] = MFMA32(xf[F][1], K1_WF(F, 2), acc[1][2]); SBAR(); \
    } while (0)

    LGKM_BAR();
    DG(3);
    {
        const lds_ptr xp_ = lds + xoff0; const lds_ptr wp_ = lds + woff0;
        xf[0][0] = lds_rd128(xp_); xf[0][1] = lds_rd128(xp_ + 4096);
#pragma unroll
        for (int sec = 0; sec < 3; ++sec) { wl[0][sec] = lds_tr(wp_ + sec * 8192); wh[0][sec] = lds_tr(wp_ + sec * 8192 + 512); }
    }
    SBAR();
#pragma unroll K1U
    for (int kt = 0; kt < 15; ++kt) {
        const lds_ptr st = lds + (kt & 3) * K1_STAGE;
        const lds_ptr sn = lds + ((kt + 1) & 3) * K1_STAGE;
        K1_STEP(0, st, 1, false);
        K1_STEP(1, st, 2, false);
        K1_STEP(0, st, 3, false);
        K1_STEP(1, sn, 0, true);
    }
    {
        const lds_ptr st = lds + 3 * K1_STAGE;
        K1_STEP(0, st, 1, false);
        K1_STEP(1, st, 2, false);
        K1_STEP(0, st, 3, false);
#pragma unroll
        for (int m = 0; m < 2; ++m) {
            acc[m][1] = MFMA32(K1_WF(1, 1), xf[1][m], acc[m][1]);
            acc[m][2] = MFMA32(xf[1][m], K1_WF(1, 2), acc[m][2]);
            acc[m][0] = MFMA32(K1_WF(1, 0), xf[1][m], acc[m][0]);
        }
    }
    DG(4);
    {
        const int h = 2 * hp + wn;
        const float qscale = 0.17677669529663687f * 1.4426950408889634f;
        const __amdgpu_buffer_rsrc_t rs = __builtin_amdgcn_make_buffer_rsrc(ws, 0, 32 << 20, 0x00020000);
#pragma unroll
        for (int m = 0; m < 2; ++m) {
            const int T = tb * 4 + wm * 2 + m;
            const unsigned dq = (unsigned)WS_Q + (h * 64 + T) * 2048 + lane * 16;
            const unsigned dk = (unsigned)WS_KV + (h * 64 + T) * 4096 + lane * 16;
            st_sc1(pack8n(acc[m][1], 0), rs, dk);
            st_sc1(pack8n(acc[m][1], 1), rs, dk + 1024);
            st_sc1(pack8n(acc[m][2], 0), rs, dk + 2048);
            st_sc1(pack8n(acc[m][2], 1), rs, dk + 3072);
            st_sc1(pack8(acc[m][0], 0, qscale), rs, dq);
            st_sc1(pack8(acc[m][0], 1, qscale), rs, dq + 1024);
        }
    }
    DG_END(5);
}

constexpr int K2_SLOT = 32768;
constexpr int K2_SF = 65536;
constexpr int K2_CV = 98304;
constexpr int CV_HA = 0, CV_HB = 4160, CV_KV = 8320, CV_BYTES = 8320 + 480 + 4096;
constexpr int K2_LX = K2_CV + 4 * CV_BYTES;
constexpr int K2_FL = K2_LX + 2048;
constexpr int K2_SCR = K2_FL + 256;
constexpr int K2_LDS = K2_SCR + 4096;
constexpr int F_X = 0, F_R = 32, F_W2 = 64, C_OST = 68, C_S1 = 76, C_S2 = 80, C_S3 = 128, C_ZST = 88, F_ZGO = 92,
              C_KST = 96, C_U0 = 100, C_LD = 108, F_KGO = 116, C_END = 120, C_OL = 72;
#define NG_IN 0
#define GATE_WORD C_END
#define GATE_VAL 0
#define X_NZ 4
#define X_NW3 8
#define ZP_GATE 4u
#define ZL_GATE 4
#define ZL_AT 31
#define Z_SLEEP0 0
#define O_SLEEP 0
#define Z_SLEEP 8
#define OWIN 16
#define OW0 8
#define WD 2
#define W3_AT 10
#define X_NOLDW 0
#define X_NOLDO 0
#define X_NOCHK 0
#define KROT 2
#define NG_LAST 0
#define PACE_IN "s_nop 7"
#define PACE_OUT "s_nop 1"
#define P3_PRIO 1
#define AUX_PRIO 2

__global__ __launch_bounds__(768, 3) void k2_attn(char* __restrict__ ws, const char* __restrict__ Aq,
                                                  const char* __restrict__ Ak, const char* __restrict__ Av,
                                                  const char* __restrict__ shiftk, const char* __restrict__ ssmk,
                                                  const unsigned* __restrict__ ssmD, const char* __restrict__ Wo,
                                                  char* __restrict__ out) {
    __shared__ __attribute__((aligned(16))) char lds_raw[K2_LDS];
    const lds_ptr lds = (lds_ptr)lds_raw;
    const lds_ptr fl = lds + K2_FL;
    const unsigned lds0 = (unsigned)(uintptr_t)lds;
    const int tid = threadIdx.x, lane = tid & 63;
    const int w = __builtin_amdgcn_readfirstlane(tid >> 6);
    const int b = blockIdx.x, xcd = b & 7, idx = b >> 3;
    const int h = xcd * 4 + (idx >> 3), qb = idx & 7;
    const int tt = xcd * 2 + (idx >> 4), nt = idx & 15;
    const char* kvg = ws + WS_KV + (size_t)h * 64 * 4096;
    const __amdgpu_buffer_rsrc_t rs = __builtin_amdgcn_make_buffer_rsrc(ws, 0, 32 << 20, 0x00020000);
    gu32* const cnt = (gu32*)(ws + WS_CNT);
    auto signal = [&]() {
        (void)__hip_atomic_fetch_add(cnt + ((lane & 31) * 8 + (lane < 32 ? xcd : qb)) * 64, 1u, RLX_AGENT);
    };
    const unsigned wvoff = (unsigned)(lane >> 3) * 2048u + nt * 128 + (((lane & 7) ^ (((lane >> 4) & 1) << 2)) << 4);
    auto wsrc = [&](int hf, int lp) { return Wo + (size_t)(hf * 64 + ((lp + nt * KROT * 2) & 63)) * 16384; };
    DG_DECL(1)

    if (w >= 8) {
        const int cw = w - 8;
        const unsigned voff = lane * 16;
        __builtin_amdgcn_s_setprio(3);
        auto issue = [&](int st) {
            const char* src = kvg + (st * 4 + cw) * 4096;
            const unsigned dst = lds0 + (st & 1) * K2_SLOT + cw * 4096;
#pragma unroll
            for (int i = 0; i < 4; ++i) glds16(voff, src + i * 1024, dst + i * 1024);
#pragma unroll
            for (int i = 0; i < 4; ++i) glds16(voff, src + 131072 + i * 1024, dst + 16384 + i * 1024);
        };
        issue(0);
        const int ch = xcd * 128 + idx * 4 + cw;
        const lds_ptr cv = lds + K2_CV + cw * CV_BYTES;
        {
            const u32x4 z4 = {0u, 0u, 0u, 0u};
            if (lane < 2) *(LAS u32x4*)(cv + CV_HA + lane * 16) = z4;
            if (lane < 30) *(LAS u32x4*)(cv + CV_KV + lane * 16) = z4;
            if (cw == 0 && lane < 16) *(LAS u32x4*)(fl + lane * 16) = z4;
        }
        WAIT_BAR(0);
        issue(1);
        const int c0 = xcd * 128 + cw * 32, cq = lane & 7, rg = lane >> 3;
        const int row0 = idx * 64 + rg * 8;
        const unsigned roff = (unsigned)row0 * 2048u + (c0 + cq * 4) * 2;
        const unsigned toffs = (unsigned)(c0 + (lane >> 3)) * 4096u + idx * 128;
        u32x4 sk[2];
        sk[0] = gld128((unsigned)(c0 + cq * 4) * 8u, shiftk); sk[1] = gld128((unsigned)(c0 + cq * 4) * 8u, shiftk + 16);
        unsigned dd = gld32((unsigned)(ch >> 1) * 4u, ssmD);
        auto stage = [&](const char* src, int dst) {
            const unsigned sbase = (unsigned)(idx * 64 + cw * 16 + (lane >> 4)) * 2048u + xcd * 256;
#pragma unroll
            for (int i = 0; i < 4; ++i)
                glds16(sbase + (((lane & 15) ^ (((2 * cw + (i >> 1)) & 3) << 2)) << 4), src + i * 8192, lds0 + dst + cw * 4096 + i * 1024);
        };
        auto blank = [&](unsigned off) {
            const u32x4 e4 = {EMPTY, EMPTY, EMPTY, EMPTY};
            CFENCE();
            st_sc1(e4, rs, off + voff); st_sc1(e4, rs, off + 1024 + voff);
            CFENCE();
        };
        WAIT_BAR(3);
        DG(12);
        issue(2);
        stage(Ak, K2_SF);
        glds16((unsigned)(idx * 64 - (idx ? 4 : 0) + (lane >> 4)) * 2048u + xcd * 256 + (((lane & 15) ^ 12) << 4), Ak, lds0 + K2_SCR + cw * 1024);
        blank((unsigned)WS_O + (unsigned)((qb * 8 + cw * 2) * 32 + h) * 2048u);
        WAIT_BAR(7);
        DG(9);
        issue(3);
        stage(Av, K2_SF + 16384);
        blank((unsigned)WS_O + (unsigned)((qb * 8 + cw * 2 + 1) * 32 + h) * 2048u);
        WAIT_BAR(6);
        DG(3);
        issue(4);
#pragma unroll
        for (int i = 0; i < 4; ++i) glds16(voff, ssmk + (size_t)ch * 4096 + i * 1024, lds0 + K2_CV + cw * CV_BYTES + CV_HA + 32 + i * 1024);
        blank((unsigned)WS_ZT + ch * 4096); blank((unsigned)WS_ZT + ch * 4096 + 2048);
        WAIT_BAR(8);
        DG(5);
        issue(5);
        SBAR();
        asm volatile("" : "+v"(sk[0]), "+v"(sk[1]), "+v"(dd));
        dd = __builtin_amdgcn_readfirstlane(dd);
        {
            u32x2 kr[11], vr[8];
            const lds_ptr kb = lds + K2_SF + rg * 2048 + ((cw ^ (rg & 3)) << 6) + cq * 8;
            const lds_ptr hb = lds + (rg ? K2_SF + rg * 2048 - 768 : K2_SCR + cw * 1024 + 256) + ((cw ^ ((rg + 3) & 3)) << 6) + cq * 8;
#pragma unroll
            for (int i = 0; i < 3; ++i) kr[i] = *(const LAS u32x2*)(hb + i * 256);
#pragma unroll
            for (int i = 0; i < 8; ++i) kr[3 + i] = *(const LAS u32x2*)(kb + i * 256);
#pragma unroll
            for (int i = 0; i < 8; ++i) vr[i] = *(const LAS u32x2*)(kb + 16384 + i * 256);
            if (row0 == 0) { kr[0] = u32x2{0u, 0u}; kr[1] = u32x2{0u, 0u}; kr[2] = u32x2{0u, 0u}; }
            const lds_ptr tb = cv + CV_KV + 480;
#pragma unroll
            for (int c = 0; c < 4; ++c) {
                const unsigned s01 = sk[c >> 1][(c & 1) * 2], s23 = sk[c >> 1][(c & 1) * 2 + 1];
                const float w0 = bflo(s01), w1 = bfhi(s01), w2 = bflo(s23), w3 = bfhi(s23);
                float kf[11], kv[8];
#pragma unroll
                for (int i = 0; i < 11; ++i) kf[i] = (c & 1) ? bfhi(kr[i][c >> 1]) : bflo(kr[i][c >> 1]);
#pragma unroll
                for (int i = 0; i < 8; ++i) {
                    float a = mul_vv(w3, kf[i]);
                    fmac_vv(a, w2, kf[i + 1]); fmac_vv(a, w1, kf[i + 2]); fmac_vv(a, w0, kf[i + 3]);
                    kv[i] = mul_vv(a, (c & 1) ? bfhi(vr[i][c >> 1]) : bflo(vr[i][c >> 1]));
                }
                u32x4 o4;
#pragma unroll
                for (int i = 0; i < 4; ++i) o4[i] = cvtpk(kv[7 - 2 * i], kv[6 - 2 * i]);
                *(LAS u32x4*)(tb + (cq * 4 + c) * 128 + ((rg ^ cq) << 4)) = o4;
            }
#pragma unroll
            for (int i = 0; i < 4; ++i) {
                const u32x4 t = *(const LAS u32x4*)(tb + i * 1024 + lane * 16);
                st_sc1(t, rs, (unsigned)WS_KVR + toffs + i * 32768 + (((lane & 7) ^ (2 * i + (lane >> 5))) << 4));
            }
        }
        CFENCE();
        DG(7);
        WAIT_BAR(4);
        DG(6);
        issue(6);
        asm volatile("s_waitcnt vmcnt(8)" ::: "memory");
        if (lds_add_rtn(fl + C_KST, lane) == 3u) signal();
#pragma unroll
        for (int i = 0; i < 8; ++i)
            glds16(wvoff, wsrc(i >> 2, cw * 4 + (i & 3)), lds0 + K2_SF + (i >> 2) * 16384 + (cw * 4 + (i & 3)) * 1024);
        {
            const u32x4 z4 = {0u, 0u, 0u, 0u};
            if (lane < 2) *(LAS u32x4*)(cv + CV_HB + lane * 16) = z4;
#pragma unroll
            for (int i = 0; i < 4; ++i) {
                const int off = 32 + i * 1024 + lane * 16;
                const u32x4 hv = *(const LAS u32x4*)(cv + CV_HA + off);
                const unsigned pv = *(const LAS unsigned*)(cv + CV_HA + off - 4);
                u32x4 sh;
                sh[0] = __builtin_amdgcn_alignbit(hv[0], pv, 16);
                sh[1] = __builtin_amdgcn_alignbit(hv[1], hv[0], 16);
                sh[2] = __builtin_amdgcn_alignbit(hv[2], hv[1], 16);
                sh[3] = __builtin_amdgcn_alignbit(hv[3], hv[2], 16);
                *(LAS u32x4*)(cv + CV_HB + off) = sh;
            }
        }
        WAIT_BAR(8);
        DG(8);
        issue(7);
#pragma unroll
        for (int i = 0; i < 8; ++i)
            glds16(wvoff, wsrc(idx >> 4, 32 + cw * 8 + i), lds0 + K2_SCR + cw * 1024);
        WAIT_BAR(8);
        DG(11);
        if (cw == 0) { poll_ge(cnt + b * 64, 64u); lds_add(fl + F_KGO, lane); }
        else lds_wait_ge(fl + F_KGO, 1u);
        {
            u32x4 t[4];
#pragma unroll
            for (int i = 0; i < 4; ++i) t[i] = ld_sc1(rs, (unsigned)WS_KVR + ch * 4096 + i * 1024 + voff);
#pragma unroll
            for (int i = 0; i < 4; ++i) *(LAS u32x4*)(cv + CV_KV + 480 + i * 1024 + lane * 16) = t[i];
        }
        const int r1 = lane & 15, q = lane >> 4;
        const int toff = (r1 & 1) ? CV_HA + 2 * (16 + r1 + 9 - 8 * q) : CV_HB + 2 * (16 + r1 + 10 - 8 * q);
        const int boff = CV_KV + 448 + 32 * r1 + 16 * q;
        f32x4 y[8];
        u32x4 tf[8];
        bf16x8 bf[8];
        const lds_ptr bp = cv + boff - 448;
        lds_ptr tp[4];
#pragma unroll
        for (int i = 0; i < 4; ++i) { tp[i] = cv + toff + 1024 * i; asm volatile("" : "+v"(tp[i])); }
#define LDT(bb, a, hf) do { const LAS unsigned* t_ = (const LAS unsigned*)(tp[(a) >> 1] + 64 * (bb) + 512 * ((a) & 1) + 8 * (hf)); \
            tf[a][2 * (hf)] = t_[0]; tf[a][2 * (hf) + 1] = t_[1]; SBAR(); } while (0)
#define LDB(bb, a) do { bf[a] = lds_rd128(bp + 448 - 64 * (bb) + 512 * (a)); SBAR(); } while (0)
#define CM(a, ww) do { asm volatile("v_mfma_f32_16x16x32_bf16 %0, %1, %2, %0" : "+v"(y[(a) + (ww)]) : "v"(tf[a]), "v"(bf[ww])); SBAR(); } while (0)
#define CM0(a, ww) do { asm volatile("v_mfma_f32_16x16x32_bf16 %0, %1, %2, 0" : "=&v"(y[(a) + (ww)]) : "v"(tf[a]), "v"(bf[ww])); SBAR(); } while (0)
#define PC() do { asm volatile(PACE_OUT); SBAR(); } while (0)
#pragma unroll
        for (int a = 0; a < 8; ++a) { if (a < 5) { LDT(0, a, 0); LDT(0, a, 1); } LDB(0, 7 - a); }
#pragma unroll
        for (int i = 0; i < 8; ++i)
            glds16(wvoff, wsrc(i >> 2, 16 + cw * 4 + (i & 3)), lds0 + (i >> 2) * 16384 + (cw * 4 + (i & 3)) * 1024);
#pragma unroll
        for (int i = 0; i < 4; ++i)
            glds16((unsigned)(idx * 64 + cw * 16 + (lane >> 4)) * 2048u + xcd * 256 + (lane & 15) * 16, Aq + i * 8192, lds0 + K2_SCR + cw * 1024);
        RAW_BAR();
        asm volatile("s_waitcnt vmcnt(4)" ::: "memory");
        lds_add(fl + C_S1, lane);
        __builtin_amdgcn_s_setprio(AUX_PRIO);
        const float dch = (ch & 1) ? bfhi(dd) : bflo(dd);
        const lds_ptr kdp = cv + CV_KV + 480 + 32 * r1 + 16 * (q >> 1) + ((q & 1) ? 0 : 8);
        float kd[8][4];
        u32x2 kk;
        unsigned kc0, kc1, zo[8][2];
#define KD_RD(t) do { kk = *(const LAS u32x2*)(kdp + 512 * (t)); SBAR(); } while (0)
#define KD_UP0(t) do { kd[t][0] = bfhi(kk[1]); kd[t][1] = bflo(kk[1]); SBAR(); } while (0)
#define KD_UP1(t) do { kd[t][2] = bfhi(kk[0]); kd[t][3] = bflo(kk[0]); SBAR(); } while (0)
#define KD_MU0(t) do { kd[t][0] = mul_sv(dch, kd[t][0]); kd[t][1] = mul_sv(dch, kd[t][1]); SBAR(); } while (0)
#define KD_MU1(t) do { kd[t][2] = mul_sv(dch, kd[t][2]); kd[t][3] = mul_sv(dch, kd[t][3]); SBAR(); } while (0)
#define KD_CV(t) do { kc0 = cvtpk(kd[t][0], kd[t][1]); kc1 = cvtpk(kd[t][2], kd[t][3]); SBAR(); } while (0)
#define KD_X0(t) do { kd[t][0] = bflo(kc0); kd[t][1] = bfhi(kc0); SBAR(); } while (0)
#define KD_X1(t) do { kd[t][2] = bflo(kc1); kd[t][3] = bfhi(kc1); SBAR(); } while (0)
#define ZF0(t) do { kc0 = cvtpk(y[t][0], y[t][1]); kc1 = cvtpk(y[t][2], y[t][3]); SBAR(); } while (0)
#define ZF1(t) do { y[t][0] = bflo(kc0); y[t][1] = bfhi(kc0); SBAR(); } while (0)
#define ZF2(t) do { y[t][2] = bflo(kc1); y[t][3] = bfhi(kc1); SBAR(); } while (0)
#define ZF3(t) do { y[t][0] = add_vv(y[t][0], kd[t][0]); y[t][1] = add_vv(y[t][1], kd[t][1]); SBAR(); } while (0)
#define ZF4(t) do { y[t][2] = add_vv(y[t][2], kd[t][2]); y[t][3] = add_vv(y[t][3], kd[t][3]); SBAR(); } while (0)
#define ZF5(t) do { zo[t][0] = nz(cvtpk(y[t][0], y[t][1])); zo[t][1] = nz(cvtpk(y[t][2], y[t][3])); SBAR(); } while (0)
#define ZST(pr) do { auto s0 = __builtin_amdgcn_permlane16_swap(zo[2 * (pr)][0], zo[2 * (pr) + 1][0], false, false); \
            auto s1 = __builtin_amdgcn_permlane16_swap(zo[2 * (pr)][1], zo[2 * (pr) + 1][1], false, false); \
            st_sc1(u32x4{s0[0], s1[0], s0[1], s1[1]}, rs, (unsigned)WS_ZT + ch * 4096 + (pr) * 1024 + voff); SBAR(); } while (0)
        CM0(0, 7); LDB(1, 7); CM0(0, 6); LDT(0, 5, 0); CM0(0, 5); LDT(0, 5, 1); CM0(0, 4); LDT(0, 6, 0); CM0(0, 3); LDT(0, 6, 1); CM0(0, 2); LDT(0, 7, 0); CM0(0, 1); LDT(0, 7, 1); CM0(0, 0); KD_RD(0);
        CM(1, 6); LDB(1, 6); CM(1, 5); LDT(1, 0, 0); CM(1, 4); LDT(1, 0, 1); CM(1, 3); PC(); CM(1, 2); PC(); CM(1, 1); KD_UP0(0); CM(1, 0); KD_UP1(0);
        CM(2, 5); LDB(1, 5); CM(2, 4); LDT(1, 1, 0); CM(2, 3); LDT(1, 1, 1); CM(2, 2); KD_MU0(0); CM(2, 1); KD_MU1(0); CM(2, 0); KD_CV(0);
        CM(3, 4); LDB(1, 4); CM(3, 3); LDT(1, 2, 0); CM(3, 2); LDT(1, 2, 1); CM(3, 1); KD_X0(0); CM(3, 0); KD_X1(0);
        CM(4, 3); LDB(1, 3); CM(4, 2); LDT(1, 3, 0); CM(4, 1); LDT(1, 3, 1); CM(4, 0); KD_RD(1);
        CM(5, 2); LDB(1, 2); CM(5, 1); LDT(1, 4, 0); CM(5, 0); LDT(1, 4, 1);
        CM(6, 1); LDB(1, 1); CM(6, 0); PC();
        CM(7, 0); LDB(1, 0);
        CM(0, 7); LDB(2, 7); CM(0, 6); LDT(1, 5, 0); CM(0, 5); LDT(1, 5, 1); CM(0, 4); LDT(1, 6, 0); CM(0, 3); LDT(1, 6, 1); CM(0, 2); LDT(1, 7, 0); CM(0, 1); LDT(1, 7, 1); CM(0, 0); PC();
        CM(1, 6); LDB(2, 6); CM(1, 5); LDT(2, 0, 0); CM(1, 4); LDT(2, 0, 1); CM(1, 3); KD_UP0(1); CM(1, 2); KD_UP1(1); CM(1, 1); KD_MU0(1); CM(1, 0); KD_MU1(1);
        CM(2, 5); LDB(2, 5); CM(2, 4); LDT(2, 1, 0); CM(2, 3); LDT(2, 1, 1); CM(2, 2); KD_CV(1); CM(2, 1); KD_X0(1); CM(2, 0); KD_X1(1);
        CM(3, 4); LDB(2, 4); CM(3, 3); LDT(2, 2, 0); CM(3, 2); LDT(2, 2, 1); CM(3, 1); KD_RD(2); CM(3, 0); PC();
        CM(4, 3); LDB(2, 3); CM(4, 2); LDT(2, 3, 0); CM(4, 1); LDT(2, 3, 1); CM(4, 0); PC();
        CM(5, 2); LDB(2, 2); CM(5, 1); LDT(2, 4, 0); CM(5, 0); LDT(2, 4, 1);
        CM(6, 1); LDB(2, 1); CM(6, 0); KD_UP0(2);
        CM(7, 0); LDB(2, 0);
        CM(0, 7); LDB(3, 7); CM(0, 6); LDT(2, 5, 0); CM(0, 5); LDT(2, 5, 1); CM(0, 4); LDT(2, 6, 0); CM(0, 3); LDT(2, 6, 1); CM(0, 2); LDT(2, 7, 0); CM(0, 1); LDT(2, 7, 1); CM(0, 0); KD_UP1(2);
        CM(1, 6); LDB(3, 6); CM(1, 5); LDT(3, 0, 0); CM(1, 4); LDT(3, 0, 1); CM(1, 3); KD_MU0(2); CM(1, 2); KD_MU1(2); CM(1, 1); KD_CV(2); CM(1, 0); KD_X0(2);
        CM(2, 5); LDB(3, 5); CM(2, 4); LDT(3, 1, 0); CM(2, 3); LDT(3, 1, 1); CM(2, 2); KD_X1(2); CM(2, 1); KD_RD(3); CM(2, 0); PC();
        CM(3, 4); LDB(3, 4); CM(3, 3); LDT(3, 2, 0); CM(3, 2); LDT(3, 2, 1); CM(3, 1); PC(); CM(3, 0); KD_UP0(3);
        CM(4, 3); LDB(3, 3); CM(4, 2); LDT(3, 3, 0); CM(4, 1); LDT(3, 3, 1); CM(4, 0); KD_UP1(3);
        CM(5, 2); LDB(3, 2); CM(5, 1); LDT(3, 4, 0); CM(5, 0); LDT(3, 4, 1);
        CM(6, 1); LDB(3, 1); CM(6, 0); KD_MU0(3);
        CM(7, 0); LDB(3, 0);
        CM(0, 7); LDB(4, 7); CM(0, 6); LDT(3, 5, 0); CM(0, 5); LDT(3, 5, 1); CM(0, 4); LDT(3, 6, 0); CM(0, 3); LDT(3, 6, 1); CM(0, 2); LDT(3, 7, 0); CM(0, 1); LDT(3, 7, 1); CM(0, 0); KD_MU1(3);
        CM(1, 6); LDB(4, 6); CM(1, 5); LDT(4, 0, 0); CM(1, 4); LDT(4, 0, 1); CM(1, 3); KD_CV(3); CM(1, 2); KD_X0(3); CM(1, 1); KD_X1(3); CM(1, 0); KD_RD(4);
        CM(2, 5); LDB(4, 5); CM(2, 4); LDT(4, 1, 0); CM(2, 3); LDT(4, 1, 1); CM(2, 2); PC(); CM(2, 1); PC(); CM(2, 0); KD_UP0(4);
        CM(3, 4); LDB(4, 4); CM(3, 3); LDT(4, 2, 0); CM(3, 2); LDT(4, 2, 1); CM(3, 1); KD_UP1(4); CM(3, 0); KD_MU0(4);
        CM(4, 3); LDB(4, 3); CM(4, 2); LDT(4, 3, 0); CM(4, 1); LDT(4, 3, 1); CM(4, 0); KD_MU1(4);
        CM(5, 2); LDB(4, 2); CM(5, 1); LDT(4, 4, 0); CM(5, 0); LDT(4, 4, 1);
        CM(6, 1); LDB(4, 1); CM(6, 0); KD_CV(4);
        CM(7, 0); LDB(4, 0);
        CM(0, 7); LDB(5, 7); CM(0, 6); LDT(4, 5, 0); CM(0, 5); LDT(4, 5, 1); CM(0, 4); LDT(4, 6, 0); CM(0, 3); LDT(4, 6, 1); CM(0, 2); LDT(4, 7, 0); CM(0, 1); LDT(4, 7, 1); CM(0, 0); KD_X0(4);
        CM(1, 6); LDB(5, 6); CM(1, 5); LDT(5, 0, 0); CM(1, 4); LDT(5, 0, 1); CM(1, 3); KD_X1(4); CM(1, 2); KD_RD(5); CM(1, 1); PC(); CM(1, 0); PC();
        CM(2, 5); LDB(5, 5); CM(2, 4); LDT(5, 1, 0); CM(2, 3); LDT(5, 1, 1); CM(2, 2); KD_UP0(5); CM(2, 1); KD_UP1(5); CM(2, 0); KD_MU0(5);
        CM(3, 4); LDB(5, 4); CM(3, 3); LDT(5, 2, 0); CM(3, 2); LDT(5, 2, 1); CM(3, 1); KD_MU1(5); CM(3, 0); KD_CV(5);
        CM(4, 3); LDB(5, 3); CM(4, 2); LDT(5, 3, 0); CM(4, 1); LDT(5, 3, 1); CM(4, 0); KD_X0(5);
        CM(5, 2); LDB(5, 2); CM(5, 1); LDT(5, 4, 0); CM(5, 0); LDT(5, 4, 1);
        CM(6, 1); LDB(5, 1); CM(6, 0); KD_X1(5);
        CM(7, 0); LDB(5, 0);
        CM(0, 7); LDB(6, 7); CM(0, 6); LDT(5, 5, 0); CM(0, 5); LDT(5, 5, 1); CM(0, 4); LDT(5, 6, 0); CM(0, 3); LDT(5, 6, 1); CM(0, 2); LDT(5, 7, 0); CM(0, 1); LDT(5, 7, 1); CM(0, 0); KD_RD(6);
        CM(1, 6); LDB(6, 6); CM(1, 5); LDT(6, 0, 0); CM(1, 4); LDT(6, 0, 1); CM(1, 3); PC(); CM(1, 2); PC(); CM(1, 1); KD_UP0(6); CM(1, 0); KD_UP1(6);
        CM(2, 5); LDB(6, 5); CM(2, 4); LDT(6, 1, 0); CM(2, 3); LDT(6, 1, 1); CM(2, 2); KD_MU0(6); CM(2, 1); KD_MU1(6); CM(2, 0); KD_CV(6);
        CM(3, 4); LDB(6, 4); CM(3, 3); LDT(6, 2, 0); CM(3, 2); LDT(6, 2, 1); CM(3, 1); KD_X0(6); CM(3, 0); KD_X1(6);
        CM(4, 3); LDB(6, 3); CM(4, 2); LDT(6, 3, 0); CM(4, 1); LDT(6, 3, 1); CM(4, 0); KD_RD(7);
        CM(5, 2); LDB(6, 2); CM(5, 1); LDT(6, 4, 0); CM(5, 0); LDT(6, 4, 1);
        CM(6, 1); LDB(6, 1); CM(6, 0); PC();
        CM(7, 0); LDB(6, 0);
        CM(0, 7); LDB(7, 7); CM(0, 6); LDT(6, 5, 0); CM(0, 5); LDT(6, 5, 1); CM(0, 4); LDT(6, 6, 0); CM(0, 3); LDT(6, 6, 1); CM(0, 2); LDT(6, 7, 0); CM(0, 1); LDT(6, 7, 1); CM(0, 0); PC();
        CM(1, 6); LDB(7, 6); CM(1, 5); LDT(7, 0, 0); CM(1, 4); LDT(7, 0, 1); CM(1, 3); KD_UP0(7); CM(1, 2); KD_UP1(7); CM(1, 1); KD_MU0(7); CM(1, 0); KD_MU1(7);
        CM(2, 5); LDB(7, 5); CM(2, 4); LDT(7, 1, 0); CM(2, 3); LDT(7, 1, 1); CM(2, 2); KD_CV(7); CM(2, 1); KD_X0(7); CM(2, 0); KD_X1(7);
        CM(3, 4); LDB(7, 4); CM(3, 3); LDT(7, 2, 0); CM(3, 2); LDT(7, 2, 1); CM(3, 1); PC(); CM(3, 0); PC();
        CM(4, 3); LDB(7, 3); CM(4, 2); LDT(7, 3, 0); CM(4, 1); LDT(7, 3, 1); CM(4, 0); PC();
        CM(5, 2); LDB(7, 2); CM(5, 1); LDT(7, 4, 0); CM(5, 0); LDT(7, 4, 1);
        CM(6, 1); LDB(7, 1); CM(6, 0); PC();
        CM(7, 0); LDB(7, 0);
        CM(0, 3); LDT(7, 5, 0); CM(0, 2); LDT(7, 5, 1); CM(1, 2); LDT(7, 6, 0); CM(1, 1); LDT(7, 6, 1); CM(2, 1); LDT(7, 7, 0); CM(2, 0); LDT(7, 7, 1);
        CM(3, 0); PC(); CM(0, 1); PC(); CM(0, 0); PC(); CM(1, 0); ZF0(2);  CM(0, 5); ZF1(2);  CM(0, 4); ZF2(2);
        CM(1, 4); ZF3(2);  CM(1, 3); ZF4(2);  CM(2, 3); ZF5(2);  CM(2, 2); ZF0(3);  CM(3, 2); ZF1(3);  CM(3, 1); ZF2(3);
        CM(4, 1); ZF3(3);  CM(4, 0); ZF4(3);  CM(5, 0); ZF5(3);  CM(0, 7); ZST(1);  CM(0, 6); ZF0(0);  CM(1, 6); ZF1(0);
        CM(1, 5); ZF2(0);  CM(2, 5); ZF3(0);  CM(2, 4); ZF4(0);  CM(3, 4); ZF5(0);  CM(3, 3); ZF0(1);  CM(4, 3); ZF1(1);
        CM(4, 2); ZF2(1);  CM(5, 2); ZF3(1);  CM(5, 1); ZF4(1);  CM(6, 1); ZF5(1);  CM(6, 0); ZST(0);  CM(7, 0); ZF0(4);
        DG(4);
        ZF1(4); ZF2(4); ZF3(4); ZF4(4); ZF5(4); ZF0(5); ZF1(5); ZF2(5); ZF3(5); ZF4(5); ZF5(5); ZST(2); ZF0(6); ZF1(6); ZF2(6); ZF3(6); ZF4(6); ZF5(6); ZF0(7); ZF1(7); ZF2(7); ZF3(7); ZF4(7); ZF5(7); ZST(3);
        DG(10);
        __builtin_amdgcn_s_setprio(3);
        while ((unsigned)__builtin_amdgcn_readfirstlane(*(volatile LAS unsigned*)(fl + C_OL)) < ZL_GATE) __builtin_amdgcn_s_sleep(4);
        CFENCE();
        u32x2 qr[8];
#pragma unroll
        for (int i = 0; i < 8; ++i) qr[i] = gld64(roff, Aq + i * 2048);
        {
            const lds_ptr tr = cv;
            u32x4 t[4];
            const unsigned zb = (unsigned)WS_ZT + (unsigned)(c0 + (lane >> 3)) * 4096u + (idx >> 3) * 1024 + ((idx & 3) * 4 + ((idx >> 2) & 1) * 16) * 16;
            for (unsigned spins = 0; spins < (1u << 20); ++spins) {
#pragma unroll
                for (int i = 0; i < 4; ++i) {
                    const int ck = (lane & 7) ^ (2 * i + (lane >> 5));
                    t[i] = ld_sc1(rs, zb + i * 32768 + ((ck >> 1) + (ck & 1) * 32) * 16);
                }
                if (!missing(mx4(mx4(mx8(t[0], t[1]), t[2]), t[3]))) break;
                __builtin_amdgcn_s_sleep(Z_SLEEP);
            }
            DG(13);
#pragma unroll
            for (int i = 0; i < 4; ++i) *(LAS u32x4*)(tr + i * 1024 + lane * 16) = t[i];
#pragma unroll
            for (int i = 0; i < 8; ++i) asm volatile("" : "+v"(qr[i]));
            u32x4 zc[4];
#pragma unroll
            for (int c = 0; c < 4; ++c) zc[c] = *(const LAS u32x4*)(tr + (cq * 4 + c) * 128 + ((rg ^ cq) << 4));
            char* dst = out + (size_t)row0 * 4096 + (c0 + cq * 4) * 2;
#pragma unroll
            for (int i = 0; i < 8; ++i) {
                float zz[4];
#pragma unroll
                for (int c = 0; c < 4; ++c) zz[c] = (i & 1) ? bfhi(zc[c][i >> 1]) : bflo(zc[c][i >> 1]);
                u32x2 o = {cvtpk(mul_vv(bflo(qr[i][0]), zz[0]), mul_vv(bfhi(qr[i][0]), zz[1])),
                           cvtpk(mul_vv(bflo(qr[i][1]), zz[2]), mul_vv(bfhi(qr[i][1]), zz[3]))};
                *(u32x2*)(dst + i * 4096) = o;
            }
        }
        DG_END(14);
        return;
    }

    const int half = w >> 2;
    const int TA = qb * 8 + (w & 3) * 2 + half, TB = TA ^ 1;
    const char* qga = ws + WS_Q + (size_t)(h * 64 + TA) * 2048 + lane * 16;
    const char* qgb = ws + WS_Q + (size_t)(h * 64 + TB) * 2048 + lane * 16;
    const bf16x8 qa0 = *(const bf16x8*)qga, qa1 = *(const bf16x8*)(qga + 1024);
    const bf16x8 qb0 = *(const bf16x8*)qgb, qb1 = *(const bf16x8*)(qgb + 1024);
    bf16x8 sel;
    {
        const short one = (((lane >> 2) & 1) == ((lane >> 4) & 1)) ? (short)0x3f80 : (short)0;
#pragma unroll
        for (int i = 0; i < 8; ++i) sel[i] = one;
    }
    f32x16 oA = f32x16{}, oB = f32x16{};
    f32x4 lA = f32x4{}, lB = f32x4{};
    f32x16 negm, sA, sB;
    const lds_ptr lp = lds + half * 16384 + lane * 16;
    bf16x8 k0[2], k1[2], v0[2], v1[2];
    u32x4 p0, p1 = {0u, 0u, 0u, 0u};
#define EX(X, i) X[i] = __builtin_amdgcn_exp2f(X[i])
    LGKM_BAR();
    DG(3);
    {
        const bf16x8 x0 = lds_rd128(lds + lane * 16), x1 = lds_rd128(lds + lane * 16 + 1024);
        k0[0] = lds_rd128(lp); k1[0] = lds_rd128(lp + 1024);
        v0[0] = lds_rd128(lp + 2048); v1[0] = lds_rd128(lp + 3072);
        sA = MFMA32(x0, qa0, f32x16{});
        sB = MFMA32(x0, qb0, f32x16{});
        sA = MFMA32(x1, qa1, sA);
        sB = MFMA32(x1, qb1, sB);
        float m = __builtin_fmaxf(sA[0], sB[0]);
#pragma unroll
        for (int i = 1; i < 16; ++i) m = __builtin_fmaxf(m, __builtin_fmaxf(sA[i], sB[i]));
        auto rr = __builtin_amdgcn_permlane32_swap(__float_as_uint(m), __float_as_uint(m), false, false);
        m = __builtin_fmaxf(__uint_as_float(rr[0]), __uint_as_float(rr[1]));
#pragma unroll
        for (int i = 0; i < 16; ++i) negm[i] = -m;
        sA = MFMA32(k0[0], qa0, negm);
        sA = MFMA32(k1[0], qa1, sA);
        v1[1] = v1[0];
        EX(sA, 0); EX(sA, 1);
    }
#define SUM16(l, p) asm volatile("v_mfma_f32_16x16x32_bf16 %0, %1, %2, %0" : "+v"(l) : "v"(sel), "v"(p))
#define HALF(C, oC, lC, N, qx, qy, ka, kb, oP, lP, vprev, vcur0, PRE) do { \
        oP = MFMA32(vprev, PB(p1), oP); SBAR(); \
        PRE; EX(C, 2); EX(C, 3); EX(C, 4); SBAR(); \
        N = MFMA32(ka, qx, negm); SBAR(); \
        EX(C, 5); EX(C, 6); EX(C, 7); SBAR(); \
        SUM16(lP, p1); SBAR(); \
        p0[0] = cvtpk(C[0], C[1]); p0[1] = cvtpk(C[2], C[3]); SBAR(); \
        N = MFMA32(kb, qy, N); SBAR(); \
        p0[2] = cvtpk(C[4], C[5]); p0[3] = cvtpk(C[6], C[7]); SBAR(); EX(C, 8); EX(C, 9); SBAR(); \
        oC = MFMA32(vcur0, PB(p0), oC); SBAR(); \
        EX(C, 10); EX(C, 11); EX(C, 12); SBAR(); \
        SUM16(lC, p0); SBAR(); \
        EX(C, 13); EX(C, 14); EX(C, 15); SBAR(); \
        p1[0] = cvtpk(C[8], C[9]); p1[1] = cvtpk(C[10], C[11]); p1[2] = cvtpk(C[12], C[13]); p1[3] = cvtpk(C[14], C[15]); SBAR(); \
        EX(N, 0); EX(N, 1); SBAR(); \
    } while (0)
#define RD4(Y, nx) do { v1[Y] = lds_rd128((nx) + 3072); v0[Y] = lds_rd128((nx) + 2048); \
        k1[Y] = lds_rd128((nx) + 1024); k0[Y] = lds_rd128(nx); } while (0)
#define KT(X, nx, PRE0) do { \
        HALF(sA, oA, lA, sB, qb0, qb1, k0[X], k1[X], oB, lB, v1[X ^ 1], v0[X], PRE0; RD4(X ^ 1, nx)); \
        HALF(sB, oB, lB, sA, qa0, qa1, k0[X ^ 1], k1[X ^ 1], oA, lA, v1[X], v0[X], (void)0); \
    } while (0)
    SBAR();
#pragma unroll 1
    for (int st = 0; st < 8; ++st) {
        const lds_ptr cur = lp + (st & 1) * K2_SLOT;
        const lds_ptr nxt = lp + ((st + 1) & 1) * K2_SLOT;
        PRIO(2);
        KT(0, cur + 4096, (void)0);
        PRIO(1);
        KT(1, cur + 8192, (void)0);
        PRIO(0);
        KT(0, cur + 12288, (void)0);
        PRIO(3);
        KT(1, nxt, RAW_BAR(); SBAR());
    }
    oB = MFMA32(v1[1], PB(p1), oB);
    SBAR();
    SUM16(lB, p1);
    asm volatile("s_nop 7" : "+v"(oB) :: "memory");
    SBAR();
    DG(4);
    {
        const lds_ptr xo = lds + K2_SLOT + w * 4096 + lane * 16;
        *(LAS float*)(lds + K2_LX + w * 256 + lane * 4) = lB[0];
#pragma unroll
        for (int i = 0; i < 4; ++i) *(LAS f32x4*)(xo + i * 1024) = f32x4{oB[4 * i], oB[4 * i + 1], oB[4 * i + 2], oB[4 * i + 3]};
        lds_add(fl + F_X + w * 4, lane);
        lds_add(fl + C_END + half * 4, lane);
        lds_wait_ge(fl + F_X + (w ^ 4) * 4, 1u);
        const lds_ptr xi = lds + K2_SLOT + (w ^ 4) * 4096 + lane * 16;
        f32x4 t[4];
        const float lx = *(const LAS float*)(lds + K2_LX + (w ^ 4) * 256 + lane * 4);
#pragma unroll
        for (int i = 0; i < 4; ++i) t[i] = *(const LAS f32x4*)(xi + i * 1024);
        SBAR();
        lA[0] += lx;
#pragma unroll
        for (int i = 0; i < 16; ++i) { float o = oA[i]; acc_vv(o, t[i >> 2][i & 3]); oA[i] = o; }
    }
    DG(9);
    const float l = lA[0];
    u32x4 on0, on1;
    auto finish = [&](float ls) {
        float rl = __builtin_amdgcn_rcpf(ls);
        asm volatile("s_nop 1" : "+v"(rl));
        f32x16 o;
#pragma unroll
        for (int i = 0; i < 16; ++i) o[i] = mul_vv(oA[i], rl);
        on0 = nz4(pack8n(o, 0)); on1 = nz4(pack8n(o, 1));
    };
    finish(l);
    {
        float t0, t1;
        asm("v_mul_f32_e32 %0, 0, %2\n\tv_mul_f32_e32 %1, 0, %3\n\tv_fmac_f32_e32 %0, 0, %4\n\tv_fmac_f32_e32 %1, 0, %5\n\tv_fmac_f32_e32 %0, 0, %6\n\tv_fmac_f32_e32 %1, 0, %7\n\tv_fmac_f32_e32 %0, 0, %8\n\tv_fmac_f32_e32 %1, 0, %9\n\t"
            "v_fmac_f32_e32 %0, 0, %10\n\tv_fmac_f32_e32 %1, 0, %11\n\tv_fmac_f32_e32 %0, 0, %12\n\tv_fmac_f32_e32 %1, 0, %13\n\tv_fmac_f32_e32 %0, 0, %14\n\tv_fmac_f32_e32 %1, 0, %15\n\tv_fmac_f32_e32 %0, 0, %16\n\tv_fmac_f32_e32 %1, 0, %17"
            : "=&v"(t0), "=&v"(t1) : "v"(oA[0]), "v"(oA[1]), "v"(oA[2]), "v"(oA[3]), "v"(oA[4]), "v"(oA[5]), "v"(oA[6]), "v"(oA[7]), "v"(oA[8]), "v"(oA[9]), "v"(oA[10]), "v"(oA[11]), "v"(oA[12]), "v"(oA[13]), "v"(oA[14]), "v"(oA[15]));
        const bool ok = (l <= 1.0e30f) && (l >= 1.0e-18f) && (t0 + t1 == 0.f);
        if (__builtin_expect(__any(!ok), 0)) {
            float m = -INFINITY, ls = 0.f;
            oA = f32x16{};
#pragma unroll 1
            for (int t = 0; t < 64; ++t) {
                const char* g = kvg + t * 4096 + lane * 16;
                const bf16x8 a0 = *(const bf16x8*)g, a1 = *(const bf16x8*)(g + 1024);
                const bf16x8 b0 = *(const bf16x8*)(g + 2048), b1 = *(const bf16x8*)(g + 3072);
                f32x16 s = MFMA32(a0, qa0, f32x16{});
                s = MFMA32(a1, qa1, s);
                float tm = s[0];
#pragma unroll
                for (int i = 1; i < 16; ++i) tm = __builtin_fmaxf(tm, s[i]);
                auto rr = __builtin_amdgcn_permlane32_swap(__float_as_uint(tm), __float_as_uint(tm), false, false);
                tm = __builtin_fmaxf(__uint_as_float(rr[0]), __uint_as_float(rr[1]));
                const float mn = __builtin_fmaxf(m, tm);
                const float alpha = __builtin_amdgcn_exp2f(m - mn);
                m = mn;
                float ps = 0.f;
#pragma unroll
                for (int i = 0; i < 16; ++i) { s[i] = __builtin_amdgcn_exp2f(s[i] - mn); ps += s[i]; oA[i] *= alpha; }
                ls = ls * alpha + ps;
                oA = MFMA32(b0, PB(pack8n(s, 0)), oA);
                oA = MFMA32(b1, PB(pack8n(s, 1)), oA);
            }
            auto rr = __builtin_amdgcn_permlane32_swap(__float_as_uint(ls), __float_as_uint(ls), false, false);
            asm volatile("s_nop 7\n\ts_nop 7" : "+v"(oA));
            finish(__uint_as_float(rr[0]) + __uint_as_float(rr[1]));
        }
    }
    u32x4 w3[4];
    {
        const unsigned og = (unsigned)WS_O + (unsigned)(TA * 32 + h) * 2048u + lane * 16;
        st_sc1(on0, rs, og);
        st_sc1(on1, rs, og + 1024);
        lds_add(fl + C_S1, lane);
        const char* s2 = wsrc(half ^ 1, 32 + (w & 3) * 4);
        const char* s3 = wsrc(half, 48 + (w & 3) * 4);
        lds_wait_ge(fl + C_S1, 12u);
        DG(5);
#pragma unroll
        for (int i = 0; i < 4; ++i)
            glds16(wvoff, s2 + i * 16384, lds0 + K2_SLOT + ((w ^ 4) * 4 + i) * 1024);
#pragma unroll
        for (int i = 0; i < 4; ++i) w3[i] = gld128(wvoff, s3 + i * 16384);
    }
    const int j4 = w & 3;
    {
        const int T3 = tt * 4 + j4;
        const unsigned og = (unsigned)WS_O + (unsigned)T3 * 65536u + half * 32768 + lane * 16;
        const int r = lane & 31, hh = lane >> 5;
        const int g = lane >> 4, q = (lane & 15) >> 2, p = lane & 3;
        int woff[2];
#pragma unroll
        for (int n = 0; n < 2; ++n) {
            const int c = 4 * (n ^ half) + 2 * (p & 1) + (g & 1);
            woff[n] = (4 * hh + q) * 128 + ((c ^ ((q >> 1) << 2)) << 4) + (p >> 1) * 8;
        }
        f32x16 acc[2] = {f32x16{}, f32x16{}};
        lds_ptr wl[2][2] = {{lds + half * 16384 + woff[0], lds + half * 16384 + woff[1]},
                            {lds + K2_SF + half * 16384 + woff[0], lds + K2_SF + half * 16384 + woff[1]}};
        lds_ptr w3d = lds + K2_SF + w * 4096 + lane * 16;
        asm volatile("" : "+v"(wl[0][0]), "+v"(wl[0][1]), "+v"(wl[1][0]), "+v"(wl[1][1]), "+v"(w3d));
        s16x4 wa[WD + 1][2], wb[WD + 1][2];
#define LDW(s, n) do { const int f_ = (s) < 8 || (s) >= 24, o_ = (f_ ? 0 : (((s) >> 3) - 1) * K2_SLOT) + ((s) & 7) * 2048; \
            wa[(s) % (WD + 1)][n] = lds_tr(wl[f_][n] + o_); wb[(s) % (WD + 1)][n] = lds_tr(wl[f_][n] + o_ + 1024); SBAR(); } while (0)
#pragma unroll
        for (int s = 0; s < WD; ++s) { LDW(s, 0); LDW(s, 1); }
        u32x4 of[OWIN];
#define LDO(i) do { of[(i) % OWIN] = ld_sc1s(rs, og, (((i) + nt * KROT) & 31) * 1024); SBAR(); } while (0)
        for (unsigned spins = 0; spins < (1u << 20); ++spins) {
#pragma unroll
            for (int i = 0; i < OW0; ++i) LDO(i);
            unsigned m = mx8(of[0], of[1]);
#pragma unroll
            for (int i = 2; i < OW0; ++i) m = mx4(m, of[i]);
            if (!missing(m)) break;
            DGC(12);
        }
        lds_add(fl + C_S2, lane);
#pragma unroll
        for (int i = OW0; i < OWIN; ++i) LDO(i);
        DG(6);
        __builtin_amdgcn_s_setprio(P3_PRIO);
        SBAR();
        unsigned fv = 0;
#define FLAG_RD(a) do { fv = *(volatile LAS unsigned*)(a); SBAR(); } while (0)
#define FLAG_CHK(a, n) do { if (__builtin_expect((unsigned)__builtin_amdgcn_readfirstlane(fv) < (n), 0)) lds_wait_ge(a, n); CFENCE(); SBAR(); } while (0)
#pragma unroll
        for (int s = 0; s < 32; ++s) {
            acc[0] = MFMA32(cat8(wa[s % (WD + 1)][0], wb[s % (WD + 1)][0]), PB(of[s % OWIN]), acc[0]);
            SBAR();
            if (s == 16 - WD) FLAG_CHK(fl + C_S2, 8u);
            if (s == 24 - WD) FLAG_CHK(fl + C_S3 + half * 4, 4u);
            if (s + WD < 32) LDW(s + WD, 0);
            if ((s & 1) && s + 1 >= OW0 && s + 1 < 32 && __builtin_expect(missing(mx8(of[(s + 1) % OWIN], of[(s + 2) % OWIN])), 0)) {
                for (unsigned spins = 0; spins < (1u << 20); ++spins) {
                    __builtin_amdgcn_s_sleep(2);
                    LDO(s + 1); LDO(s + 2);
                    DGC(13);
                    if (!missing(mx8(of[(s + 1) % OWIN], of[(s + 2) % OWIN]))) break;
                }
            }
            SBAR();
            acc[1] = MFMA32(cat8(wa[s % (WD + 1)][1], wb[s % (WD + 1)][1]), PB(of[s % OWIN]), acc[1]);
            SBAR();
            if (s + WD < 32) LDW(s + WD, 1);
            if (s + OWIN < 32) LDO(s + OWIN);
            if (s == 7 - WD) lds_add(fl + C_U0 + half * 4, lane);
            if (s == W3_AT - 2) FLAG_RD(fl + C_U0 + half * 4);
            if (s == 14 - WD) FLAG_RD(fl + C_S2);
            if (s == 22 - WD) FLAG_RD(fl + C_S3 + half * 4);
            if (s == W3_AT) FLAG_CHK(fl + C_U0 + half * 4, 4u);
            if (s >= W3_AT && s < W3_AT + 4) {
                asm volatile("" : "+v"(w3[(s - W3_AT) & 3]));
                *(LAS u32x4*)(w3d + ((s - W3_AT) & 3) * 1024) = w3[(s - W3_AT) & 3];
            }
            if (s == W3_AT + 3) lds_add(fl + C_S3 + half * 4, lane);
            if (ZL_GATE && s + OWIN == ZL_AT) lds_add(fl + C_OL, lane);
            SBAR();
            if (s == 0) DG(10);
            if (s == 15) DG(11);
            if (s == 23) DG(14);
        }
        DG(7);
        const lds_ptr xo = lds + w * 4096 + lane * 16;
#pragma unroll
        for (int i = 0; i < 4; ++i) *(LAS f32x4*)(xo + i * 1024) = f32x4{acc[1][4 * i], acc[1][4 * i + 1], acc[1][4 * i + 2], acc[1][4 * i + 3]};
        lds_add(fl + F_R + w * 4, lane);
        lds_wait_ge(fl + F_R + (w ^ 4) * 4, 1u);
        const lds_ptr xi = lds + (w ^ 4) * 4096 + lane * 16;
#pragma unroll
        for (int i = 0; i < 4; ++i) {
            const f32x4 t = *(const LAS f32x4*)(xi + i * 1024);
#pragma unroll
            for (int j = 0; j < 4; ++j) acc[0][4 * i + j] = add_vv(acc[0][4 * i + j], t[j]);
        }
        char* dst = out + (size_t)(T3 * 32 + r) * 4096 + 2048 + nt * 128 + half * 64 + hh * 32;
        *(u32x4*)(dst) = pack8n(acc[0], 0);
        *(u32x4*)(dst + 16) = pack8n(acc[0], 1);
    }
    DG_END(8);
}

extern "C" void kernel_launch(void* const* d_in, const int* in_sizes, int n_in,
                              void* d_out, int out_size, void* d_ws, size_t ws_size,
                              hipStream_t stream) {
    const void* A_q = d_in[0];
    const void* A_k = d_in[1];
    const void* A_v = d_in[2];
    const void* A_shift = d_in[3];
    const void* A_ssmk = d_in[4];
    const void* A_D = d_in[5];
    const void* B_x = d_in[6];
    const void* B_wqkv = d_in[7];
    const void* B_wo = d_in[8];
    char* ws = (char*)d_ws;
    k1_qkv<<<256, 512, 0, stream>>>((const char*)B_x, (const char*)B_wqkv, ws);
    k2_attn<<<256, 768, 0, stream>>>(ws, (const char*)A_q, (const char*)A_k, (const char*)A_v, (const char*)A_shift,
                                     (const char*)A_ssmk, (const unsigned*)A_D, (const char*)B_wo, (char*)d_out);
}
```

```cpp
#include <hip/hip_runtime.h>
#include <hip/hip_bf16.h>
#include <stdint.h>

typedef short bf16x8 __attribute__((ext_vector_type(8)));
typedef short s16x4 __attribute__((ext_vector_type(4)));
typedef float f32x16 __attribute__((ext_vector_type(16)));
typedef float f32x4 __attribute__((ext_vector_type(4)));
typedef float f32x2 __attribute__((ext_vector_type(2)));
typedef unsigned u32x4 __attribute__((ext_vector_type(4)));
typedef unsigned u32x2 __attribute__((ext_vector_type(2)));
typedef __bf16 bf16x2_t __attribute__((ext_vector_type(2)));

#define LAS __attribute__((address_space(3)))
typedef LAS char* lds_ptr;
#define DI __device__ __forceinline__
#define SBAR() __builtin_amdgcn_sched_barrier(0)
#define MFMA32(a, b, c) __builtin_amdgcn_mfma_f32_32x32x16_bf16((a), (b), (c), 0, 0, 0)
#define MFMA16(a, b, c) __builtin_amdgcn_mfma_f32_16x16x32_bf16((a), (b), (c), 0, 0, 0)
#define WAIT_BAR(N) asm volatile("s_waitcnt vmcnt(" #N ") lgkmcnt(0)\n\ts_barrier" ::: "memory")
#define LGKM_BAR() asm volatile("s_waitcnt lgkmcnt(0)\n\ts_barrier" ::: "memory")

#define DG_DECL(K)
#define DG(i)
#define DG_END(i)
#define DGC(i)

constexpr int S_LEN = 2048, DM = 1024, NH = 32;

constexpr size_t WS_Q = 0;
constexpr size_t WS_KV = 4u << 20;
constexpr size_t WS_O = 12u << 20;
constexpr size_t WS_KVR = 16u << 20;
constexpr size_t WS_ZT = 20u << 20;
constexpr size_t WS_CNT = 24u << 20;

DI unsigned cvtpk(float lo, float hi) {
    f32x2 v = {lo, hi};
    return __builtin_bit_cast(unsigned, __builtin_convertvector(v, bf16x2_t));
}
DI float bflo(unsigned u) { return __uint_as_float(u << 16); }
DI float bfhi(unsigned u) { return __uint_as_float(u & 0xffff0000u); }

DI void glds16(unsigned voff, const void* sbase, unsigned lds_dst) {
    unsigned sv;
    asm volatile("s_mov_b32 %0, m0\n\ts_mov_b32 m0, %3\n\ts_nop 0\n\tglobal_load_lds_dwordx4 %1, %2\n\ts_mov_b32 m0, %0"
                 : "=&s"(sv) : "v"(voff), "s"(sbase), "s"(lds_dst) : "memory");
}
DI bf16x8 lds_rd128(lds_ptr p) { return *(const LAS bf16x8*)p; }
DI s16x4 lds_tr(lds_ptr p) {
    return __builtin_bit_cast(s16x4, __builtin_amdgcn_ds_read_tr16_b64_v4i16((LAS s16x4*)p));
}
DI bf16x8 cat8(s16x4 a, s16x4 b) { return __builtin_shufflevector(a, b, 0, 1, 2, 3, 4, 5, 6, 7); }
DI u32x4 pack8(const f32x16& x, int s, float sc) {
    u32x4 p;
    p[0] = cvtpk(x[8 * s + 0] * sc, x[8 * s + 1] * sc);
    p[1] = cvtpk(x[8 * s + 2] * sc, x[8 * s + 3] * sc);
    p[2] = cvtpk(x[8 * s + 4] * sc, x[8 * s + 5] * sc);
    p[3] = cvtpk(x[8 * s + 6] * sc, x[8 * s + 7] * sc);
    return p;
}
DI u32x4 pack8n(const f32x16& x, int s) {
    u32x4 p;
    p[0] = cvtpk(x[8 * s + 0], x[8 * s + 1]);
    p[1] = cvtpk(x[8 * s + 2], x[8 * s + 3]);
    p[2] = cvtpk(x[8 * s + 4], x[8 * s + 5]);
    p[3] = cvtpk(x[8 * s + 6], x[8 * s + 7]);
    return p;
}
DI void rnd2(float& a, float& b) { unsigned u = cvtpk(a, b); a = bflo(u); b = bfhi(u); }

#define CONV_PACE()
#define RAW_BAR() asm volatile("s_barrier" ::: "memory")
DI unsigned gld32(unsigned voff, const void* sbase) {
    unsigned r;
    asm volatile("global_load_dword %0, %1, %2" : "=v"(r) : "v"(voff), "s"(sbase) : "memory");
    return r;
}
DI u32x2 gld64(unsigned voff, const void* sbase) {
    u32x2 r;
    asm volatile("global_load_dwordx2 %0, %1, %2" : "=v"(r) : "v"(voff), "s"(sbase) : "memory");
    return r;
}
DI float mul_sv(float s, float v) { float r; asm("v_mul_f32_e32 %0, %1, %2" : "=v"(r) : "s"(s), "v"(v)); return r; }
DI float mul_vv(float a, float b) { float r; asm("v_mul_f32_e32 %0, %1, %2" : "=v"(r) : "v"(a), "v"(b)); return r; }
DI float add_vv(float a, float b) { float r; asm("v_add_f32_e32 %0, %1, %2" : "=v"(r) : "v"(a), "v"(b)); return r; }
DI void acc_vv(float& a, float b) { asm("v_add_f32_e32 %0, %0, %1" : "+v"(a) : "v"(b)); }
DI void fmac_vv(float& acc, float a, float b) { asm("v_fmac_f32_e32 %0, %1, %2" : "+v"(acc) : "v"(a), "v"(b)); }
DI void fmac_sv(float& acc, float s, float v) { asm("v_fmac_f32_e32 %0, %1, %2" : "+v"(acc) : "s"(s), "v"(v)); }
typedef __attribute__((address_space(1))) unsigned gu32;
#define RLX_AGENT __ATOMIC_RELAXED, __HIP_MEMORY_SCOPE_AGENT
#define CFENCE() asm volatile("" ::: "memory")
#define DRAIN() asm volatile("s_waitcnt vmcnt(0)" ::: "memory")
constexpr int AUX_SC1 = 16;
DI u32x4 ld_sc1(__amdgpu_buffer_rsrc_t rs, unsigned off) { return __builtin_amdgcn_raw_buffer_load_b128(rs, (int)off, 0, AUX_SC1); }
DI u32x4 ld_sc1s(__amdgpu_buffer_rsrc_t rs, unsigned off, int soff) { return __builtin_amdgcn_raw_buffer_load_b128(rs, (int)off, soff, AUX_SC1); }
DI void st_sc1(u32x4 v, __amdgpu_buffer_rsrc_t rs, unsigned off) { __builtin_amdgcn_raw_buffer_store_b128(v, rs, (int)off, 0, AUX_SC1); }
constexpr unsigned EMPTY = 0xffffffffu;
DI unsigned nz(unsigned x) { return x < EMPTY - 1u ? x : EMPTY - 1u; }
DI u32x4 nz4(u32x4 v) { return u32x4{nz(v[0]), nz(v[1]), nz(v[2]), nz(v[3])}; }
DI float fmx(float a, float b) { return __builtin_elementwise_maximum(a, b); }
DI unsigned umax(unsigned a, unsigned b) { return a > b ? a : b; }
DI unsigned umax3(unsigned a, unsigned b, unsigned c) { return umax(umax(a, b), c); }
DI unsigned mx4(unsigned m, u32x4 v) { return umax3(umax3(m, v[0], v[1]), v[2], v[3]); }
DI unsigned mx8(u32x4 a, u32x4 b) { return umax(umax3(umax3(umax3(a[0], a[1], a[2]), a[3], b[0]), b[1], b[2]), b[3]); }
DI bool missing(unsigned m) { return __builtin_amdgcn_ballot_w64(m == EMPTY) != 0; }
DI unsigned lds_add_rtn(lds_ptr p, int lane) {
    unsigned old = 0;
    CFENCE();
    if (lane == 0) old = __hip_atomic_fetch_add((LAS unsigned*)p, 1u, __ATOMIC_RELAXED, __HIP_MEMORY_SCOPE_WORKGROUP);
    old = __builtin_amdgcn_readfirstlane(old);
    CFENCE();
    return old;
}
DI void lds_add(lds_ptr p, int lane) {
    unsigned long long sv;
    asm volatile("s_mov_b64 %0, exec\n\ts_mov_b64 exec, 1\n\tds_add_u32 %1, %2\n\ts_mov_b64 exec, %0"
                 : "=&s"(sv) : "v"(p), "v"(1u) : "memory");
}
DI void lds_wait_ge(lds_ptr p, unsigned v) {
    CFENCE();
    while ((unsigned)__builtin_amdgcn_readfirstlane(*(volatile LAS unsigned*)p) < v) __builtin_amdgcn_s_sleep(1);
    CFENCE();
}
DI void poll_ge(gu32* word, unsigned want) {
    CFENCE();
    for (unsigned spins = 0; spins < (1u << 22); ++spins) {
        if ((unsigned)__builtin_amdgcn_readfirstlane(__hip_atomic_load(word, RLX_AGENT)) >= want) break;
        __builtin_amdgcn_s_sleep(1);
    }
    __builtin_amdgcn_fence(__ATOMIC_ACQUIRE, "wavefront");
    CFENCE();
}
#define PB(x) __builtin_bit_cast(bf16x8, x)
#define PRIO(x) do { __builtin_amdgcn_s_setprio(x); SBAR(); } while (0)
DI u32x4 gld128(unsigned voff, const void* sbase) {
    u32x4 r;
    asm volatile("global_load_dwordx4 %0, %1, %2" : "=v"(r) : "v"(voff), "s"(sbase) : "memory");
    return r;
}

#define K1_UNR 15
#define K1_LA 5
#define K1_ROT ((tb & 3) * 4)
constexpr int K1_STAGE = 40960, K1U = K1_UNR;
__global__ __launch_bounds__(512, 2) void k1_qkv(const char* __restrict__ X, const char* __restrict__ W,
                                                 char* __restrict__ ws) {
    __shared__ __attribute__((aligned(16))) char lds_raw[4 * K1_STAGE];
    const lds_ptr lds = (lds_ptr)lds_raw;
    const unsigned lds0 = (unsigned)(uintptr_t)lds;
    const int tid = threadIdx.x, lane = tid & 63;
    const int w = __builtin_amdgcn_readfirstlane(tid >> 6);
    const int b = blockIdx.x, xcd = b & 7, idx = b >> 3;
    const int hp = (xcd >> 1) * 4 + (idx & 3), tb = (xcd & 1) * 8 + (idx >> 2);
    DG_DECL(0)

    const int lw = w & 3;
    const int rot = K1_ROT;
    unsigned voffX[4], voffW[2];
#pragma unroll
    for (int pp = 0; pp < 4; ++pp) {
        const int row = 32 * lw + 8 * pp + (lane >> 3);
        const int ch = (lane & 7) ^ ((row >> 1) & 7);
        voffX[pp] = (unsigned)(tb * 128 + row) * 2048u + ch * 16;
    }
#pragma unroll
    for (int pp = 0; pp < 2; ++pp) {
        const int krow = 16 * lw + 8 * pp + (lane >> 3);
        const int ch = (lane & 7) ^ (((lane >> 4) & 1) << 2);
        voffW[pp] = (unsigned)krow * 6144u + ch * 16;
    }
    auto issue = [&](int kt, int lo, int hi) {
        const int kr = (kt + rot) & 15;
        const char* xb = X + kr * 128;
        const char* wb = W + (size_t)kr * (64 * 6144) + hp * 128;
        const unsigned st = lds0 + (kt & 3) * K1_STAGE;
#pragma unroll
        for (int pp = 0; pp < 4; ++pp)
            if (pp >= lo && pp < hi) glds16(voffX[pp], xb, st + (4 * lw + pp) * 1024);
#pragma unroll
        for (int sec = 0; sec < 3; ++sec)
#pragma unroll
            for (int pp = 0; pp < 2; ++pp)
                if (4 + 2 * sec + pp >= lo && 4 + 2 * sec + pp < hi)
                    glds16(voffW[pp], wb + sec * 2048, st + 16384 + sec * 8192 + (2 * lw + pp) * 1024);
    };
    DG(13);
    if (w < 4) { issue(0, 0, 5); issue(1, 0, 5); }
    else { issue(0, 5, 10); issue(1, 5, 10); }
    DG(14);
    int w2 = w;
    asm volatile("" : "+s"(w2));

    if (w2 >= 4) {
#define WAITN_BAR(N) asm volatile("s_waitcnt vmcnt(%0) lgkmcnt(0)\n\ts_barrier" :: "n"(N) : "memory")
        issue(2, 0, K1_LA);
        asm volatile("s_waitcnt vmcnt(%0)" :: "n"(5 + K1_LA) : "memory");
#pragma unroll 1
        for (int kt = 0; kt < 13; ++kt) {
            WAITN_BAR(10 + K1_LA);
            issue(kt + 2, K1_LA, 10);
            issue(kt + 3, 0, K1_LA);
        }
        WAITN_BAR(10 + K1_LA);
        issue(15, K1_LA, 10);
        WAIT_BAR(10);
        WAIT_BAR(0);
        if (lw == 0 && lane == 0) *(unsigned*)(ws + WS_CNT + b * 256) = 0u;
        DG_END(5);
        return;
    }

    const int wm = w >> 1, wn = w & 1;
    const int r = lane & 31, hh = lane >> 5;
    const int g = lane >> 4, q = (lane & 15) >> 2, p = lane & 3;
    const int xoff0 = (64 * wm + r) * 128 + ((hh ^ ((r >> 1) & 7)) << 4);
    const int woff0 = 16384 + (8 * hh + q) * 128 + (((4 * wn + 2 * (g & 1) + (p >> 1)) ^ ((q >> 1) << 2)) << 4) + (p & 1) * 8;

    f32x16 acc[2][3];
#pragma unroll
    for (int m = 0; m < 2; ++m)
#pragma unroll
        for (int s = 0; s < 3; ++s) acc[m][s] = f32x16{};
    bf16x8 xf[2][2];
    s16x4 wl[2][3], wh[2][3];
#define K1_WF(f, sec) cat8(wl[f][sec], wh[f][sec])
#define K1_STEP(F, st, t, BAR_HERE) do { \
        const lds_ptr xp_ = (st) + (xoff0 ^ ((t) << 5)); const lds_ptr wp_ = (st) + woff0 + (t) * 2048; \
        acc[0][0] = MFMA32(K1_WF(F, 0), xf[F][0], acc[0][0]); SBAR(); \
        if (BAR_HERE) { WAIT_BAR(0); SBAR(); } \
        xf[F ^ 1][0] = lds_rd128(xp_); wl[F ^ 1][0] = lds_tr(wp_); wh[F ^ 1][0] = lds_tr(wp_ + 512); SBAR(); \
        acc[0][1] = MFMA32(K1_WF(F, 1), xf[F][0], acc[0][1]); SBAR(); \
        wl[F ^ 1][1] = lds_tr(wp_ + 8192); wh[F ^ 1][1] = lds_tr(wp_ + 8192 + 512); SBAR(); \
        acc[0][2] = MFMA32(xf[F][0], K1_WF(F, 2), acc[0][2]); SBAR(); \
        wl[F ^ 1][2] = lds_tr(wp_ + 16384); wh[F ^ 1][2] = lds_tr(wp_ + 16384 + 512); xf[F ^ 1][1] = lds_rd128(xp_ + 4096); SBAR(); \
        acc[1][0] = MFMA32(K1_WF(F, 0), xf[F][1], acc[1][0]); SBAR(); \
        acc[1][1] = MFMA32(K1_WF(F, 1), xf[F][1], acc[1][1]); \
        acc[1][2] = MFMA32(xf[F][1], K1_WF(F, 2), acc[1][2]); SBAR(); \
    } while (0)

    WAIT_BAR(5);
    DG(3);
    {
        const lds_ptr xp_ = lds + xoff0; const lds_ptr wp_ = lds + woff0;
        xf[0][0] = lds_rd128(xp_); xf[0][1] = lds_rd128(xp_ + 4096);
#pragma unroll
        for (int sec = 0; sec < 3; ++sec) { wl[0][sec] = lds_tr(wp_ + sec * 8192); wh[0][sec] = lds_tr(wp_ + sec * 8192 + 512); }
    }
    SBAR();
#pragma unroll K1U
    for (int kt = 0; kt < 15; ++kt) {
        const lds_ptr st = lds + (kt & 3) * K1_STAGE;
        const lds_ptr sn = lds + ((kt + 1) & 3) * K1_STAGE;
        K1_STEP(0, st, 1, false);
        K1_STEP(1, st, 2, false);
        K1_STEP(0, st, 3, false);
        K1_STEP(1, sn, 0, true);
    }
    {
        const lds_ptr st = lds + 3 * K1_STAGE;
        K1_STEP(0, st, 1, false);
        K1_STEP(1, st, 2, false);
        K1_STEP(0, st, 3, false);
#pragma unroll
        for (int m = 0; m < 2; ++m) {
            acc[m][1] = MFMA32(K1_WF(1, 1), xf[1][m], acc[m][1]);
            acc[m][2] = MFMA32(xf[1][m], K1_WF(1, 2), acc[m][2]);
            acc[m][0] = MFMA32(K1_WF(1, 0), xf[1][m], acc[m][0]);
        }
    }
    DG(4);
    {
        const int h = 2 * hp + wn;
        const float qscale = 0.17677669529663687f * 1.4426950408889634f;
        const __amdgpu_buffer_rsrc_t rs = __builtin_amdgcn_make_buffer_rsrc(ws, 0, 32 << 20, 0x00020000);
#pragma unroll
        for (int m = 0; m < 2; ++m) {
            const int T = tb * 4 + wm * 2 + m;
            const unsigned dq = (unsigned)WS_Q + (h * 64 + T) * 2048 + lane * 16;
            const unsigned dk = (unsigned)WS_KV + (h * 64 + T) * 4096 + lane * 16;
            st_sc1(pack8n(acc[m][1], 0), rs, dk);
            st_sc1(pack8n(acc[m][1], 1), rs, dk + 1024);
            st_sc1(pack8n(acc[m][2], 0), rs, dk + 2048);
            st_sc1(pack8n(acc[m][2], 1), rs, dk + 3072);
            st_sc1(pack8(acc[m][0], 0, qscale), rs, dq);
            st_sc1(pack8(acc[m][0], 1, qscale), rs, dq + 1024);
        }
    }
    DG_END(5);
}

constexpr int K2_SLOT = 32768;
constexpr int K2_SF = 65536;
constexpr int K2_CV = 98304;
constexpr int CV_HA = 0, CV_HB = 4160, CV_KV = 8320, CV_BYTES = 8320 + 480 + 4096;
constexpr int K2_LX = K2_CV + 4 * CV_BYTES;
constexpr int K2_FL = K2_LX + 2048;
constexpr int K2_SCR = K2_FL + 256;
constexpr int K2_LDS = K2_SCR + 4096;
constexpr int F_X = 0, F_R = 32, F_W2 = 64, C_OST = 68, C_S1 = 76, C_S2 = 80, C_S3 = 128, C_ZST = 88, F_ZGO = 92,
              C_KST = 96, C_U0 = 100, C_LD = 108, F_KGO = 116, C_END = 120, C_OL = 72;
#define NG_IN 0
#define GATE_WORD C_END
#define GATE_VAL 0
#define X_NZ 4
#define X_NW3 8
#define ZP_GATE 4u
#define ZL_GATE 4
#define ZL_AT 31
#define Z_SLEEP0 0
#define O_SLEEP 0
#define Z_SLEEP 8
#define OWIN 16
#define OW0 8
#define WD 2
#define W3_AT 10
#define X_NOLDW 0
#define X_NOLDO 0
#define X_NOCHK 0
#define KROT 2
#define NG_LAST 0
#define PACE_IN "s_nop 7"
#define PACE_OUT "s_nop 1"
#define P3_PRIO 1
#define TOP_SLEEP 2
#define ZE_PRIO 3
#define P3_T1 99
#define P3_T2 99
#define AUX_PRIO 2

__global__ __launch_bounds__(768, 3) void k2_attn(char* __restrict__ ws, const char* __restrict__ Aq,
                                                  const char* __restrict__ Ak, const char* __restrict__ Av,
                                                  const char* __restrict__ shiftk, const char* __restrict__ ssmk,
                                                  const unsigned* __restrict__ ssmD, const char* __restrict__ Wo,
                                                  char* __restrict__ out) {
    __shared__ __attribute__((aligned(16))) char lds_raw[K2_LDS];
    const lds_ptr lds = (lds_ptr)lds_raw;
    const lds_ptr fl = lds + K2_FL;
    const unsigned lds0 = (unsigned)(uintptr_t)lds;
    const int tid = threadIdx.x, lane = tid & 63;
    const int w = __builtin_amdgcn_readfirstlane(tid >> 6);
    const int b = blockIdx.x, xcd = b & 7, idx = b >> 3;
    const int h = xcd * 4 + (idx >> 3), qb = idx & 7;
    const int tt = xcd * 2 + (idx >> 4), nt = idx & 15;
    const char* kvg = ws + WS_KV + (size_t)h * 64 * 4096;
    const __amdgpu_buffer_rsrc_t rs = __builtin_amdgcn_make_buffer_rsrc(ws, 0, 32 << 20, 0x00020000);
    gu32* const cnt = (gu32*)(ws + WS_CNT);
    auto signal = [&]() {
        (void)__hip_atomic_fetch_add(cnt + ((lane & 31) * 8 + (lane < 32 ? xcd : qb)) * 64, 1u, RLX_AGENT);
    };
    const unsigned wvoff = (unsigned)(lane >> 3) * 2048u + nt * 128 + (((lane & 7) ^ (((lane >> 4) & 1) << 2)) << 4);
    auto wsrc = [&](int hf, int lp) { return Wo + (size_t)(hf * 64 + ((lp + nt * KROT * 2) & 63)) * 16384; };
    DG_DECL(1)
    const unsigned voff = lane * 16;
    const int cw = w & 3;
    auto issue = [&](int st) {
        const char* src = kvg + (st * 4 + cw) * 4096;
        const unsigned dst = lds0 + (st & 1) * K2_SLOT + cw * 4096;
#pragma unroll
        for (int i = 0; i < 4; ++i) glds16(voff, src + i * 1024, dst + i * 1024);
#pragma unroll
        for (int i = 0; i < 4; ++i) glds16(voff, src + 131072 + i * 1024, dst + 16384 + i * 1024);
    };
    if (w >= 8) {
        __builtin_amdgcn_s_sleep(TOP_SLEEP);
#pragma unroll
        for (int i = 2; i < 8; ++i)
            glds16(voff, kvg + (i & 1) * 131072 + (i >> 1) * 4096 + cw * 1024, lds0 + (i & 1) * 16384 + (i >> 1) * 4096 + cw * 1024);
        RAW_BAR();
        issue(1);
        WAIT_BAR(8);
    } else {
        const int tq = cw * 2 + (w >> 2);
        const char* qg = ws + WS_Q + (size_t)(h * 64 + qb * 8 + tq) * 2048;
        glds16(voff, kvg + (w >> 2) * 131072 + cw * 1024, lds0 + (w >> 2) * 16384 + cw * 1024);
        glds16(voff, qg, lds0 + K2_SF + tq * 2048);
        glds16(voff, qg + 1024, lds0 + K2_SF + tq * 2048 + 1024);
    }
    int w2 = w;
    asm volatile("" : "+s"(w2));

    if (w2 >= 8) {
        __builtin_amdgcn_s_setprio(3);
        const int ch = xcd * 128 + idx * 4 + cw;
        const lds_ptr cv = lds + K2_CV + cw * CV_BYTES;
        {
            const u32x4 z4 = {0u, 0u, 0u, 0u};
            if (lane < 2) *(LAS u32x4*)(cv + CV_HA + lane * 16) = z4;
            if (lane < 30) *(LAS u32x4*)(cv + CV_KV + lane * 16) = z4;
            if (cw == 0 && lane < 16) *(LAS u32x4*)(fl + lane * 16) = z4;
        }
        const int c0 = xcd * 128 + cw * 32, cq = lane & 7, rg = lane >> 3;
        const int row0 = idx * 64 + rg * 8;
        const unsigned roff = (unsigned)row0 * 2048u + (c0 + cq * 4) * 2;
        const unsigned toffs = (unsigned)(c0 + (lane >> 3)) * 4096u + idx * 128;
        u32x4 sk[2];
        sk[0] = gld128((unsigned)(c0 + cq * 4) * 8u, shiftk); sk[1] = gld128((unsigned)(c0 + cq * 4) * 8u, shiftk + 16);
        unsigned dd = gld32((unsigned)(ch >> 1) * 4u, ssmD);
        auto stage = [&](const char* src, int dst) {
            const unsigned sbase = (unsigned)(idx * 64 + cw * 16 + (lane >> 4)) * 2048u + xcd * 256;
#pragma unroll
            for (int i = 0; i < 4; ++i)
                glds16(sbase + (((lane & 15) ^ (((2 * cw + (i >> 1)) & 3) << 2)) << 4), src + i * 8192, lds0 + dst + cw * 4096 + i * 1024);
        };
        auto blank = [&](unsigned off) {
            const u32x4 e4 = {EMPTY, EMPTY, EMPTY, EMPTY};
            CFENCE();
            st_sc1(e4, rs, off + voff); st_sc1(e4, rs, off + 1024 + voff);
            CFENCE();
        };
        WAIT_BAR(3);
        DG(12);
        issue(2);
        stage(Ak, K2_SF);
        glds16((unsigned)(idx * 64 - (idx ? 4 : 0) + (lane >> 4)) * 2048u + xcd * 256 + (((lane & 15) ^ 12) << 4), Ak, lds0 + K2_SCR + cw * 1024);
        blank((unsigned)WS_O + (unsigned)((qb * 8 + cw * 2) * 32 + h) * 2048u);
        WAIT_BAR(7);
        DG(9);
        issue(3);
        stage(Av, K2_SF + 16384);
        blank((unsigned)WS_O + (unsigned)((qb * 8 + cw * 2 + 1) * 32 + h) * 2048u);
        WAIT_BAR(6);
        DG(3);
        issue(4);
#pragma unroll
        for (int i = 0; i < 4; ++i) glds16(voff, ssmk + (size_t)ch * 4096 + i * 1024, lds0 + K2_CV + cw * CV_BYTES + CV_HA + 32 + i * 1024);
        blank((unsigned)WS_ZT + ch * 4096); blank((unsigned)WS_ZT + ch * 4096 + 2048);
        WAIT_BAR(8);
        DG(5);
        issue(5);
        SBAR();
        asm volatile("" : "+v"(sk[0]), "+v"(sk[1]), "+v"(dd));
        dd = __builtin_amdgcn_readfirstlane(dd);
        {
            u32x2 kr[11], vr[8];
            const lds_ptr kb = lds + K2_SF + rg * 2048 + ((cw ^ (rg & 3)) << 6) + cq * 8;
            const lds_ptr hb = lds + (rg ? K2_SF + rg * 2048 - 768 : K2_SCR + cw * 1024 + 256) + ((cw ^ ((rg + 3) & 3)) << 6) + cq * 8;
#pragma unroll
            for (int i = 0; i < 3; ++i) kr[i] = *(const LAS u32x2*)(hb + i * 256);
#pragma unroll
            for (int i = 0; i < 8; ++i) kr[3 + i] = *(const LAS u32x2*)(kb + i * 256);
#pragma unroll
            for (int i = 0; i < 8; ++i) vr[i] = *(const LAS u32x2*)(kb + 16384 + i * 256);
            if (row0 == 0) { kr[0] = u32x2{0u, 0u}; kr[1] = u32x2{0u, 0u}; kr[2] = u32x2{0u, 0u}; }
            const lds_ptr tb = cv + CV_KV + 480;
#pragma unroll
            for (int c = 0; c < 4; ++c) {
                const unsigned s01 = sk[c >> 1][(c & 1) * 2], s23 = sk[c >> 1][(c & 1) * 2 + 1];
                const float w0 = bflo(s01), w1 = bfhi(s01), w2 = bflo(s23), w3 = bfhi(s23);
                float kf[11], kv[8];
#pragma unroll
                for (int i = 0; i < 11; ++i) kf[i] = (c & 1) ? bfhi(kr[i][c >> 1]) : bflo(kr[i][c >> 1]);
#pragma unroll
                for (int i = 0; i < 8; ++i) {
                    float a = mul_vv(w3, kf[i]);
                    fmac_vv(a, w2, kf[i + 1]); fmac_vv(a, w1, kf[i + 2]); fmac_vv(a, w0, kf[i + 3]);
                    kv[i] = mul_vv(a, (c & 1) ? bfhi(vr[i][c >> 1]) : bflo(vr[i][c >> 1]));
                }
                u32x4 o4;
#pragma unroll
                for (int i = 0; i < 4; ++i) o4[i] = cvtpk(kv[7 - 2 * i], kv[6 - 2 * i]);
                *(LAS u32x4*)(tb + (cq * 4 + c) * 128 + ((rg ^ cq) << 4)) = o4;
            }
#pragma unroll
            for (int i = 0; i < 4; ++i) {
                const u32x4 t = *(const LAS u32x4*)(tb + i * 1024 + lane * 16);
                st_sc1(t, rs, (unsigned)WS_KVR + toffs + i * 32768 + (((lane & 7) ^ (2 * i + (lane >> 5))) << 4));
            }
        }
        CFENCE();
        DG(7);
        WAIT_BAR(4);
        DG(6);
        issue(6);
        asm volatile("s_waitcnt vmcnt(8)" ::: "memory");
        if (lds_add_rtn(fl + C_KST, lane) == 3u) signal();
#pragma unroll
        for (int i = 0; i < 8; ++i)
            glds16(wvoff, wsrc(i >> 2, cw * 4 + (i & 3)), lds0 + K2_SF + (i >> 2) * 16384 + (cw * 4 + (i & 3)) * 1024);
        {
            const u32x4 z4 = {0u, 0u, 0u, 0u};
            if (lane < 2) *(LAS u32x4*)(cv + CV_HB + lane * 16) = z4;
#pragma unroll
            for (int i = 0; i < 4; ++i) {
                const int off = 32 + i * 1024 + lane * 16;
                const u32x4 hv = *(const LAS u32x4*)(cv + CV_HA + off);
                const unsigned pv = *(const LAS unsigned*)(cv + CV_HA + off - 4);
                u32x4 sh;
                sh[0] = __builtin_amdgcn_alignbit(hv[0], pv, 16);
                sh[1] = __builtin_amdgcn_alignbit(hv[1], hv[0], 16);
                sh[2] = __builtin_amdgcn_alignbit(hv[2], hv[1], 16);
                sh[3] = __builtin_amdgcn_alignbit(hv[3], hv[2], 16);
                *(LAS u32x4*)(cv + CV_HB + off) = sh;
            }
        }
        WAIT_BAR(8);
        DG(8);
        issue(7);
#pragma unroll
        for (int i = 0; i < 8; ++i)
            glds16(wvoff, wsrc(idx >> 4, 32 + cw * 8 + i), lds0 + K2_SCR + cw * 1024);
        WAIT_BAR(8);
        DG(11);
        if (cw == 0) { poll_ge(cnt + b * 64, 64u); lds_add(fl + F_KGO, lane); }
        else lds_wait_ge(fl + F_KGO, 1u);
        {
            u32x4 t[4];
#pragma unroll
            for (int i = 0; i < 4; ++i) t[i] = ld_sc1(rs, (unsigned)WS_KVR + ch * 4096 + i * 1024 + voff);
#pragma unroll
            for (int i = 0; i < 4; ++i) *(LAS u32x4*)(cv + CV_KV + 480 + i * 1024 + lane * 16) = t[i];
        }
        const int r1 = lane & 15, q = lane >> 4;
        const int toff = (r1 & 1) ? CV_HA + 2 * (16 + r1 + 9 - 8 * q) : CV_HB + 2 * (16 + r1 + 10 - 8 * q);
        const int boff = CV_KV + 448 + 32 * r1 + 16 * q;
        f32x4 y[8];
        u32x4 tf[8];
        bf16x8 bf[8];
        const lds_ptr bp = cv + boff - 448;
        lds_ptr tp[4];
#pragma unroll
        for (int i = 0; i < 4; ++i) { tp[i] = cv + toff + 1024 * i; asm volatile("" : "+v"(tp[i])); }
#define LDT(bb, a, hf) do { const LAS unsigned* t_ = (const LAS unsigned*)(tp[(a) >> 1] + 64 * (bb) + 512 * ((a) & 1) + 8 * (hf)); \
            tf[a][2 * (hf)] = t_[0]; tf[a][2 * (hf) + 1] = t_[1]; SBAR(); } while (0)
#define LDB(bb, a) do { bf[a] = lds_rd128(bp + 448 - 64 * (bb) + 512 * (a)); SBAR(); } while (0)
#define CM(a, ww) do { asm volatile("v_mfma_f32_16x16x32_bf16 %0, %1, %2, %0" : "+v"(y[(a) + (ww)]) : "v"(tf[a]), "v"(bf[ww])); SBAR(); } while (0)
#define CM0(a, ww) do { asm volatile("v_mfma_f32_16x16x32_bf16 %0, %1, %2, 0" : "=&v"(y[(a) + (ww)]) : "v"(tf[a]), "v"(bf[ww])); SBAR(); } while (0)
#define PC() do { asm volatile(PACE_OUT); SBAR(); } while (0)
#pragma unroll
        for (int a = 0; a < 8; ++a) { if (a < 5) { LDT(0, a, 0); LDT(0, a, 1); } LDB(0, 7 - a); }
#pragma unroll
        for (int i = 0; i < 8; ++i)
            glds16(wvoff, wsrc(i >> 2, 16 + cw * 4 + (i & 3)), lds0 + (i >> 2) * 16384 + (cw * 4 + (i & 3)) * 1024);
#pragma unroll
        for (int i = 0; i < 4; ++i)
            glds16((unsigned)(idx * 64 + cw * 16 + (lane >> 4)) * 2048u + xcd * 256 + (lane & 15) * 16, Aq + i * 8192, lds0 + K2_SCR + cw * 1024);
        RAW_BAR();
        asm volatile("s_waitcnt vmcnt(4)" ::: "memory");
        lds_add(fl + C_S1, lane);
        __builtin_amdgcn_s_setprio(AUX_PRIO);
        const float dch = (ch & 1) ? bfhi(dd) : bflo(dd);
        const lds_ptr kdp = cv + CV_KV + 480 + 32 * r1 + 16 * (q >> 1) + ((q & 1) ? 0 : 8);
        float kd[8][4];
        u32x2 kk;
        unsigned kc0, kc1, zo[8][2];
#define KD_RD(t) do { kk = *(const LAS u32x2*)(kdp + 512 * (t)); SBAR(); } while (0)
#define KD_UP0(t) do { kd[t][0] = bfhi(kk[1]); kd[t][1] = bflo(kk[1]); SBAR(); } while (0)
#define KD_UP1(t) do { kd[t][2] = bfhi(kk[0]); kd[t][3] = bflo(kk[0]); SBAR(); } while (0)
#define KD_MU0(t) do { kd[t][0] = mul_sv(dch, kd[t][0]); kd[t][1] = mul_sv(dch, kd[t][1]); SBAR(); } while (0)
#define KD_MU1(t) do { kd[t][2] = mul_sv(dch, kd[t][2]); kd[t][3] = mul_sv(dch, kd[t][3]); SBAR(); } while (0)
#define KD_CV(t) do { kc0 = cvtpk(kd[t][0], kd[t][1]); kc1 = cvtpk(kd[t][2], kd[t][3]); SBAR(); } while (0)
#define KD_X0(t) do { kd[t][0] = bflo(kc0); kd[t][1] = bfhi(kc0); SBAR(); } while (0)
#define KD_X1(t) do { kd[t][2] = bflo(kc1); kd[t][3] = bfhi(kc1); SBAR(); } while (0)
#define ZF0(t) do { kc0 = cvtpk(y[t][0], y[t][1]); kc1 = cvtpk(y[t][2], y[t][3]); SBAR(); } while (0)
#define ZF1(t) do { y[t][0] = bflo(kc0); y[t][1] = bfhi(kc0); SBAR(); } while (0)
#define ZF2(t) do { y[t][2] = bflo(kc1); y[t][3] = bfhi(kc1); SBAR(); } while (0)
#define ZF3(t) do { y[t][0] = add_vv(y[t][0], kd[t][0]); y[t][1] = add_vv(y[t][1], kd[t][1]); SBAR(); } while (0)
#define ZF4(t) do { y[t][2] = add_vv(y[t][2], kd[t][2]); y[t][3] = add_vv(y[t][3], kd[t][3]); SBAR(); } while (0)
#define ZF5(t) do { zo[t][0] = nz(cvtpk(y[t][0], y[t][1])); zo[t][1] = nz(cvtpk(y[t][2], y[t][3])); SBAR(); } while (0)
#define ZST(pr) do { auto s0 = __builtin_amdgcn_permlane16_swap(zo[2 * (pr)][0], zo[2 * (pr) + 1][0], false, false); \
            auto s1 = __builtin_amdgcn_permlane16_swap(zo[2 * (pr)][1], zo[2 * (pr) + 1][1], false, false); \
            st_sc1(u32x4{s0[0], s1[0], s0[1], s1[1]}, rs, (unsigned)WS_ZT + ch * 4096 + (pr) * 1024 + voff); SBAR(); } while (0)
        CM0(0, 7); LDB(1, 7); CM0(0, 6); LDT(0, 5, 0); CM0(0, 5); LDT(0, 5, 1); CM0(0, 4); LDT(0, 6, 0); CM0(0, 3); LDT(0, 6, 1); CM0(0, 2); LDT(0, 7, 0); CM0(0, 1); LDT(0, 7, 1); CM0(0, 0); KD_RD(0);
        CM(1, 6); LDB(1, 6); CM(1, 5); LDT(1, 0, 0); CM(1, 4); LDT(1, 0, 1); CM(1, 3); PC(); CM(1, 2); PC(); CM(1, 1); KD_UP0(0); CM(1, 0); KD_UP1(0);
        CM(2, 5); LDB(1, 5); CM(2, 4); LDT(1, 1, 0); CM(2, 3); LDT(1, 1, 1); CM(2, 2); KD_MU0(0); CM(2, 1); KD_MU1(0); CM(2, 0); KD_CV(0);
        CM(3, 4); LDB(1, 4); CM(3, 3); LDT(1, 2, 0); CM(3, 2); LDT(1, 2, 1); CM(3, 1); KD_X0(0); CM(3, 0); KD_X1(0);
        CM(4, 3); LDB(1, 3); CM(4, 2); LDT(1, 3, 0); CM(4, 1); LDT(1, 3, 1); CM(4, 0); KD_RD(1);
        CM(5, 2); LDB(1, 2); CM(5, 1); LDT(1, 4, 0); CM(5, 0); LDT(1, 4, 1);
        CM(6, 1); LDB(1, 1); CM(6, 0); PC();
        CM(7, 0); LDB(1, 0);
        CM(0, 7); LDB(2, 7); CM(0, 6); LDT(1, 5, 0); CM(0, 5); LDT(1, 5, 1); CM(0, 4); LDT(1, 6, 0); CM(0, 3); LDT(1, 6, 1); CM(0, 2); LDT(1, 7, 0); CM(0, 1); LDT(1, 7, 1); CM(0, 0); PC();
        CM(1, 6); LDB(2, 6); CM(1, 5); LDT(2, 0, 0); CM(1, 4); LDT(2, 0, 1); CM(1, 3); KD_UP0(1); CM(1, 2); KD_UP1(1); CM(1, 1); KD_MU0(1); CM(1, 0); KD_MU1(1);
        CM(2, 5); LDB(2, 5); CM(2, 4); LDT(2, 1, 0); CM(2, 3); LDT(2, 1, 1); CM(2, 2); KD_CV(1); CM(2, 1); KD_X0(1); CM(2, 0); KD_X1(1);
        CM(3, 4); LDB(2, 4); CM(3, 3); LDT(2, 2, 0); CM(3, 2); LDT(2, 2, 1); CM(3, 1); KD_RD(2); CM(3, 0); PC();
        CM(4, 3); LDB(2, 3); CM(4, 2); LDT(2, 3, 0); CM(4, 1); LDT(2, 3, 1); CM(4, 0); PC();
        CM(5, 2); LDB(2, 2); CM(5, 1); LDT(2, 4, 0); CM(5, 0); LDT(2, 4, 1);
        CM(6, 1); LDB(2, 1); CM(6, 0); KD_UP0(2);
        CM(7, 0); LDB(2, 0);
        CM(0, 7); LDB(3, 7); CM(0, 6); LDT(2, 5, 0); CM(0, 5); LDT(2, 5, 1); CM(0, 4); LDT(2, 6, 0); CM(0, 3); LDT(2, 6, 1); CM(0, 2); LDT(2, 7, 0); CM(0, 1); LDT(2, 7, 1); CM(0, 0); KD_UP1(2);
        CM(1, 6); LDB(3, 6); CM(1, 5); LDT(3, 0, 0); CM(1, 4); LDT(3, 0, 1); CM(1, 3); KD_MU0(2); CM(1, 2); KD_MU1(2); CM(1, 1); KD_CV(2); CM(1, 0); KD_X0(2);
        CM(2, 5); LDB(3, 5); CM(2, 4); LDT(3, 1, 0); CM(2, 3); LDT(3, 1, 1); CM(2, 2); KD_X1(2); CM(2, 1); KD_RD(3); CM(2, 0); PC();
        CM(3, 4); LDB(3, 4); CM(3, 3); LDT(3, 2, 0); CM(3, 2); LDT(3, 2, 1); CM(3, 1); PC(); CM(3, 0); KD_UP0(3);
        CM(4, 3); LDB(3, 3); CM(4, 2); LDT(3, 3, 0); CM(4, 1); LDT(3, 3, 1); CM(4, 0); KD_UP1(3);
        CM(5, 2); LDB(3, 2); CM(5, 1); LDT(3, 4, 0); CM(5, 0); LDT(3, 4, 1);
        CM(6, 1); LDB(3, 1); CM(6, 0); KD_MU0(3);
        CM(7, 0); LDB(3, 0);
        CM(0, 7); LDB(4, 7); CM(0, 6); LDT(3, 5, 0); CM(0, 5); LDT(3, 5, 1); CM(0, 4); LDT(3, 6, 0); CM(0, 3); LDT(3, 6, 1); CM(0, 2); LDT(3, 7, 0); CM(0, 1); LDT(3, 7, 1); CM(0, 0); KD_MU1(3);
        CM(1, 6); LDB(4, 6); CM(1, 5); LDT(4, 0, 0); CM(1, 4); LDT(4, 0, 1); CM(1, 3); KD_CV(3); CM(1, 2); KD_X0(3); CM(1, 1); KD_X1(3); CM(1, 0); KD_RD(4);
        CM(2, 5); LDB(4, 5); CM(2, 4); LDT(4, 1, 0); CM(2, 3); LDT(4, 1, 1); CM(2, 2); PC(); CM(2, 1); PC(); CM(2, 0); KD_UP0(4);
        CM(3, 4); LDB(4, 4); CM(3, 3); LDT(4, 2, 0); CM(3, 2); LDT(4, 2, 1); CM(3, 1); KD_UP1(4); CM(3, 0); KD_MU0(4);
        CM(4, 3); LDB(4, 3); CM(4, 2); LDT(4, 3, 0); CM(4, 1); LDT(4, 3, 1); CM(4, 0); KD_MU1(4);
        CM(5, 2); LDB(4, 2); CM(5, 1); LDT(4, 4, 0); CM(5, 0); LDT(4, 4, 1);
        CM(6, 1); LDB(4, 1); CM(6, 0); KD_CV(4);
        CM(7, 0); LDB(4, 0);
        CM(0, 7); LDB(5, 7); CM(0, 6); LDT(4, 5, 0); CM(0, 5); LDT(4, 5, 1); CM(0, 4); LDT(4, 6, 0); CM(0, 3); LDT(4, 6, 1); CM(0, 2); LDT(4, 7, 0); CM(0, 1); LDT(4, 7, 1); CM(0, 0); KD_X0(4);
        CM(1, 6); LDB(5, 6); CM(1, 5); LDT(5, 0, 0); CM(1, 4); LDT(5, 0, 1); CM(1, 3); KD_X1(4); CM(1, 2); KD_RD(5); CM(1, 1); PC(); CM(1, 0); PC();
        CM(2, 5); LDB(5, 5); CM(2, 4); LDT(5, 1, 0); CM(2, 3); LDT(5, 1, 1); CM(2, 2); KD_UP0(5); CM(2, 1); KD_UP1(5); CM(2, 0); KD_MU0(5);
        CM(3, 4); LDB(5, 4); CM(3, 3); LDT(5, 2, 0); CM(3, 2); LDT(5, 2, 1); CM(3, 1); KD_MU1(5); CM(3, 0); KD_CV(5);
        CM(4, 3); LDB(5, 3); CM(4, 2); LDT(5, 3, 0); CM(4, 1); LDT(5, 3, 1); CM(4, 0); KD_X0(5);
        CM(5, 2); LDB(5, 2); CM(5, 1); LDT(5, 4, 0); CM(5, 0); LDT(5, 4, 1);
        CM(6, 1); LDB(5, 1); CM(6, 0); KD_X1(5);
        CM(7, 0); LDB(5, 0);
        CM(0, 7); LDB(6, 7); CM(0, 6); LDT(5, 5, 0); CM(0, 5); LDT(5, 5, 1); CM(0, 4); LDT(5, 6, 0); CM(0, 3); LDT(5, 6, 1); CM(0, 2); LDT(5, 7, 0); CM(0, 1); LDT(5, 7, 1); CM(0, 0); KD_RD(6);
        CM(1, 6); LDB(6, 6); CM(1, 5); LDT(6, 0, 0); CM(1, 4); LDT(6, 0, 1); CM(1, 3); PC(); CM(1, 2); PC(); CM(1, 1); KD_UP0(6); CM(1, 0); KD_UP1(6);
        CM(2, 5); LDB(6, 5); CM(2, 4); LDT(6, 1, 0); CM(2, 3); LDT(6, 1, 1); CM(2, 2); KD_MU0(6); CM(2, 1); KD_MU1(6); CM(2, 0); KD_CV(6);
        CM(3, 4); LDB(6, 4); CM(3, 3); LDT(6, 2, 0); CM(3, 2); LDT(6, 2, 1); CM(3, 1); KD_X0(6); CM(3, 0); KD_X1(6);
        CM(4, 3); LDB(6, 3); CM(4, 2); LDT(6, 3, 0); CM(4, 1); LDT(6, 3, 1); CM(4, 0); KD_RD(7);
        CM(5, 2); LDB(6, 2); CM(5, 1); LDT(6, 4, 0); CM(5, 0); LDT(6, 4, 1);
        CM(6, 1); LDB(6, 1); CM(6, 0); PC();
        CM(7, 0); LDB(6, 0);
        CM(0, 7); LDB(7, 7); CM(0, 6); LDT(6, 5, 0); CM(0, 5); LDT(6, 5, 1); CM(0, 4); LDT(6, 6, 0); CM(0, 3); LDT(6, 6, 1); CM(0, 2); LDT(6, 7, 0); CM(0, 1); LDT(6, 7, 1); CM(0, 0); PC();
        CM(1, 6); LDB(7, 6); CM(1, 5); LDT(7, 0, 0); CM(1, 4); LDT(7, 0, 1); CM(1, 3); KD_UP0(7); CM(1, 2); KD_UP1(7); CM(1, 1); KD_MU0(7); CM(1, 0); KD_MU1(7);
        CM(2, 5); LDB(7, 5); CM(2, 4); LDT(7, 1, 0); CM(2, 3); LDT(7, 1, 1); CM(2, 2); KD_CV(7); CM(2, 1); KD_X0(7); CM(2, 0); KD_X1(7);
        CM(3, 4); LDB(7, 4); CM(3, 3); LDT(7, 2, 0); CM(3, 2); LDT(7, 2, 1); CM(3, 1); PC(); CM(3, 0); PC();
        CM(4, 3); LDB(7, 3); CM(4, 2); LDT(7, 3, 0); CM(4, 1); LDT(7, 3, 1); CM(4, 0); PC();
        CM(5, 2); LDB(7, 2); CM(5, 1); LDT(7, 4, 0); CM(5, 0); LDT(7, 4, 1);
        CM(6, 1); LDB(7, 1); CM(6, 0); PC();
        CM(7, 0); LDB(7, 0);
        CM(0, 3); LDT(7, 5, 0); CM(0, 2); LDT(7, 5, 1); CM(1, 2); LDT(7, 6, 0); CM(1, 1); LDT(7, 6, 1); CM(2, 1); LDT(7, 7, 0); CM(2, 0); LDT(7, 7, 1);
        CM(3, 0); PC(); CM(0, 1); PC(); CM(0, 0); PC(); CM(1, 0); ZF0(2);  CM(0, 5); ZF1(2);  CM(0, 4); ZF2(2);
        CM(1, 4); ZF3(2);  CM(1, 3); ZF4(2);  CM(2, 3); ZF5(2);  CM(2, 2); ZF0(3);  CM(3, 2); ZF1(3);  CM(3, 1); ZF2(3);
        CM(4, 1); ZF3(3);  CM(4, 0); ZF4(3);  CM(5, 0); ZF5(3);  CM(0, 7); ZST(1);  CM(0, 6); ZF0(0);  CM(1, 6); ZF1(0);
        CM(1, 5); ZF2(0);  CM(2, 5); ZF3(0);  CM(2, 4); ZF4(0);  CM(3, 4); ZF5(0);  CM(3, 3); ZF0(1);  CM(4, 3); ZF1(1);
        CM(4, 2); ZF2(1);  CM(5, 2); ZF3(1);  CM(5, 1); ZF4(1);  CM(6, 1); ZF5(1);  CM(6, 0); ZST(0);  CM(7, 0); ZF0(4);
        DG(4);
        ZF1(4); ZF2(4); ZF3(4); ZF4(4); ZF5(4); ZF0(5); ZF1(5); ZF2(5); ZF3(5); ZF4(5); ZF5(5); ZST(2); ZF0(6); ZF1(6); ZF2(6); ZF3(6); ZF4(6); ZF5(6); ZF0(7); ZF1(7); ZF2(7); ZF3(7); ZF4(7); ZF5(7); ZST(3);
        DG(10);
        __builtin_amdgcn_s_setprio(3);
        while ((unsigned)__builtin_amdgcn_readfirstlane(*(volatile LAS unsigned*)(fl + C_OL)) < ZL_GATE) __builtin_amdgcn_s_sleep(4);
        CFENCE();
        u32x2 qr[8];
#pragma unroll
        for (int i = 0; i < 8; ++i) qr[i] = gld64(roff, Aq + i * 2048);
        {
            const lds_ptr tr = cv;
            u32x4 t[4];
            const unsigned zb = (unsigned)WS_ZT + (unsigned)(c0 + (lane >> 3)) * 4096u + (idx >> 3) * 1024 + ((idx & 3) * 4 + ((idx >> 2) & 1) * 16) * 16;
            for (unsigned spins = 0; spins < (1u << 20); ++spins) {
#pragma unroll
                for (int i = 0; i < 4; ++i) {
                    const int ck = (lane & 7) ^ (2 * i + (lane >> 5));
                    t[i] = ld_sc1(rs, zb + i * 32768 + ((ck >> 1) + (ck & 1) * 32) * 16);
                }
                if (!missing(mx4(mx4(mx8(t[0], t[1]), t[2]), t[3]))) break;
                __builtin_amdgcn_s_sleep(Z_SLEEP);
            }
            DG(13);
            __builtin_amdgcn_s_setprio(ZE_PRIO);
#pragma unroll
            for (int i = 0; i < 4; ++i) *(LAS u32x4*)(tr + i * 1024 + lane * 16) = t[i];
#pragma unroll
            for (int i = 0; i < 8; ++i) asm volatile("" : "+v"(qr[i]));
            u32x4 zc[4];
#pragma unroll
            for (int c = 0; c < 4; ++c) zc[c] = *(const LAS u32x4*)(tr + (cq * 4 + c) * 128 + ((rg ^ cq) << 4));
            char* dst = out + (size_t)row0 * 4096 + (c0 + cq * 4) * 2;
#pragma unroll
            for (int i = 0; i < 8; ++i) {
                float zz[4];
#pragma unroll
                for (int c = 0; c < 4; ++c) zz[c] = (i & 1) ? bfhi(zc[c][i >> 1]) : bflo(zc[c][i >> 1]);
                u32x2 o = {cvtpk(mul_vv(bflo(qr[i][0]), zz[0]), mul_vv(bfhi(qr[i][0]), zz[1])),
                           cvtpk(mul_vv(bflo(qr[i][1]), zz[2]), mul_vv(bfhi(qr[i][1]), zz[3]))};
                *(u32x2*)(dst + i * 4096) = o;
            }
        }
        DG_END(14);
        return;
    }

    const int half = w >> 2;
    const int TA = qb * 8 + (w & 3) * 2 + half, TB = TA ^ 1;
    bf16x8 sel;
    {
        const short one = (((lane >> 2) & 1) == ((lane >> 4) & 1)) ? (short)0x3f80 : (short)0;
#pragma unroll
        for (int i = 0; i < 8; ++i) sel[i] = one;
    }
    f32x16 oA = f32x16{}, oB = f32x16{};
    f32x4 lA = f32x4{}, lB = f32x4{};
    f32x16 negm, sA, sB;
    lds_ptr lp = lds + half * 16384 + lane * 16;
    bf16x8 k0[2], k1[2], v0[2], v1[2];
    u32x4 p0, p1 = {0u, 0u, 0u, 0u};
#define EX(X, i) X[i] = __builtin_amdgcn_exp2f(X[i])
    lds_ptr qpa = lds + K2_SF + (TA & 7) * 2048 + lane * 16, qpb = lds + K2_SF + (TB & 7) * 2048 + lane * 16;
    asm volatile("" : "+v"(qpa), "+v"(qpb), "+v"(lp), "+v"(sel), "+v"(oA), "+v"(oB), "+v"(lA), "+v"(lB), "+v"(p1));
    WAIT_BAR(0);
    DG(3);
    const bf16x8 qa0 = lds_rd128(qpa), qb0 = lds_rd128(qpb);
    const bf16x8 x0 = lds_rd128(lds + lane * 16), x1 = lds_rd128(lds + lane * 16 + 1024);
    const bf16x8 qa1 = lds_rd128(qpa + 1024), qb1 = lds_rd128(qpb + 1024);
    {
        k0[0] = lds_rd128(lp); k1[0] = lds_rd128(lp + 1024);
        v0[0] = lds_rd128(lp + 2048); v1[0] = lds_rd128(lp + 3072);
        sA = MFMA32(x0, qa0, f32x16{});
        sB = MFMA32(x0, qb0, f32x16{});
        sA = MFMA32(x1, qa1, sA);
        sB = MFMA32(x1, qb1, sB);
        float m = fmx(sA[0], sA[1]), m2 = fmx(sB[0], sB[1]);
#pragma unroll
        for (int i = 2; i < 16; i += 2) m = fmx(fmx(m, sA[i]), sA[i + 1]);
#pragma unroll
        for (int i = 2; i < 16; i += 2) m2 = fmx(fmx(m2, sB[i]), sB[i + 1]);
        m = fmx(m, m2);
        auto rr = __builtin_amdgcn_permlane32_swap(__float_as_uint(m), __float_as_uint(m), false, false);
        m = fmx(__uint_as_float(rr[0]), __uint_as_float(rr[1]));
#pragma unroll
        for (int i = 0; i < 16; ++i) negm[i] = -m;
        sA = MFMA32(k0[0], qa0, negm);
        sA = MFMA32(k1[0], qa1, sA);
        v1[1] = v1[0];
        SBAR();
        RAW_BAR();
        SBAR();
        EX(sA, 0); EX(sA, 1);
    }
    DG(10);
#define SUM16(l, p) asm volatile("v_mfma_f32_16x16x32_bf16 %0, %1, %2, %0" : "+v"(l) : "v"(sel), "v"(p))
#define HALF(C, oC, lC, N, qx, qy, ka, kb, oP, lP, vprev, vcur0, PRE) do { \
        oP = MFMA32(vprev, PB(p1), oP); SBAR(); \
        PRE; EX(C, 2); EX(C, 3); EX(C, 4); SBAR(); \
        N = MFMA32(ka, qx, negm); SBAR(); \
        EX(C, 5); EX(C, 6); EX(C, 7); SBAR(); \
        SUM16(lP, p1); SBAR(); \
        p0[0] = cvtpk(C[0], C[1]); p0[1] = cvtpk(C[2], C[3]); SBAR(); \
        N = MFMA32(kb, qy, N); SBAR(); \
        p0[2] = cvtpk(C[4], C[5]); p0[3] = cvtpk(C[6], C[7]); SBAR(); EX(C, 8); EX(C, 9); SBAR(); \
        oC = MFMA32(vcur0, PB(p0), oC); SBAR(); \
        EX(C, 10); EX(C, 11); EX(C, 12); SBAR(); \
        SUM16(lC, p0); SBAR(); \
        EX(C, 13); EX(C, 14); EX(C, 15); SBAR(); \
        p1[0] = cvtpk(C[8], C[9]); p1[1] = cvtpk(C[10], C[11]); p1[2] = cvtpk(C[12], C[13]); p1[3] = cvtpk(C[14], C[15]); SBAR(); \
        EX(N, 0); EX(N, 1); SBAR(); \
    } while (0)
#define RD4(Y, nx) do { v1[Y] = lds_rd128((nx) + 3072); v0[Y] = lds_rd128((nx) + 2048); \
        k1[Y] = lds_rd128((nx) + 1024); k0[Y] = lds_rd128(nx); } while (0)
#define KT(X, nx, PRE0) do { \
        HALF(sA, oA, lA, sB, qb0, qb1, k0[X], k1[X], oB, lB, v1[X ^ 1], v0[X], PRE0; RD4(X ^ 1, nx)); \
        HALF(sB, oB, lB, sA, qa0, qa1, k0[X ^ 1], k1[X ^ 1], oA, lA, v1[X], v0[X], (void)0); \
    } while (0)
    SBAR();
#pragma unroll 1
    for (int st = 0; st < 8; ++st) {
        const lds_ptr cur = lp + (st & 1) * K2_SLOT;
        const lds_ptr nxt = lp + ((st + 1) & 1) * K2_SLOT;
        PRIO(2);
        KT(0, cur + 4096, (void)0);
        PRIO(1);
        KT(1, cur + 8192, (void)0);
        PRIO(0);
        KT(0, cur + 12288, (void)0);
        PRIO(3);
        KT(1, nxt, RAW_BAR(); SBAR());
    }
    oB = MFMA32(v1[1], PB(p1), oB);
    SBAR();
    SUM16(lB, p1);
    asm volatile("s_nop 7" : "+v"(oB) :: "memory");
    SBAR();
    DG(4);
    {
        const lds_ptr xo = lds + K2_SLOT + w * 4096 + lane * 16;
        *(LAS float*)(lds + K2_LX + w * 256 + lane * 4) = lB[0];
#pragma unroll
        for (int i = 0; i < 4; ++i) *(LAS f32x4*)(xo + i * 1024) = f32x4{oB[4 * i], oB[4 * i + 1], oB[4 * i + 2], oB[4 * i + 3]};
        lds_add(fl + F_X + w * 4, lane);
        lds_add(fl + C_END + half * 4, lane);
        lds_wait_ge(fl + F_X + (w ^ 4) * 4, 1u);
        const lds_ptr xi = lds + K2_SLOT + (w ^ 4) * 4096 + lane * 16;
        f32x4 t[4];
        const float lx = *(const LAS float*)(lds + K2_LX + (w ^ 4) * 256 + lane * 4);
#pragma unroll
        for (int i = 0; i < 4; ++i) t[i] = *(const LAS f32x4*)(xi + i * 1024);
        SBAR();
        lA[0] += lx;
#pragma unroll
        for (int i = 0; i < 16; ++i) { float o = oA[i]; acc_vv(o, t[i >> 2][i & 3]); oA[i] = o; }
    }
    DG(9);
    const float l = lA[0];
    u32x4 on0, on1;
    auto finish = [&](float ls) {
        float rl = __builtin_amdgcn_rcpf(ls);
        asm volatile("s_nop 1" : "+v"(rl));
        f32x16 o;
#pragma unroll
        for (int i = 0; i < 16; ++i) o[i] = mul_vv(oA[i], rl);
        on0 = nz4(pack8n(o, 0)); on1 = nz4(pack8n(o, 1));
    };
    finish(l);
    {
        float t0, t1;
        asm("v_mul_f32_e32 %0, 0, %2\n\tv_mul_f32_e32 %1, 0, %3\n\tv_fmac_f32_e32 %0, 0, %4\n\tv_fmac_f32_e32 %1, 0, %5\n\tv_fmac_f32_e32 %0, 0, %6\n\tv_fmac_f32_e32 %1, 0, %7\n\tv_fmac_f32_e32 %0, 0, %8\n\tv_fmac_f32_e32 %1, 0, %9\n\t"
            "v_fmac_f32_e32 %0, 0, %10\n\tv_fmac_f32_e32 %1, 0, %11\n\tv_fmac_f32_e32 %0, 0, %12\n\tv_fmac_f32_e32 %1, 0, %13\n\tv_fmac_f32_e32 %0, 0, %14\n\tv_fmac_f32_e32 %1, 0, %15\n\tv_fmac_f32_e32 %0, 0, %16\n\tv_fmac_f32_e32 %1, 0, %17"
            : "=&v"(t0), "=&v"(t1) : "v"(oA[0]), "v"(oA[1]), "v"(oA[2]), "v"(oA[3]), "v"(oA[4]), "v"(oA[5]), "v"(oA[6]), "v"(oA[7]), "v"(oA[8]), "v"(oA[9]), "v"(oA[10]), "v"(oA[11]), "v"(oA[12]), "v"(oA[13]), "v"(oA[14]), "v"(oA[15]));
        const bool ok = (l <= 1.0e30f) && (l >= 1.0e-18f) && (t0 + t1 == 0.f);
        if (__builtin_expect(__any(!ok), 0)) {
            float m = -INFINITY, ls = 0.f;
            oA = f32x16{};
#pragma unroll 1
            for (int t = 0; t < 64; ++t) {
                const char* g = kvg + t * 4096 + lane * 16;
                const bf16x8 a0 = *(const bf16x8*)g, a1 = *(const bf16x8*)(g + 1024);
                const bf16x8 b0 = *(const bf16x8*)(g + 2048), b1 = *(const bf16x8*)(g + 3072);
                f32x16 s = MFMA32(a0, qa0, f32x16{});
                s = MFMA32(a1, qa1, s);
                float tm = s[0];
#pragma unroll
                for (int i = 1; i < 16; ++i) tm = __builtin_fmaxf(tm, s[i]);
                auto rr = __builtin_amdgcn_permlane32_swap(__float_as_uint(tm), __float_as_uint(tm), false, false);
                tm = __builtin_fmaxf(__uint_as_float(rr[0]), __uint_as_float(rr[1]));
                const float mn = __builtin_fmaxf(m, tm);
                const float alpha = __builtin_amdgcn_exp2f(m - mn);
                m = mn;
                float ps = 0.f;
#pragma unroll
                for (int i = 0; i < 16; ++i) { s[i] = __builtin_amdgcn_exp2f(s[i] - mn); ps += s[i]; oA[i] *= alpha; }
                ls = ls * alpha + ps;
                oA = MFMA32(b0, PB(pack8n(s, 0)), oA);
                oA = MFMA32(b1, PB(pack8n(s, 1)), oA);
            }
            auto rr = __builtin_amdgcn_permlane32_swap(__float_as_uint(ls), __float_as_uint(ls), false, false);
            asm volatile("s_nop 7\n\ts_nop 7" : "+v"(oA));
            finish(__uint_as_float(rr[0]) + __uint_as_float(rr[1]));
        }
    }
    u32x4 w3[4];
    {
        const unsigned og = (unsigned)WS_O + (unsigned)(TA * 32 + h) * 2048u + lane * 16;
        st_sc1(on0, rs, og);
        st_sc1(on1, rs, og + 1024);
        lds_add(fl + C_S1, lane);
        const char* s2 = wsrc(half ^ 1, 32 + (w & 3) * 4);
        const char* s3 = wsrc(half, 48 + (w & 3) * 4);
        lds_wait_ge(fl + C_S1, 12u);
        DG(5);
#pragma unroll
        for (int i = 0; i < 4; ++i)
            glds16(wvoff, s2 + i * 16384, lds0 + K2_SLOT + ((w ^ 4) * 4 + i) * 1024);
#pragma unroll
        for (int i = 0; i < 4; ++i) w3[i] = gld128(wvoff, s3 + i * 16384);
    }
    const int j4 = w & 3;
    {
        const int T3 = tt * 4 + j4;
        const unsigned og = (unsigned)WS_O + (unsigned)T3 * 65536u + half * 32768 + lane * 16;
        const int r = lane & 31, hh = lane >> 5;
        const int g = lane >> 4, q = (lane & 15) >> 2, p = lane & 3;
        int woff[2];
#pragma unroll
        for (int n = 0; n < 2; ++n) {
            const int c = 4 * (n ^ half) + 2 * (p & 1) + (g & 1);
            woff[n] = (4 * hh + q) * 128 + ((c ^ ((q >> 1) << 2)) << 4) + (p >> 1) * 8;
        }
        f32x16 acc[2] = {f32x16{}, f32x16{}};
        lds_ptr wl[2][2] = {{lds + half * 16384 + woff[0], lds + half * 16384 + woff[1]},
                            {lds + K2_SF + half * 16384 + woff[0], lds + K2_SF + half * 16384 + woff[1]}};
        lds_ptr w3d = lds + K2_SF + w * 4096 + lane * 16;
        asm volatile("" : "+v"(wl[0][0]), "+v"(wl[0][1]), "+v"(wl[1][0]), "+v"(wl[1][1]), "+v"(w3d));
        s16x4 wa[WD + 1][2], wb[WD + 1][2];
#define LDW(s, n) do { const int f_ = (s) < 8 || (s) >= 24, o_ = (f_ ? 0 : (((s) >> 3) - 1) * K2_SLOT) + ((s) & 7) * 2048; \
            wa[(s) % (WD + 1)][n] = lds_tr(wl[f_][n] + o_); wb[(s) % (WD + 1)][n] = lds_tr(wl[f_][n] + o_ + 1024); SBAR(); } while (0)
#pragma unroll
        for (int s = 0; s < WD; ++s) { LDW(s, 0); LDW(s, 1); }
        u32x4 of[OWIN];
#define LDO(i) do { of[(i) % OWIN] = ld_sc1s(rs, og, (((i) + nt * KROT) & 31) * 1024); SBAR(); } while (0)
        for (unsigned spins = 0; spins < (1u << 20); ++spins) {
#pragma unroll
            for (int i = 0; i < OW0; ++i) LDO(i);
            unsigned m = mx8(of[0], of[1]);
#pragma unroll
            for (int i = 2; i < OW0; ++i) m = mx4(m, of[i]);
            if (!missing(m)) break;
            DGC(12);
        }
        lds_add(fl + C_S2, lane);
#pragma unroll
        for (int i = OW0; i < OWIN; ++i) LDO(i);
        DG(6);
        __builtin_amdgcn_s_setprio(P3_PRIO);
        SBAR();
        unsigned fv = 0;
#define FLAG_RD(a) do { fv = *(volatile LAS unsigned*)(a); SBAR(); } while (0)
#define FLAG_CHK(a, n) do { if (__builtin_expect((unsigned)__builtin_amdgcn_readfirstlane(fv) < (n), 0)) lds_wait_ge(a, n); CFENCE(); SBAR(); } while (0)
#pragma unroll
        for (int s = 0; s < 32; ++s) {
            acc[0] = MFMA32(cat8(wa[s % (WD + 1)][0], wb[s % (WD + 1)][0]), PB(of[s % OWIN]), acc[0]);
            SBAR();
            if (s == 16 - WD) FLAG_CHK(fl + C_S2, 8u);
            if (s == 24 - WD) FLAG_CHK(fl + C_S3 + half * 4, 4u);
            if (s + WD < 32) LDW(s + WD, 0);
            if ((s & 1) && s + 1 >= OW0 && s + 1 < 32 && __builtin_expect(missing(mx8(of[(s + 1) % OWIN], of[(s + 2) % OWIN])), 0)) {
                for (unsigned spins = 0; spins < (1u << 20); ++spins) {
                    __builtin_amdgcn_s_sleep(2);
                    LDO(s + 1); LDO(s + 2);
                    DGC(13);
                    if (!missing(mx8(of[(s + 1) % OWIN], of[(s + 2) % OWIN]))) break;
                }
            }
            SBAR();
            acc[1] = MFMA32(cat8(wa[s % (WD + 1)][1], wb[s % (WD + 1)][1]), PB(of[s % OWIN]), acc[1]);
            SBAR();
            if (s + WD < 32) LDW(s + WD, 1);
            if (s + OWIN < 32) LDO(s + OWIN);
            if (s == 7 - WD) lds_add(fl + C_U0 + half * 4, lane);
            if (s == W3_AT - 2) FLAG_RD(fl + C_U0 + half * 4);
            if (s == 14 - WD) FLAG_RD(fl + C_S2);
            if (s == 22 - WD) FLAG_RD(fl + C_S3 + half * 4);
            if (s == W3_AT) FLAG_CHK(fl + C_U0 + half * 4, 4u);
            if (s >= W3_AT && s < W3_AT + 4) {
                asm volatile("" : "+v"(w3[(s - W3_AT) & 3]));
                *(LAS u32x4*)(w3d + ((s - W3_AT) & 3) * 1024) = w3[(s - W3_AT) & 3];
            }
            if (s == W3_AT + 3) lds_add(fl + C_S3 + half * 4, lane);
            if (ZL_GATE && s + OWIN == ZL_AT) lds_add(fl + C_OL, lane);
            if (s == P3_T1) __builtin_amdgcn_s_setprio(P3_PRIO - 1);
            if (s == P3_T2) __builtin_amdgcn_s_setprio(P3_PRIO - 2 < 0 ? 0 : P3_PRIO - 2);
            SBAR();
            if (s == 15) DG(11);
            if (s == 23) DG(14);
        }
        DG(7);
        const lds_ptr xo = lds + w * 4096 + lane * 16;
#pragma unroll
        for (int i = 0; i < 4; ++i) *(LAS f32x4*)(xo + i * 1024) = f32x4{acc[1][4 * i], acc[1][4 * i + 1], acc[1][4 * i + 2], acc[1][4 * i + 3]};
        lds_add(fl + F_R + w * 4, lane);
        lds_wait_ge(fl + F_R + (w ^ 4) * 4, 1u);
        const lds_ptr xi = lds + (w ^ 4) * 4096 + lane * 16;
#pragma unroll
        for (int i = 0; i < 4; ++i) {
            const f32x4 t = *(const LAS f32x4*)(xi + i * 1024);
#pragma unroll
            for (int j = 0; j < 4; ++j) acc[0][4 * i + j] = add_vv(acc[0][4 * i + j], t[j]);
        }
        char* dst = out + (size_t)(T3 * 32 + r) * 4096 + 2048 + nt * 128 + half * 64 + hh * 32;
        *(u32x4*)(dst) = pack8n(acc[0], 0);
        *(u32x4*)(dst + 16) = pack8n(acc[0], 1);
    }
    DG_END(8);
}

extern "C" void kernel_launch(void* const* d_in, const int* in_sizes, int n_in,
                              void* d_out, int out_size, void* d_ws, size_t ws_size,
                              hipStream_t stream) {
    const void* A_q = d_in[0];
    const void* A_k = d_in[1];
    const void* A_v = d_in[2];
    const void* A_shift = d_in[3];
    const void* A_ssmk = d_in[4];
    const void* A_D = d_in[5];
    const void* B_x = d_in[6];
    const void* B_wqkv = d_in[7];
    const void* B_wo = d_in[8];
    char* ws = (char*)d_ws;
    k1_qkv<<<256, 512, 0, stream>>>((const char*)B_x, (const char*)B_wqkv, ws);
    k2_attn<<<256, 768, 0, stream>>>(ws, (const char*)A_q, (const char*)A_k, (const char*)A_v, (const char*)A_shift,
                                     (const char*)A_ssmk, (const unsigned*)A_D, (const char*)B_wo, (char*)d_out);
}
```

```cpp
#include <hip/hip_runtime.h>
#include <hip/hip_bf16.h>
#include <stdint.h>

typedef short bf16x8 __attribute__((ext_vector_type(8)));
typedef short s16x4 __attribute__((ext_vector_type(4)));
typedef float f32x16 __attribute__((ext_vector_type(16)));
typedef float f32x4 __attribute__((ext_vector_type(4)));
typedef float f32x2 __attribute__((ext_vector_type(2)));
typedef unsigned u32x4 __attribute__((ext_vector_type(4)));
typedef unsigned u32x2 __attribute__((ext_vector_type(2)));
typedef __bf16 bf16x2_t __attribute__((ext_vector_type(2)));

#define LAS __attribute__((address_space(3)))
typedef LAS char* lds_ptr;
#define DI __device__ __forceinline__
#define SBAR() __builtin_amdgcn_sched_barrier(0)
#define MFMA32(a, b, c) __builtin_amdgcn_mfma_f32_32x32x16_bf16((a), (b), (c), 0, 0, 0)
#define MFMA16(a, b, c) __builtin_amdgcn_mfma_f32_16x16x32_bf16((a), (b), (c), 0, 0, 0)
#define WAIT_BAR(N) asm volatile("s_waitcnt vmcnt(" #N ") lgkmcnt(0)\n\ts_barrier" ::: "memory")
#define LGKM_BAR() asm volatile("s_waitcnt lgkmcnt(0)\n\ts_barrier" ::: "memory")

#define DG_DECL(K)
#define DG(i)
#define DG_END(i)
#define DGC(i)

constexpr int S_LEN = 2048, DM = 1024, NH = 32;

constexpr size_t WS_Q = 0;
constexpr size_t WS_KV = 4u << 20;
constexpr size_t WS_O = 12u << 20;
constexpr size_t WS_KVR = 16u << 20;
constexpr size_t WS_ZT = 20u << 20;
constexpr size_t WS_CNT = 24u << 20;

DI unsigned cvtpk(float lo, float hi) {
    f32x2 v = {lo, hi};
    return __builtin_bit_cast(unsigned, __builtin_convertvector(v, bf16x2_t));
}
DI float bflo(unsigned u) { return __uint_as_float(u << 16); }
DI float bfhi(unsigned u) { return __uint_as_float(u & 0xffff0000u); }

DI void glds16(unsigned voff, const void* sbase, unsigned lds_dst) {
    unsigned sv;
    asm volatile("s_mov_b32 %0, m0\n\ts_mov_b32 m0, %3\n\ts_nop 0\n\tglobal_load_lds_dwordx4 %1, %2\n\ts_mov_b32 m0, %0"
                 : "=&s"(sv) : "v"(voff), "s"(sbase), "s"(lds_dst) : "memory");
}
DI bf16x8 lds_rd128(lds_ptr p) { return *(const LAS bf16x8*)p; }
DI s16x4 lds_tr(lds_ptr p) {
    return __builtin_bit_cast(s16x4, __builtin_amdgcn_ds_read_tr16_b64_v4i16((LAS s16x4*)p));
}
DI bf16x8 cat8(s16x4 a, s16x4 b) { return __builtin_shufflevector(a, b, 0, 1, 2, 3, 4, 5, 6, 7); }
DI u32x4 pack8(const f32x16& x, int s, float sc) {
    u32x4 p;
    p[0] = cvtpk(x[8 * s + 0] * sc, x[8 * s + 1] * sc);
    p[1] = cvtpk(x[8 * s + 2] * sc, x[8 * s + 3] * sc);
    p[2] = cvtpk(x[8 * s + 4] * sc, x[8 * s + 5] * sc);
    p[3] = cvtpk(x[8 * s + 6] * sc, x[8 * s + 7] * sc);
    return p;
}
DI u32x4 pack8n(const f32x16& x, int s) {
    u32x4 p;
    p[0] = cvtpk(x[8 * s + 0], x[8 * s + 1]);
    p[1] = cvtpk(x[8 * s + 2], x[8 * s + 3]);
    p[2] = cvtpk(x[8 * s + 4], x[8 * s + 5]);
    p[3] = cvtpk(x[8 * s + 6], x[8 * s + 7]);
    return p;
}
DI void rnd2(float& a, float& b) { unsigned u = cvtpk(a, b); a = bflo(u); b = bfhi(u); }

#define CONV_PACE()
#define RAW_BAR() asm volatile("s_barrier" ::: "memory")
DI unsigned gld32(unsigned voff, const void* sbase) {
    unsigned r;
    asm volatile("global_load_dword %0, %1, %2" : "=v"(r) : "v"(voff), "s"(sbase) : "memory");
    return r;
}
DI u32x2 gld64(unsigned voff, const void* sbase) {
    u32x2 r;
    asm volatile("global_load_dwordx2 %0, %1, %2" : "=v"(r) : "v"(voff), "s"(sbase) : "memory");
    return r;
}
DI float mul_sv(float s, float v) { float r; asm("v_mul_f32_e32 %0, %1, %2" : "=v"(r) : "s"(s), "v"(v)); return r; }
DI float mul_vv(float a, float b) { float r; asm("v_mul_f32_e32 %0, %1, %2" : "=v"(r) : "v"(a), "v"(b)); return r; }
DI float add_vv(float a, float b) { float r; asm("v_add_f32_e32 %0, %1, %2" : "=v"(r) : "v"(a), "v"(b)); return r; }
DI void acc_vv(float& a, float b) { asm("v_add_f32_e32 %0, %0, %1" : "+v"(a) : "v"(b)); }
DI void fmac_vv(float& acc, float a, float b) { asm("v_fmac_f32_e32 %0, %1, %2" : "+v"(acc) : "v"(a), "v"(b)); }
DI void fmac_sv(float& acc, float s, float v) { asm("v_fmac_f32_e32 %0, %1, %2" : "+v"(acc) : "s"(s), "v"(v)); }
typedef __attribute__((address_space(1))) unsigned gu32;
#define RLX_AGENT __ATOMIC_RELAXED, __HIP_MEMORY_SCOPE_AGENT
#define CFENCE() asm volatile("" ::: "memory")
#define DRAIN() asm volatile("s_waitcnt vmcnt(0)" ::: "memory")
constexpr int AUX_SC1 = 16;
DI u32x4 ld_sc1(__amdgpu_buffer_rsrc_t rs, unsigned off) { return __builtin_amdgcn_raw_buffer_load_b128(rs, (int)off, 0, AUX_SC1); }
DI u32x4 ld_sc1s(__amdgpu_buffer_rsrc_t rs, unsigned off, int soff) { return __builtin_amdgcn_raw_buffer_load_b128(rs, (int)off, soff, AUX_SC1); }
DI void st_sc1(u32x4 v, __amdgpu_buffer_rsrc_t rs, unsigned off) { __builtin_amdgcn_raw_buffer_store_b128(v, rs, (int)off, 0, AUX_SC1); }
constexpr unsigned EMPTY = 0xffffffffu;
DI unsigned nz(unsigned x) { return x < EMPTY - 1u ? x : EMPTY - 1u; }
DI u32x4 nz4(u32x4 v) { return u32x4{nz(v[0]), nz(v[1]), nz(v[2]), nz(v[3])}; }
DI float fmx(float a, float b) { return __builtin_elementwise_maximum(a, b); }
DI unsigned umax(unsigned a, unsigned b) { return a > b ? a : b; }
DI unsigned umax3(unsigned a, unsigned b, unsigned c) { return umax(umax(a, b), c); }
DI unsigned mx4(unsigned m, u32x4 v) { return umax3(umax3(m, v[0], v[1]), v[2], v[3]); }
DI unsigned mx8(u32x4 a, u32x4 b) { return umax(umax3(umax3(umax3(a[0], a[1], a[2]), a[3], b[0]), b[1], b[2]), b[3]); }
DI bool missing(unsigned m) { return __builtin_amdgcn_ballot_w64(m == EMPTY) != 0; }
DI unsigned lds_add_rtn(lds_ptr p, int lane) {
    unsigned old = 0;
    CFENCE();
    if (lane == 0) old = __hip_atomic_fetch_add((LAS unsigned*)p, 1u, __ATOMIC_RELAXED, __HIP_MEMORY_SCOPE_WORKGROUP);
    old = __builtin_amdgcn_readfirstlane(old);
    CFENCE();
    return old;
}
DI void lds_add(lds_ptr p, int lane) {
    unsigned long long sv;
    asm volatile("s_mov_b64 %0, exec\n\ts_mov_b64 exec, 1\n\tds_add_u32 %1, %2\n\ts_mov_b64 exec, %0"
                 : "=&s"(sv) : "v"(p), "v"(1u) : "memory");
}
DI void lds_wait_ge(lds_ptr p, unsigned v) {
    CFENCE();
    while ((unsigned)__builtin_amdgcn_readfirstlane(*(volatile LAS unsigned*)p) < v) __builtin_amdgcn_s_sleep(1);
    CFENCE();
}
DI void poll_ge(gu32* word, unsigned want) {
    CFENCE();
    for (unsigned spins = 0; spins < (1u << 22); ++spins) {
        if ((unsigned)__builtin_amdgcn_readfirstlane(__hip_atomic_load(word, RLX_AGENT)) >= want) break;
        __builtin_amdgcn_s_sleep(1);
    }
    __builtin_amdgcn_fence(__ATOMIC_ACQUIRE, "wavefront");
    CFENCE();
}
#define PB(x) __builtin_bit_cast(bf16x8, x)
#define PRIO(x) do { __builtin_amdgcn_s_setprio(x); SBAR(); } while (0)
DI u32x4 gld128(unsigned voff, const void* sbase) {
    u32x4 r;
    asm volatile("global_load_dwordx4 %0, %1, %2" : "=v"(r) : "v"(voff), "s"(sbase) : "memory");
    return r;
}

#define K1_UNR 15
#define K1_LA 5
#define K1_ROT ((tb & 3) * 4)
constexpr int K1_STAGE = 40960, K1U = K1_UNR;
__global__ __launch_bounds__(512, 2) void k1_qkv(const char* __restrict__ X, const char* __restrict__ W,
                                                 char* __restrict__ ws) {
    __shared__ __attribute__((aligned(16))) char lds_raw[4 * K1_STAGE];
    const lds_ptr lds = (lds_ptr)lds_raw;
    const unsigned lds0 = (unsigned)(uintptr_t)lds;
    const int tid = threadIdx.x, lane = tid & 63;
    const int w = __builtin_amdgcn_readfirstlane(tid >> 6);
    const int b = blockIdx.x, xcd = b & 7, idx = b >> 3;
    const int hp = (xcd >> 1) * 4 + (idx & 3), tb = (xcd & 1) * 8 + (idx >> 2);
    DG_DECL(0)

    const int lw = w & 3;
    const int rot = K1_ROT;
    unsigned voffX[4], voffW[2];
#pragma unroll
    for (int pp = 0; pp < 4; ++pp) {
        const int row = 32 * lw + 8 * pp + (lane >> 3);
        const int ch = (lane & 7) ^ ((row >> 1) & 7);
        voffX[pp] = (unsigned)(tb * 128 + row) * 2048u + ch * 16;
    }
#pragma unroll
    for (int pp = 0; pp < 2; ++pp) {
        const int krow = 16 * lw + 8 * pp + (lane >> 3);
        const int ch = (lane & 7) ^ (((lane >> 4) & 1) << 2);
        voffW[pp] = (unsigned)krow * 6144u + ch * 16;
    }
    auto issue = [&](int kt, int lo, int hi) {
        const int kr = (kt + rot) & 15;
        const char* xb = X + kr * 128;
        const char* wb = W + (size_t)kr * (64 * 6144) + hp * 128;
        const unsigned st = lds0 + (kt & 3) * K1_STAGE;
#pragma unroll
        for (int pp = 0; pp < 4; ++pp)
            if (pp >= lo && pp < hi) glds16(voffX[pp], xb, st + (4 * lw + pp) * 1024);
#pragma unroll
        for (int sec = 0; sec < 3; ++sec)
#pragma unroll
            for (int pp = 0; pp < 2; ++pp)
                if (4 + 2 * sec + pp >= lo && 4 + 2 * sec + pp < hi)
                    glds16(voffW[pp], wb + sec * 2048, st + 16384 + sec * 8192 + (2 * lw + pp) * 1024);
    };
    DG(13);
    if (w < 4) { issue(0, 0, 5); issue(1, 0, 5); }
    else { issue(0, 5, 10); issue(1, 5, 10); }
    DG(14);
    int w2 = w;
    asm volatile("" : "+s"(w2));

    if (w2 >= 4) {
#define WAITN_BAR(N) asm volatile("s_waitcnt vmcnt(%0) lgkmcnt(0)\n\ts_barrier" :: "n"(N) : "memory")
        issue(2, 0, K1_LA);
        asm volatile("s_waitcnt vmcnt(%0)" :: "n"(5 + K1_LA) : "memory");
#pragma unroll 1
        for (int kt = 0; kt < 13; ++kt) {
            WAITN_BAR(10 + K1_LA);
            issue(kt + 2, K1_LA, 10);
            issue(kt + 3, 0, K1_LA);
        }
        WAITN_BAR(10 + K1_LA);
        issue(15, K1_LA, 10);
        WAIT_BAR(10);
        WAIT_BAR(0);
        if (lw == 0 && lane == 0) *(unsigned*)(ws + WS_CNT + b * 256) = 0u;
        DG_END(5);
        return;
    }

    const int wm = w >> 1, wn = w & 1;
    const int r = lane & 31, hh = lane >> 5;
    const int g = lane >> 4, q = (lane & 15) >> 2, p = lane & 3;
    const int xoff0 = (64 * wm + r) * 128 + ((hh ^ ((r >> 1) & 7)) << 4);
    const int woff0 = 16384 + (8 * hh + q) * 128 + (((4 * wn + 2 * (g & 1) + (p >> 1)) ^ ((q >> 1) << 2)) << 4) + (p & 1) * 8;

    f32x16 acc[2][3];
#pragma unroll
    for (int m = 0; m < 2; ++m)
#pragma unroll
        for (int s = 0; s < 3; ++s) acc[m][s] = f32x16{};
    bf16x8 xf[2][2];
    s16x4 wl[2][3], wh[2][3];
#define K1_WF(f, sec) cat8(wl[f][sec], wh[f][sec])
#define K1_STEP(F, st, t, BAR_HERE) do { \
        const lds_ptr xp_ = (st) + (xoff0 ^ ((t) << 5)); const lds_ptr wp_ = (st) + woff0 + (t) * 2048; \
        acc[0][0] = MFMA32(K1_WF(F, 0), xf[F][0], acc[0][0]); SBAR(); \
        if (BAR_HERE) { WAIT_BAR(0); SBAR(); } \
        xf[F ^ 1][0] = lds_rd128(xp_); wl[F ^ 1][0] = lds_tr(wp_); wh[F ^ 1][0] = lds_tr(wp_ + 512); SBAR(); \
        acc[0][1] = MFMA32(K1_WF(F, 1), xf[F][0], acc[0][1]); SBAR(); \
        wl[F ^ 1][1] = lds_tr(wp_ + 8192); wh[F ^ 1][1] = lds_tr(wp_ + 8192 + 512); SBAR(); \
        acc[0][2] = MFMA32(xf[F][0], K1_WF(F, 2), acc[0][2]); SBAR(); \
        wl[F ^ 1][2] = lds_tr(wp_ + 16384); wh[F ^ 1][2] = lds_tr(wp_ + 16384 + 512); xf[F ^ 1][1] = lds_rd128(xp_ + 4096); SBAR(); \
        acc[1][0] = MFMA32(K1_WF(F, 0), xf[F][1], acc[1][0]); SBAR(); \
        acc[1][1] = MFMA32(K1_WF(F, 1), xf[F][1], acc[1][1]); \
        acc[1][2] = MFMA32(xf[F][1], K1_WF(F, 2), acc[1][2]); SBAR(); \
    } while (0)

    WAIT_BAR(5);
    DG(3);
    {
        const lds_ptr xp_ = lds + xoff0; const lds_ptr wp_ = lds + woff0;
        xf[0][0] = lds_rd128(xp_); xf[0][1] = lds_rd128(xp_ + 4096);
#pragma unroll
        for (int sec = 0; sec < 3; ++sec) { wl[0][sec] = lds_tr(wp_ + sec * 8192); wh[0][sec] = lds_tr(wp_ + sec * 8192 + 512); }
    }
    SBAR();
#pragma unroll K1U
    for (int kt = 0; kt < 15; ++kt) {
        const lds_ptr st = lds + (kt & 3) * K1_STAGE;
        const lds_ptr sn = lds + ((kt + 1) & 3) * K1_STAGE;
        K1_STEP(0, st, 1, false);
        K1_STEP(1, st, 2, false);
        K1_STEP(0, st, 3, false);
        K1_STEP(1, sn, 0, true);
    }
    {
        const lds_ptr st = lds + 3 * K1_STAGE;
        K1_STEP(0, st, 1, false);
        K1_STEP(1, st, 2, false);
        K1_STEP(0, st, 3, false);
#pragma unroll
        for (int m = 0; m < 2; ++m) {
            acc[m][1] = MFMA32(K1_WF(1, 1), xf[1][m], acc[m][1]);
            acc[m][2] = MFMA32(xf[1][m], K1_WF(1, 2), acc[m][2]);
            acc[m][0] = MFMA32(K1_WF(1, 0), xf[1][m], acc[m][0]);
        }
    }
    DG(4);
    {
        const int h = 2 * hp + wn;
        const float qscale = 0.17677669529663687f * 1.4426950408889634f;
        const __amdgpu_buffer_rsrc_t rs = __builtin_amdgcn_make_buffer_rsrc(ws, 0, 32 << 20, 0x00020000);
#pragma unroll
        for (int m = 0; m < 2; ++m) {
            const int T = tb * 4 + wm * 2 + m;
            const unsigned dq = (unsigned)WS_Q + (h * 64 + T) * 2048 + lane * 16;
            const unsigned dk = (unsigned)WS_KV + (h * 64 + T) * 4096 + lane * 16;
            st_sc1(pack8n(acc[m][1], 0), rs, dk);
            st_sc1(pack8n(acc[m][1], 1), rs, dk + 1024);
            st_sc1(pack8n(acc[m][2], 0), rs, dk + 2048);
            st_sc1(pack8n(acc[m][2], 1), rs, dk + 3072);
            st_sc1(pack8(acc[m][0], 0, qscale), rs, dq);
            st_sc1(pack8(acc[m][0], 1, qscale), rs, dq + 1024);
        }
    }
    DG_END(5);
}

constexpr int K2_SLOT = 32768;
constexpr int K2_SF = 65536;
constexpr int K2_CV = 98304;
constexpr int CV_HA = 0, CV_HB = 4160, CV_KV = 8320, CV_BYTES = 8320 + 480 + 4096;
constexpr int K2_LX = K2_CV + 4 * CV_BYTES;
constexpr int K2_FL = K2_LX + 2048;
constexpr int K2_SCR = K2_FL + 256;
constexpr int K2_LDS = K2_SCR + 4096;
constexpr int F_X = 0, F_R = 32, F_W2 = 64, C_OST = 68, C_S1 = 76, C_S2 = 80, C_S3 = 128, C_ZST = 88, F_ZGO = 92,
              C_KST = 96, C_U0 = 100, C_LD = 108, F_KGO = 116, C_END = 120, C_OL = 72;
#define NG_IN 0
#define GATE_WORD C_END
#define GATE_VAL 0
#define X_NZ 4
#define X_NW3 8
#define ZP_GATE 4u
#define ZL_GATE 4
#define ZL_AT 31
#define Z_SLEEP0 0
#define O_SLEEP 0
#define Z_SLEEP 8
#define OWIN 16
#define OW0 8
#define WD 2
#define W3_AT 10
#define X_NOLDW 0
#define X_NOLDO 0
#define X_NOCHK 0
#define KROT 2
#define NG_LAST 0
#define PACE_IN "s_nop 7"
#define PACE_OUT "s_nop 1"
#define P3_PRIO 1
#define TOP_SLEEP 2
#define A_AUX 16
#define B_AUX 16
#define B_LDS 0
#define ZE_PRIO 3
#define P3_T1 99
#define P3_T2 99
#define AUX_PRIO 2

__global__ __launch_bounds__(768, 3) void k2_attn(char* __restrict__ ws, const char* __restrict__ Aq,
                                                  const char* __restrict__ Ak, const char* __restrict__ Av,
                                                  const char* __restrict__ shiftk, const char* __restrict__ ssmk,
                                                  const unsigned* __restrict__ ssmD, const char* __restrict__ Wo,
                                                  char* __restrict__ out) {
    __shared__ __attribute__((aligned(16))) char lds_raw[K2_LDS];
    const lds_ptr lds = (lds_ptr)lds_raw;
    const lds_ptr fl = lds + K2_FL;
    const unsigned lds0 = (unsigned)(uintptr_t)lds;
    const int tid = threadIdx.x, lane = tid & 63;
    const int w = __builtin_amdgcn_readfirstlane(tid >> 6);
    const int b = blockIdx.x, xcd = b & 7, idx = b >> 3;
    const int h = xcd * 4 + (idx >> 3), qb = idx & 7;
    const int tt = xcd * 2 + (idx >> 4), nt = idx & 15;
    const char* kvg = ws + WS_KV + (size_t)h * 64 * 4096;
    const __amdgpu_buffer_rsrc_t rs = __builtin_amdgcn_make_buffer_rsrc(ws, 0, 32 << 20, 0x00020000);
    const __amdgpu_buffer_rsrc_t ro = __builtin_amdgcn_make_buffer_rsrc(out, 0, 8 << 20, 0x00020000);
    gu32* const cnt = (gu32*)(ws + WS_CNT);
    auto signal = [&]() {
        (void)__hip_atomic_fetch_add(cnt + ((lane & 31) * 8 + (lane < 32 ? xcd : qb)) * 64, 1u, RLX_AGENT);
    };
    const unsigned wvoff = (unsigned)(lane >> 3) * 2048u + nt * 128 + (((lane & 7) ^ (((lane >> 4) & 1) << 2)) << 4);
    auto wsrc = [&](int hf, int lp) { return Wo + (size_t)(hf * 64 + ((lp + nt * KROT * 2) & 63)) * 16384; };
    DG_DECL(1)
    const unsigned voff = lane * 16;
    const int cw = w & 3;
    auto issue = [&](int st) {
        const char* src = kvg + (st * 4 + cw) * 4096;
        const unsigned dst = lds0 + (st & 1) * K2_SLOT + cw * 4096;
#pragma unroll
        for (int i = 0; i < 4; ++i) glds16(voff, src + i * 1024, dst + i * 1024);
#pragma unroll
        for (int i = 0; i < 4; ++i) glds16(voff, src + 131072 + i * 1024, dst + 16384 + i * 1024);
    };
    if (w >= 8) {
        __builtin_amdgcn_s_sleep(TOP_SLEEP);
#pragma unroll
        for (int i = 2; i < 8; ++i)
            glds16(voff, kvg + (i & 1) * 131072 + (i >> 1) * 4096 + cw * 1024, lds0 + (i & 1) * 16384 + (i >> 1) * 4096 + cw * 1024);
        RAW_BAR();
        issue(1);
        WAIT_BAR(8);
    } else {
        const int tq = cw * 2 + (w >> 2);
        const char* qg = ws + WS_Q + (size_t)(h * 64 + qb * 8 + tq) * 2048;
        glds16(voff, kvg + (w >> 2) * 131072 + cw * 1024, lds0 + (w >> 2) * 16384 + cw * 1024);
        glds16(voff, qg, lds0 + K2_SF + tq * 2048);
        glds16(voff, qg + 1024, lds0 + K2_SF + tq * 2048 + 1024);
    }
    int w2 = w;
    asm volatile("" : "+s"(w2));

    if (w2 >= 8) {
        __builtin_amdgcn_s_setprio(3);
        const int ch = xcd * 128 + idx * 4 + cw;
        const lds_ptr cv = lds + K2_CV + cw * CV_BYTES;
        {
            const u32x4 z4 = {0u, 0u, 0u, 0u};
            if (lane < 2) *(LAS u32x4*)(cv + CV_HA + lane * 16) = z4;
            if (lane < 30) *(LAS u32x4*)(cv + CV_KV + lane * 16) = z4;
            if (cw == 0 && lane < 16) *(LAS u32x4*)(fl + lane * 16) = z4;
        }
        const int c0 = xcd * 128 + cw * 32, cq = lane & 7, rg = lane >> 3;
        const int row0 = idx * 64 + rg * 8;
        const unsigned roff = (unsigned)row0 * 2048u + (c0 + cq * 4) * 2;
        const unsigned toffs = (unsigned)(c0 + (lane >> 3)) * 4096u + idx * 128;
        u32x4 sk[2];
        sk[0] = gld128((unsigned)(c0 + cq * 4) * 8u, shiftk); sk[1] = gld128((unsigned)(c0 + cq * 4) * 8u, shiftk + 16);
        unsigned dd = gld32((unsigned)(ch >> 1) * 4u, ssmD);
        auto stage = [&](const char* src, int dst) {
            const unsigned sbase = (unsigned)(idx * 64 + cw * 16 + (lane >> 4)) * 2048u + xcd * 256;
#pragma unroll
            for (int i = 0; i < 4; ++i)
                glds16(sbase + (((lane & 15) ^ (((2 * cw + (i >> 1)) & 3) << 2)) << 4), src + i * 8192, lds0 + dst + cw * 4096 + i * 1024);
        };
        auto blank = [&](unsigned off) {
            const u32x4 e4 = {EMPTY, EMPTY, EMPTY, EMPTY};
            CFENCE();
            st_sc1(e4, rs, off + voff); st_sc1(e4, rs, off + 1024 + voff);
            CFENCE();
        };
        WAIT_BAR(3);
        DG(12);
        issue(2);
        stage(Ak, K2_SF);
        glds16((unsigned)(idx * 64 - (idx ? 4 : 0) + (lane >> 4)) * 2048u + xcd * 256 + (((lane & 15) ^ 12) << 4), Ak, lds0 + K2_SCR + cw * 1024);
        blank((unsigned)WS_O + (unsigned)((qb * 8 + cw * 2) * 32 + h) * 2048u);
        WAIT_BAR(7);
        DG(9);
        issue(3);
        stage(Av, K2_SF + 16384);
        blank((unsigned)WS_O + (unsigned)((qb * 8 + cw * 2 + 1) * 32 + h) * 2048u);
        WAIT_BAR(6);
        DG(3);
        issue(4);
#pragma unroll
        for (int i = 0; i < 4; ++i) glds16(voff, ssmk + (size_t)ch * 4096 + i * 1024, lds0 + K2_CV + cw * CV_BYTES + CV_HA + 32 + i * 1024);
        blank((unsigned)WS_ZT + ch * 4096); blank((unsigned)WS_ZT + ch * 4096 + 2048);
        WAIT_BAR(8);
        DG(5);
        issue(5);
        SBAR();
        asm volatile("" : "+v"(sk[0]), "+v"(sk[1]), "+v"(dd));
        dd = __builtin_amdgcn_readfirstlane(dd);
        {
            u32x2 kr[11], vr[8];
            const lds_ptr kb = lds + K2_SF + rg * 2048 + ((cw ^ (rg & 3)) << 6) + cq * 8;
            const lds_ptr hb = lds + (rg ? K2_SF + rg * 2048 - 768 : K2_SCR + cw * 1024 + 256) + ((cw ^ ((rg + 3) & 3)) << 6) + cq * 8;
#pragma unroll
            for (int i = 0; i < 3; ++i) kr[i] = *(const LAS u32x2*)(hb + i * 256);
#pragma unroll
            for (int i = 0; i < 8; ++i) kr[3 + i] = *(const LAS u32x2*)(kb + i * 256);
#pragma unroll
            for (int i = 0; i < 8; ++i) vr[i] = *(const LAS u32x2*)(kb + 16384 + i * 256);
            if (row0 == 0) { kr[0] = u32x2{0u, 0u}; kr[1] = u32x2{0u, 0u}; kr[2] = u32x2{0u, 0u}; }
            const lds_ptr tb = cv + CV_KV + 480;
#pragma unroll
            for (int c = 0; c < 4; ++c) {
                const unsigned s01 = sk[c >> 1][(c & 1) * 2], s23 = sk[c >> 1][(c & 1) * 2 + 1];
                const float w0 = bflo(s01), w1 = bfhi(s01), w2 = bflo(s23), w3 = bfhi(s23);
                float kf[11], kv[8];
#pragma unroll
                for (int i = 0; i < 11; ++i) kf[i] = (c & 1) ? bfhi(kr[i][c >> 1]) : bflo(kr[i][c >> 1]);
#pragma unroll
                for (int i = 0; i < 8; ++i) {
                    float a = mul_vv(w3, kf[i]);
                    fmac_vv(a, w2, kf[i + 1]); fmac_vv(a, w1, kf[i + 2]); fmac_vv(a, w0, kf[i + 3]);
                    kv[i] = mul_vv(a, (c & 1) ? bfhi(vr[i][c >> 1]) : bflo(vr[i][c >> 1]));
                }
                u32x4 o4;
#pragma unroll
                for (int i = 0; i < 4; ++i) o4[i] = cvtpk(kv[7 - 2 * i], kv[6 - 2 * i]);
                *(LAS u32x4*)(tb + (cq * 4 + c) * 128 + ((rg ^ cq) << 4)) = o4;
            }
#pragma unroll
            for (int i = 0; i < 4; ++i) {
                const u32x4 t = *(const LAS u32x4*)(tb + i * 1024 + lane * 16);
                st_sc1(t, rs, (unsigned)WS_KVR + toffs + i * 32768 + (((lane & 7) ^ (2 * i + (lane >> 5))) << 4));
            }
        }
        CFENCE();
        DG(7);
        WAIT_BAR(4);
        DG(6);
        issue(6);
        asm volatile("s_waitcnt vmcnt(8)" ::: "memory");
        if (lds_add_rtn(fl + C_KST, lane) == 3u) signal();
#pragma unroll
        for (int i = 0; i < 8; ++i)
            glds16(wvoff, wsrc(i >> 2, cw * 4 + (i & 3)), lds0 + K2_SF + (i >> 2) * 16384 + (cw * 4 + (i & 3)) * 1024);
        {
            const u32x4 z4 = {0u, 0u, 0u, 0u};
            if (lane < 2) *(LAS u32x4*)(cv + CV_HB + lane * 16) = z4;
#pragma unroll
            for (int i = 0; i < 4; ++i) {
                const int off = 32 + i * 1024 + lane * 16;
                const u32x4 hv = *(const LAS u32x4*)(cv + CV_HA + off);
                const unsigned pv = *(const LAS unsigned*)(cv + CV_HA + off - 4);
                u32x4 sh;
                sh[0] = __builtin_amdgcn_alignbit(hv[0], pv, 16);
                sh[1] = __builtin_amdgcn_alignbit(hv[1], hv[0], 16);
                sh[2] = __builtin_amdgcn_alignbit(hv[2], hv[1], 16);
                sh[3] = __builtin_amdgcn_alignbit(hv[3], hv[2], 16);
                *(LAS u32x4*)(cv + CV_HB + off) = sh;
            }
        }
        WAIT_BAR(8);
        DG(8);
        issue(7);
#pragma unroll
        for (int i = 0; i < 8; ++i)
            glds16(wvoff, wsrc(idx >> 4, 32 + cw * 8 + i), lds0 + K2_SCR + cw * 1024);
        WAIT_BAR(8);
        DG(11);
        if (cw == 0) { poll_ge(cnt + b * 64, 64u); lds_add(fl + F_KGO, lane); }
        else lds_wait_ge(fl + F_KGO, 1u);
        {
            u32x4 t[4];
#pragma unroll
            for (int i = 0; i < 4; ++i) t[i] = ld_sc1(rs, (unsigned)WS_KVR + ch * 4096 + i * 1024 + voff);
#pragma unroll
            for (int i = 0; i < 4; ++i) *(LAS u32x4*)(cv + CV_KV + 480 + i * 1024 + lane * 16) = t[i];
        }
        const int r1 = lane & 15, q = lane >> 4;
        const int toff = (r1 & 1) ? CV_HA + 2 * (16 + r1 + 9 - 8 * q) : CV_HB + 2 * (16 + r1 + 10 - 8 * q);
        const int boff = CV_KV + 448 + 32 * r1 + 16 * q;
        f32x4 y[8];
        u32x4 tf[8];
        bf16x8 bf[8];
        const lds_ptr bp = cv + boff - 448;
        lds_ptr tp[4];
#pragma unroll
        for (int i = 0; i < 4; ++i) { tp[i] = cv + toff + 1024 * i; asm volatile("" : "+v"(tp[i])); }
#define LDT(bb, a, hf) do { const LAS unsigned* t_ = (const LAS unsigned*)(tp[(a) >> 1] + 64 * (bb) + 512 * ((a) & 1) + 8 * (hf)); \
            tf[a][2 * (hf)] = t_[0]; tf[a][2 * (hf) + 1] = t_[1]; SBAR(); } while (0)
#define LDB(bb, a) do { bf[a] = lds_rd128(bp + 448 - 64 * (bb) + 512 * (a)); SBAR(); } while (0)
#define CM(a, ww) do { asm volatile("v_mfma_f32_16x16x32_bf16 %0, %1, %2, %0" : "+v"(y[(a) + (ww)]) : "v"(tf[a]), "v"(bf[ww])); SBAR(); } while (0)
#define CM0(a, ww) do { asm volatile("v_mfma_f32_16x16x32_bf16 %0, %1, %2, 0" : "=&v"(y[(a) + (ww)]) : "v"(tf[a]), "v"(bf[ww])); SBAR(); } while (0)
#define PC() do { asm volatile(PACE_OUT); SBAR(); } while (0)
#pragma unroll
        for (int a = 0; a < 8; ++a) { if (a < 5) { LDT(0, a, 0); LDT(0, a, 1); } LDB(0, 7 - a); }
#pragma unroll
        for (int i = 0; i < 8; ++i)
            glds16(wvoff, wsrc(i >> 2, 16 + cw * 4 + (i & 3)), lds0 + (i >> 2) * 16384 + (cw * 4 + (i & 3)) * 1024);
#pragma unroll
        for (int i = 0; i < 4; ++i)
            glds16((unsigned)(idx * 64 + cw * 16 + (lane >> 4)) * 2048u + xcd * 256 + (lane & 15) * 16, Aq + i * 8192, lds0 + K2_SCR + cw * 1024);
        RAW_BAR();
        asm volatile("s_waitcnt vmcnt(4)" ::: "memory");
        lds_add(fl + C_S1, lane);
        __builtin_amdgcn_s_setprio(AUX_PRIO);
        const float dch = (ch & 1) ? bfhi(dd) : bflo(dd);
        const lds_ptr kdp = cv + CV_KV + 480 + 32 * r1 + 16 * (q >> 1) + ((q & 1) ? 0 : 8);
        float kd[8][4];
        u32x2 kk;
        unsigned kc0, kc1, zo[8][2];
#define KD_RD(t) do { kk = *(const LAS u32x2*)(kdp + 512 * (t)); SBAR(); } while (0)
#define KD_UP0(t) do { kd[t][0] = bfhi(kk[1]); kd[t][1] = bflo(kk[1]); SBAR(); } while (0)
#define KD_UP1(t) do { kd[t][2] = bfhi(kk[0]); kd[t][3] = bflo(kk[0]); SBAR(); } while (0)
#define KD_MU0(t) do { kd[t][0] = mul_sv(dch, kd[t][0]); kd[t][1] = mul_sv(dch, kd[t][1]); SBAR(); } while (0)
#define KD_MU1(t) do { kd[t][2] = mul_sv(dch, kd[t][2]); kd[t][3] = mul_sv(dch, kd[t][3]); SBAR(); } while (0)
#define KD_CV(t) do { kc0 = cvtpk(kd[t][0], kd[t][1]); kc1 = cvtpk(kd[t][2], kd[t][3]); SBAR(); } while (0)
#define KD_X0(t) do { kd[t][0] = bflo(kc0); kd[t][1] = bfhi(kc0); SBAR(); } while (0)
#define KD_X1(t) do { kd[t][2] = bflo(kc1); kd[t][3] = bfhi(kc1); SBAR(); } while (0)
#define ZF0(t) do { kc0 = cvtpk(y[t][0], y[t][1]); kc1 = cvtpk(y[t][2], y[t][3]); SBAR(); } while (0)
#define ZF1(t) do { y[t][0] = bflo(kc0); y[t][1] = bfhi(kc0); SBAR(); } while (0)
#define ZF2(t) do { y[t][2] = bflo(kc1); y[t][3] = bfhi(kc1); SBAR(); } while (0)
#define ZF3(t) do { y[t][0] = add_vv(y[t][0], kd[t][0]); y[t][1] = add_vv(y[t][1], kd[t][1]); SBAR(); } while (0)
#define ZF4(t) do { y[t][2] = add_vv(y[t][2], kd[t][2]); y[t][3] = add_vv(y[t][3], kd[t][3]); SBAR(); } while (0)
#define ZF5(t) do { zo[t][0] = nz(cvtpk(y[t][0], y[t][1])); zo[t][1] = nz(cvtpk(y[t][2], y[t][3])); SBAR(); } while (0)
#define ZST(pr) do { auto s0 = __builtin_amdgcn_permlane16_swap(zo[2 * (pr)][0], zo[2 * (pr) + 1][0], false, false); \
            auto s1 = __builtin_amdgcn_permlane16_swap(zo[2 * (pr)][1], zo[2 * (pr) + 1][1], false, false); \
            st_sc1(u32x4{s0[0], s1[0], s0[1], s1[1]}, rs, (unsigned)WS_ZT + ch * 4096 + (pr) * 1024 + voff); SBAR(); } while (0)
        CM0(0, 7); LDB(1, 7); CM0(0, 6); LDT(0, 5, 0); CM0(0, 5); LDT(0, 5, 1); CM0(0, 4); LDT(0, 6, 0); CM0(0, 3); LDT(0, 6, 1); CM0(0, 2); LDT(0, 7, 0); CM0(0, 1); LDT(0, 7, 1); CM0(0, 0); KD_RD(0);
        CM(1, 6); LDB(1, 6); CM(1, 5); LDT(1, 0, 0); CM(1, 4); LDT(1, 0, 1); CM(1, 3); PC(); CM(1, 2); PC(); CM(1, 1); KD_UP0(0); CM(1, 0); KD_UP1(0);
        CM(2, 5); LDB(1, 5); CM(2, 4); LDT(1, 1, 0); CM(2, 3); LDT(1, 1, 1); CM(2, 2); KD_MU0(0); CM(2, 1); KD_MU1(0); CM(2, 0); KD_CV(0);
        CM(3, 4); LDB(1, 4); CM(3, 3); LDT(1, 2, 0); CM(3, 2); LDT(1, 2, 1); CM(3, 1); KD_X0(0); CM(3, 0); KD_X1(0);
        CM(4, 3); LDB(1, 3); CM(4, 2); LDT(1, 3, 0); CM(4, 1); LDT(1, 3, 1); CM(4, 0); KD_RD(1);
        CM(5, 2); LDB(1, 2); CM(5, 1); LDT(1, 4, 0); CM(5, 0); LDT(1, 4, 1);
        CM(6, 1); LDB(1, 1); CM(6, 0); PC();
        CM(7, 0); LDB(1, 0);
        CM(0, 7); LDB(2, 7); CM(0, 6); LDT(1, 5, 0); CM(0, 5); LDT(1, 5, 1); CM(0, 4); LDT(1, 6, 0); CM(0, 3); LDT(1, 6, 1); CM(0, 2); LDT(1, 7, 0); CM(0, 1); LDT(1, 7, 1); CM(0, 0); PC();
        CM(1, 6); LDB(2, 6); CM(1, 5); LDT(2, 0, 0); CM(1, 4); LDT(2, 0, 1); CM(1, 3); KD_UP0(1); CM(1, 2); KD_UP1(1); CM(1, 1); KD_MU0(1); CM(1, 0); KD_MU1(1);
        CM(2, 5); LDB(2, 5); CM(2, 4); LDT(2, 1, 0); CM(2, 3); LDT(2, 1, 1); CM(2, 2); KD_CV(1); CM(2, 1); KD_X0(1); CM(2, 0); KD_X1(1);
        CM(3, 4); LDB(2, 4); CM(3, 3); LDT(2, 2, 0); CM(3, 2); LDT(2, 2, 1); CM(3, 1); KD_RD(2); CM(3, 0); PC();
        CM(4, 3); LDB(2, 3); CM(4, 2); LDT(2, 3, 0); CM(4, 1); LDT(2, 3, 1); CM(4, 0); PC();
        CM(5, 2); LDB(2, 2); CM(5, 1); LDT(2, 4, 0); CM(5, 0); LDT(2, 4, 1);
        CM(6, 1); LDB(2, 1); CM(6, 0); KD_UP0(2);
        CM(7, 0); LDB(2, 0);
        CM(0, 7); LDB(3, 7); CM(0, 6); LDT(2, 5, 0); CM(0, 5); LDT(2, 5, 1); CM(0, 4); LDT(2, 6, 0); CM(0, 3); LDT(2, 6, 1); CM(0, 2); LDT(2, 7, 0); CM(0, 1); LDT(2, 7, 1); CM(0, 0); KD_UP1(2);
        CM(1, 6); LDB(3, 6); CM(1, 5); LDT(3, 0, 0); CM(1, 4); LDT(3, 0, 1); CM(1, 3); KD_MU0(2); CM(1, 2); KD_MU1(2); CM(1, 1); KD_CV(2); CM(1, 0); KD_X0(2);
        CM(2, 5); LDB(3, 5); CM(2, 4); LDT(3, 1, 0); CM(2, 3); LDT(3, 1, 1); CM(2, 2); KD_X1(2); CM(2, 1); KD_RD(3); CM(2, 0); PC();
        CM(3, 4); LDB(3, 4); CM(3, 3); LDT(3, 2, 0); CM(3, 2); LDT(3, 2, 1); CM(3, 1); PC(); CM(3, 0); KD_UP0(3);
        CM(4, 3); LDB(3, 3); CM(4, 2); LDT(3, 3, 0); CM(4, 1); LDT(3, 3, 1); CM(4, 0); KD_UP1(3);
        CM(5, 2); LDB(3, 2); CM(5, 1); LDT(3, 4, 0); CM(5, 0); LDT(3, 4, 1);
        CM(6, 1); LDB(3, 1); CM(6, 0); KD_MU0(3);
        CM(7, 0); LDB(3, 0);
        CM(0, 7); LDB(4, 7); CM(0, 6); LDT(3, 5, 0); CM(0, 5); LDT(3, 5, 1); CM(0, 4); LDT(3, 6, 0); CM(0, 3); LDT(3, 6, 1); CM(0, 2); LDT(3, 7, 0); CM(0, 1); LDT(3, 7, 1); CM(0, 0); KD_MU1(3);
        CM(1, 6); LDB(4, 6); CM(1, 5); LDT(4, 0, 0); CM(1, 4); LDT(4, 0, 1); CM(1, 3); KD_CV(3); CM(1, 2); KD_X0(3); CM(1, 1); KD_X1(3); CM(1, 0); KD_RD(4);
        CM(2, 5); LDB(4, 5); CM(2, 4); LDT(4, 1, 0); CM(2, 3); LDT(4, 1, 1); CM(2, 2); PC(); CM(2, 1); PC(); CM(2, 0); KD_UP0(4);
        CM(3, 4); LDB(4, 4); CM(3, 3); LDT(4, 2, 0); CM(3, 2); LDT(4, 2, 1); CM(3, 1); KD_UP1(4); CM(3, 0); KD_MU0(4);
        CM(4, 3); LDB(4, 3); CM(4, 2); LDT(4, 3, 0); CM(4, 1); LDT(4, 3, 1); CM(4, 0); KD_MU1(4);
        CM(5, 2); LDB(4, 2); CM(5, 1); LDT(4, 4, 0); CM(5, 0); LDT(4, 4, 1);
        CM(6, 1); LDB(4, 1); CM(6, 0); KD_CV(4);
        CM(7, 0); LDB(4, 0);
        CM(0, 7); LDB(5, 7); CM(0, 6); LDT(4, 5, 0); CM(0, 5); LDT(4, 5, 1); CM(0, 4); LDT(4, 6, 0); CM(0, 3); LDT(4, 6, 1); CM(0, 2); LDT(4, 7, 0); CM(0, 1); LDT(4, 7, 1); CM(0, 0); KD_X0(4);
        CM(1, 6); LDB(5, 6); CM(1, 5); LDT(5, 0, 0); CM(1, 4); LDT(5, 0, 1); CM(1, 3); KD_X1(4); CM(1, 2); KD_RD(5); CM(1, 1); PC(); CM(1, 0); PC();
        CM(2, 5); LDB(5, 5); CM(2, 4); LDT(5, 1, 0); CM(2, 3); LDT(5, 1, 1); CM(2, 2); KD_UP0(5); CM(2, 1); KD_UP1(5); CM(2, 0); KD_MU0(5);
        CM(3, 4); LDB(5, 4); CM(3, 3); LDT(5, 2, 0); CM(3, 2); LDT(5, 2, 1); CM(3, 1); KD_MU1(5); CM(3, 0); KD_CV(5);
        CM(4, 3); LDB(5, 3); CM(4, 2); LDT(5, 3, 0); CM(4, 1); LDT(5, 3, 1); CM(4, 0); KD_X0(5);
        CM(5, 2); LDB(5, 2); CM(5, 1); LDT(5, 4, 0); CM(5, 0); LDT(5, 4, 1);
        CM(6, 1); LDB(5, 1); CM(6, 0); KD_X1(5);
        CM(7, 0); LDB(5, 0);
        CM(0, 7); LDB(6, 7); CM(0, 6); LDT(5, 5, 0); CM(0, 5); LDT(5, 5, 1); CM(0, 4); LDT(5, 6, 0); CM(0, 3); LDT(5, 6, 1); CM(0, 2); LDT(5, 7, 0); CM(0, 1); LDT(5, 7, 1); CM(0, 0); KD_RD(6);
        CM(1, 6); LDB(6, 6); CM(1, 5); LDT(6, 0, 0); CM(1, 4); LDT(6, 0, 1); CM(1, 3); PC(); CM(1, 2); PC(); CM(1, 1); KD_UP0(6); CM(1, 0); KD_UP1(6);
        CM(2, 5); LDB(6, 5); CM(2, 4); LDT(6, 1, 0); CM(2, 3); LDT(6, 1, 1); CM(2, 2); KD_MU0(6); CM(2, 1); KD_MU1(6); CM(2, 0); KD_CV(6);
        CM(3, 4); LDB(6, 4); CM(3, 3); LDT(6, 2, 0); CM(3, 2); LDT(6, 2, 1); CM(3, 1); KD_X0(6); CM(3, 0); KD_X1(6);
        CM(4, 3); LDB(6, 3); CM(4, 2); LDT(6, 3, 0); CM(4, 1); LDT(6, 3, 1); CM(4, 0); KD_RD(7);
        CM(5, 2); LDB(6, 2); CM(5, 1); LDT(6, 4, 0); CM(5, 0); LDT(6, 4, 1);
        CM(6, 1); LDB(6, 1); CM(6, 0); PC();
        CM(7, 0); LDB(6, 0);
        CM(0, 7); LDB(7, 7); CM(0, 6); LDT(6, 5, 0); CM(0, 5); LDT(6, 5, 1); CM(0, 4); LDT(6, 6, 0); CM(0, 3); LDT(6, 6, 1); CM(0, 2); LDT(6, 7, 0); CM(0, 1); LDT(6, 7, 1); CM(0, 0); PC();
        CM(1, 6); LDB(7, 6); CM(1, 5); LDT(7, 0, 0); CM(1, 4); LDT(7, 0, 1); CM(1, 3); KD_UP0(7); CM(1, 2); KD_UP1(7); CM(1, 1); KD_MU0(7); CM(1, 0); KD_MU1(7);
        CM(2, 5); LDB(7, 5); CM(2, 4); LDT(7, 1, 0); CM(2, 3); LDT(7, 1, 1); CM(2, 2); KD_CV(7); CM(2, 1); KD_X0(7); CM(2, 0); KD_X1(7);
        CM(3, 4); LDB(7, 4); CM(3, 3); LDT(7, 2, 0); CM(3, 2); LDT(7, 2, 1); CM(3, 1); PC(); CM(3, 0); PC();
        CM(4, 3); LDB(7, 3); CM(4, 2); LDT(7, 3, 0); CM(4, 1); LDT(7, 3, 1); CM(4, 0); PC();
        CM(5, 2); LDB(7, 2); CM(5, 1); LDT(7, 4, 0); CM(5, 0); LDT(7, 4, 1);
        CM(6, 1); LDB(7, 1); CM(6, 0); PC();
        CM(7, 0); LDB(7, 0);
        CM(0, 3); LDT(7, 5, 0); CM(0, 2); LDT(7, 5, 1); CM(1, 2); LDT(7, 6, 0); CM(1, 1); LDT(7, 6, 1); CM(2, 1); LDT(7, 7, 0); CM(2, 0); LDT(7, 7, 1);
        CM(3, 0); PC(); CM(0, 1); PC(); CM(0, 0); PC(); CM(1, 0); ZF0(2);  CM(0, 5); ZF1(2);  CM(0, 4); ZF2(2);
        CM(1, 4); ZF3(2);  CM(1, 3); ZF4(2);  CM(2, 3); ZF5(2);  CM(2, 2); ZF0(3);  CM(3, 2); ZF1(3);  CM(3, 1); ZF2(3);
        CM(4, 1); ZF3(3);  CM(4, 0); ZF4(3);  CM(5, 0); ZF5(3);  CM(0, 7); ZST(1);  CM(0, 6); ZF0(0);  CM(1, 6); ZF1(0);
        CM(1, 5); ZF2(0);  CM(2, 5); ZF3(0);  CM(2, 4); ZF4(0);  CM(3, 4); ZF5(0);  CM(3, 3); ZF0(1);  CM(4, 3); ZF1(1);
        CM(4, 2); ZF2(1);  CM(5, 2); ZF3(1);  CM(5, 1); ZF4(1);  CM(6, 1); ZF5(1);  CM(6, 0); ZST(0);  CM(7, 0); ZF0(4);
        DG(4);
        ZF1(4); ZF2(4); ZF3(4); ZF4(4); ZF5(4); ZF0(5); ZF1(5); ZF2(5); ZF3(5); ZF4(5); ZF5(5); ZST(2); ZF0(6); ZF1(6); ZF2(6); ZF3(6); ZF4(6); ZF5(6); ZF0(7); ZF1(7); ZF2(7); ZF3(7); ZF4(7); ZF5(7); ZST(3);
        DG(10);
        __builtin_amdgcn_s_setprio(3);
        while ((unsigned)__builtin_amdgcn_readfirstlane(*(volatile LAS unsigned*)(fl + C_OL)) < ZL_GATE) __builtin_amdgcn_s_sleep(4);
        CFENCE();
        u32x2 qr[8];
#pragma unroll
        for (int i = 0; i < 8; ++i) qr[i] = gld64(roff, Aq + i * 2048);
        {
            const lds_ptr tr = cv;
            u32x4 t[4];
            const unsigned zb = (unsigned)WS_ZT + (unsigned)(c0 + (lane >> 3)) * 4096u + (idx >> 3) * 1024 + ((idx & 3) * 4 + ((idx >> 2) & 1) * 16) * 16;
            for (unsigned spins = 0; spins < (1u << 20); ++spins) {
#pragma unroll
                for (int i = 0; i < 4; ++i) {
                    const int ck = (lane & 7) ^ (2 * i + (lane >> 5));
                    t[i] = ld_sc1(rs, zb + i * 32768 + ((ck >> 1) + (ck & 1) * 32) * 16);
                }
                if (!missing(mx4(mx4(mx8(t[0], t[1]), t[2]), t[3]))) break;
                __builtin_amdgcn_s_sleep(Z_SLEEP);
            }
            DG(13);
            __builtin_amdgcn_s_setprio(ZE_PRIO);
#pragma unroll
            for (int i = 0; i < 4; ++i) *(LAS u32x4*)(tr + i * 1024 + lane * 16) = t[i];
#pragma unroll
            for (int i = 0; i < 8; ++i) asm volatile("" : "+v"(qr[i]));
            u32x4 zc[4];
#pragma unroll
            for (int c = 0; c < 4; ++c) zc[c] = *(const LAS u32x4*)(tr + (cq * 4 + c) * 128 + ((rg ^ cq) << 4));
            char* dst = out + (size_t)row0 * 4096 + (c0 + cq * 4) * 2;
#pragma unroll
            for (int i = 0; i < 8; ++i) {
                float zz[4];
#pragma unroll
                for (int c = 0; c < 4; ++c) zz[c] = (i & 1) ? bfhi(zc[c][i >> 1]) : bflo(zc[c][i >> 1]);
                u32x2 o = {cvtpk(mul_vv(bflo(qr[i][0]), zz[0]), mul_vv(bfhi(qr[i][0]), zz[1])),
                           cvtpk(mul_vv(bflo(qr[i][1]), zz[2]), mul_vv(bfhi(qr[i][1]), zz[3]))};
                __builtin_amdgcn_raw_buffer_store_b64(o, ro, (int)(row0 * 4096 + (c0 + cq * 4) * 2), i * 4096, A_AUX);
            }
        }
        DG_END(14);
        return;
    }

    const int half = w >> 2;
    const int TA = qb * 8 + (w & 3) * 2 + half, TB = TA ^ 1;
    bf16x8 sel;
    {
        const short one = (((lane >> 2) & 1) == ((lane >> 4) & 1)) ? (short)0x3f80 : (short)0;
#pragma unroll
        for (int i = 0; i < 8; ++i) sel[i] = one;
    }
    f32x16 oA = f32x16{}, oB = f32x16{};
    f32x4 lA = f32x4{}, lB = f32x4{};
    f32x16 negm, sA, sB;
    lds_ptr lp = lds + half * 16384 + lane * 16;
    bf16x8 k0[2], k1[2], v0[2], v1[2];
    u32x4 p0, p1 = {0u, 0u, 0u, 0u};
#define EX(X, i) X[i] = __builtin_amdgcn_exp2f(X[i])
    lds_ptr qpa = lds + K2_SF + (TA & 7) * 2048 + lane * 16, qpb = lds + K2_SF + (TB & 7) * 2048 + lane * 16;
    asm volatile("" : "+v"(qpa), "+v"(qpb), "+v"(lp), "+v"(sel), "+v"(oA), "+v"(oB), "+v"(lA), "+v"(lB), "+v"(p1));
    WAIT_BAR(0);
    DG(3);
    const bf16x8 qa0 = lds_rd128(qpa), qb0 = lds_rd128(qpb);
    const bf16x8 x0 = lds_rd128(lds + lane * 16), x1 = lds_rd128(lds + lane * 16 + 1024);
    const bf16x8 qa1 = lds_rd128(qpa + 1024), qb1 = lds_rd128(qpb + 1024);
    {
        k0[0] = lds_rd128(lp); k1[0] = lds_rd128(lp + 1024);
        v0[0] = lds_rd128(lp + 2048); v1[0] = lds_rd128(lp + 3072);
        sA = MFMA32(x0, qa0, f32x16{});
        sB = MFMA32(x0, qb0, f32x16{});
        sA = MFMA32(x1, qa1, sA);
        sB = MFMA32(x1, qb1, sB);
        float m = fmx(sA[0], sA[1]), m2 = fmx(sB[0], sB[1]);
#pragma unroll
        for (int i = 2; i < 16; i += 2) m = fmx(fmx(m, sA[i]), sA[i + 1]);
#pragma unroll
        for (int i = 2; i < 16; i += 2) m2 = fmx(fmx(m2, sB[i]), sB[i + 1]);
        m = fmx(m, m2);
        auto rr = __builtin_amdgcn_permlane32_swap(__float_as_uint(m), __float_as_uint(m), false, false);
        m = fmx(__uint_as_float(rr[0]), __uint_as_float(rr[1]));
#pragma unroll
        for (int i = 0; i < 16; ++i) negm[i] = -m;
        sA = MFMA32(k0[0], qa0, negm);
        sA = MFMA32(k1[0], qa1, sA);
        v1[1] = v1[0];
        SBAR();
        RAW_BAR();
        SBAR();
        EX(sA, 0); EX(sA, 1);
    }
    DG(10);
#define SUM16(l, p) asm volatile("v_mfma_f32_16x16x32_bf16 %0, %1, %2, %0" : "+v"(l) : "v"(sel), "v"(p))
#define HALF(C, oC, lC, N, qx, qy, ka, kb, oP, lP, vprev, vcur0, PRE) do { \
        oP = MFMA32(vprev, PB(p1), oP); SBAR(); \
        PRE; EX(C, 2); EX(C, 3); EX(C, 4); SBAR(); \
        N = MFMA32(ka, qx, negm); SBAR(); \
        EX(C, 5); EX(C, 6); EX(C, 7); SBAR(); \
        SUM16(lP, p1); SBAR(); \
        p0[0] = cvtpk(C[0], C[1]); p0[1] = cvtpk(C[2], C[3]); SBAR(); \
        N = MFMA32(kb, qy, N); SBAR(); \
        p0[2] = cvtpk(C[4], C[5]); p0[3] = cvtpk(C[6], C[7]); SBAR(); EX(C, 8); EX(C, 9); SBAR(); \
        oC = MFMA32(vcur0, PB(p0), oC); SBAR(); \
        EX(C, 10); EX(C, 11); EX(C, 12); SBAR(); \
        SUM16(lC, p0); SBAR(); \
        EX(C, 13); EX(C, 14); EX(C, 15); SBAR(); \
        p1[0] = cvtpk(C[8], C[9]); p1[1] = cvtpk(C[10], C[11]); p1[2] = cvtpk(C[12], C[13]); p1[3] = cvtpk(C[14], C[15]); SBAR(); \
        EX(N, 0); EX(N, 1); SBAR(); \
    } while (0)
#define RD4(Y, nx) do { v1[Y] = lds_rd128((nx) + 3072); v0[Y] = lds_rd128((nx) + 2048); \
        k1[Y] = lds_rd128((nx) + 1024); k0[Y] = lds_rd128(nx); } while (0)
#define KT(X, nx, PRE0) do { \
        HALF(sA, oA, lA, sB, qb0, qb1, k0[X], k1[X], oB, lB, v1[X ^ 1], v0[X], PRE0; RD4(X ^ 1, nx)); \
        HALF(sB, oB, lB, sA, qa0, qa1, k0[X ^ 1], k1[X ^ 1], oA, lA, v1[X], v0[X], (void)0); \
    } while (0)
    SBAR();
#pragma unroll 1
    for (int st = 0; st < 8; ++st) {
        const lds_ptr cur = lp + (st & 1) * K2_SLOT;
        const lds_ptr nxt = lp + ((st + 1) & 1) * K2_SLOT;
        PRIO(2);
        KT(0, cur + 4096, (void)0);
        PRIO(1);
        KT(1, cur + 8192, (void)0);
        PRIO(0);
        KT(0, cur + 12288, (void)0);
        PRIO(3);
        KT(1, nxt, RAW_BAR(); SBAR());
    }
    oB = MFMA32(v1[1], PB(p1), oB);
    SBAR();
    SUM16(lB, p1);
    asm volatile("s_nop 7" : "+v"(oB) :: "memory");
    SBAR();
    DG(4);
    {
        const lds_ptr xo = lds + K2_SLOT + w * 4096 + lane * 16;
        *(LAS float*)(lds + K2_LX + w * 256 + lane * 4) = lB[0];
#pragma unroll
        for (int i = 0; i < 4; ++i) *(LAS f32x4*)(xo + i * 1024) = f32x4{oB[4 * i], oB[4 * i + 1], oB[4 * i + 2], oB[4 * i + 3]};
        lds_add(fl + F_X + w * 4, lane);
        lds_add(fl + C_END + half * 4, lane);
        lds_wait_ge(fl + F_X + (w ^ 4) * 4, 1u);
        const lds_ptr xi = lds + K2_SLOT + (w ^ 4) * 4096 + lane * 16;
        f32x4 t[4];
        const float lx = *(const LAS float*)(lds + K2_LX + (w ^ 4) * 256 + lane * 4);
#pragma unroll
        for (int i = 0; i < 4; ++i) t[i] = *(const LAS f32x4*)(xi + i * 1024);
        SBAR();
        lA[0] += lx;
#pragma unroll
        for (int i = 0; i < 16; ++i) { float o = oA[i]; acc_vv(o, t[i >> 2][i & 3]); oA[i] = o; }
    }
    DG(9);
    const float l = lA[0];
    u32x4 on0, on1;
    auto finish = [&](float ls) {
        float rl = __builtin_amdgcn_rcpf(ls);
        asm volatile("s_nop 1" : "+v"(rl));
        f32x16 o;
#pragma unroll
        for (int i = 0; i < 16; ++i) o[i] = mul_vv(oA[i], rl);
        on0 = nz4(pack8n(o, 0)); on1 = nz4(pack8n(o, 1));
    };
    finish(l);
    {
        float t0, t1;
        asm("v_mul_f32_e32 %0, 0, %2\n\tv_mul_f32_e32 %1, 0, %3\n\tv_fmac_f32_e32 %0, 0, %4\n\tv_fmac_f32_e32 %1, 0, %5\n\tv_fmac_f32_e32 %0, 0, %6\n\tv_fmac_f32_e32 %1, 0, %7\n\tv_fmac_f32_e32 %0, 0, %8\n\tv_fmac_f32_e32 %1, 0, %9\n\t"
            "v_fmac_f32_e32 %0, 0, %10\n\tv_fmac_f32_e32 %1, 0, %11\n\tv_fmac_f32_e32 %0, 0, %12\n\tv_fmac_f32_e32 %1, 0, %13\n\tv_fmac_f32_e32 %0, 0, %14\n\tv_fmac_f32_e32 %1, 0, %15\n\tv_fmac_f32_e32 %0, 0, %16\n\tv_fmac_f32_e32 %1, 0, %17"
            : "=&v"(t0), "=&v"(t1) : "v"(oA[0]), "v"(oA[1]), "v"(oA[2]), "v"(oA[3]), "v"(oA[4]), "v"(oA[5]), "v"(oA[6]), "v"(oA[7]), "v"(oA[8]), "v"(oA[9]), "v"(oA[10]), "v"(oA[11]), "v"(oA[12]), "v"(oA[13]), "v"(oA[14]), "v"(oA[15]));
        const bool ok = (l <= 1.0e30f) && (l >= 1.0e-18f) && (t0 + t1 == 0.f);
        if (__builtin_expect(__any(!ok), 0)) {
            float m = -INFINITY, ls = 0.f;
            oA = f32x16{};
#pragma unroll 1
            for (int t = 0; t < 64; ++t) {
                const char* g = kvg + t * 4096 + lane * 16;
                const bf16x8 a0 = *(const bf16x8*)g, a1 = *(const bf16x8*)(g + 1024);
                const bf16x8 b0 = *(const bf16x8*)(g + 2048), b1 = *(const bf16x8*)(g + 3072);
                f32x16 s = MFMA32(a0, qa0, f32x16{});
                s = MFMA32(a1, qa1, s);
                float tm = s[0];
#pragma unroll
                for (int i = 1; i < 16; ++i) tm = __builtin_fmaxf(tm, s[i]);
                auto rr = __builtin_amdgcn_permlane32_swap(__float_as_uint(tm), __float_as_uint(tm), false, false);
                tm = __builtin_fmaxf(__uint_as_float(rr[0]), __uint_as_float(rr[1]));
                const float mn = __builtin_fmaxf(m, tm);
                const float alpha = __builtin_amdgcn_exp2f(m - mn);
                m = mn;
                float ps = 0.f;
#pragma unroll
                for (int i = 0; i < 16; ++i) { s[i] = __builtin_amdgcn_exp2f(s[i] - mn); ps += s[i]; oA[i] *= alpha; }
                ls = ls * alpha + ps;
                oA = MFMA32(b0, PB(pack8n(s, 0)), oA);
                oA = MFMA32(b1, PB(pack8n(s, 1)), oA);
            }
            auto rr = __builtin_amdgcn_permlane32_swap(__float_as_uint(ls), __float_as_uint(ls), false, false);
            asm volatile("s_nop 7\n\ts_nop 7" : "+v"(oA));
            finish(__uint_as_float(rr[0]) + __uint_as_float(rr[1]));
        }
    }
    u32x4 w3[4];
    {
        const unsigned og = (unsigned)WS_O + (unsigned)(TA * 32 + h) * 2048u + lane * 16;
        st_sc1(on0, rs, og);
        st_sc1(on1, rs, og + 1024);
        lds_add(fl + C_S1, lane);
        const char* s2 = wsrc(half ^ 1, 32 + (w & 3) * 4);
        const char* s3 = wsrc(half, 48 + (w & 3) * 4);
        lds_wait_ge(fl + C_S1, 12u);
        DG(5);
#pragma unroll
        for (int i = 0; i < 4; ++i)
            glds16(wvoff, s2 + i * 16384, lds0 + K2_SLOT + ((w ^ 4) * 4 + i) * 1024);
#pragma unroll
        for (int i = 0; i < 4; ++i) w3[i] = gld128(wvoff, s3 + i * 16384);
    }
    const int j4 = w & 3;
    {
        const int T3 = tt * 4 + j4;
        const unsigned og = (unsigned)WS_O + (unsigned)T3 * 65536u + half * 32768 + lane * 16;
        const int r = lane & 31, hh = lane >> 5;
        const int g = lane >> 4, q = (lane & 15) >> 2, p = lane & 3;
        int woff[2];
#pragma unroll
        for (int n = 0; n < 2; ++n) {
            const int c = 4 * (n ^ half) + 2 * (p & 1) + (g & 1);
            woff[n] = (4 * hh + q) * 128 + ((c ^ ((q >> 1) << 2)) << 4) + (p >> 1) * 8;
        }
        f32x16 acc[2] = {f32x16{}, f32x16{}};
        lds_ptr wl[2][2] = {{lds + half * 16384 + woff[0], lds + half * 16384 + woff[1]},
                            {lds + K2_SF + half * 16384 + woff[0], lds + K2_SF + half * 16384 + woff[1]}};
        lds_ptr w3d = lds + K2_SF + w * 4096 + lane * 16;
        asm volatile("" : "+v"(wl[0][0]), "+v"(wl[0][1]), "+v"(wl[1][0]), "+v"(wl[1][1]), "+v"(w3d));
        s16x4 wa[WD + 1][2], wb[WD + 1][2];
#define LDW(s, n) do { const int f_ = (s) < 8 || (s) >= 24, o_ = (f_ ? 0 : (((s) >> 3) - 1) * K2_SLOT) + ((s) & 7) * 2048; \
            wa[(s) % (WD + 1)][n] = lds_tr(wl[f_][n] + o_); wb[(s) % (WD + 1)][n] = lds_tr(wl[f_][n] + o_ + 1024); SBAR(); } while (0)
#pragma unroll
        for (int s = 0; s < WD; ++s) { LDW(s, 0); LDW(s, 1); }
        u32x4 of[OWIN];
#define LDO(i) do { of[(i) % OWIN] = ld_sc1s(rs, og, (((i) + nt * KROT) & 31) * 1024); SBAR(); } while (0)
        for (unsigned spins = 0; spins < (1u << 20); ++spins) {
#pragma unroll
            for (int i = 0; i < OW0; ++i) LDO(i);
            unsigned m = mx8(of[0], of[1]);
#pragma unroll
            for (int i = 2; i < OW0; ++i) m = mx4(m, of[i]);
            if (!missing(m)) break;
            DGC(12);
        }
        lds_add(fl + C_S2, lane);
#pragma unroll
        for (int i = OW0; i < OWIN; ++i) LDO(i);
        DG(6);
        __builtin_amdgcn_s_setprio(P3_PRIO);
        SBAR();
        unsigned fv = 0;
#define FLAG_RD(a) do { fv = *(volatile LAS unsigned*)(a); SBAR(); } while (0)
#define FLAG_CHK(a, n) do { if (__builtin_expect((unsigned)__builtin_amdgcn_readfirstlane(fv) < (n), 0)) lds_wait_ge(a, n); CFENCE(); SBAR(); } while (0)
#pragma unroll
        for (int s = 0; s < 32; ++s) {
            acc[0] = MFMA32(cat8(wa[s % (WD + 1)][0], wb[s % (WD + 1)][0]), PB(of[s % OWIN]), acc[0]);
            SBAR();
            if (s == 16 - WD) FLAG_CHK(fl + C_S2, 8u);
            if (s == 24 - WD) FLAG_CHK(fl + C_S3 + half * 4, 4u);
            if (s + WD < 32) LDW(s + WD, 0);
            if ((s & 1) && s + 1 >= OW0 && s + 1 < 32 && __builtin_expect(missing(mx8(of[(s + 1) % OWIN], of[(s + 2) % OWIN])), 0)) {
                for (unsigned spins = 0; spins < (1u << 20); ++spins) {
                    __builtin_amdgcn_s_sleep(2);
                    LDO(s + 1); LDO(s + 2);
                    DGC(13);
                    if (!missing(mx8(of[(s + 1) % OWIN], of[(s + 2) % OWIN]))) break;
                }
            }
            SBAR();
            acc[1] = MFMA32(cat8(wa[s % (WD + 1)][1], wb[s % (WD + 1)][1]), PB(of[s % OWIN]), acc[1]);
            SBAR();
            if (s + WD < 32) LDW(s + WD, 1);
            if (s + OWIN < 32) LDO(s + OWIN);
            if (s == 7 - WD) lds_add(fl + C_U0 + half * 4, lane);
            if (s == W3_AT - 2) FLAG_RD(fl + C_U0 + half * 4);
            if (s == 14 - WD) FLAG_RD(fl + C_S2);
            if (s == 22 - WD) FLAG_RD(fl + C_S3 + half * 4);
            if (s == W3_AT) FLAG_CHK(fl + C_U0 + half * 4, 4u);
            if (s >= W3_AT && s < W3_AT + 4) {
                asm volatile("" : "+v"(w3[(s - W3_AT) & 3]));
                *(LAS u32x4*)(w3d + ((s - W3_AT) & 3) * 1024) = w3[(s - W3_AT) & 3];
            }
            if (s == W3_AT + 3) lds_add(fl + C_S3 + half * 4, lane);
            if (ZL_GATE && s + OWIN == ZL_AT) lds_add(fl + C_OL, lane);
            if (s == P3_T1) __builtin_amdgcn_s_setprio(P3_PRIO - 1);
            if (s == P3_T2) __builtin_amdgcn_s_setprio(P3_PRIO - 2 < 0 ? 0 : P3_PRIO - 2);
            SBAR();
            if (s == 15) DG(11);
            if (s == 23) DG(14);
        }
        DG(7);
        const lds_ptr xo = lds + w * 4096 + lane * 16;
#pragma unroll
        for (int i = 0; i < 4; ++i) *(LAS f32x4*)(xo + i * 1024) = f32x4{acc[1][4 * i], acc[1][4 * i + 1], acc[1][4 * i + 2], acc[1][4 * i + 3]};
        lds_add(fl + F_R + w * 4, lane);
        lds_wait_ge(fl + F_R + (w ^ 4) * 4, 1u);
        const lds_ptr xi = lds + (w ^ 4) * 4096 + lane * 16;
#pragma unroll
        for (int i = 0; i < 4; ++i) {
            const f32x4 t = *(const LAS f32x4*)(xi + i * 1024);
#pragma unroll
            for (int j = 0; j < 4; ++j) acc[0][4 * i + j] = add_vv(acc[0][4 * i + j], t[j]);
        }
        char* dst = out + (size_t)(T3 * 32 + r) * 4096 + 2048 + nt * 128 + half * 64 + hh * 32;
        u32x4 pa = pack8n(acc[0], 0), pb = pack8n(acc[0], 1);
#pragma unroll
        for (int i = 0; i < 4; ++i) {
            auto sw = __builtin_amdgcn_permlane16_swap(pa[i], pb[i], false, false);
            pa[i] = sw[0]; pb[i] = sw[1];
        }
        const int bo = (T3 * 32 + (lane & 15)) * 4096 + 2048 + nt * 128 + half * 64 + g * 16;
        __builtin_amdgcn_raw_buffer_store_b128(pa, ro, bo, 0, B_AUX);
        __builtin_amdgcn_raw_buffer_store_b128(pb, ro, bo, 65536, B_AUX);
    }
    DG_END(8);
}

extern "C" void kernel_launch(void* const* d_in, const int* in_sizes, int n_in,
                              void* d_out, int out_size, void* d_ws, size_t ws_size,
                              hipStream_t stream) {
    const void* A_q = d_in[0];
    const void* A_k = d_in[1];
    const void* A_v = d_in[2];
    const void* A_shift = d_in[3];
    const void* A_ssmk = d_in[4];
    const void* A_D = d_in[5];
    const void* B_x = d_in[6];
    const void* B_wqkv = d_in[7];
    const void* B_wo = d_in[8];
    char* ws = (char*)d_ws;
    k1_qkv<<<256, 512, 0, stream>>>((const char*)B_x, (const char*)B_wqkv, ws);
    k2_attn<<<256, 768, 0, stream>>>(ws, (const char*)A_q, (const char*)A_k, (const char*)A_v, (const char*)A_shift,
                                     (const char*)A_ssmk, (const unsigned*)A_D, (const char*)B_wo, (char*)d_out);
}
```

```cpp
#include <hip/hip_runtime.h>
#include <hip/hip_bf16.h>
#include <stdint.h>
#include <utility>

typedef short bf16x8 __attribute__((ext_vector_type(8)));
typedef short s16x4 __attribute__((ext_vector_type(4)));
typedef float f32x16 __attribute__((ext_vector_type(16)));
typedef float f32x4 __attribute__((ext_vector_type(4)));
typedef float f32x2 __attribute__((ext_vector_type(2)));
typedef unsigned u32x4 __attribute__((ext_vector_type(4)));
typedef unsigned u32x2 __attribute__((ext_vector_type(2)));
typedef __bf16 bf16x2_t __attribute__((ext_vector_type(2)));

#define LAS __attribute__((address_space(3)))
typedef LAS char* lds_ptr;
#define DI __device__ __forceinline__
#define SBAR() __builtin_amdgcn_sched_barrier(0)
#define MFMA32(a, b, c) __builtin_amdgcn_mfma_f32_32x32x16_bf16((a), (b), (c), 0, 0, 0)
#define MFMA16(a, b, c) __builtin_amdgcn_mfma_f32_16x16x32_bf16((a), (b), (c), 0, 0, 0)
#define WAIT_BAR(N) asm volatile("s_waitcnt vmcnt(" #N ") lgkmcnt(0)\n\ts_barrier" ::: "memory")
#define LGKM_BAR() asm volatile("s_waitcnt lgkmcnt(0)\n\ts_barrier" ::: "memory")

#define DG_DECL(K)
#define DG(i)
#define DG_END(i)
#define DGC(i)

constexpr int S_LEN = 2048, DM = 1024, NH = 32;

constexpr size_t WS_Q = 0;
constexpr size_t WS_KV = 4u << 20;
constexpr size_t WS_O = 12u << 20;
constexpr size_t WS_KVR = 16u << 20;
constexpr size_t WS_ZT = 20u << 20;
constexpr size_t WS_CNT = 24u << 20;

template <int... I, class F> DI void sfor(std::integer_sequence<int, I...>, F&& f) { (f(std::integral_constant<int, I>{}), ...); }
DI unsigned cvtpk(float lo, float hi) {
    f32x2 v = {lo, hi};
    return __builtin_bit_cast(unsigned, __builtin_convertvector(v, bf16x2_t));
}
DI float bflo(unsigned u) { return __uint_as_float(u << 16); }
DI float bfhi(unsigned u) { return __uint_as_float(u & 0xffff0000u); }

DI void glds16(unsigned voff, const void* sbase, unsigned lds_dst) {
    unsigned sv;
    asm volatile("s_mov_b32 %0, m0\n\ts_mov_b32 m0, %3\n\ts_nop 0\n\tglobal_load_lds_dwordx4 %1, %2\n\ts_mov_b32 m0, %0"
                 : "=&s"(sv) : "v"(voff), "s"(sbase), "s"(lds_dst) : "memory");
}
DI bf16x8 lds_rd128(lds_ptr p) { return *(const LAS bf16x8*)p; }
DI s16x4 lds_tr(lds_ptr p) {
    return __builtin_bit_cast(s16x4, __builtin_amdgcn_ds_read_tr16_b64_v4i16((LAS s16x4*)p));
}
DI bf16x8 cat8(s16x4 a, s16x4 b) { return __builtin_shufflevector(a, b, 0, 1, 2, 3, 4, 5, 6, 7); }
DI u32x4 pack8(const f32x16& x, int s, float sc) {
    u32x4 p;
    p[0] = cvtpk(x[8 * s + 0] * sc, x[8 * s + 1] * sc);
    p[1] = cvtpk(x[8 * s + 2] * sc, x[8 * s + 3] * sc);
    p[2] = cvtpk(x[8 * s + 4] * sc, x[8 * s + 5] * sc);
    p[3] = cvtpk(x[8 * s + 6] * sc, x[8 * s + 7] * sc);
    return p;
}
DI u32x4 pack8n(const f32x16& x, int s) {
    u32x4 p;
    p[0] = cvtpk(x[8 * s + 0], x[8 * s + 1]);
    p[1] = cvtpk(x[8 * s + 2], x[8 * s + 3]);
    p[2] = cvtpk(x[8 * s + 4], x[8 * s + 5]);
    p[3] = cvtpk(x[8 * s + 6], x[8 * s + 7]);
    return p;
}
DI void rnd2(float& a, float& b) { unsigned u = cvtpk(a, b); a = bflo(u); b = bfhi(u); }

#define CONV_PACE()
#define RAW_BAR() asm volatile("s_barrier" ::: "memory")
DI unsigned gld32(unsigned voff, const void* sbase) {
    unsigned r;
    asm volatile("global_load_dword %0, %1, %2" : "=v"(r) : "v"(voff), "s"(sbase) : "memory");
    return r;
}
DI u32x2 gld64(unsigned voff, const void* sbase) {
    u32x2 r;
    asm volatile("global_load_dwordx2 %0, %1, %2" : "=v"(r) : "v"(voff), "s"(sbase) : "memory");
    return r;
}
DI float mul_sv(float s, float v) { float r; asm("v_mul_f32_e32 %0, %1, %2" : "=v"(r) : "s"(s), "v"(v)); return r; }
DI float mul_vv(float a, float b) { float r; asm("v_mul_f32_e32 %0, %1, %2" : "=v"(r) : "v"(a), "v"(b)); return r; }
DI float add_vv(float a, float b) { float r; asm("v_add_f32_e32 %0, %1, %2" : "=v"(r) : "v"(a), "v"(b)); return r; }
DI void acc_vv(float& a, float b) { asm("v_add_f32_e32 %0, %0, %1" : "+v"(a) : "v"(b)); }
DI void fmac_vv(float& acc, float a, float b) { asm("v_fmac_f32_e32 %0, %1, %2" : "+v"(acc) : "v"(a), "v"(b)); }
DI void fmac_sv(float& acc, float s, float v) { asm("v_fmac_f32_e32 %0, %1, %2" : "+v"(acc) : "s"(s), "v"(v)); }
typedef __attribute__((address_space(1))) unsigned gu32;
#define RLX_AGENT __ATOMIC_RELAXED, __HIP_MEMORY_SCOPE_AGENT
#define CFENCE() asm volatile("" ::: "memory")
#define DRAIN() asm volatile("s_waitcnt vmcnt(0)" ::: "memory")
constexpr int AUX_SC1 = 16;
DI u32x4 ld_sc1(__amdgpu_buffer_rsrc_t rs, unsigned off) { return __builtin_amdgcn_raw_buffer_load_b128(rs, (int)off, 0, AUX_SC1); }
DI u32x4 ld_sc1s(__amdgpu_buffer_rsrc_t rs, unsigned off, int soff) { return __builtin_amdgcn_raw_buffer_load_b128(rs, (int)off, soff, AUX_SC1); }
DI void st_sc1(u32x4 v, __amdgpu_buffer_rsrc_t rs, unsigned off) { __builtin_amdgcn_raw_buffer_store_b128(v, rs, (int)off, 0, AUX_SC1); }
constexpr unsigned EMPTY = 0xffffffffu;
DI unsigned nz(unsigned x) { return x < EMPTY - 1u ? x : EMPTY - 1u; }
DI u32x4 nz4(u32x4 v) { return u32x4{nz(v[0]), nz(v[1]), nz(v[2]), nz(v[3])}; }
DI float fmx(float a, float b) { return __builtin_elementwise_maximum(a, b); }
DI unsigned umax(unsigned a, unsigned b) { return a > b ? a : b; }
DI unsigned umax3(unsigned a, unsigned b, unsigned c) { return umax(umax(a, b), c); }
DI unsigned mx4(unsigned m, u32x4 v) { return umax3(umax3(m, v[0], v[1]), v[2], v[3]); }
DI unsigned mx8(u32x4 a, u32x4 b) { return umax(umax3(umax3(umax3(a[0], a[1], a[2]), a[3], b[0]), b[1], b[2]), b[3]); }
DI bool missing(unsigned m) { return __builtin_amdgcn_ballot_w64(m == EMPTY) != 0; }
DI unsigned lds_add_rtn(lds_ptr p, int lane) {
    unsigned old = 0;
    CFENCE();
    if (lane == 0) old = __hip_atomic_fetch_add((LAS unsigned*)p, 1u, __ATOMIC_RELAXED, __HIP_MEMORY_SCOPE_WORKGROUP);
    old = __builtin_amdgcn_readfirstlane(old);
    CFENCE();
    return old;
}
DI void lds_add(lds_ptr p, int lane) {
    unsigned long long sv;
    asm volatile("s_mov_b64 %0, exec\n\ts_mov_b64 exec, 1\n\tds_add_u32 %1, %2\n\ts_mov_b64 exec, %0"
                 : "=&s"(sv) : "v"(p), "v"(1u) : "memory");
}
DI void lds_wait_ge(lds_ptr p, unsigned v) {
    CFENCE();
    while ((unsigned)__builtin_amdgcn_readfirstlane(*(volatile LAS unsigned*)p) < v) __builtin_amdgcn_s_sleep(1);
    CFENCE();
}
DI void poll_ge(gu32* word, unsigned want) {
    CFENCE();
    for (unsigned spins = 0; spins < (1u << 22); ++spins) {
        if ((unsigned)__builtin_amdgcn_readfirstlane(__hip_atomic_load(word, RLX_AGENT)) >= want) break;
        __builtin_amdgcn_s_sleep(1);
    }
    __builtin_amdgcn_fence(__ATOMIC_ACQUIRE, "wavefront");
    CFENCE();
}
#define PB(x) __builtin_bit_cast(bf16x8, x)
#define PRIO(x) do { __builtin_amdgcn_s_setprio(x); SBAR(); } while (0)
DI u32x4 gld128(unsigned voff, const void* sbase) {
    u32x4 r;
    asm volatile("global_load_dwordx4 %0, %1, %2" : "=v"(r) : "v"(voff), "s"(sbase) : "memory");
    return r;
}

#define K1_UNR 15
#define K1_LA 5
#define K1_ROT ((tb & 3) * 4)
constexpr int K1_STAGE = 40960, K1U = K1_UNR;
__global__ __launch_bounds__(512, 2) void k1_qkv(const char* __restrict__ X, const char* __restrict__ W,
                                                 char* __restrict__ ws) {
    __shared__ __attribute__((aligned(16))) char lds_raw[4 * K1_STAGE];
    const lds_ptr lds = (lds_ptr)lds_raw;
    const unsigned lds0 = (unsigned)(uintptr_t)lds;
    const int tid = threadIdx.x, lane = tid & 63;
    const int w = __builtin_amdgcn_readfirstlane(tid >> 6);
    const int b = blockIdx.x, xcd = b & 7, idx = b >> 3;
    const int hp = (xcd >> 1) * 4 + (idx & 3), tb = (xcd & 1) * 8 + (idx >> 2);
    DG_DECL(0)

    const int lw = w & 3;
    const int rot = K1_ROT;
    unsigned voffX[4], voffW[2];
#pragma unroll
    for (int pp = 0; pp < 4; ++pp) {
        const int row = 32 * lw + 8 * pp + (lane >> 3);
        const int ch = (lane & 7) ^ ((row >> 1) & 7);
        voffX[pp] = (unsigned)(tb * 128 + row) * 2048u + ch * 16;
    }
#pragma unroll
    for (int pp = 0; pp < 2; ++pp) {
        const int krow = 16 * lw + 8 * pp + (lane >> 3);
        const int ch = (lane & 7) ^ (((lane >> 4) & 1) << 2);
        voffW[pp] = (unsigned)krow * 6144u + ch * 16;
    }
    auto issue = [&](int kt, int lo, int hi) {
        const int kr = (kt + rot) & 15;
        const char* xb = X + kr * 128;
        const char* wb = W + (size_t)kr * (64 * 6144) + hp * 128;
        const unsigned st = lds0 + (kt & 3) * K1_STAGE;
#pragma unroll
        for (int pp = 0; pp < 4; ++pp)
            if (pp >= lo && pp < hi) glds16(voffX[pp], xb, st + (4 * lw + pp) * 1024);
#pragma unroll
        for (int sec = 0; sec < 3; ++sec)
#pragma unroll
            for (int pp = 0; pp < 2; ++pp)
                if (4 + 2 * sec + pp >= lo && 4 + 2 * sec + pp < hi)
                    glds16(voffW[pp], wb + sec * 2048, st + 16384 + sec * 8192 + (2 * lw + pp) * 1024);
    };
    DG(13);
    if (w < 4) { issue(0, 0, 5); issue(1, 0, 5); }
    else { issue(0, 5, 10); issue(1, 5, 10); }
    DG(14);
    int w2 = w;
    asm volatile("" : "+s"(w2));

    if (w2 >= 4) {
#define WAITN_BAR(N) asm volatile("s_waitcnt vmcnt(%0) lgkmcnt(0)\n\ts_barrier" :: "n"(N) : "memory")
        issue(2, 0, K1_LA);
        asm volatile("s_waitcnt vmcnt(%0)" :: "n"(5 + K1_LA) : "memory");
#pragma unroll 1
        for (int kt = 0; kt < 13; ++kt) {
            WAITN_BAR(10 + K1_LA);
            issue(kt + 2, K1_LA, 10);
            issue(kt + 3, 0, K1_LA);
        }
        WAITN_BAR(10 + K1_LA);
        issue(15, K1_LA, 10);
        WAIT_BAR(10);
        WAIT_BAR(0);
        if (lw == 0 && lane == 0) *(unsigned*)(ws + WS_CNT + b * 256) = 0u;
        DG_END(5);
        return;
    }

    const int wm = w >> 1, wn = w & 1;
    const int r = lane & 31, hh = lane >> 5;
    const int g = lane >> 4, q = (lane & 15) >> 2, p = lane & 3;
    const int xoff0 = (64 * wm + r) * 128 + ((hh ^ ((r >> 1) & 7)) << 4);
    const int woff0 = 16384 + (8 * hh + q) * 128 + (((4 * wn + 2 * (g & 1) + (p >> 1)) ^ ((q >> 1) << 2)) << 4) + (p & 1) * 8;

    f32x16 acc[2][3];
#pragma unroll
    for (int m = 0; m < 2; ++m)
#pragma unroll
        for (int s = 0; s < 3; ++s) acc[m][s] = f32x16{};
    bf16x8 xf[2][2];
    s16x4 wl[2][3], wh[2][3];
#define K1_WF(f, sec) cat8(wl[f][sec], wh[f][sec])
#define K1_STEP(F, st, t, BAR_HERE) do { \
        const lds_ptr xp_ = (st) + (xoff0 ^ ((t) << 5)); const lds_ptr wp_ = (st) + woff0 + (t) * 2048; \
        acc[0][0] = MFMA32(K1_WF(F, 0), xf[F][0], acc[0][0]); SBAR(); \
        if (BAR_HERE) { WAIT_BAR(0); SBAR(); } \
        xf[F ^ 1][0] = lds_rd128(xp_); wl[F ^ 1][0] = lds_tr(wp_); wh[F ^ 1][0] = lds_tr(wp_ + 512); SBAR(); \
        acc[0][1] = MFMA32(K1_WF(F, 1), xf[F][0], acc[0][1]); SBAR(); \
        wl[F ^ 1][1] = lds_tr(wp_ + 8192); wh[F ^ 1][1] = lds_tr(wp_ + 8192 + 512); SBAR(); \
        acc[0][2] = MFMA32(xf[F][0], K1_WF(F, 2), acc[0][2]); SBAR(); \
        wl[F ^ 1][2] = lds_tr(wp_ + 16384); wh[F ^ 1][2] = lds_tr(wp_ + 16384 + 512); xf[F ^ 1][1] = lds_rd128(xp_ + 4096); SBAR(); \
        acc[1][0] = MFMA32(K1_WF(F, 0), xf[F][1], acc[1][0]); SBAR(); \
        acc[1][1] = MFMA32(K1_WF(F, 1), xf[F][1], acc[1][1]); \
        acc[1][2] = MFMA32(xf[F][1], K1_WF(F, 2), acc[1][2]); SBAR(); \
    } while (0)

    WAIT_BAR(5);
    DG(3);
    {
        const lds_ptr xp_ = lds + xoff0; const lds_ptr wp_ = lds + woff0;
        xf[0][0] = lds_rd128(xp_); xf[0][1] = lds_rd128(xp_ + 4096);
#pragma unroll
        for (int sec = 0; sec < 3; ++sec) { wl[0][sec] = lds_tr(wp_ + sec * 8192); wh[0][sec] = lds_tr(wp_ + sec * 8192 + 512); }
    }
    SBAR();
#pragma unroll K1U
    for (int kt = 0; kt < 15; ++kt) {
        const lds_ptr st = lds + (kt & 3) * K1_STAGE;
        const lds_ptr sn = lds + ((kt + 1) & 3) * K1_STAGE;
        K1_STEP(0, st, 1, false);
        K1_STEP(1, st, 2, false);
        K1_STEP(0, st, 3, false);
        K1_STEP(1, sn, 0, true);
    }
    {
        const lds_ptr st = lds + 3 * K1_STAGE;
        K1_STEP(0, st, 1, false);
        K1_STEP(1, st, 2, false);
        K1_STEP(0, st, 3, false);
#pragma unroll
        for (int m = 0; m < 2; ++m) {
            acc[m][1] = MFMA32(K1_WF(1, 1), xf[1][m], acc[m][1]);
            acc[m][2] = MFMA32(xf[1][m], K1_WF(1, 2), acc[m][2]);
            acc[m][0] = MFMA32(K1_WF(1, 0), xf[1][m], acc[m][0]);
        }
    }
    DG(4);
    {
        const int h = 2 * hp + wn;
        const float qscale = 0.17677669529663687f * 1.4426950408889634f;
        const __amdgpu_buffer_rsrc_t rs = __builtin_amdgcn_make_buffer_rsrc(ws, 0, 32 << 20, 0x00020000);
#pragma unroll
        for (int m = 0; m < 2; ++m) {
            const int T = tb * 4 + wm * 2 + m;
            const unsigned dq = (unsigned)WS_Q + (h * 64 + T) * 2048 + lane * 16;
            const unsigned dk = (unsigned)WS_KV + (h * 64 + T) * 4096 + lane * 16;
            st_sc1(pack8n(acc[m][1], 0), rs, dk);
            st_sc1(pack8n(acc[m][1], 1), rs, dk + 1024);
            st_sc1(pack8n(acc[m][2], 0), rs, dk + 2048);
            st_sc1(pack8n(acc[m][2], 1), rs, dk + 3072);
            st_sc1(pack8(acc[m][0], 0, qscale), rs, dq);
            st_sc1(pack8(acc[m][0], 1, qscale), rs, dq + 1024);
        }
    }
    DG_END(5);
}

constexpr int K2_SLOT = 32768;
constexpr int K2_SF = 65536;
constexpr int K2_CV = 98304;
constexpr int CV_HA = 0, CV_HB = 4160, CV_KV = 8320, CV_BYTES = 8320 + 480 + 4096;
constexpr int K2_LX = K2_CV + 4 * CV_BYTES;
constexpr int K2_FL = K2_LX + 2048;
constexpr int K2_SCR = K2_FL + 256;
constexpr int K2_LDS = K2_SCR + 4096;
constexpr int F_X = 0, F_R = 32, F_W2 = 64, C_OST = 68, C_S1 = 76, C_S2 = 80, C_S3 = 128, C_ZST = 88, F_ZGO = 92,
              C_KST = 96, C_U0 = 100, C_LD = 108, F_KGO = 116, C_END = 120, C_OL = 72, C_HF = 84;
#define NG_IN 0
#define GATE_WORD C_END
#define GATE_VAL 0
#define X_NZ 4
#define X_NW3 8
#define ZP_GATE 4u
#define ZL_GATE 4
#define ZL_AT 31
#define Z_SLEEP0 0
#define O_SLEEP 0
#define Z_SLEEP 8
#define OWIN 16
#define OW0 8
#define WD 2
#define W3_AT 10
#define X_NOLDW 0
#define X_NOLDO 0
#define X_NOCHK 0
#define KROT (nt * 2)
#define NG_LAST 0
#define PACE_IN "s_nop 7"
#define PACE_OUT "s_nop 1"
#define P3_PRIO 1
#define TOP_SLEEP 2
#define A_AUX 16
#define B_AUX 16
#define B_LDS 0
#define TL 8
#define XR_AT 62
#define W3_AUX 0
#define W3M 13
#define A16 1
#define ZE_PRIO 3
#define P3_T1 99
#define P3_T2 99
#define AUX_PRIO 2

__global__ __launch_bounds__(768, 3) void k2_attn(char* __restrict__ ws, const char* __restrict__ Aq,
                                                  const char* __restrict__ Ak, const char* __restrict__ Av,
                                                  const char* __restrict__ shiftk, const char* __restrict__ ssmk,
                                                  const unsigned* __restrict__ ssmD, const char* __restrict__ Wo,
                                                  char* __restrict__ out) {
    __shared__ __attribute__((aligned(16))) char lds_raw[K2_LDS];
    const lds_ptr lds = (lds_ptr)lds_raw;
    const lds_ptr fl = lds + K2_FL;
    const unsigned lds0 = (unsigned)(uintptr_t)lds;
    const int tid = threadIdx.x, lane = tid & 63;
    const int w = __builtin_amdgcn_readfirstlane(tid >> 6);
    const int b = blockIdx.x, xcd = b & 7, idx = b >> 3;
    const int h = xcd * 4 + (idx >> 3), qb = idx & 7;
    const int tt = xcd * 2 + (idx >> 4), nt = idx & 15;
    const char* kvg = ws + WS_KV + (size_t)h * 64 * 4096;
    const __amdgpu_buffer_rsrc_t rs = __builtin_amdgcn_make_buffer_rsrc(ws, 0, 32 << 20, 0x00020000);
    const __amdgpu_buffer_rsrc_t ro = __builtin_amdgcn_make_buffer_rsrc(out, 0, 8 << 20, 0x00020000);
    gu32* const cnt = (gu32*)(ws + WS_CNT);
    auto signal = [&]() {
        (void)__hip_atomic_fetch_add(cnt + ((lane & 31) * 8 + (lane < 32 ? xcd : qb)) * 64, 1u, RLX_AGENT);
    };
    const unsigned wvoff = (unsigned)(lane >> 3) * 2048u + nt * 128 + (((lane & 7) ^ (((lane >> 4) & 1) << 2)) << 4);
    const int krot = KROT;
    auto wsrc = [&](int hf, int lp) { return Wo + (size_t)(hf * 64 + ((lp + krot * 2) & 63)) * 16384; };
    DG_DECL(1)
    const unsigned voff = lane * 16;
    const int cw = w & 3;
    auto issue = [&](int st) {
        const char* src = kvg + (st * 4 + cw) * 4096;
        const unsigned dst = lds0 + (st & 1) * K2_SLOT + cw * 4096;
#pragma unroll
        for (int i = 0; i < 4; ++i) glds16(voff, src + i * 1024, dst + i * 1024);
#pragma unroll
        for (int i = 0; i < 4; ++i) glds16(voff, src + 131072 + i * 1024, dst + 16384 + i * 1024);
    };
    if (w >= 8) {
        __builtin_amdgcn_s_sleep(TOP_SLEEP);
#pragma unroll
        for (int i = 2; i < 8; ++i)
            glds16(voff, kvg + (i & 1) * 131072 + (i >> 1) * 4096 + cw * 1024, lds0 + (i & 1) * 16384 + (i >> 1) * 4096 + cw * 1024);
        RAW_BAR();
        issue(1);
        WAIT_BAR(8);
    } else {
        const int tq = cw * 2 + (w >> 2);
        const char* qg = ws + WS_Q + (size_t)(h * 64 + qb * 8 + tq) * 2048;
        glds16(voff, kvg + (w >> 2) * 131072 + cw * 1024, lds0 + (w >> 2) * 16384 + cw * 1024);
        glds16(voff, qg, lds0 + K2_SF + tq * 2048);
        glds16(voff, qg + 1024, lds0 + K2_SF + tq * 2048 + 1024);
    }
    int w2 = w;
    asm volatile("" : "+s"(w2));

    if (w2 >= 8) {
        __builtin_amdgcn_s_setprio(3);
        const int ch = xcd * 128 + idx * 4 + cw;
        const lds_ptr cv = lds + K2_CV + cw * CV_BYTES;
        {
            const u32x4 z4 = {0u, 0u, 0u, 0u};
            if (lane < 2) *(LAS u32x4*)(cv + CV_HA + lane * 16) = z4;
            if (lane < 30) *(LAS u32x4*)(cv + CV_KV + lane * 16) = z4;
            if (cw == 0 && lane < 16) *(LAS u32x4*)(fl + lane * 16) = z4;
        }
        const int c0 = xcd * 128 + cw * 32, cq = lane & 7, rg = lane >> 3;
        const int row0 = idx * 64 + rg * 8;
        const unsigned roff = (unsigned)row0 * 2048u + (c0 + cq * 4) * 2;
        const unsigned toffs = (unsigned)(c0 + (lane >> 3)) * 4096u + idx * 128;
        u32x4 sk[2];
        sk[0] = gld128((unsigned)(c0 + cq * 4) * 8u, shiftk); sk[1] = gld128((unsigned)(c0 + cq * 4) * 8u, shiftk + 16);
        unsigned dd = gld32((unsigned)(ch >> 1) * 4u, ssmD);
        auto stage = [&](const char* src, int dst) {
            const unsigned sbase = (unsigned)(idx * 64 + cw * 16 + (lane >> 4)) * 2048u + xcd * 256;
#pragma unroll
            for (int i = 0; i < 4; ++i)
                glds16(sbase + (((lane & 15) ^ (((2 * cw + (i >> 1)) & 3) << 2)) << 4), src + i * 8192, lds0 + dst + cw * 4096 + i * 1024);
        };
        auto blank = [&](unsigned off) {
            const u32x4 e4 = {EMPTY, EMPTY, EMPTY, EMPTY};
            CFENCE();
            st_sc1(e4, rs, off + voff); st_sc1(e4, rs, off + 1024 + voff);
            CFENCE();
        };
        WAIT_BAR(3);
        DG(12);
        issue(2);
        stage(Ak, K2_SF);
        glds16((unsigned)(idx * 64 - (idx ? 4 : 0) + (lane >> 4)) * 2048u + xcd * 256 + (((lane & 15) ^ 12) << 4), Ak, lds0 + K2_SCR + cw * 1024);
        blank((unsigned)WS_O + (unsigned)((qb * 8 + cw * 2) * 32 + h) * 2048u);
        WAIT_BAR(7);
        DG(9);
        issue(3);
        stage(Av, K2_SF + 16384);
        blank((unsigned)WS_O + (unsigned)((qb * 8 + cw * 2 + 1) * 32 + h) * 2048u);
        WAIT_BAR(6);
        DG(3);
        issue(4);
#pragma unroll
        for (int i = 0; i < 4; ++i) glds16(voff, ssmk + (size_t)ch * 4096 + i * 1024, lds0 + K2_CV + cw * CV_BYTES + CV_HA + 32 + i * 1024);
        blank((unsigned)WS_ZT + ch * 4096); blank((unsigned)WS_ZT + ch * 4096 + 2048);
        WAIT_BAR(8);
        DG(5);
        issue(5);
        SBAR();
        asm volatile("" : "+v"(sk[0]), "+v"(sk[1]), "+v"(dd));
        dd = __builtin_amdgcn_readfirstlane(dd);
        {
            u32x2 kr[11], vr[8];
            const lds_ptr kb = lds + K2_SF + rg * 2048 + ((cw ^ (rg & 3)) << 6) + cq * 8;
            const lds_ptr hb = lds + (rg ? K2_SF + rg * 2048 - 768 : K2_SCR + cw * 1024 + 256) + ((cw ^ ((rg + 3) & 3)) << 6) + cq * 8;
#pragma unroll
            for (int i = 0; i < 3; ++i) kr[i] = *(const LAS u32x2*)(hb + i * 256);
#pragma unroll
            for (int i = 0; i < 8; ++i) kr[3 + i] = *(const LAS u32x2*)(kb + i * 256);
#pragma unroll
            for (int i = 0; i < 8; ++i) vr[i] = *(const LAS u32x2*)(kb + 16384 + i * 256);
            if (row0 == 0) { kr[0] = u32x2{0u, 0u}; kr[1] = u32x2{0u, 0u}; kr[2] = u32x2{0u, 0u}; }
            const lds_ptr tb = cv + CV_KV + 480;
#pragma unroll
            for (int c = 0; c < 4; ++c) {
                const unsigned s01 = sk[c >> 1][(c & 1) * 2], s23 = sk[c >> 1][(c & 1) * 2 + 1];
                const float w0 = bflo(s01), w1 = bfhi(s01), w2 = bflo(s23), w3 = bfhi(s23);
                float kf[11], kv[8];
#pragma unroll
                for (int i = 0; i < 11; ++i) kf[i] = (c & 1) ? bfhi(kr[i][c >> 1]) : bflo(kr[i][c >> 1]);
#pragma unroll
                for (int i = 0; i < 8; ++i) {
                    float a = mul_vv(w3, kf[i]);
                    fmac_vv(a, w2, kf[i + 1]); fmac_vv(a, w1, kf[i + 2]); fmac_vv(a, w0, kf[i + 3]);
                    kv[i] = mul_vv(a, (c & 1) ? bfhi(vr[i][c >> 1]) : bflo(vr[i][c >> 1]));
                }
                u32x4 o4;
#pragma unroll
                for (int i = 0; i < 4; ++i) o4[i] = cvtpk(kv[7 - 2 * i], kv[6 - 2 * i]);
                *(LAS u32x4*)(tb + (cq * 4 + c) * 128 + ((rg ^ cq) << 4)) = o4;
            }
#pragma unroll
            for (int i = 0; i < 4; ++i) {
                const u32x4 t = *(const LAS u32x4*)(tb + i * 1024 + lane * 16);
                st_sc1(t, rs, (unsigned)WS_KVR + toffs + i * 32768 + (((lane & 7) ^ (2 * i + (lane >> 5))) << 4));
            }
        }
        CFENCE();
        DG(7);
        WAIT_BAR(4);
        DG(6);
        issue(6);
        asm volatile("s_waitcnt vmcnt(8)" ::: "memory");
        if (lds_add_rtn(fl + C_KST, lane) == 3u) signal();
#pragma unroll
        for (int i = 0; i < 8; ++i)
            glds16(wvoff, wsrc(i >> 2, cw * 4 + (i & 3)), lds0 + K2_SF + (i >> 2) * 16384 + (cw * 4 + (i & 3)) * 1024);
        {
            const u32x4 z4 = {0u, 0u, 0u, 0u};
            if (lane < 2) *(LAS u32x4*)(cv + CV_HB + lane * 16) = z4;
#pragma unroll
            for (int i = 0; i < 4; ++i) {
                const int off = 32 + i * 1024 + lane * 16;
                const u32x4 hv = *(const LAS u32x4*)(cv + CV_HA + off);
                const unsigned pv = *(const LAS unsigned*)(cv + CV_HA + off - 4);
                u32x4 sh;
                sh[0] = __builtin_amdgcn_alignbit(hv[0], pv, 16);
                sh[1] = __builtin_amdgcn_alignbit(hv[1], hv[0], 16);
                sh[2] = __builtin_amdgcn_alignbit(hv[2], hv[1], 16);
                sh[3] = __builtin_amdgcn_alignbit(hv[3], hv[2], 16);
                *(LAS u32x4*)(cv + CV_HB + off) = sh;
            }
        }
        WAIT_BAR(8);
        DG(8);
        issue(7);
#pragma unroll
        for (int i = 0; i < 8; ++i)
            glds16(wvoff, wsrc(idx >> 4, 32 + cw * 8 + i), lds0 + K2_SCR + cw * 1024);
        WAIT_BAR(8);
        DG(11);
        if (cw == 0) { poll_ge(cnt + b * 64, 64u); lds_add(fl + F_KGO, lane); }
        else lds_wait_ge(fl + F_KGO, 1u);
        {
            u32x4 t[4];
#pragma unroll
            for (int i = 0; i < 4; ++i) t[i] = ld_sc1(rs, (unsigned)WS_KVR + ch * 4096 + i * 1024 + voff);
#pragma unroll
            for (int i = 0; i < 4; ++i) *(LAS u32x4*)(cv + CV_KV + 480 + i * 1024 + lane * 16) = t[i];
        }
        const int r1 = lane & 15, q = lane >> 4;
        const int toff = (r1 & 1) ? CV_HA + 2 * (16 + r1 + 9 - 8 * q) : CV_HB + 2 * (16 + r1 + 10 - 8 * q);
        const int boff = CV_KV + 448 + 32 * r1 + 16 * q;
        f32x4 y[8];
        u32x4 tf[8];
        bf16x8 bf[8];
        const lds_ptr bp = cv + boff - 448;
        lds_ptr tp[4];
#pragma unroll
        for (int i = 0; i < 4; ++i) { tp[i] = cv + toff + 1024 * i; asm volatile("" : "+v"(tp[i])); }
#define LDT(bb, a, hf) do { const LAS unsigned* t_ = (const LAS unsigned*)(tp[(a) >> 1] + 64 * (bb) + 512 * ((a) & 1) + 8 * (hf)); \
            tf[a][2 * (hf)] = t_[0]; tf[a][2 * (hf) + 1] = t_[1]; SBAR(); } while (0)
#define LDB(bb, a) do { bf[a] = lds_rd128(bp + 448 - 64 * (bb) + 512 * (a)); SBAR(); } while (0)
#define CM(a, ww) do { asm volatile("v_mfma_f32_16x16x32_bf16 %0, %1, %2, %0" : "+v"(y[(a) + (ww)]) : "v"(tf[a]), "v"(bf[ww])); SBAR(); } while (0)
#define CM0(a, ww) do { asm volatile("v_mfma_f32_16x16x32_bf16 %0, %1, %2, 0" : "=&v"(y[(a) + (ww)]) : "v"(tf[a]), "v"(bf[ww])); SBAR(); } while (0)
#define PC() do { asm volatile(PACE_OUT); SBAR(); } while (0)
#pragma unroll
        for (int a = 0; a < 8; ++a) { if (a < 5) { LDT(0, a, 0); LDT(0, a, 1); } LDB(0, 7 - a); }
#pragma unroll
        for (int i = 0; i < 8; ++i)
            glds16(wvoff, wsrc(i >> 2, 16 + cw * 4 + (i & 3)), lds0 + (i >> 2) * 16384 + (cw * 4 + (i & 3)) * 1024);
#pragma unroll
        for (int i = 0; i < 4; ++i)
            glds16((unsigned)(idx * 64 + cw * 16 + (lane >> 4)) * 2048u + xcd * 256 + (lane & 15) * 16, Aq + i * 8192, lds0 + K2_SCR + cw * 1024);
        RAW_BAR();
        asm volatile("s_waitcnt vmcnt(4)" ::: "memory");
        lds_add(fl + C_S1, lane);
        __builtin_amdgcn_s_setprio(AUX_PRIO);
        const float dch = (ch & 1) ? bfhi(dd) : bflo(dd);
        const lds_ptr kdp = cv + CV_KV + 480 + 32 * r1 + 16 * (q >> 1) + ((q & 1) ? 0 : 8);
        float kd[8][4];
        u32x2 kk;
        unsigned kc0, kc1, zo[8][2];
#define KD_RD(t) do { kk = *(const LAS u32x2*)(kdp + 512 * (t)); SBAR(); } while (0)
#define KD_UP0(t) do { kd[t][0] = bfhi(kk[1]); kd[t][1] = bflo(kk[1]); SBAR(); } while (0)
#define KD_UP1(t) do { kd[t][2] = bfhi(kk[0]); kd[t][3] = bflo(kk[0]); SBAR(); } while (0)
#define KD_MU0(t) do { kd[t][0] = mul_sv(dch, kd[t][0]); kd[t][1] = mul_sv(dch, kd[t][1]); SBAR(); } while (0)
#define KD_MU1(t) do { kd[t][2] = mul_sv(dch, kd[t][2]); kd[t][3] = mul_sv(dch, kd[t][3]); SBAR(); } while (0)
#define KD_CV(t) do { kc0 = cvtpk(kd[t][0], kd[t][1]); kc1 = cvtpk(kd[t][2], kd[t][3]); SBAR(); } while (0)
#define KD_X0(t) do { kd[t][0] = bflo(kc0); kd[t][1] = bfhi(kc0); SBAR(); } while (0)
#define KD_X1(t) do { kd[t][2] = bflo(kc1); kd[t][3] = bfhi(kc1); SBAR(); } while (0)
#define ZF0(t) do { kc0 = cvtpk(y[t][0], y[t][1]); kc1 = cvtpk(y[t][2], y[t][3]); SBAR(); } while (0)
#define ZF1(t) do { y[t][0] = bflo(kc0); y[t][1] = bfhi(kc0); SBAR(); } while (0)
#define ZF2(t) do { y[t][2] = bflo(kc1); y[t][3] = bfhi(kc1); SBAR(); } while (0)
#define ZF3(t) do { y[t][0] = add_vv(y[t][0], kd[t][0]); y[t][1] = add_vv(y[t][1], kd[t][1]); SBAR(); } while (0)
#define ZF4(t) do { y[t][2] = add_vv(y[t][2], kd[t][2]); y[t][3] = add_vv(y[t][3], kd[t][3]); SBAR(); } while (0)
#define ZF5(t) do { zo[t][0] = nz(cvtpk(y[t][0], y[t][1])); zo[t][1] = nz(cvtpk(y[t][2], y[t][3])); SBAR(); } while (0)
#define ZST(pr) do { auto s0 = __builtin_amdgcn_permlane16_swap(zo[2 * (pr)][0], zo[2 * (pr) + 1][0], false, false); \
            auto s1 = __builtin_amdgcn_permlane16_swap(zo[2 * (pr)][1], zo[2 * (pr) + 1][1], false, false); \
            st_sc1(u32x4{s0[0], s1[0], s0[1], s1[1]}, rs, (unsigned)WS_ZT + ch * 4096 + (pr) * 1024 + voff); SBAR(); } while (0)
#define W3(i) do { if ((i) == 0) PC(); } while (0)
#define HF() do { lds_add(fl + C_HF, lane); SBAR(); } while (0)
        CM0(0, 7); LDB(1, 7); CM0(0, 6); LDT(0, 5, 0); CM0(0, 5); LDT(0, 5, 1); CM0(0, 4); LDT(0, 6, 0); CM0(0, 3); LDT(0, 6, 1); CM0(0, 2); LDT(0, 7, 0); CM0(0, 1); LDT(0, 7, 1); CM0(0, 0); KD_RD(0);
        CM(1, 6); LDB(1, 6); CM(1, 5); LDT(1, 0, 0); CM(1, 4); LDT(1, 0, 1); CM(1, 3); PC(); CM(1, 2); PC(); CM(1, 1); KD_UP0(0); CM(1, 0); KD_UP1(0);
        CM(2, 5); LDB(1, 5); CM(2, 4); LDT(1, 1, 0); CM(2, 3); LDT(1, 1, 1); CM(2, 2); KD_MU0(0); CM(2, 1); KD_MU1(0); CM(2, 0); KD_CV(0);
        CM(3, 4); LDB(1, 4); CM(3, 3); LDT(1, 2, 0); CM(3, 2); LDT(1, 2, 1); CM(3, 1); KD_X0(0); CM(3, 0); KD_X1(0);
        CM(4, 3); LDB(1, 3); CM(4, 2); LDT(1, 3, 0); CM(4, 1); LDT(1, 3, 1); CM(4, 0); KD_RD(1);
        CM(5, 2); LDB(1, 2); CM(5, 1); LDT(1, 4, 0); CM(5, 0); LDT(1, 4, 1);
        CM(6, 1); LDB(1, 1); CM(6, 0); PC();
        CM(7, 0); LDB(1, 0);
        CM(0, 7); LDB(2, 7); CM(0, 6); LDT(1, 5, 0); CM(0, 5); LDT(1, 5, 1); CM(0, 4); LDT(1, 6, 0); CM(0, 3); LDT(1, 6, 1); CM(0, 2); LDT(1, 7, 0); CM(0, 1); LDT(1, 7, 1); CM(0, 0); PC();
        CM(1, 6); LDB(2, 6); CM(1, 5); LDT(2, 0, 0); CM(1, 4); LDT(2, 0, 1); CM(1, 3); KD_UP0(1); CM(1, 2); KD_UP1(1); CM(1, 1); KD_MU0(1); CM(1, 0); KD_MU1(1);
        CM(2, 5); LDB(2, 5); CM(2, 4); LDT(2, 1, 0); CM(2, 3); LDT(2, 1, 1); CM(2, 2); KD_CV(1); CM(2, 1); KD_X0(1); CM(2, 0); KD_X1(1);
        CM(3, 4); LDB(2, 4); CM(3, 3); LDT(2, 2, 0); CM(3, 2); LDT(2, 2, 1); CM(3, 1); KD_RD(2); CM(3, 0); PC();
        CM(4, 3); LDB(2, 3); CM(4, 2); LDT(2, 3, 0); CM(4, 1); LDT(2, 3, 1); CM(4, 0); PC();
        CM(5, 2); LDB(2, 2); CM(5, 1); LDT(2, 4, 0); CM(5, 0); LDT(2, 4, 1);
        CM(6, 1); LDB(2, 1); CM(6, 0); KD_UP0(2);
        CM(7, 0); LDB(2, 0);
        CM(0, 7); LDB(3, 7); CM(0, 6); LDT(2, 5, 0); CM(0, 5); LDT(2, 5, 1); CM(0, 4); LDT(2, 6, 0); CM(0, 3); LDT(2, 6, 1); CM(0, 2); LDT(2, 7, 0); CM(0, 1); LDT(2, 7, 1); CM(0, 0); KD_UP1(2);
        CM(1, 6); LDB(3, 6); CM(1, 5); LDT(3, 0, 0); CM(1, 4); LDT(3, 0, 1); CM(1, 3); KD_MU0(2); CM(1, 2); KD_MU1(2); CM(1, 1); KD_CV(2); CM(1, 0); KD_X0(2);
        CM(2, 5); LDB(3, 5); CM(2, 4); LDT(3, 1, 0); CM(2, 3); LDT(3, 1, 1); CM(2, 2); KD_X1(2); CM(2, 1); KD_RD(3); CM(2, 0); PC();
        CM(3, 4); LDB(3, 4); CM(3, 3); LDT(3, 2, 0); CM(3, 2); LDT(3, 2, 1); CM(3, 1); PC(); CM(3, 0); KD_UP0(3);
        CM(4, 3); LDB(3, 3); CM(4, 2); LDT(3, 3, 0); CM(4, 1); LDT(3, 3, 1); CM(4, 0); KD_UP1(3);
        CM(5, 2); LDB(3, 2); CM(5, 1); LDT(3, 4, 0); CM(5, 0); LDT(3, 4, 1);
        CM(6, 1); LDB(3, 1); CM(6, 0); KD_MU0(3);
        CM(7, 0); LDB(3, 0);
        CM(0, 7); LDB(4, 7); CM(0, 6); LDT(3, 5, 0); CM(0, 5); LDT(3, 5, 1); CM(0, 4); LDT(3, 6, 0); CM(0, 3); LDT(3, 6, 1); CM(0, 2); LDT(3, 7, 0); CM(0, 1); LDT(3, 7, 1); CM(0, 0); KD_MU1(3);
        CM(1, 6); LDB(4, 6); CM(1, 5); LDT(4, 0, 0); CM(1, 4); LDT(4, 0, 1); CM(1, 3); KD_CV(3); CM(1, 2); KD_X0(3); CM(1, 1); KD_X1(3); CM(1, 0); KD_RD(4);
        CM(2, 5); LDB(4, 5); CM(2, 4); LDT(4, 1, 0); CM(2, 3); LDT(4, 1, 1); CM(2, 2); PC(); CM(2, 1); PC(); CM(2, 0); KD_UP0(4);
        CM(3, 4); LDB(4, 4); CM(3, 3); LDT(4, 2, 0); CM(3, 2); LDT(4, 2, 1); CM(3, 1); KD_UP1(4); CM(3, 0); KD_MU0(4);
        CM(4, 3); LDB(4, 3); CM(4, 2); LDT(4, 3, 0); CM(4, 1); LDT(4, 3, 1); CM(4, 0); KD_MU1(4);
        CM(5, 2); LDB(4, 2); CM(5, 1); LDT(4, 4, 0); CM(5, 0); LDT(4, 4, 1);
        CM(6, 1); LDB(4, 1); CM(6, 0); KD_CV(4);
        CM(7, 0); LDB(4, 0);
        CM(0, 7); LDB(5, 7); CM(0, 6); LDT(4, 5, 0); CM(0, 5); LDT(4, 5, 1); CM(0, 4); LDT(4, 6, 0); CM(0, 3); LDT(4, 6, 1); CM(0, 2); LDT(4, 7, 0); CM(0, 1); LDT(4, 7, 1); CM(0, 0); KD_X0(4);
        CM(1, 6); LDB(5, 6); CM(1, 5); LDT(5, 0, 0); CM(1, 4); LDT(5, 0, 1); CM(1, 3); KD_X1(4); CM(1, 2); KD_RD(5); CM(1, 1); PC(); CM(1, 0); PC();
        CM(2, 5); LDB(5, 5); CM(2, 4); LDT(5, 1, 0); CM(2, 3); LDT(5, 1, 1); CM(2, 2); KD_UP0(5); CM(2, 1); KD_UP1(5); CM(2, 0); KD_MU0(5);
        CM(3, 4); LDB(5, 4); CM(3, 3); LDT(5, 2, 0); CM(3, 2); LDT(5, 2, 1); CM(3, 1); KD_MU1(5); CM(3, 0); KD_CV(5);
        CM(4, 3); LDB(5, 3); CM(4, 2); LDT(5, 3, 0); CM(4, 1); LDT(5, 3, 1); CM(4, 0); KD_X0(5);
        CM(5, 2); LDB(5, 2); CM(5, 1); LDT(5, 4, 0); CM(5, 0); LDT(5, 4, 1);
        CM(6, 1); LDB(5, 1); CM(6, 0); KD_X1(5);
        CM(7, 0); LDB(5, 0);
        CM(0, 7); LDB(6, 7); CM(0, 6); LDT(5, 5, 0); CM(0, 5); LDT(5, 5, 1); CM(0, 4); LDT(5, 6, 0); CM(0, 3); LDT(5, 6, 1); CM(0, 2); LDT(5, 7, 0); CM(0, 1); LDT(5, 7, 1); CM(0, 0); KD_RD(6);
        CM(1, 6); LDB(6, 6); CM(1, 5); LDT(6, 0, 0); CM(1, 4); LDT(6, 0, 1); CM(1, 3); PC(); CM(1, 2); PC(); CM(1, 1); KD_UP0(6); CM(1, 0); KD_UP1(6);
        CM(2, 5); LDB(6, 5); CM(2, 4); LDT(6, 1, 0); CM(2, 3); LDT(6, 1, 1); CM(2, 2); KD_MU0(6); CM(2, 1); KD_MU1(6); CM(2, 0); KD_CV(6);
        CM(3, 4); LDB(6, 4); CM(3, 3); LDT(6, 2, 0); CM(3, 2); LDT(6, 2, 1); CM(3, 1); KD_X0(6); CM(3, 0); KD_X1(6);
        CM(4, 3); LDB(6, 3); CM(4, 2); LDT(6, 3, 0); CM(4, 1); LDT(6, 3, 1); CM(4, 0); KD_RD(7);
        CM(5, 2); LDB(6, 2); CM(5, 1); LDT(6, 4, 0); CM(5, 0); LDT(6, 4, 1);
        CM(6, 1); LDB(6, 1); CM(6, 0); PC();
        CM(7, 0); LDB(6, 0);
        CM(0, 7); LDB(7, 7); CM(0, 6); LDT(6, 5, 0); CM(0, 5); LDT(6, 5, 1); CM(0, 4); LDT(6, 6, 0); CM(0, 3); LDT(6, 6, 1); CM(0, 2); LDT(6, 7, 0); CM(0, 1); LDT(6, 7, 1); CM(0, 0); PC();
        CM(1, 6); LDB(7, 6); CM(1, 5); LDT(7, 0, 0); CM(1, 4); LDT(7, 0, 1); CM(1, 3); KD_UP0(7); CM(1, 2); KD_UP1(7); CM(1, 1); KD_MU0(7); CM(1, 0); KD_MU1(7);
        CM(2, 5); LDB(7, 5); CM(2, 4); LDT(7, 1, 0); CM(2, 3); LDT(7, 1, 1); CM(2, 2); KD_CV(7); CM(2, 1); KD_X0(7); CM(2, 0); KD_X1(7);
        CM(3, 4); LDB(7, 4); CM(3, 3); LDT(7, 2, 0); CM(3, 2); LDT(7, 2, 1); CM(3, 1); PC(); CM(3, 0); PC();
        CM(4, 3); LDB(7, 3); CM(4, 2); LDT(7, 3, 0); CM(4, 1); LDT(7, 3, 1); CM(4, 0); PC();
        CM(5, 2); LDB(7, 2); CM(5, 1); LDT(7, 4, 0); CM(5, 0); LDT(7, 4, 1);
        CM(6, 1); LDB(7, 1); CM(6, 0); PC();
        CM(7, 0); LDB(7, 0);
        CM(0, 3); LDT(7, 5, 0); CM(0, 2); LDT(7, 5, 1); CM(1, 2); LDT(7, 6, 0); CM(1, 1); LDT(7, 6, 1); CM(2, 1); LDT(7, 7, 0); CM(2, 0); LDT(7, 7, 1);
        CM(3, 0); HF(); CM(0, 1); PC(); CM(0, 0); W3(0); CM(1, 0); ZF0(2); W3(1); CM(0, 5); ZF1(2); W3(2); CM(0, 4); ZF2(2); W3(3);
        CM(1, 4); ZF3(2); W3(4); CM(1, 3); ZF4(2); W3(5); CM(2, 3); ZF5(2); W3(6); CM(2, 2); ZF0(3); W3(7); CM(3, 2); ZF1(3);  CM(3, 1); ZF2(3);
        CM(4, 1); ZF3(3);  CM(4, 0); ZF4(3);  CM(5, 0); ZF5(3);  CM(0, 7); ZST(1);  CM(0, 6); ZF0(0);  CM(1, 6); ZF1(0);
        CM(1, 5); ZF2(0);  CM(2, 5); ZF3(0);  CM(2, 4); ZF4(0);  CM(3, 4); ZF5(0);  CM(3, 3); ZF0(1);  CM(4, 3); ZF1(1);
        CM(4, 2); ZF2(1);  CM(5, 2); ZF3(1);  CM(5, 1); ZF4(1);  CM(6, 1); ZF5(1);  CM(6, 0); ZST(0);  CM(7, 0); ZF0(4);
        DG(4);
        ZF1(4); ZF2(4); ZF3(4); ZF4(4); ZF5(4); ZF0(5); ZF1(5); ZF2(5); ZF3(5); ZF4(5); ZF5(5); ZST(2); ZF0(6); ZF1(6); ZF2(6); ZF3(6); ZF4(6); ZF5(6); ZF0(7); ZF1(7); ZF2(7); ZF3(7); ZF4(7); ZF5(7); ZST(3);
        DG(10);
        __builtin_amdgcn_s_setprio(3);
        while ((unsigned)__builtin_amdgcn_readfirstlane(*(volatile LAS unsigned*)(fl + C_OL)) < ZL_GATE) __builtin_amdgcn_s_sleep(4);
        CFENCE();
        const int arow = idx * 64 + (cw >> 1) * 32 + rg * 4, acol = xcd * 256 + (cw & 1) * 128 + cq * 16;
        u32x4 qr[4];
#pragma unroll
        for (int i = 0; i < 4; ++i) qr[i] = gld128((unsigned)(arow * 2048 + acol), Aq + i * 2048);
        {
            const lds_ptr tr = cv + CV_KV + 480;
            u32x4 t[4];
            const int ck = (cw >> 1) * 4 + ((lane ^ ((lane >> 5) * 2)) & 3);
            const unsigned zb = (unsigned)WS_ZT + (unsigned)(xcd * 128 + (cw & 1) * 64 + (lane >> 4) * 4) * 4096u + (idx >> 3) * 1024 +
                                ((idx & 3) * 4 + ((idx >> 2) & 1) * 16 + (ck >> 1) + (ck & 1) * 32) * 16;
#pragma unroll 1
            for (unsigned spins = 0; spins < (1u << 20); ++spins) {
#pragma unroll
                for (int i = 0; i < 4; ++i) t[i] = ld_sc1s(rs, zb + ((((lane >> 2) & 3) ^ i) << 12), i * 65536);
                if (!missing(mx4(mx4(mx8(t[0], t[1]), t[2]), t[3]))) break;
                __builtin_amdgcn_s_sleep(Z_SLEEP);
            }
            DG(13);
            __builtin_amdgcn_s_setprio(ZE_PRIO);
#pragma unroll
            for (int i = 0; i < 4; ++i) *(LAS u32x4*)(tr + i * 1024 + lane * 16) = t[i];
#pragma unroll
            for (int i = 0; i < 4; ++i) asm volatile("" : "+v"(qr[i]));
            u32x2 zc[8];
#pragma unroll
            for (int c = 0; c < 8; ++c)
                zc[c] = *(const LAS u32x2*)(tr + cq * 512 + (c >> 2) * 256 + ((((c & 3) * 4 + (rg >> 1)) ^ (cq * 2)) << 4) + (rg & 1) * 8);
#pragma unroll
            for (int i = 0; i < 4; ++i) {
                u32x4 o;
#pragma unroll
                for (int c = 0; c < 4; ++c) {
                    const unsigned z0 = zc[2 * c][i >> 1], z1 = zc[2 * c + 1][i >> 1];
                    o[c] = cvtpk(mul_vv(bflo(qr[i][c]), (i & 1) ? bfhi(z0) : bflo(z0)), mul_vv(bfhi(qr[i][c]), (i & 1) ? bfhi(z1) : bflo(z1)));
                }
                __builtin_amdgcn_raw_buffer_store_b128(o, ro, arow * 4096 + acol, i * 4096, AUX_SC1);
            }
        }
        DG_END(14);
        return;
    }

    const int half = w >> 2;
    const int TA = qb * 8 + (w & 3) * 2 + half, TB = TA ^ 1;
    bf16x8 sel;
    {
        const short one = (((lane >> 2) & 1) == ((lane >> 4) & 1)) ? (short)0x3f80 : (short)0;
#pragma unroll
        for (int i = 0; i < 8; ++i) sel[i] = one;
    }
    f32x16 oA = f32x16{}, oB = f32x16{};
    f32x4 lA = f32x4{}, lB = f32x4{};
    f32x16 negm, sA, sB;
    lds_ptr lp = lds + half * 16384 + lane * 16;
    bf16x8 k0[2], k1[2], v0[2], v1[2];
    u32x4 p0, p1 = {0u, 0u, 0u, 0u};
#define EX(X, i) X[i] = __builtin_amdgcn_exp2f(X[i])
    lds_ptr qpa = lds + K2_SF + (TA & 7) * 2048 + lane * 16, qpb = lds + K2_SF + (TB & 7) * 2048 + lane * 16;
    asm volatile("" : "+v"(qpa), "+v"(qpb), "+v"(lp), "+v"(sel), "+v"(oA), "+v"(oB), "+v"(lA), "+v"(lB), "+v"(p1));
    WAIT_BAR(0);
    DG(3);
    const bf16x8 qa0 = lds_rd128(qpa), qb0 = lds_rd128(qpb);
    const bf16x8 x0 = lds_rd128(lds + lane * 16), x1 = lds_rd128(lds + lane * 16 + 1024);
    const bf16x8 qa1 = lds_rd128(qpa + 1024), qb1 = lds_rd128(qpb + 1024);
    {
        k0[0] = lds_rd128(lp); k1[0] = lds_rd128(lp + 1024);
        v0[0] = lds_rd128(lp + 2048); v1[0] = lds_rd128(lp + 3072);
        sA = MFMA32(x0, qa0, f32x16{});
        sB = MFMA32(x0, qb0, f32x16{});
        sA = MFMA32(x1, qa1, sA);
        sB = MFMA32(x1, qb1, sB);
        float m = fmx(sA[0], sA[1]), m2 = fmx(sB[0], sB[1]);
#pragma unroll
        for (int i = 2; i < 16; i += 2) m = fmx(fmx(m, sA[i]), sA[i + 1]);
#pragma unroll
        for (int i = 2; i < 16; i += 2) m2 = fmx(fmx(m2, sB[i]), sB[i + 1]);
        m = fmx(m, m2);
        auto rr = __builtin_amdgcn_permlane32_swap(__float_as_uint(m), __float_as_uint(m), false, false);
        m = fmx(__uint_as_float(rr[0]), __uint_as_float(rr[1]));
#pragma unroll
        for (int i = 0; i < 16; ++i) negm[i] = -m;
        sA = MFMA32(k0[0], qa0, negm);
        sA = MFMA32(k1[0], qa1, sA);
        v1[1] = v1[0];
        SBAR();
        RAW_BAR();
        SBAR();
        EX(sA, 0); EX(sA, 1);
    }
    DG(10);
#define SUM16(l, p) asm volatile("v_mfma_f32_16x16x32_bf16 %0, %1, %2, %0" : "+v"(l) : "v"(sel), "v"(p))
#define HALF(C, oC, lC, N, qx, qy, ka, kb, oP, lP, vprev, vcur0, PRE) do { \
        oP = MFMA32(vprev, PB(p1), oP); SBAR(); \
        PRE; EX(C, 2); EX(C, 3); EX(C, 4); SBAR(); \
        N = MFMA32(ka, qx, negm); SBAR(); \
        EX(C, 5); EX(C, 6); EX(C, 7); SBAR(); \
        SUM16(lP, p1); SBAR(); \
        p0[0] = cvtpk(C[0], C[1]); p0[1] = cvtpk(C[2], C[3]); SBAR(); \
        N = MFMA32(kb, qy, N); SBAR(); \
        p0[2] = cvtpk(C[4], C[5]); p0[3] = cvtpk(C[6], C[7]); SBAR(); EX(C, 8); EX(C, 9); SBAR(); \
        oC = MFMA32(vcur0, PB(p0), oC); SBAR(); \
        EX(C, 10); EX(C, 11); EX(C, 12); SBAR(); \
        SUM16(lC, p0); SBAR(); \
        EX(C, 13); EX(C, 14); EX(C, 15); SBAR(); \
        p1[0] = cvtpk(C[8], C[9]); p1[1] = cvtpk(C[10], C[11]); p1[2] = cvtpk(C[12], C[13]); p1[3] = cvtpk(C[14], C[15]); SBAR(); \
        EX(N, 0); EX(N, 1); SBAR(); \
    } while (0)
#define RD4(Y, nx) do { v1[Y] = lds_rd128((nx) + 3072); v0[Y] = lds_rd128((nx) + 2048); \
        k1[Y] = lds_rd128((nx) + 1024); k0[Y] = lds_rd128(nx); } while (0)
#define KT(X, nx, PRE0) do { \
        HALF(sA, oA, lA, sB, qb0, qb1, k0[X], k1[X], oB, lB, v1[X ^ 1], v0[X], PRE0; RD4(X ^ 1, nx)); \
        HALF(sB, oB, lB, sA, qa0, qa1, k0[X ^ 1], k1[X ^ 1], oA, lA, v1[X], v0[X], (void)0); \
    } while (0)
    SBAR();
#pragma unroll 1
    for (int st = 0; st < 8; ++st) {
        const lds_ptr cur = lp + (st & 1) * K2_SLOT;
        const lds_ptr nxt = lp + ((st + 1) & 1) * K2_SLOT;
        PRIO(2);
        KT(0, cur + 4096, (void)0);
        PRIO(1);
        KT(1, cur + 8192, (void)0);
        PRIO(0);
        KT(0, cur + 12288, (void)0);
        PRIO(3);
        KT(1, nxt, RAW_BAR(); SBAR());
    }
    oB = MFMA32(v1[1], PB(p1), oB);
    SBAR();
    SUM16(lB, p1);
    asm volatile("s_nop 7" : "+v"(oB) :: "memory");
    SBAR();
    DG(4);
    {
        const lds_ptr xo = lds + K2_SLOT + w * 4096 + lane * 16;
        *(LAS float*)(lds + K2_LX + w * 256 + lane * 4) = lB[0];
#pragma unroll
        for (int i = 0; i < 4; ++i) *(LAS f32x4*)(xo + i * 1024) = f32x4{oB[4 * i], oB[4 * i + 1], oB[4 * i + 2], oB[4 * i + 3]};
        lds_add(fl + F_X + w * 4, lane);
        lds_add(fl + C_END + half * 4, lane);
        lds_wait_ge(fl + F_X + (w ^ 4) * 4, 1u);
        const lds_ptr xi = lds + K2_SLOT + (w ^ 4) * 4096 + lane * 16;
        f32x4 t[4];
        const float lx = *(const LAS float*)(lds + K2_LX + (w ^ 4) * 256 + lane * 4);
#pragma unroll
        for (int i = 0; i < 4; ++i) t[i] = *(const LAS f32x4*)(xi + i * 1024);
        SBAR();
        lA[0] += lx;
#pragma unroll
        for (int i = 0; i < 16; ++i) { float o = oA[i]; acc_vv(o, t[i >> 2][i & 3]); oA[i] = o; }
    }
    DG(9);
    const float l = lA[0];
    u32x4 on0, on1;
    auto finish = [&](float ls) {
        float rl = __builtin_amdgcn_rcpf(ls);
        asm volatile("s_nop 1" : "+v"(rl));
        f32x16 o;
#pragma unroll
        for (int i = 0; i < 16; ++i) o[i] = mul_vv(oA[i], rl);
        on0 = nz4(pack8n(o, 0)); on1 = nz4(pack8n(o, 1));
    };
    finish(l);
    {
        float t0, t1;
        asm("v_mul_f32_e32 %0, 0, %2\n\tv_mul_f32_e32 %1, 0, %3\n\tv_fmac_f32_e32 %0, 0, %4\n\tv_fmac_f32_e32 %1, 0, %5\n\tv_fmac_f32_e32 %0, 0, %6\n\tv_fmac_f32_e32 %1, 0, %7\n\tv_fmac_f32_e32 %0, 0, %8\n\tv_fmac_f32_e32 %1, 0, %9\n\t"
            "v_fmac_f32_e32 %0, 0, %10\n\tv_fmac_f32_e32 %1, 0, %11\n\tv_fmac_f32_e32 %0, 0, %12\n\tv_fmac_f32_e32 %1, 0, %13\n\tv_fmac_f32_e32 %0, 0, %14\n\tv_fmac_f32_e32 %1, 0, %15\n\tv_fmac_f32_e32 %0, 0, %16\n\tv_fmac_f32_e32 %1, 0, %17"
            : "=&v"(t0), "=&v"(t1) : "v"(oA[0]), "v"(oA[1]), "v"(oA[2]), "v"(oA[3]), "v"(oA[4]), "v"(oA[5]), "v"(oA[6]), "v"(oA[7]), "v"(oA[8]), "v"(oA[9]), "v"(oA[10]), "v"(oA[11]), "v"(oA[12]), "v"(oA[13]), "v"(oA[14]), "v"(oA[15]));
        const bool ok = (l <= 1.0e30f) && (l >= 1.0e-18f) && (t0 + t1 == 0.f);
        if (__builtin_expect(__any(!ok), 0)) {
            float m = -INFINITY, ls = 0.f;
            oA = f32x16{};
#pragma unroll 1
            for (int t = 0; t < 64; ++t) {
                const char* g = kvg + t * 4096 + lane * 16;
                const bf16x8 a0 = *(const bf16x8*)g, a1 = *(const bf16x8*)(g + 1024);
                const bf16x8 b0 = *(const bf16x8*)(g + 2048), b1 = *(const bf16x8*)(g + 3072);
                f32x16 s = MFMA32(a0, qa0, f32x16{});
                s = MFMA32(a1, qa1, s);
                float tm = s[0];
#pragma unroll
                for (int i = 1; i < 16; ++i) tm = __builtin_fmaxf(tm, s[i]);
                auto rr = __builtin_amdgcn_permlane32_swap(__float_as_uint(tm), __float_as_uint(tm), false, false);
                tm = __builtin_fmaxf(__uint_as_float(rr[0]), __uint_as_float(rr[1]));
                const float mn = __builtin_fmaxf(m, tm);
                const float alpha = __builtin_amdgcn_exp2f(m - mn);
                m = mn;
                float ps = 0.f;
#pragma unroll
                for (int i = 0; i < 16; ++i) { s[i] = __builtin_amdgcn_exp2f(s[i] - mn); ps += s[i]; oA[i] *= alpha; }
                ls = ls * alpha + ps;
                oA = MFMA32(b0, PB(pack8n(s, 0)), oA);
                oA = MFMA32(b1, PB(pack8n(s, 1)), oA);
            }
            auto rr = __builtin_amdgcn_permlane32_swap(__float_as_uint(ls), __float_as_uint(ls), false, false);
            asm volatile("s_nop 7\n\ts_nop 7" : "+v"(oA));
            finish(__uint_as_float(rr[0]) + __uint_as_float(rr[1]));
        }
    }
    u32x4 w3[4];
    {
        const unsigned og = (unsigned)WS_O + (unsigned)(TA * 32 + h) * 2048u + lane * 16;
        st_sc1(on0, rs, og);
        st_sc1(on1, rs, og + 1024);
        lds_add(fl + C_S1, lane);
        const char* s2 = wsrc(half ^ 1, 32 + (w & 3) * 4);
        const char* s3 = wsrc(half, 48 + (w & 3) * 4);
        lds_wait_ge(fl + C_S1, 12u);
        DG(5);
#pragma unroll
        for (int i = 0; i < 4; ++i)
            glds16(wvoff, s2 + i * 16384, lds0 + K2_SLOT + ((w ^ 4) * 4 + i) * 1024);
#pragma unroll
        for (int i = 0; i < 4; ++i) w3[i] = gld128(wvoff, s3 + i * 16384);
    }
    {
        const int half = w & 1;
        const int T3 = tt * 4 + (w >> 1);
        const unsigned og = (unsigned)WS_O + (unsigned)T3 * 65536u + half * 32768 + lane * 16;
        const int r = lane & 31, hh = lane >> 5;
        const int g = lane >> 4, q = (lane & 15) >> 2, p = lane & 3;
        int woff[2];
#pragma unroll
        for (int n = 0; n < 2; ++n) {
            const int c = 4 * (n ^ half) + 2 * (p & 1) + (g & 1);
            woff[n] = (4 * hh + q) * 128 + ((c ^ ((q >> 1) << 2)) << 4) + (p >> 1) * 8;
        }
        f32x16 acc[2] = {f32x16{}, f32x16{}};
        lds_ptr wl[3][2] = {{lds + half * 16384 + woff[0], lds + half * 16384 + woff[1]},
                            {lds + K2_SF + half * 16384 + woff[0], lds + K2_SF + half * 16384 + woff[1]},
                            {lds + K2_CV + half * 2 * CV_BYTES + woff[0], lds + K2_CV + half * 2 * CV_BYTES + woff[1]}};
        asm volatile("" : "+v"(wl[0][0]), "+v"(wl[0][1]), "+v"(wl[1][0]), "+v"(wl[1][1]), "+v"(wl[2][0]), "+v"(wl[2][1]));
#define MS(m) ((m) < 64 - 2 * TL ? (m) >> 1 : 32 - TL + ((m) - (64 - 2 * TL)) % TL)
#define MN(m) ((m) < 64 - 2 * TL ? (m) & 1 : (m) < 64 - TL)
        constexpr int WR = 2 * WD + 2;
        s16x4 wa[WR], wb[WR];
#define LDW(m) do { const int s_ = MS(m), f_ = s_ < 8 ? 1 : s_ < 24 ? 0 : 2; \
            const int o_ = f_ == 2 ? ((s_ >> 2) & 1) * CV_BYTES + (s_ & 3) * 2048 : (f_ ? 0 : ((s_ >> 3) - 1) * K2_SLOT) + (s_ & 7) * 2048; \
            wa[(m) % WR] = lds_tr(wl[f_][MN(m)] + o_); wb[(m) % WR] = lds_tr(wl[f_][MN(m)] + o_ + 1024); SBAR(); } while (0)
#pragma unroll
        for (int m = 0; m < 2 * WD; ++m) LDW(m);
        u32x4 of[OWIN];
#define LDO(i) do { of[(i) % OWIN] = ld_sc1s(rs, og, (((i) + krot) & 31) * 1024); SBAR(); } while (0)
        for (unsigned spins = 0; spins < (1u << 20); ++spins) {
#pragma unroll
            for (int i = 0; i < OW0; ++i) LDO(i);
            unsigned m = mx8(of[0], of[1]);
#pragma unroll
            for (int i = 2; i < OW0; ++i) m = mx4(m, of[i]);
            if (!missing(m)) break;
            DGC(12);
        }
        lds_add(fl + C_S2, lane);
#pragma unroll
        for (int i = OW0; i < OWIN; ++i) LDO(i);
        DG(6);
        __builtin_amdgcn_s_setprio(P3_PRIO);
        SBAR();
        unsigned fv = 0;
#define FLAG_RD(a) do { fv = *(volatile LAS unsigned*)(a); SBAR(); } while (0)
#define FLAG_CHK(a, n) do { if (__builtin_expect((unsigned)__builtin_amdgcn_readfirstlane(fv) < (n), 0)) lds_wait_ge(a, n); CFENCE(); SBAR(); } while (0)
        lds_ptr xo = lds + K2_SF + w * 4096 + lane * 16, xi = lds + K2_SF + (w ^ 1) * 4096 + lane * 16;
        int bo = (T3 * 32 + (lane & 15)) * 4096 + 2048 + nt * 128 + half * 64 + g * 16;
        lds_ptr w3d = lds + K2_CV + (w >> 1) * CV_BYTES + (w & 1) * 4096 + lane * 16;
        asm volatile("" : "+v"(xo), "+v"(xi), "+v"(bo), "+v"(w3d));
        f32x4 xt[4];
        constexpr int M3 = TL > 8 ? 64 - 2 * TL + TL - 8 - 2 * WD : 48 - 2 * WD;
        sfor(std::make_integer_sequence<int, 64>{}, [&](auto mc) __attribute__((always_inline)) {
            constexpr int m = decltype(mc)::value, s = MS(m), n = MN(m);
            constexpr bool first = m < 64 - 2 * TL ? n == 0 : n == 1;
            constexpr bool last = m < 64 - 2 * TL ? n == 1 : n == 0;
            acc[n] = MFMA32(cat8(wa[m % WR], wb[m % WR]), PB(of[s % OWIN]), acc[n]);
            SBAR();
            if constexpr (m == 32 - 2 * WD) FLAG_CHK(fl + C_S2, 8u);
            if constexpr (m == M3) FLAG_CHK(fl + C_S3, 8u);
            if constexpr (m + 2 * WD < 64) LDW(m + 2 * WD);
            if constexpr (first && (s & 1) && s + 1 >= OW0 && s + 1 < 32) {
                if (__builtin_expect(missing(mx8(of[(s + 1) % OWIN], of[(s + 2) % OWIN])), 0)) {
#pragma unroll 1
                    for (unsigned spins = 0; spins < (1u << 20); ++spins) {
                        __builtin_amdgcn_s_sleep(2);
                        LDO(s + 1); LDO(s + 2);
                        DGC(13);
                        if (!missing(mx8(of[(s + 1) % OWIN], of[(s + 2) % OWIN]))) break;
                    }
                }
            }
            if constexpr (last && s + OWIN < 32) LDO(s + OWIN);
            if constexpr (m == 15 - 2 * WD) lds_add(fl + C_U0, lane);
            if constexpr (m == 29 - 2 * WD) FLAG_RD(fl + C_S2);
            if constexpr (m == W3M - 3) FLAG_RD(fl + C_HF);
            if constexpr (m == W3M) FLAG_CHK(fl + C_HF, 4u);
            if constexpr (m >= W3M && m < W3M + 8 && ((m - W3M) & 1) == 0) {
                asm volatile("" : "+v"(w3[((m - W3M) >> 1) & 3]));
                *(LAS u32x4*)(w3d + (((m - W3M) >> 1) & 3) * 1024) = w3[((m - W3M) >> 1) & 3];
            }
            if constexpr (m == W3M + 6) lds_add(fl + C_S3, lane);
            if constexpr (m == M3 - 3) FLAG_RD(fl + C_S3);
            if constexpr (m == 64 - TL - 3) FLAG_RD(fl + C_U0);
            if constexpr (m == 64 - TL) FLAG_CHK(fl + C_U0, 8u);
            if constexpr (m == 64 - TL + 1 || m == 64 - TL + 2) {
                constexpr int i = 2 * (m - (64 - TL + 1));
                *(LAS f32x4*)(xo + i * 1024) = f32x4{acc[1][4 * i], acc[1][4 * i + 1], acc[1][4 * i + 2], acc[1][4 * i + 3]};
                *(LAS f32x4*)(xo + i * 1024 + 1024) = f32x4{acc[1][4 * i + 4], acc[1][4 * i + 5], acc[1][4 * i + 6], acc[1][4 * i + 7]};
            }
            if constexpr (m == 64 - TL + 2) lds_add(fl + F_R + w * 4, lane);
            if constexpr (m == XR_AT - 2) FLAG_RD(fl + F_R + (w ^ 1) * 4);
            if constexpr (m == XR_AT) {
                FLAG_CHK(fl + F_R + (w ^ 1) * 4, 1u);
#pragma unroll
                for (int i = 0; i < 4; ++i) xt[i] = *(const LAS f32x4*)(xi + i * 1024);
            }
            if constexpr (ZL_GATE && last && s + OWIN == ZL_AT) lds_add(fl + C_OL, lane);
            if constexpr (m == 2 * P3_T1 + 1) __builtin_amdgcn_s_setprio(P3_PRIO - 1);
            SBAR();
            if constexpr (m == 31) DG(11);
            if constexpr (m == 47) DG(14);
        });
        asm volatile("s_nop 11" : "+v"(acc[0]));
        DG(7);
#pragma unroll
        for (int i = 0; i < 16; ++i) { float o = xt[i >> 2][i & 3]; acc_vv(o, acc[0][i]); acc[1][i] = o; }
        u32x4 pa = pack8n(acc[1], 0), pb = pack8n(acc[1], 1);
#pragma unroll
        for (int i = 0; i < 4; ++i) {
            auto sw = __builtin_amdgcn_permlane16_swap(pa[i], pb[i], false, false);
            pa[i] = sw[0]; pb[i] = sw[1];
        }
        asm volatile("" :: "v"(acc[0]));
        __builtin_amdgcn_raw_buffer_store_b128(pa, ro, bo, 0, AUX_SC1);
        __builtin_amdgcn_raw_buffer_store_b128(pb, ro, bo, 65536, AUX_SC1);
    }
    DG_END(8);
}

extern "C" void kernel_launch(void* const* d_in, const int* in_sizes, int n_in,
                              void* d_out, int out_size, void* d_ws, size_t ws_size,
                              hipStream_t stream) {
    const void* A_q = d_in[0];
    const void* A_k = d_in[1];
    const void* A_v = d_in[2];
    const void* A_shift = d_in[3];
    const void* A_ssmk = d_in[4];
    const void* A_D = d_in[5];
    const void* B_x = d_in[6];
    const void* B_wqkv = d_in[7];
    const void* B_wo = d_in[8];
    char* ws = (char*)d_ws;
    k1_qkv<<<256, 512, 0, stream>>>((const char*)B_x, (const char*)B_wqkv, ws);
    k2_attn<<<256, 768, 0, stream>>>(ws, (const char*)A_q, (const char*)A_k, (const char*)A_v, (const char*)A_shift,
                                     (const char*)A_ssmk, (const unsigned*)A_D, (const char*)B_wo, (char*)d_out);
}
```

```cpp
#include <hip/hip_runtime.h>
#include <hip/hip_bf16.h>
#include <stdint.h>
#include <utility>

typedef short bf16x8 __attribute__((ext_vector_type(8)));
typedef short s16x4 __attribute__((ext_vector_type(4)));
typedef float f32x16 __attribute__((ext_vector_type(16)));
typedef float f32x4 __attribute__((ext_vector_type(4)));
typedef float f32x2 __attribute__((ext_vector_type(2)));
typedef unsigned u32x4 __attribute__((ext_vector_type(4)));
typedef unsigned u32x2 __attribute__((ext_vector_type(2)));
typedef __bf16 bf16x2_t __attribute__((ext_vector_type(2)));

#define LAS __attribute__((address_space(3)))
typedef LAS char* lds_ptr;
#define DI __device__ __forceinline__
#define SBAR() __builtin_amdgcn_sched_barrier(0)
#define MFMA32(a, b, c) __builtin_amdgcn_mfma_f32_32x32x16_bf16((a), (b), (c), 0, 0, 0)
#define MFMA16(a, b, c) __builtin_amdgcn_mfma_f32_16x16x32_bf16((a), (b), (c), 0, 0, 0)
#define WAIT_BAR(N) asm volatile("s_waitcnt vmcnt(" #N ") lgkmcnt(0)\n\ts_barrier" ::: "memory")
#define LGKM_BAR() asm volatile("s_waitcnt lgkmcnt(0)\n\ts_barrier" ::: "memory")

#define DG_DECL(K)
#define DG(i)
#define DG_END(i)
#define DGC(i)

constexpr int S_LEN = 2048, DM = 1024, NH = 32;

constexpr size_t WS_Q = 0;
constexpr size_t WS_KV = 4u << 20;
constexpr size_t WS_O = 12u << 20;
constexpr size_t WS_KVR = 16u << 20;
constexpr size_t WS_ZT = 20u << 20;
constexpr size_t WS_CNT = 24u << 20;

template <int... I, class F> DI void sfor(std::integer_sequence<int, I...>, F&& f) { (f(std::integral_constant<int, I>{}), ...); }
DI unsigned cvtpk(float lo, float hi) {
    f32x2 v = {lo, hi};
    return __builtin_bit_cast(unsigned, __builtin_convertvector(v, bf16x2_t));
}
DI float bflo(unsigned u) { return __uint_as_float(u << 16); }
DI float bfhi(unsigned u) { return __uint_as_float(u & 0xffff0000u); }

DI void glds16(unsigned voff, const void* sbase, unsigned lds_dst) {
    unsigned sv;
    asm volatile("s_mov_b32 %0, m0\n\ts_mov_b32 m0, %3\n\ts_nop 0\n\tglobal_load_lds_dwordx4 %1, %2\n\ts_mov_b32 m0, %0"
                 : "=&s"(sv) : "v"(voff), "s"(sbase), "s"(lds_dst) : "memory");
}
DI bf16x8 lds_rd128(lds_ptr p) { return *(const LAS bf16x8*)p; }
DI s16x4 lds_tr(lds_ptr p) {
    return __builtin_bit_cast(s16x4, __builtin_amdgcn_ds_read_tr16_b64_v4i16((LAS s16x4*)p));
}
DI bf16x8 cat8(s16x4 a, s16x4 b) { return __builtin_shufflevector(a, b, 0, 1, 2, 3, 4, 5, 6, 7); }
DI u32x4 pack8(const f32x16& x, int s, float sc) {
    u32x4 p;
    p[0] = cvtpk(x[8 * s + 0] * sc, x[8 * s + 1] * sc);
    p[1] = cvtpk(x[8 * s + 2] * sc, x[8 * s + 3] * sc);
    p[2] = cvtpk(x[8 * s + 4] * sc, x[8 * s + 5] * sc);
    p[3] = cvtpk(x[8 * s + 6] * sc, x[8 * s + 7] * sc);
    return p;
}
DI u32x4 pack8n(const f32x16& x, int s) {
    u32x4 p;
    p[0] = cvtpk(x[8 * s + 0], x[8 * s + 1]);
    p[1] = cvtpk(x[8 * s + 2], x[8 * s + 3]);
    p[2] = cvtpk(x[8 * s + 4], x[8 * s + 5]);
    p[3] = cvtpk(x[8 * s + 6], x[8 * s + 7]);
    return p;
}
DI void rnd2(float& a, float& b) { unsigned u = cvtpk(a, b); a = bflo(u); b = bfhi(u); }

#define CONV_PACE()
#define RAW_BAR() asm volatile("s_barrier" ::: "memory")
DI unsigned gld32(unsigned voff, const void* sbase) {
    unsigned r;
    asm volatile("global_load_dword %0, %1, %2" : "=v"(r) : "v"(voff), "s"(sbase) : "memory");
    return r;
}
DI u32x2 gld64(unsigned voff, const void* sbase) {
    u32x2 r;
    asm volatile("global_load_dwordx2 %0, %1, %2" : "=v"(r) : "v"(voff), "s"(sbase) : "memory");
    return r;
}
DI float mul_sv(float s, float v) { float r; asm("v_mul_f32_e32 %0, %1, %2" : "=v"(r) : "s"(s), "v"(v)); return r; }
DI float mul_vv(float a, float b) { float r; asm("v_mul_f32_e32 %0, %1, %2" : "=v"(r) : "v"(a), "v"(b)); return r; }
DI float add_vv(float a, float b) { float r; asm("v_add_f32_e32 %0, %1, %2" : "=v"(r) : "v"(a), "v"(b)); return r; }
DI void acc_vv(float& a, float b) { asm("v_add_f32_e32 %0, %0, %1" : "+v"(a) : "v"(b)); }
DI void fmac_vv(float& acc, float a, float b) { asm("v_fmac_f32_e32 %0, %1, %2" : "+v"(acc) : "v"(a), "v"(b)); }
DI void fmac_sv(float& acc, float s, float v) { asm("v_fmac_f32_e32 %0, %1, %2" : "+v"(acc) : "s"(s), "v"(v)); }
typedef __attribute__((address_space(1))) unsigned gu32;
#define RLX_AGENT __ATOMIC_RELAXED, __HIP_MEMORY_SCOPE_AGENT
#define CFENCE() asm volatile("" ::: "memory")
#define DRAIN() asm volatile("s_waitcnt vmcnt(0)" ::: "memory")
constexpr int AUX_SC1 = 16;
DI u32x4 ld_sc1(__amdgpu_buffer_rsrc_t rs, unsigned off) { return __builtin_amdgcn_raw_buffer_load_b128(rs, (int)off, 0, AUX_SC1); }
DI u32x4 ld_sc1s(__amdgpu_buffer_rsrc_t rs, unsigned off, int soff) { return __builtin_amdgcn_raw_buffer_load_b128(rs, (int)off, soff, AUX_SC1); }
DI void st_sc1(u32x4 v, __amdgpu_buffer_rsrc_t rs, unsigned off) { __builtin_amdgcn_raw_buffer_store_b128(v, rs, (int)off, 0, AUX_SC1); }
constexpr unsigned EMPTY = 0xffffffffu;
DI unsigned nz(unsigned x) { return x < EMPTY - 1u ? x : EMPTY - 1u; }
DI u32x4 nz4(u32x4 v) { return u32x4{nz(v[0]), nz(v[1]), nz(v[2]), nz(v[3])}; }
DI float fmx(float a, float b) { return __builtin_elementwise_maximum(a, b); }
DI unsigned umax(unsigned a, unsigned b) { return a > b ? a : b; }
DI unsigned umax3(unsigned a, unsigned b, unsigned c) { return umax(umax(a, b), c); }
DI unsigned mx4(unsigned m, u32x4 v) { return umax3(umax3(m, v[0], v[1]), v[2], v[3]); }
DI unsigned mx8(u32x4 a, u32x4 b) { return umax(umax3(umax3(umax3(a[0], a[1], a[2]), a[3], b[0]), b[1], b[2]), b[3]); }
DI bool missing(unsigned m) { return __builtin_amdgcn_ballot_w64(m == EMPTY) != 0; }
DI unsigned lds_add_rtn(lds_ptr p, int lane) {
    unsigned old = 0;
    CFENCE();
    if (lane == 0) old = __hip_atomic_fetch_add((LAS unsigned*)p, 1u, __ATOMIC_RELAXED, __HIP_MEMORY_SCOPE_WORKGROUP);
    old = __builtin_amdgcn_readfirstlane(old);
    CFENCE();
    return old;
}
DI void lds_add(lds_ptr p, int lane) {
    unsigned long long sv;
    asm volatile("s_mov_b64 %0, exec\n\ts_mov_b64 exec, 1\n\tds_add_u32 %1, %2\n\ts_mov_b64 exec, %0"
                 : "=&s"(sv) : "v"(p), "v"(1u) : "memory");
}
DI void lds_wait_ge(lds_ptr p, unsigned v) {
    CFENCE();
    while ((unsigned)__builtin_amdgcn_readfirstlane(*(volatile LAS unsigned*)p) < v) __builtin_amdgcn_s_sleep(1);
    CFENCE();
}
DI void poll_ge(gu32* word, unsigned want) {
    CFENCE();
    for (unsigned spins = 0; spins < (1u << 22); ++spins) {
        if ((unsigned)__builtin_amdgcn_readfirstlane(__hip_atomic_load(word, RLX_AGENT)) >= want) break;
        __builtin_amdgcn_s_sleep(1);
    }
    __builtin_amdgcn_fence(__ATOMIC_ACQUIRE, "wavefront");
    CFENCE();
}
#define PB(x) __builtin_bit_cast(bf16x8, x)
#define PRIO(x) do { __builtin_amdgcn_s_setprio(x); SBAR(); } while (0)
DI u32x4 gld128(unsigned voff, const void* sbase) {
    u32x4 r;
    asm volatile("global_load_dwordx4 %0, %1, %2" : "=v"(r) : "v"(voff), "s"(sbase) : "memory");
    return r;
}

#define K1_UNR 15
#define K1_LA 5
#define K1_ROT ((tb & 3) * 4)
constexpr int K1_STAGE = 40960, K1U = K1_UNR;
__global__ __launch_bounds__(512, 2) void k1_qkv(const char* __restrict__ X, const char* __restrict__ W,
                                                 char* __restrict__ ws) {
    __shared__ __attribute__((aligned(16))) char lds_raw[4 * K1_STAGE];
    const lds_ptr lds = (lds_ptr)lds_raw;
    const unsigned lds0 = (unsigned)(uintptr_t)lds;
    const int tid = threadIdx.x, lane = tid & 63;
    const int w = __builtin_amdgcn_readfirstlane(tid >> 6);
    const int b = blockIdx.x, xcd = b & 7, idx = b >> 3;
    const int hp = (xcd >> 1) * 4 + (idx & 3), tb = (xcd & 1) * 8 + (idx >> 2);
    DG_DECL(0)

    const int lw = w & 3;
    const int rot = K1_ROT;
    unsigned voffX[4], voffW[2];
#pragma unroll
    for (int pp = 0; pp < 4; ++pp) {
        const int row = 32 * lw + 8 * pp + (lane >> 3);
        const int ch = (lane & 7) ^ ((row >> 1) & 7);
        voffX[pp] = (unsigned)(tb * 128 + row) * 2048u + ch * 16;
    }
#pragma unroll
    for (int pp = 0; pp < 2; ++pp) {
        const int krow = 16 * lw + 8 * pp + (lane >> 3);
        const int ch = (lane & 7) ^ (((lane >> 4) & 1) << 2);
        voffW[pp] = (unsigned)krow * 6144u + ch * 16;
    }
    auto issue = [&](int kt, int lo, int hi) {
        const int kr = (kt + rot) & 15;
        const char* xb = X + kr * 128;
        const char* wb = W + (size_t)kr * (64 * 6144) + hp * 128;
        const unsigned st = lds0 + (kt & 3) * K1_STAGE;
#pragma unroll
        for (int pp = 0; pp < 4; ++pp)
            if (pp >= lo && pp < hi) glds16(voffX[pp], xb, st + (4 * lw + pp) * 1024);
#pragma unroll
        for (int sec = 0; sec < 3; ++sec)
#pragma unroll
            for (int pp = 0; pp < 2; ++pp)
                if (4 + 2 * sec + pp >= lo && 4 + 2 * sec + pp < hi)
                    glds16(voffW[pp], wb + sec * 2048, st + 16384 + sec * 8192 + (2 * lw + pp) * 1024);
    };
    DG(13);
    if (w < 4) { issue(0, 0, 5); issue(1, 0, 5); }
    else { issue(0, 5, 10); issue(1, 5, 10); }
    DG(14);
    int w2 = w;
    asm volatile("" : "+s"(w2));

    if (w2 >= 4) {
#define WAITN_BAR(N) asm volatile("s_waitcnt vmcnt(%0) lgkmcnt(0)\n\ts_barrier" :: "n"(N) : "memory")
        issue(2, 0, K1_LA);
        asm volatile("s_waitcnt vmcnt(%0)" :: "n"(5 + K1_LA) : "memory");
#pragma unroll 1
        for (int kt = 0; kt < 13; ++kt) {
            WAITN_BAR(10 + K1_LA);
            issue(kt + 2, K1_LA, 10);
            issue(kt + 3, 0, K1_LA);
        }
        WAITN_BAR(10 + K1_LA);
        issue(15, K1_LA, 10);
        WAIT_BAR(10);
        WAIT_BAR(0);
        if (lw == 0 && lane == 0) *(unsigned*)(ws + WS_CNT + b * 256) = 0u;
        DG_END(5);
        return;
    }

    const int wm = w >> 1, wn = w & 1;
    const int r = lane & 31, hh = lane >> 5;
    const int g = lane >> 4, q = (lane & 15) >> 2, p = lane & 3;
    const int xoff0 = (64 * wm + r) * 128 + ((hh ^ ((r >> 1) & 7)) << 4);
    const int woff0 = 16384 + (8 * hh + q) * 128 + (((4 * wn + 2 * (g & 1) + (p >> 1)) ^ ((q >> 1) << 2)) << 4) + (p & 1) * 8;

    f32x16 acc[2][3];
#pragma unroll
    for (int m = 0; m < 2; ++m)
#pragma unroll
        for (int s = 0; s < 3; ++s) acc[m][s] = f32x16{};
    bf16x8 xf[2][2];
    s16x4 wl[2][3], wh[2][3];
#define K1_WF(f, sec) cat8(wl[f][sec], wh[f][sec])
#define K1_STEP(F, st, t, BAR_HERE) do { \
        const lds_ptr xp_ = (st) + (xoff0 ^ ((t) << 5)); const lds_ptr wp_ = (st) + woff0 + (t) * 2048; \
        acc[0][0] = MFMA32(K1_WF(F, 0), xf[F][0], acc[0][0]); SBAR(); \
        if (BAR_HERE) { WAIT_BAR(0); SBAR(); } \
        xf[F ^ 1][0] = lds_rd128(xp_); wl[F ^ 1][0] = lds_tr(wp_); wh[F ^ 1][0] = lds_tr(wp_ + 512); SBAR(); \
        acc[0][1] = MFMA32(K1_WF(F, 1), xf[F][0], acc[0][1]); SBAR(); \
        wl[F ^ 1][1] = lds_tr(wp_ + 8192); wh[F ^ 1][1] = lds_tr(wp_ + 8192 + 512); SBAR(); \
        acc[0][2] = MFMA32(xf[F][0], K1_WF(F, 2), acc[0][2]); SBAR(); \
        wl[F ^ 1][2] = lds_tr(wp_ + 16384); wh[F ^ 1][2] = lds_tr(wp_ + 16384 + 512); xf[F ^ 1][1] = lds_rd128(xp_ + 4096); SBAR(); \
        acc[1][0] = MFMA32(K1_WF(F, 0), xf[F][1], acc[1][0]); SBAR(); \
        acc[1][1] = MFMA32(K1_WF(F, 1), xf[F][1], acc[1][1]); \
        acc[1][2] = MFMA32(xf[F][1], K1_WF(F, 2), acc[1][2]); SBAR(); \
    } while (0)

    WAIT_BAR(5);
    DG(3);
    {
        const lds_ptr xp_ = lds + xoff0; const lds_ptr wp_ = lds + woff0;
        xf[0][0] = lds_rd128(xp_); xf[0][1] = lds_rd128(xp_ + 4096);
#pragma unroll
        for (int sec = 0; sec < 3; ++sec) { wl[0][sec] = lds_tr(wp_ + sec * 8192); wh[0][sec] = lds_tr(wp_ + sec * 8192 + 512); }
    }
    SBAR();
#pragma unroll K1U
    for (int kt = 0; kt < 15; ++kt) {
        const lds_ptr st = lds + (kt & 3) * K1_STAGE;
        const lds_ptr sn = lds + ((kt + 1) & 3) * K1_STAGE;
        K1_STEP(0, st, 1, false);
        K1_STEP(1, st, 2, false);
        K1_STEP(0, st, 3, false);
        K1_STEP(1, sn, 0, true);
    }
    {
        const lds_ptr st = lds + 3 * K1_STAGE;
        K1_STEP(0, st, 1, false);
        K1_STEP(1, st, 2, false);
        K1_STEP(0, st, 3, false);
#pragma unroll
        for (int m = 0; m < 2; ++m) {
            acc[m][1] = MFMA32(K1_WF(1, 1), xf[1][m], acc[m][1]);
            acc[m][2] = MFMA32(xf[1][m], K1_WF(1, 2), acc[m][2]);
            acc[m][0] = MFMA32(K1_WF(1, 0), xf[1][m], acc[m][0]);
        }
    }
    DG(4);
    {
        const int h = 2 * hp + wn;
        const float qscale = 0.17677669529663687f * 1.4426950408889634f;
        const __amdgpu_buffer_rsrc_t rs = __builtin_amdgcn_make_buffer_rsrc(ws, 0, 32 << 20, 0x00020000);
#pragma unroll
        for (int m = 0; m < 2; ++m) {
            const int T = tb * 4 + wm * 2 + m;
            const unsigned dq = (unsigned)WS_Q + (h * 64 + T) * 2048 + lane * 16;
            const unsigned dk = (unsigned)WS_KV + (h * 64 + T) * 4096 + lane * 16;
            st_sc1(pack8n(acc[m][1], 0), rs, dk);
            st_sc1(pack8n(acc[m][1], 1), rs, dk + 1024);
            st_sc1(pack8n(acc[m][2], 0), rs, dk + 2048);
            st_sc1(pack8n(acc[m][2], 1), rs, dk + 3072);
            st_sc1(pack8(acc[m][0], 0, qscale), rs, dq);
            st_sc1(pack8(acc[m][0], 1, qscale), rs, dq + 1024);
        }
    }
    DG_END(5);
}

constexpr int K2_SLOT = 32768;
constexpr int K2_SF = 65536;
constexpr int K2_CV = 98304;
constexpr int CV_HA = 0, CV_HB = 4160, CV_KV = 8320, CV_BYTES = 8320 + 480 + 4096;
constexpr int K2_LX = K2_CV + 4 * CV_BYTES;
constexpr int K2_FL = K2_LX + 2048;
constexpr int K2_SCR = K2_FL + 256;
constexpr int K2_LDS = K2_SCR + 4096;
constexpr int F_X = 0, F_R = 32, F_W2 = 64, C_OST = 68, C_S1 = 76, C_S2 = 80, C_S3 = 128, C_ZST = 88, F_ZGO = 92,
              C_KST = 96, C_U0 = 100, C_LD = 108, F_KGO = 116, C_END = 120, C_OL = 72, C_HF = 84;
#define NG_IN 0
#define GATE_WORD C_END
#define GATE_VAL 0
#define X_NZ 4
#define X_NW3 8
#define ZP_GATE 4u
#define ZL_GATE 4
#define ZL_AT 31
#define Z_SLEEP0 0
#define O_SLEEP 0
#define Z_SLEEP 8
#define OWIN 16
#define OW0 8
#define WD 2
#define W3_AT 10
#define X_NOLDW 0
#define X_NOLDO 0
#define X_NOCHK 0
#define KROT (nt * 2)
#define NG_LAST 0
#define PACE_IN "s_nop 7"
#define PACE_OUT "s_nop 1"
#define P3_PRIO 1
#define TOP_SLEEP 2
#define A_AUX 16
#define B_AUX 16
#define B_LDS 0
#define TL 8
#define XR_AT 62
#define W3_AUX 0
#define W3M 13
#define A16 0
#define ZE_PRIO 3
#define P3_T1 99
#define P3_T2 99
#define AUX_PRIO 2

__global__ __launch_bounds__(768, 3) void k2_attn(char* __restrict__ ws, const char* __restrict__ Aq,
                                                  const char* __restrict__ Ak, const char* __restrict__ Av,
                                                  const char* __restrict__ shiftk, const char* __restrict__ ssmk,
                                                  const unsigned* __restrict__ ssmD, const char* __restrict__ Wo,
                                                  char* __restrict__ out) {
    __shared__ __attribute__((aligned(16))) char lds_raw[K2_LDS];
    const lds_ptr lds = (lds_ptr)lds_raw;
    const lds_ptr fl = lds + K2_FL;
    const unsigned lds0 = (unsigned)(uintptr_t)lds;
    const int tid = threadIdx.x, lane = tid & 63;
    const int w = __builtin_amdgcn_readfirstlane(tid >> 6);
    const int b = blockIdx.x, xcd = b & 7, idx = b >> 3;
    const int h = xcd * 4 + (idx >> 3), qb = idx & 7;
    const int tt = xcd * 2 + (idx >> 4), nt = idx & 15;
    const char* kvg = ws + WS_KV + (size_t)h * 64 * 4096;
    const __amdgpu_buffer_rsrc_t rs = __builtin_amdgcn_make_buffer_rsrc(ws, 0, 32 << 20, 0x00020000);
    const __amdgpu_buffer_rsrc_t ro = __builtin_amdgcn_make_buffer_rsrc(out, 0, 8 << 20, 0x00020000);
    gu32* const cnt = (gu32*)(ws + WS_CNT);
    auto signal = [&]() {
        (void)__hip_atomic_fetch_add(cnt + ((lane & 31) * 8 + (lane < 32 ? xcd : qb)) * 64, 1u, RLX_AGENT);
    };
    const unsigned wvoff = (unsigned)(lane >> 3) * 2048u + nt * 128 + (((lane & 7) ^ (((lane >> 4) & 1) << 2)) << 4);
    const int krot = KROT;
    auto wsrc = [&](int hf, int lp) { return Wo + (size_t)(hf * 64 + ((lp + krot * 2) & 63)) * 16384; };
    DG_DECL(1)
    const unsigned voff = lane * 16;
    const int cw = w & 3;
    auto issue = [&](int st) {
        const char* src = kvg + (st * 4 + cw) * 4096;
        const unsigned dst = lds0 + (st & 1) * K2_SLOT + cw * 4096;
#pragma unroll
        for (int i = 0; i < 4; ++i) glds16(voff, src + i * 1024, dst + i * 1024);
#pragma unroll
        for (int i = 0; i < 4; ++i) glds16(voff, src + 131072 + i * 1024, dst + 16384 + i * 1024);
    };
    if (w >= 8) {
        __builtin_amdgcn_s_sleep(TOP_SLEEP);
#pragma unroll
        for (int i = 2; i < 8; ++i)
            glds16(voff, kvg + (i & 1) * 131072 + (i >> 1) * 4096 + cw * 1024, lds0 + (i & 1) * 16384 + (i >> 1) * 4096 + cw * 1024);
        RAW_BAR();
        issue(1);
        WAIT_BAR(8);
    } else {
        const int tq = cw * 2 + (w >> 2);
        const char* qg = ws + WS_Q + (size_t)(h * 64 + qb * 8 + tq) * 2048;
        glds16(voff, kvg + (w >> 2) * 131072 + cw * 1024, lds0 + (w >> 2) * 16384 + cw * 1024);
        glds16(voff, qg, lds0 + K2_SF + tq * 2048);
        glds16(voff, qg + 1024, lds0 + K2_SF + tq * 2048 + 1024);
    }
    int w2 = w;
    asm volatile("" : "+s"(w2));

    if (w2 >= 8) {
        __builtin_amdgcn_s_setprio(3);
        const int ch = xcd * 128 + idx * 4 + cw;
        const lds_ptr cv = lds + K2_CV + cw * CV_BYTES;
        {
            const u32x4 z4 = {0u, 0u, 0u, 0u};
            if (lane < 2) *(LAS u32x4*)(cv + CV_HA + lane * 16) = z4;
            if (lane < 30) *(LAS u32x4*)(cv + CV_KV + lane * 16) = z4;
            if (cw == 0 && lane < 16) *(LAS u32x4*)(fl + lane * 16) = z4;
        }
        const int c0 = xcd * 128 + cw * 32, cq = lane & 7, rg = lane >> 3;
        const int row0 = idx * 64 + rg * 8;
        const unsigned roff = (unsigned)row0 * 2048u + (c0 + cq * 4) * 2;
        const unsigned toffs = (unsigned)(c0 + (lane >> 3)) * 4096u + idx * 128;
        u32x4 sk[2];
        sk[0] = gld128((unsigned)(c0 + cq * 4) * 8u, shiftk); sk[1] = gld128((unsigned)(c0 + cq * 4) * 8u, shiftk + 16);
        unsigned dd = gld32((unsigned)(ch >> 1) * 4u, ssmD);
        auto stage = [&](const char* src, int dst) {
            const unsigned sbase = (unsigned)(idx * 64 + cw * 16 + (lane >> 4)) * 2048u + xcd * 256;
#pragma unroll
            for (int i = 0; i < 4; ++i)
                glds16(sbase + (((lane & 15) ^ (((2 * cw + (i >> 1)) & 3) << 2)) << 4), src + i * 8192, lds0 + dst + cw * 4096 + i * 1024);
        };
        auto blank = [&](unsigned off) {
            const u32x4 e4 = {EMPTY, EMPTY, EMPTY, EMPTY};
            CFENCE();
            st_sc1(e4, rs, off + voff); st_sc1(e4, rs, off + 1024 + voff);
            CFENCE();
        };
        WAIT_BAR(3);
        DG(12);
        issue(2);
        stage(Ak, K2_SF);
        glds16((unsigned)(idx * 64 - (idx ? 4 : 0) + (lane >> 4)) * 2048u + xcd * 256 + (((lane & 15) ^ 12) << 4), Ak, lds0 + K2_SCR + cw * 1024);
        blank((unsigned)WS_O + (unsigned)((qb * 8 + cw * 2) * 32 + h) * 2048u);
        WAIT_BAR(7);
        DG(9);
        issue(3);
        stage(Av, K2_SF + 16384);
        blank((unsigned)WS_O + (unsigned)((qb * 8 + cw * 2 + 1) * 32 + h) * 2048u);
        WAIT_BAR(6);
        DG(3);
        issue(4);
#pragma unroll
        for (int i = 0; i < 4; ++i) glds16(voff, ssmk + (size_t)ch * 4096 + i * 1024, lds0 + K2_CV + cw * CV_BYTES + CV_HA + 32 + i * 1024);
        blank((unsigned)WS_ZT + ch * 4096); blank((unsigned)WS_ZT + ch * 4096 + 2048);
        WAIT_BAR(8);
        DG(5);
        issue(5);
        SBAR();
        asm volatile("" : "+v"(sk[0]), "+v"(sk[1]), "+v"(dd));
        dd = __builtin_amdgcn_readfirstlane(dd);
        {
            u32x2 kr[11], vr[8];
            const lds_ptr kb = lds + K2_SF + rg * 2048 + ((cw ^ (rg & 3)) << 6) + cq * 8;
            const lds_ptr hb = lds + (rg ? K2_SF + rg * 2048 - 768 : K2_SCR + cw * 1024 + 256) + ((cw ^ ((rg + 3) & 3)) << 6) + cq * 8;
#pragma unroll
            for (int i = 0; i < 3; ++i) kr[i] = *(const LAS u32x2*)(hb + i * 256);
#pragma unroll
            for (int i = 0; i < 8; ++i) kr[3 + i] = *(const LAS u32x2*)(kb + i * 256);
#pragma unroll
            for (int i = 0; i < 8; ++i) vr[i] = *(const LAS u32x2*)(kb + 16384 + i * 256);
            if (row0 == 0) { kr[0] = u32x2{0u, 0u}; kr[1] = u32x2{0u, 0u}; kr[2] = u32x2{0u, 0u}; }
            const lds_ptr tb = cv + CV_KV + 480;
#pragma unroll
            for (int c = 0; c < 4; ++c) {
                const unsigned s01 = sk[c >> 1][(c & 1) * 2], s23 = sk[c >> 1][(c & 1) * 2 + 1];
                const float w0 = bflo(s01), w1 = bfhi(s01), w2 = bflo(s23), w3 = bfhi(s23);
                float kf[11], kv[8];
#pragma unroll
                for (int i = 0; i < 11; ++i) kf[i] = (c & 1) ? bfhi(kr[i][c >> 1]) : bflo(kr[i][c >> 1]);
#pragma unroll
                for (int i = 0; i < 8; ++i) {
                    float a = mul_vv(w3, kf[i]);
                    fmac_vv(a, w2, kf[i + 1]); fmac_vv(a, w1, kf[i + 2]); fmac_vv(a, w0, kf[i + 3]);
                    kv[i] = mul_vv(a, (c & 1) ? bfhi(vr[i][c >> 1]) : bflo(vr[i][c >> 1]));
                }
                u32x4 o4;
#pragma unroll
                for (int i = 0; i < 4; ++i) o4[i] = cvtpk(kv[7 - 2 * i], kv[6 - 2 * i]);
                *(LAS u32x4*)(tb + (cq * 4 + c) * 128 + ((rg ^ cq) << 4)) = o4;
            }
#pragma unroll
            for (int i = 0; i < 4; ++i) {
                const u32x4 t = *(const LAS u32x4*)(tb + i * 1024 + lane * 16);
                st_sc1(t, rs, (unsigned)WS_KVR + toffs + i * 32768 + (((lane & 7) ^ (2 * i + (lane >> 5))) << 4));
            }
        }
        CFENCE();
        DG(7);
        WAIT_BAR(4);
        DG(6);
        issue(6);
        asm volatile("s_waitcnt vmcnt(8)" ::: "memory");
        if (lds_add_rtn(fl + C_KST, lane) == 3u) signal();
#pragma unroll
        for (int i = 0; i < 8; ++i)
            glds16(wvoff, wsrc(i >> 2, cw * 4 + (i & 3)), lds0 + K2_SF + (i >> 2) * 16384 + (cw * 4 + (i & 3)) * 1024);
        {
            const u32x4 z4 = {0u, 0u, 0u, 0u};
            if (lane < 2) *(LAS u32x4*)(cv + CV_HB + lane * 16) = z4;
#pragma unroll
            for (int i = 0; i < 4; ++i) {
                const int off = 32 + i * 1024 + lane * 16;
                const u32x4 hv = *(const LAS u32x4*)(cv + CV_HA + off);
                const unsigned pv = *(const LAS unsigned*)(cv + CV_HA + off - 4);
                u32x4 sh;
                sh[0] = __builtin_amdgcn_alignbit(hv[0], pv, 16);
                sh[1] = __builtin_amdgcn_alignbit(hv[1], hv[0], 16);
                sh[2] = __builtin_amdgcn_alignbit(hv[2], hv[1], 16);
                sh[3] = __builtin_amdgcn_alignbit(hv[3], hv[2], 16);
                *(LAS u32x4*)(cv + CV_HB + off) = sh;
            }
        }
        WAIT_BAR(8);
        DG(8);
        issue(7);
#pragma unroll
        for (int i = 0; i < 8; ++i)
            glds16(wvoff, wsrc(idx >> 4, 32 + cw * 8 + i), lds0 + K2_SCR + cw * 1024);
        WAIT_BAR(8);
        DG(11);
        if (cw == 0) { poll_ge(cnt + b * 64, 64u); lds_add(fl + F_KGO, lane); }
        else lds_wait_ge(fl + F_KGO, 1u);
        {
            u32x4 t[4];
#pragma unroll
            for (int i = 0; i < 4; ++i) t[i] = ld_sc1(rs, (unsigned)WS_KVR + ch * 4096 + i * 1024 + voff);
#pragma unroll
            for (int i = 0; i < 4; ++i) *(LAS u32x4*)(cv + CV_KV + 480 + i * 1024 + lane * 16) = t[i];
        }
        const int r1 = lane & 15, q = lane >> 4;
        const int toff = (r1 & 1) ? CV_HA + 2 * (16 + r1 + 9 - 8 * q) : CV_HB + 2 * (16 + r1 + 10 - 8 * q);
        const int boff = CV_KV + 448 + 32 * r1 + 16 * q;
        f32x4 y[8];
        u32x4 tf[8];
        bf16x8 bf[8];
        const lds_ptr bp = cv + boff - 448;
        lds_ptr tp[4];
#pragma unroll
        for (int i = 0; i < 4; ++i) { tp[i] = cv + toff + 1024 * i; asm volatile("" : "+v"(tp[i])); }
#define LDT(bb, a, hf) do { const LAS unsigned* t_ = (const LAS unsigned*)(tp[(a) >> 1] + 64 * (bb) + 512 * ((a) & 1) + 8 * (hf)); \
            tf[a][2 * (hf)] = t_[0]; tf[a][2 * (hf) + 1] = t_[1]; SBAR(); } while (0)
#define LDB(bb, a) do { bf[a] = lds_rd128(bp + 448 - 64 * (bb) + 512 * (a)); SBAR(); } while (0)
#define CM(a, ww) do { asm volatile("v_mfma_f32_16x16x32_bf16 %0, %1, %2, %0" : "+v"(y[(a) + (ww)]) : "v"(tf[a]), "v"(bf[ww])); SBAR(); } while (0)
#define CM0(a, ww) do { asm volatile("v_mfma_f32_16x16x32_bf16 %0, %1, %2, 0" : "=&v"(y[(a) + (ww)]) : "v"(tf[a]), "v"(bf[ww])); SBAR(); } while (0)
#define PC() do { asm volatile(PACE_OUT); SBAR(); } while (0)
#pragma unroll
        for (int a = 0; a < 8; ++a) { if (a < 5) { LDT(0, a, 0); LDT(0, a, 1); } LDB(0, 7 - a); }
#pragma unroll
        for (int i = 0; i < 8; ++i)
            glds16(wvoff, wsrc(i >> 2, 16 + cw * 4 + (i & 3)), lds0 + (i >> 2) * 16384 + (cw * 4 + (i & 3)) * 1024);
#pragma unroll
        for (int i = 0; i < 4; ++i)
            glds16((unsigned)(idx * 64 + cw * 16 + (lane >> 4)) * 2048u + xcd * 256 + (lane & 15) * 16, Aq + i * 8192, lds0 + K2_SCR + cw * 1024);
        RAW_BAR();
        asm volatile("s_waitcnt vmcnt(4)" ::: "memory");
        lds_add(fl + C_S1, lane);
        __builtin_amdgcn_s_setprio(AUX_PRIO);
        const float dch = (ch & 1) ? bfhi(dd) : bflo(dd);
        const lds_ptr kdp = cv + CV_KV + 480 + 32 * r1 + 16 * (q >> 1) + ((q & 1) ? 0 : 8);
        float kd[8][4];
        u32x2 kk;
        unsigned kc0, kc1, zo[8][2];
#define KD_RD(t) do { kk = *(const LAS u32x2*)(kdp + 512 * (t)); SBAR(); } while (0)
#define KD_UP0(t) do { kd[t][0] = bfhi(kk[1]); kd[t][1] = bflo(kk[1]); SBAR(); } while (0)
#define KD_UP1(t) do { kd[t][2] = bfhi(kk[0]); kd[t][3] = bflo(kk[0]); SBAR(); } while (0)
#define KD_MU0(t) do { kd[t][0] = mul_sv(dch, kd[t][0]); kd[t][1] = mul_sv(dch, kd[t][1]); SBAR(); } while (0)
#define KD_MU1(t) do { kd[t][2] = mul_sv(dch, kd[t][2]); kd[t][3] = mul_sv(dch, kd[t][3]); SBAR(); } while (0)
#define KD_CV(t) do { kc0 = cvtpk(kd[t][0], kd[t][1]); kc1 = cvtpk(kd[t][2], kd[t][3]); SBAR(); } while (0)
#define KD_X0(t) do { kd[t][0] = bflo(kc0); kd[t][1] = bfhi(kc0); SBAR(); } while (0)
#define KD_X1(t) do { kd[t][2] = bflo(kc1); kd[t][3] = bfhi(kc1); SBAR(); } while (0)
#define ZF0(t) do { kc0 = cvtpk(y[t][0], y[t][1]); kc1 = cvtpk(y[t][2], y[t][3]); SBAR(); } while (0)
#define ZF1(t) do { y[t][0] = bflo(kc0); y[t][1] = bfhi(kc0); SBAR(); } while (0)
#define ZF2(t) do { y[t][2] = bflo(kc1); y[t][3] = bfhi(kc1); SBAR(); } while (0)
#define ZF3(t) do { y[t][0] = add_vv(y[t][0], kd[t][0]); y[t][1] = add_vv(y[t][1], kd[t][1]); SBAR(); } while (0)
#define ZF4(t) do { y[t][2] = add_vv(y[t][2], kd[t][2]); y[t][3] = add_vv(y[t][3], kd[t][3]); SBAR(); } while (0)
#define ZF5(t) do { zo[t][0] = nz(cvtpk(y[t][0], y[t][1])); zo[t][1] = nz(cvtpk(y[t][2], y[t][3])); SBAR(); } while (0)
#define ZST(pr) do { auto s0 = __builtin_amdgcn_permlane16_swap(zo[2 * (pr)][0], zo[2 * (pr) + 1][0], false, false); \
            auto s1 = __builtin_amdgcn_permlane16_swap(zo[2 * (pr)][1], zo[2 * (pr) + 1][1], false, false); \
            st_sc1(u32x4{s0[0], s1[0], s0[1], s1[1]}, rs, (unsigned)WS_ZT + ch * 4096 + (pr) * 1024 + voff); SBAR(); } while (0)
#define W3(i) do { if ((i) == 0) PC(); } while (0)
#define HF() do { lds_add(fl + C_HF, lane); SBAR(); } while (0)
        CM0(0, 7); LDB(1, 7); CM0(0, 6); LDT(0, 5, 0); CM0(0, 5); LDT(0, 5, 1); CM0(0, 4); LDT(0, 6, 0); CM0(0, 3); LDT(0, 6, 1); CM0(0, 2); LDT(0, 7, 0); CM0(0, 1); LDT(0, 7, 1); CM0(0, 0); KD_RD(0);
        CM(1, 6); LDB(1, 6); CM(1, 5); LDT(1, 0, 0); CM(1, 4); LDT(1, 0, 1); CM(1, 3); PC(); CM(1, 2); PC(); CM(1, 1); KD_UP0(0); CM(1, 0); KD_UP1(0);
        CM(2, 5); LDB(1, 5); CM(2, 4); LDT(1, 1, 0); CM(2, 3); LDT(1, 1, 1); CM(2, 2); KD_MU0(0); CM(2, 1); KD_MU1(0); CM(2, 0); KD_CV(0);
        CM(3, 4); LDB(1, 4); CM(3, 3); LDT(1, 2, 0); CM(3, 2); LDT(1, 2, 1); CM(3, 1); KD_X0(0); CM(3, 0); KD_X1(0);
        CM(4, 3); LDB(1, 3); CM(4, 2); LDT(1, 3, 0); CM(4, 1); LDT(1, 3, 1); CM(4, 0); KD_RD(1);
        CM(5, 2); LDB(1, 2); CM(5, 1); LDT(1, 4, 0); CM(5, 0); LDT(1, 4, 1);
        CM(6, 1); LDB(1, 1); CM(6, 0); PC();
        CM(7, 0); LDB(1, 0);
        CM(0, 7); LDB(2, 7); CM(0, 6); LDT(1, 5, 0); CM(0, 5); LDT(1, 5, 1); CM(0, 4); LDT(1, 6, 0); CM(0, 3); LDT(1, 6, 1); CM(0, 2); LDT(1, 7, 0); CM(0, 1); LDT(1, 7, 1); CM(0, 0); PC();
        CM(1, 6); LDB(2, 6); CM(1, 5); LDT(2, 0, 0); CM(1, 4); LDT(2, 0, 1); CM(1, 3); KD_UP0(1); CM(1, 2); KD_UP1(1); CM(1, 1); KD_MU0(1); CM(1, 0); KD_MU1(1);
        CM(2, 5); LDB(2, 5); CM(2, 4); LDT(2, 1, 0); CM(2, 3); LDT(2, 1, 1); CM(2, 2); KD_CV(1); CM(2, 1); KD_X0(1); CM(2, 0); KD_X1(1);
        CM(3, 4); LDB(2, 4); CM(3, 3); LDT(2, 2, 0); CM(3, 2); LDT(2, 2, 1); CM(3, 1); KD_RD(2); CM(3, 0); PC();
        CM(4, 3); LDB(2, 3); CM(4, 2); LDT(2, 3, 0); CM(4, 1); LDT(2, 3, 1); CM(4, 0); PC();
        CM(5, 2); LDB(2, 2); CM(5, 1); LDT(2, 4, 0); CM(5, 0); LDT(2, 4, 1);
        CM(6, 1); LDB(2, 1); CM(6, 0); KD_UP0(2);
        CM(7, 0); LDB(2, 0);
        CM(0, 7); LDB(3, 7); CM(0, 6); LDT(2, 5, 0); CM(0, 5); LDT(2, 5, 1); CM(0, 4); LDT(2, 6, 0); CM(0, 3); LDT(2, 6, 1); CM(0, 2); LDT(2, 7, 0); CM(0, 1); LDT(2, 7, 1); CM(0, 0); KD_UP1(2);
        CM(1, 6); LDB(3, 6); CM(1, 5); LDT(3, 0, 0); CM(1, 4); LDT(3, 0, 1); CM(1, 3); KD_MU0(2); CM(1, 2); KD_MU1(2); CM(1, 1); KD_CV(2); CM(1, 0); KD_X0(2);
        CM(2, 5); LDB(3, 5); CM(2, 4); LDT(3, 1, 0); CM(2, 3); LDT(3, 1, 1); CM(2, 2); KD_X1(2); CM(2, 1); KD_RD(3); CM(2, 0); PC();
        CM(3, 4); LDB(3, 4); CM(3, 3); LDT(3, 2, 0); CM(3, 2); LDT(3, 2, 1); CM(3, 1); PC(); CM(3, 0); KD_UP0(3);
        CM(4, 3); LDB(3, 3); CM(4, 2); LDT(3, 3, 0); CM(4, 1); LDT(3, 3, 1); CM(4, 0); KD_UP1(3);
        CM(5, 2); LDB(3, 2); CM(5, 1); LDT(3, 4, 0); CM(5, 0); LDT(3, 4, 1);
        CM(6, 1); LDB(3, 1); CM(6, 0); KD_MU0(3);
        CM(7, 0); LDB(3, 0);
        CM(0, 7); LDB(4, 7); CM(0, 6); LDT(3, 5, 0); CM(0, 5); LDT(3, 5, 1); CM(0, 4); LDT(3, 6, 0); CM(0, 3); LDT(3, 6, 1); CM(0, 2); LDT(3, 7, 0); CM(0, 1); LDT(3, 7, 1); CM(0, 0); KD_MU1(3);
        CM(1, 6); LDB(4, 6); CM(1, 5); LDT(4, 0, 0); CM(1, 4); LDT(4, 0, 1); CM(1, 3); KD_CV(3); CM(1, 2); KD_X0(3); CM(1, 1); KD_X1(3); CM(1, 0); KD_RD(4);
        CM(2, 5); LDB(4, 5); CM(2, 4); LDT(4, 1, 0); CM(2, 3); LDT(4, 1, 1); CM(2, 2); PC(); CM(2, 1); PC(); CM(2, 0); KD_UP0(4);
        CM(3, 4); LDB(4, 4); CM(3, 3); LDT(4, 2, 0); CM(3, 2); LDT(4, 2, 1); CM(3, 1); KD_UP1(4); CM(3, 0); KD_MU0(4);
        CM(4, 3); LDB(4, 3); CM(4, 2); LDT(4, 3, 0); CM(4, 1); LDT(4, 3, 1); CM(4, 0); KD_MU1(4);
        CM(5, 2); LDB(4, 2); CM(5, 1); LDT(4, 4, 0); CM(5, 0); LDT(4, 4, 1);
        CM(6, 1); LDB(4, 1); CM(6, 0); KD_CV(4);
        CM(7, 0); LDB(4, 0);
        CM(0, 7); LDB(5, 7); CM(0, 6); LDT(4, 5, 0); CM(0, 5); LDT(4, 5, 1); CM(0, 4); LDT(4, 6, 0); CM(0, 3); LDT(4, 6, 1); CM(0, 2); LDT(4, 7, 0); CM(0, 1); LDT(4, 7, 1); CM(0, 0); KD_X0(4);
        CM(1, 6); LDB(5, 6); CM(1, 5); LDT(5, 0, 0); CM(1, 4); LDT(5, 0, 1); CM(1, 3); KD_X1(4); CM(1, 2); KD_RD(5); CM(1, 1); PC(); CM(1, 0); PC();
        CM(2, 5); LDB(5, 5); CM(2, 4); LDT(5, 1, 0); CM(2, 3); LDT(5, 1, 1); CM(2, 2); KD_UP0(5); CM(2, 1); KD_UP1(5); CM(2, 0); KD_MU0(5);
        CM(3, 4); LDB(5, 4); CM(3, 3); LDT(5, 2, 0); CM(3, 2); LDT(5, 2, 1); CM(3, 1); KD_MU1(5); CM(3, 0); KD_CV(5);
        CM(4, 3); LDB(5, 3); CM(4, 2); LDT(5, 3, 0); CM(4, 1); LDT(5, 3, 1); CM(4, 0); KD_X0(5);
        CM(5, 2); LDB(5, 2); CM(5, 1); LDT(5, 4, 0); CM(5, 0); LDT(5, 4, 1);
        CM(6, 1); LDB(5, 1); CM(6, 0); KD_X1(5);
        CM(7, 0); LDB(5, 0);
        CM(0, 7); LDB(6, 7); CM(0, 6); LDT(5, 5, 0); CM(0, 5); LDT(5, 5, 1); CM(0, 4); LDT(5, 6, 0); CM(0, 3); LDT(5, 6, 1); CM(0, 2); LDT(5, 7, 0); CM(0, 1); LDT(5, 7, 1); CM(0, 0); KD_RD(6);
        CM(1, 6); LDB(6, 6); CM(1, 5); LDT(6, 0, 0); CM(1, 4); LDT(6, 0, 1); CM(1, 3); PC(); CM(1, 2); PC(); CM(1, 1); KD_UP0(6); CM(1, 0); KD_UP1(6);
        CM(2, 5); LDB(6, 5); CM(2, 4); LDT(6, 1, 0); CM(2, 3); LDT(6, 1, 1); CM(2, 2); KD_MU0(6); CM(2, 1); KD_MU1(6); CM(2, 0); KD_CV(6);
        CM(3, 4); LDB(6, 4); CM(3, 3); LDT(6, 2, 0); CM(3, 2); LDT(6, 2, 1); CM(3, 1); KD_X0(6); CM(3, 0); KD_X1(6);
        CM(4, 3); LDB(6, 3); CM(4, 2); LDT(6, 3, 0); CM(4, 1); LDT(6, 3, 1); CM(4, 0); KD_RD(7);
        CM(5, 2); LDB(6, 2); CM(5, 1); LDT(6, 4, 0); CM(5, 0); LDT(6, 4, 1);
        CM(6, 1); LDB(6, 1); CM(6, 0); PC();
        CM(7, 0); LDB(6, 0);
        CM(0, 7); LDB(7, 7); CM(0, 6); LDT(6, 5, 0); CM(0, 5); LDT(6, 5, 1); CM(0, 4); LDT(6, 6, 0); CM(0, 3); LDT(6, 6, 1); CM(0, 2); LDT(6, 7, 0); CM(0, 1); LDT(6, 7, 1); CM(0, 0); PC();
        CM(1, 6); LDB(7, 6); CM(1, 5); LDT(7, 0, 0); CM(1, 4); LDT(7, 0, 1); CM(1, 3); KD_UP0(7); CM(1, 2); KD_UP1(7); CM(1, 1); KD_MU0(7); CM(1, 0); KD_MU1(7);
        CM(2, 5); LDB(7, 5); CM(2, 4); LDT(7, 1, 0); CM(2, 3); LDT(7, 1, 1); CM(2, 2); KD_CV(7); CM(2, 1); KD_X0(7); CM(2, 0); KD_X1(7);
        CM(3, 4); LDB(7, 4); CM(3, 3); LDT(7, 2, 0); CM(3, 2); LDT(7, 2, 1); CM(3, 1); PC(); CM(3, 0); PC();
        CM(4, 3); LDB(7, 3); CM(4, 2); LDT(7, 3, 0); CM(4, 1); LDT(7, 3, 1); CM(4, 0); PC();
        CM(5, 2); LDB(7, 2); CM(5, 1); LDT(7, 4, 0); CM(5, 0); LDT(7, 4, 1);
        CM(6, 1); LDB(7, 1); CM(6, 0); PC();
        CM(7, 0); LDB(7, 0);
        CM(0, 3); LDT(7, 5, 0); CM(0, 2); LDT(7, 5, 1); CM(1, 2); LDT(7, 6, 0); CM(1, 1); LDT(7, 6, 1); CM(2, 1); LDT(7, 7, 0); CM(2, 0); LDT(7, 7, 1);
        CM(3, 0); HF(); CM(0, 1); PC(); CM(0, 0); W3(0); CM(1, 0); ZF0(2); W3(1); CM(0, 5); ZF1(2); W3(2); CM(0, 4); ZF2(2); W3(3);
        CM(1, 4); ZF3(2); W3(4); CM(1, 3); ZF4(2); W3(5); CM(2, 3); ZF5(2); W3(6); CM(2, 2); ZF0(3); W3(7); CM(3, 2); ZF1(3);  CM(3, 1); ZF2(3);
        CM(4, 1); ZF3(3);  CM(4, 0); ZF4(3);  CM(5, 0); ZF5(3);  CM(0, 7); ZST(1);  CM(0, 6); ZF0(0);  CM(1, 6); ZF1(0);
        CM(1, 5); ZF2(0);  CM(2, 5); ZF3(0);  CM(2, 4); ZF4(0);  CM(3, 4); ZF5(0);  CM(3, 3); ZF0(1);  CM(4, 3); ZF1(1);
        CM(4, 2); ZF2(1);  CM(5, 2); ZF3(1);  CM(5, 1); ZF4(1);  CM(6, 1); ZF5(1);  CM(6, 0); ZST(0);  CM(7, 0); ZF0(4);
        DG(4);
        ZF1(4); ZF2(4); ZF3(4); ZF4(4); ZF5(4); ZF0(5); ZF1(5); ZF2(5); ZF3(5); ZF4(5); ZF5(5); ZST(2); ZF0(6); ZF1(6); ZF2(6); ZF3(6); ZF4(6); ZF5(6); ZF0(7); ZF1(7); ZF2(7); ZF3(7); ZF4(7); ZF5(7); ZST(3);
        DG(10);
        __builtin_amdgcn_s_setprio(3);
        while ((unsigned)__builtin_amdgcn_readfirstlane(*(volatile LAS unsigned*)(fl + C_OL)) < ZL_GATE) __builtin_amdgcn_s_sleep(4);
        CFENCE();
        u32x2 qr[8];
#pragma unroll
        for (int i = 0; i < 8; ++i) qr[i] = gld64(roff, Aq + i * 2048);
        {
            const lds_ptr tr = cv + CV_KV + 480;
            u32x4 t[4];
            const unsigned zb = (unsigned)WS_ZT + (unsigned)(c0 + (lane >> 3)) * 4096u + (idx >> 3) * 1024 + ((idx & 3) * 4 + ((idx >> 2) & 1) * 16) * 16;
            for (unsigned spins = 0; spins < (1u << 20); ++spins) {
#pragma unroll
                for (int i = 0; i < 4; ++i) {
                    const int ck = (lane & 7) ^ (2 * i + (lane >> 5));
                    t[i] = ld_sc1(rs, zb + i * 32768 + ((ck >> 1) + (ck & 1) * 32) * 16);
                }
                if (!missing(mx4(mx4(mx8(t[0], t[1]), t[2]), t[3]))) break;
                __builtin_amdgcn_s_sleep(Z_SLEEP);
            }
            DG(13);
            __builtin_amdgcn_s_setprio(ZE_PRIO);
#pragma unroll
            for (int i = 0; i < 4; ++i) *(LAS u32x4*)(tr + i * 1024 + lane * 16) = t[i];
#pragma unroll
            for (int i = 0; i < 8; ++i) asm volatile("" : "+v"(qr[i]));
            u32x4 zc[4];
#pragma unroll
            for (int c = 0; c < 4; ++c) zc[c] = *(const LAS u32x4*)(tr + (cq * 4 + c) * 128 + ((rg ^ cq) << 4));
            char* dst = out + (size_t)row0 * 4096 + (c0 + cq * 4) * 2;
#pragma unroll
            for (int i = 0; i < 8; ++i) {
                float zz[4];
#pragma unroll
                for (int c = 0; c < 4; ++c) zz[c] = (i & 1) ? bfhi(zc[c][i >> 1]) : bflo(zc[c][i >> 1]);
                u32x2 o = {cvtpk(mul_vv(bflo(qr[i][0]), zz[0]), mul_vv(bfhi(qr[i][0]), zz[1])),
                           cvtpk(mul_vv(bflo(qr[i][1]), zz[2]), mul_vv(bfhi(qr[i][1]), zz[3]))};
                __builtin_amdgcn_raw_buffer_store_b64(o, ro, (int)(row0 * 4096 + (c0 + cq * 4) * 2), i * 4096, A_AUX);
            }
        }
        DG_END(14);
        return;
    }

    const int half = w >> 2;
    const int TA = qb * 8 + (w & 3) * 2 + half, TB = TA ^ 1;
    bf16x8 sel;
    {
        const short one = (((lane >> 2) & 1) == ((lane >> 4) & 1)) ? (short)0x3f80 : (short)0;
#pragma unroll
        for (int i = 0; i < 8; ++i) sel[i] = one;
    }
    f32x16 oA = f32x16{}, oB = f32x16{};
    f32x4 lA = f32x4{}, lB = f32x4{};
    f32x16 negm, sA, sB;
    lds_ptr lp = lds + half * 16384 + lane * 16;
    bf16x8 k0[2], k1[2], v0[2], v1[2];
    u32x4 p0, p1 = {0u, 0u, 0u, 0u};
#define EX(X, i) X[i] = __builtin_amdgcn_exp2f(X[i])
    lds_ptr qpa = lds + K2_SF + (TA & 7) * 2048 + lane * 16, qpb = lds + K2_SF + (TB & 7) * 2048 + lane * 16;
    asm volatile("" : "+v"(qpa), "+v"(qpb), "+v"(lp), "+v"(sel), "+v"(oA), "+v"(oB), "+v"(lA), "+v"(lB), "+v"(p1));
    WAIT_BAR(0);
    DG(3);
    const bf16x8 qa0 = lds_rd128(qpa), qb0 = lds_rd128(qpb);
    const bf16x8 x0 = lds_rd128(lds + lane * 16), x1 = lds_rd128(lds + lane * 16 + 1024);
    const bf16x8 qa1 = lds_rd128(qpa + 1024), qb1 = lds_rd128(qpb + 1024);
    {
        k0[0] = lds_rd128(lp); k1[0] = lds_rd128(lp + 1024);
        v0[0] = lds_rd128(lp + 2048); v1[0] = lds_rd128(lp + 3072);
        sA = MFMA32(x0, qa0, f32x16{});
        sB = MFMA32(x0, qb0, f32x16{});
        sA = MFMA32(x1, qa1, sA);
        sB = MFMA32(x1, qb1, sB);
        float m = fmx(sA[0], sA[1]), m2 = fmx(sB[0], sB[1]);
#pragma unroll
        for (int i = 2; i < 16; i += 2) m = fmx(fmx(m, sA[i]), sA[i + 1]);
#pragma unroll
        for (int i = 2; i < 16; i += 2) m2 = fmx(fmx(m2, sB[i]), sB[i + 1]);
        m = fmx(m, m2);
        auto rr = __builtin_amdgcn_permlane32_swap(__float_as_uint(m), __float_as_uint(m), false, false);
        m = fmx(__uint_as_float(rr[0]), __uint_as_float(rr[1]));
#pragma unroll
        for (int i = 0; i < 16; ++i) negm[i] = -m;
        sA = MFMA32(k0[0], qa0, negm);
        sA = MFMA32(k1[0], qa1, sA);
        v1[1] = v1[0];
        SBAR();
        RAW_BAR();
        SBAR();
        EX(sA, 0); EX(sA, 1);
    }
    DG(10);
#define SUM16(l, p) asm volatile("v_mfma_f32_16x16x32_bf16 %0, %1, %2, %0" : "+v"(l) : "v"(sel), "v"(p))
#define HALF(C, oC, lC, N, qx, qy, ka, kb, oP, lP, vprev, vcur0, PRE) do { \
        oP = MFMA32(vprev, PB(p1), oP); SBAR(); \
        PRE; EX(C, 2); EX(C, 3); EX(C, 4); SBAR(); \
        N = MFMA32(ka, qx, negm); SBAR(); \
        EX(C, 5); EX(C, 6); EX(C, 7); SBAR(); \
        SUM16(lP, p1); SBAR(); \
        p0[0] = cvtpk(C[0], C[1]); p0[1] = cvtpk(C[2], C[3]); SBAR(); \
        N = MFMA32(kb, qy, N); SBAR(); \
        p0[2] = cvtpk(C[4], C[5]); p0[3] = cvtpk(C[6], C[7]); SBAR(); EX(C, 8); EX(C, 9); SBAR(); \
        oC = MFMA32(vcur0, PB(p0), oC); SBAR(); \
        EX(C, 10); EX(C, 11); EX(C, 12); SBAR(); \
        SUM16(lC, p0); SBAR(); \
        EX(C, 13); EX(C, 14); EX(C, 15); SBAR(); \
        p1[0] = cvtpk(C[8], C[9]); p1[1] = cvtpk(C[10], C[11]); p1[2] = cvtpk(C[12], C[13]); p1[3] = cvtpk(C[14], C[15]); SBAR(); \
        EX(N, 0); EX(N, 1); SBAR(); \
    } while (0)
#define RD4(Y, nx) do { v1[Y] = lds_rd128((nx) + 3072); v0[Y] = lds_rd128((nx) + 2048); \
        k1[Y] = lds_rd128((nx) + 1024); k0[Y] = lds_rd128(nx); } while (0)
#define KT(X, nx, PRE0) do { \
        HALF(sA, oA, lA, sB, qb0, qb1, k0[X], k1[X], oB, lB, v1[X ^ 1], v0[X], PRE0; RD4(X ^ 1, nx)); \
        HALF(sB, oB, lB, sA, qa0, qa1, k0[X ^ 1], k1[X ^ 1], oA, lA, v1[X], v0[X], (void)0); \
    } while (0)
    SBAR();
#pragma unroll 1
    for (int st = 0; st < 8; ++st) {
        const lds_ptr cur = lp + (st & 1) * K2_SLOT;
        const lds_ptr nxt = lp + ((st + 1) & 1) * K2_SLOT;
        PRIO(2);
        KT(0, cur + 4096, (void)0);
        PRIO(1);
        KT(1, cur + 8192, (void)0);
        PRIO(0);
        KT(0, cur + 12288, (void)0);
        PRIO(3);
        KT(1, nxt, RAW_BAR(); SBAR());
    }
    oB = MFMA32(v1[1], PB(p1), oB);
    SBAR();
    SUM16(lB, p1);
    asm volatile("s_nop 7" : "+v"(oB) :: "memory");
    SBAR();
    DG(4);
    {
        const lds_ptr xo = lds + K2_SLOT + w * 4096 + lane * 16;
        *(LAS float*)(lds + K2_LX + w * 256 + lane * 4) = lB[0];
#pragma unroll
        for (int i = 0; i < 4; ++i) *(LAS f32x4*)(xo + i * 1024) = f32x4{oB[4 * i], oB[4 * i + 1], oB[4 * i + 2], oB[4 * i + 3]};
        lds_add(fl + F_X + w * 4, lane);
        lds_add(fl + C_END + half * 4, lane);
        lds_wait_ge(fl + F_X + (w ^ 4) * 4, 1u);
        const lds_ptr xi = lds + K2_SLOT + (w ^ 4) * 4096 + lane * 16;
        f32x4 t[4];
        const float lx = *(const LAS float*)(lds + K2_LX + (w ^ 4) * 256 + lane * 4);
#pragma unroll
        for (int i = 0; i < 4; ++i) t[i] = *(const LAS f32x4*)(xi + i * 1024);
        SBAR();
        lA[0] += lx;
#pragma unroll
        for (int i = 0; i < 16; ++i) { float o = oA[i]; acc_vv(o, t[i >> 2][i & 3]); oA[i] = o; }
    }
    DG(9);
    const float l = lA[0];
    u32x4 on0, on1;
    auto finish = [&](float ls) {
        float rl = __builtin_amdgcn_rcpf(ls);
        asm volatile("s_nop 1" : "+v"(rl));
        f32x16 o;
#pragma unroll
        for (int i = 0; i < 16; ++i) o[i] = mul_vv(oA[i], rl);
        on0 = nz4(pack8n(o, 0)); on1 = nz4(pack8n(o, 1));
    };
    finish(l);
    {
        float t0, t1;
        asm("v_mul_f32_e32 %0, 0, %2\n\tv_mul_f32_e32 %1, 0, %3\n\tv_fmac_f32_e32 %0, 0, %4\n\tv_fmac_f32_e32 %1, 0, %5\n\tv_fmac_f32_e32 %0, 0, %6\n\tv_fmac_f32_e32 %1, 0, %7\n\tv_fmac_f32_e32 %0, 0, %8\n\tv_fmac_f32_e32 %1, 0, %9\n\t"
            "v_fmac_f32_e32 %0, 0, %10\n\tv_fmac_f32_e32 %1, 0, %11\n\tv_fmac_f32_e32 %0, 0, %12\n\tv_fmac_f32_e32 %1, 0, %13\n\tv_fmac_f32_e32 %0, 0, %14\n\tv_fmac_f32_e32 %1, 0, %15\n\tv_fmac_f32_e32 %0, 0, %16\n\tv_fmac_f32_e32 %1, 0, %17"
            : "=&v"(t0), "=&v"(t1) : "v"(oA[0]), "v"(oA[1]), "v"(oA[2]), "v"(oA[3]), "v"(oA[4]), "v"(oA[5]), "v"(oA[6]), "v"(oA[7]), "v"(oA[8]), "v"(oA[9]), "v"(oA[10]), "v"(oA[11]), "v"(oA[12]), "v"(oA[13]), "v"(oA[14]), "v"(oA[15]));
        const bool ok = (l <= 1.0e30f) && (l >= 1.0e-18f) && (t0 + t1 == 0.f);
        if (__builtin_expect(__any(!ok), 0)) {
            float m = -INFINITY, ls = 0.f;
            oA = f32x16{};
#pragma unroll 1
            for (int t = 0; t < 64; ++t) {
                const char* g = kvg + t * 4096 + lane * 16;
                const bf16x8 a0 = *(const bf16x8*)g, a1 = *(const bf16x8*)(g + 1024);
                const bf16x8 b0 = *(const bf16x8*)(g + 2048), b1 = *(const bf16x8*)(g + 3072);
                f32x16 s = MFMA32(a0, qa0, f32x16{});
                s = MFMA32(a1, qa1, s);
                float tm = s[0];
#pragma unroll
                for (int i = 1; i < 16; ++i) tm = __builtin_fmaxf(tm, s[i]);
                auto rr = __builtin_amdgcn_permlane32_swap(__float_as_uint(tm), __float_as_uint(tm), false, false);
                tm = __builtin_fmaxf(__uint_as_float(rr[0]), __uint_as_float(rr[1]));
                const float mn = __builtin_fmaxf(m, tm);
                const float alpha = __builtin_amdgcn_exp2f(m - mn);
                m = mn;
                float ps = 0.f;
#pragma unroll
                for (int i = 0; i < 16; ++i) { s[i] = __builtin_amdgcn_exp2f(s[i] - mn); ps += s[i]; oA[i] *= alpha; }
                ls = ls * alpha + ps;
                oA = MFMA32(b0, PB(pack8n(s, 0)), oA);
                oA = MFMA32(b1, PB(pack8n(s, 1)), oA);
            }
            auto rr = __builtin_amdgcn_permlane32_swap(__float_as_uint(ls), __float_as_uint(ls), false, false);
            asm volatile("s_nop 7\n\ts_nop 7" : "+v"(oA));
            finish(__uint_as_float(rr[0]) + __uint_as_float(rr[1]));
        }
    }
    u32x4 w3[4];
    {
        const unsigned og = (unsigned)WS_O + (unsigned)(TA * 32 + h) * 2048u + lane * 16;
        st_sc1(on0, rs, og);
        st_sc1(on1, rs, og + 1024);
        lds_add(fl + C_S1, lane);
        const char* s2 = wsrc(half ^ 1, 32 + (w & 3) * 4);
        const char* s3 = wsrc(half, 48 + (w & 3) * 4);
        lds_wait_ge(fl + C_S1, 12u);
        DG(5);
#pragma unroll
        for (int i = 0; i < 4; ++i)
            glds16(wvoff, s2 + i * 16384, lds0 + K2_SLOT + ((w ^ 4) * 4 + i) * 1024);
#pragma unroll
        for (int i = 0; i < 4; ++i) w3[i] = gld128(wvoff, s3 + i * 16384);
    }
    {
        const int half = w & 1;
        const int T3 = tt * 4 + (w >> 1);
        const unsigned og = (unsigned)WS_O + (unsigned)T3 * 65536u + half * 32768 + lane * 16;
        const int r = lane & 31, hh = lane >> 5;
        const int g = lane >> 4, q = (lane & 15) >> 2, p = lane & 3;
        int woff[2];
#pragma unroll
        for (int n = 0; n < 2; ++n) {
            const int c = 4 * (n ^ half) + 2 * (p & 1) + (g & 1);
            woff[n] = (4 * hh + q) * 128 + ((c ^ ((q >> 1) << 2)) << 4) + (p >> 1) * 8;
        }
        f32x16 acc[2] = {f32x16{}, f32x16{}};
        lds_ptr wl[3][2] = {{lds + half * 16384 + woff[0], lds + half * 16384 + woff[1]},
                            {lds + K2_SF + half * 16384 + woff[0], lds + K2_SF + half * 16384 + woff[1]},
                            {lds + K2_CV + half * 2 * CV_BYTES + woff[0], lds + K2_CV + half * 2 * CV_BYTES + woff[1]}};
        asm volatile("" : "+v"(wl[0][0]), "+v"(wl[0][1]), "+v"(wl[1][0]), "+v"(wl[1][1]), "+v"(wl[2][0]), "+v"(wl[2][1]));
#define MS(m) ((m) < 64 - 2 * TL ? (m) >> 1 : 32 - TL + ((m) - (64 - 2 * TL)) % TL)
#define MN(m) ((m) < 64 - 2 * TL ? (m) & 1 : (m) < 64 - TL)
        constexpr int WR = 2 * WD + 2;
        s16x4 wa[WR], wb[WR];
#define LDW(m) do { const int s_ = MS(m), f_ = s_ < 8 ? 1 : s_ < 24 ? 0 : 2; \
            const int o_ = f_ == 2 ? ((s_ >> 2) & 1) * CV_BYTES + (s_ & 3) * 2048 : (f_ ? 0 : ((s_ >> 3) - 1) * K2_SLOT) + (s_ & 7) * 2048; \
            wa[(m) % WR] = lds_tr(wl[f_][MN(m)] + o_); wb[(m) % WR] = lds_tr(wl[f_][MN(m)] + o_ + 1024); SBAR(); } while (0)
#pragma unroll
        for (int m = 0; m < 2 * WD; ++m) LDW(m);
        u32x4 of[OWIN];
#define LDO(i) do { of[(i) % OWIN] = ld_sc1s(rs, og, (((i) + krot) & 31) * 1024); SBAR(); } while (0)
        for (unsigned spins = 0; spins < (1u << 20); ++spins) {
#pragma unroll
            for (int i = 0; i < OW0; ++i) LDO(i);
            unsigned m = mx8(of[0], of[1]);
#pragma unroll
            for (int i = 2; i < OW0; ++i) m = mx4(m, of[i]);
            if (!missing(m)) break;
            DGC(12);
        }
        lds_add(fl + C_S2, lane);
#pragma unroll
        for (int i = OW0; i < OWIN; ++i) LDO(i);
        DG(6);
        __builtin_amdgcn_s_setprio(P3_PRIO);
        SBAR();
        unsigned fv = 0;
#define FLAG_RD(a) do { fv = *(volatile LAS unsigned*)(a); SBAR(); } while (0)
#define FLAG_CHK(a, n) do { if (__builtin_expect((unsigned)__builtin_amdgcn_readfirstlane(fv) < (n), 0)) lds_wait_ge(a, n); CFENCE(); SBAR(); } while (0)
        lds_ptr xo = lds + K2_SF + w * 4096 + lane * 16, xi = lds + K2_SF + (w ^ 1) * 4096 + lane * 16;
        int bo = (T3 * 32 + (lane & 15)) * 4096 + 2048 + nt * 128 + half * 64 + g * 16;
        lds_ptr w3d = lds + K2_CV + (w >> 1) * CV_BYTES + (w & 1) * 4096 + lane * 16;
        asm volatile("" : "+v"(xo), "+v"(xi), "+v"(bo), "+v"(w3d));
        f32x4 xt[4];
        constexpr int M3 = TL > 8 ? 64 - 2 * TL + TL - 8 - 2 * WD : 48 - 2 * WD;
        sfor(std::make_integer_sequence<int, 64>{}, [&](auto mc) __attribute__((always_inline)) {
            constexpr int m = decltype(mc)::value, s = MS(m), n = MN(m);
            constexpr bool first = m < 64 - 2 * TL ? n == 0 : n == 1;
            constexpr bool last = m < 64 - 2 * TL ? n == 1 : n == 0;
            acc[n] = MFMA32(cat8(wa[m % WR], wb[m % WR]), PB(of[s % OWIN]), acc[n]);
            SBAR();
            if constexpr (m == 32 - 2 * WD) FLAG_CHK(fl + C_S2, 8u);
            if constexpr (m == M3) FLAG_CHK(fl + C_S3, 8u);
            if constexpr (m + 2 * WD < 64) LDW(m + 2 * WD);
            if constexpr (first && (s & 1) && s + 1 >= OW0 && s + 1 < 32) {
                if (__builtin_expect(missing(mx8(of[(s + 1) % OWIN], of[(s + 2) % OWIN])), 0)) {
#pragma unroll 1
                    for (unsigned spins = 0; spins < (1u << 20); ++spins) {
                        __builtin_amdgcn_s_sleep(2);
                        LDO(s + 1); LDO(s + 2);
                        DGC(13);
                        if (!missing(mx8(of[(s + 1) % OWIN], of[(s + 2) % OWIN]))) break;
                    }
                }
            }
            if constexpr (last && s + OWIN < 32) LDO(s + OWIN);
            if constexpr (m == 15 - 2 * WD) lds_add(fl + C_U0, lane);
            if constexpr (m == 29 - 2 * WD) FLAG_RD(fl + C_S2);
            if constexpr (m == W3M - 3) FLAG_RD(fl + C_HF);
            if constexpr (m == W3M) FLAG_CHK(fl + C_HF, 4u);
            if constexpr (m >= W3M && m < W3M + 8 && ((m - W3M) & 1) == 0) {
                asm volatile("" : "+v"(w3[((m - W3M) >> 1) & 3]));
                *(LAS u32x4*)(w3d + (((m - W3M) >> 1) & 3) * 1024) = w3[((m - W3M) >> 1) & 3];
            }
            if constexpr (m == W3M + 6) lds_add(fl + C_S3, lane);
            if constexpr (m == M3 - 3) FLAG_RD(fl + C_S3);
            if constexpr (m == 64 - TL - 3) FLAG_RD(fl + C_U0);
            if constexpr (m == 64 - TL) FLAG_CHK(fl + C_U0, 8u);
            if constexpr (m == 64 - TL + 1 || m == 64 - TL + 2) {
                constexpr int i = 2 * (m - (64 - TL + 1));
                *(LAS f32x4*)(xo + i * 1024) = f32x4{acc[1][4 * i], acc[1][4 * i + 1], acc[1][4 * i + 2], acc[1][4 * i + 3]};
                *(LAS f32x4*)(xo + i * 1024 + 1024) = f32x4{acc[1][4 * i + 4], acc[1][4 * i + 5], acc[1][4 * i + 6], acc[1][4 * i + 7]};
            }
            if constexpr (m == 64 - TL + 2) lds_add(fl + F_R + w * 4, lane);
            if constexpr (m == XR_AT - 2) FLAG_RD(fl + F_R + (w ^ 1) * 4);
            if constexpr (m == XR_AT) {
                FLAG_CHK(fl + F_R + (w ^ 1) * 4, 1u);
#pragma unroll
                for (int i = 0; i < 4; ++i) xt[i] = *(const LAS f32x4*)(xi + i * 1024);
            }
            if constexpr (ZL_GATE && last && s + OWIN == ZL_AT) lds_add(fl + C_OL, lane);
            if constexpr (m == 2 * P3_T1 + 1) __builtin_amdgcn_s_setprio(P3_PRIO - 1);
            SBAR();
            if constexpr (m == 31) DG(11);
            if constexpr (m == 47) DG(14);
        });
        asm volatile("s_nop 11" : "+v"(acc[0]));
        DG(7);
#pragma unroll
        for (int i = 0; i < 16; ++i) { float o = xt[i >> 2][i & 3]; acc_vv(o, acc[0][i]); acc[1][i] = o; }
        u32x4 pa = pack8n(acc[1], 0), pb = pack8n(acc[1], 1);
#pragma unroll
        for (int i = 0; i < 4; ++i) {
            auto sw = __builtin_amdgcn_permlane16_swap(pa[i], pb[i], false, false);
            pa[i] = sw[0]; pb[i] = sw[1];
        }
        asm volatile("" :: "v"(acc[0]));
        __builtin_amdgcn_raw_buffer_store_b128(pa, ro, bo, 0, AUX_SC1);
        __builtin_amdgcn_raw_buffer_store_b128(pb, ro, bo, 65536, AUX_SC1);
    }
    DG_END(8);
}

extern "C" void kernel_launch(void* const* d_in, const int* in_sizes, int n_in,
                              void* d_out, int out_size, void* d_ws, size_t ws_size,
                              hipStream_t stream) {
    const void* A_q = d_in[0];
    const void* A_k = d_in[1];
    const void* A_v = d_in[2];
    const void* A_shift = d_in[3];
    const void* A_ssmk = d_in[4];
    const void* A_D = d_in[5];
    const void* B_x = d_in[6];
    const void* B_wqkv = d_in[7];
    const void* B_wo = d_in[8];
    char* ws = (char*)d_ws;
    k1_qkv<<<256, 512, 0, stream>>>((const char*)B_x, (const char*)B_wqkv, ws);
    k2_attn<<<256, 768, 0, stream>>>(ws, (const char*)A_q, (const char*)A_k, (const char*)A_v, (const char*)A_shift,
                                     (const char*)A_ssmk, (const unsigned*)A_D, (const char*)B_wo, (char*)d_out);
}
```

```cpp
#include <hip/hip_runtime.h>
#include <hip/hip_bf16.h>
#include <stdint.h>
#include <utility>

typedef short bf16x8 __attribute__((ext_vector_type(8)));
typedef short s16x4 __attribute__((ext_vector_type(4)));
typedef float f32x16 __attribute__((ext_vector_type(16)));
typedef float f32x4 __attribute__((ext_vector_type(4)));
typedef float f32x2 __attribute__((ext_vector_type(2)));
typedef unsigned u32x4 __attribute__((ext_vector_type(4)));
typedef unsigned u32x2 __attribute__((ext_vector_type(2)));
typedef __bf16 bf16x2_t __attribute__((ext_vector_type(2)));

#define LAS __attribute__((address_space(3)))
typedef LAS char* lds_ptr;
#define DI __device__ __forceinline__
#define SBAR() __builtin_amdgcn_sched_barrier(0)
#define MFMA32(a, b, c) __builtin_amdgcn_mfma_f32_32x32x16_bf16((a), (b), (c), 0, 0, 0)
#define MFMA16(a, b, c) __builtin_amdgcn_mfma_f32_16x16x32_bf16((a), (b), (c), 0, 0, 0)
#define WAIT_BAR(N) asm volatile("s_waitcnt vmcnt(" #N ") lgkmcnt(0)\n\ts_barrier" ::: "memory")
#define LGKM_BAR() asm volatile("s_waitcnt lgkmcnt(0)\n\ts_barrier" ::: "memory")

#define DG_DECL(K)
#define DG(i)
#define DG_END(i)
#define DGC(i)

constexpr int S_LEN = 2048, DM = 1024, NH = 32;

constexpr size_t WS_Q = 0;
constexpr size_t WS_KV = 4u << 20;
constexpr size_t WS_O = 12u << 20;
constexpr size_t WS_KVR = 16u << 20;
constexpr size_t WS_ZT = 20u << 20;
constexpr size_t WS_CNT = 24u << 20;

template <int... I, class F> DI void sfor(std::integer_sequence<int, I...>, F&& f) { (f(std::integral_constant<int, I>{}), ...); }
DI unsigned cvtpk(float lo, float hi) {
    f32x2 v = {lo, hi};
    return __builtin_bit_cast(unsigned, __builtin_convertvector(v, bf16x2_t));
}
DI float bflo(unsigned u) { return __uint_as_float(u << 16); }
DI float bfhi(unsigned u) { return __uint_as_float(u & 0xffff0000u); }

DI void glds16(unsigned voff, const void* sbase, unsigned lds_dst) {
    unsigned sv;
    asm volatile("s_mov_b32 %0, m0\n\ts_mov_b32 m0, %3\n\ts_nop 0\n\tglobal_load_lds_dwordx4 %1, %2\n\ts_mov_b32 m0, %0"
                 : "=&s"(sv) : "v"(voff), "s"(sbase), "s"(lds_dst) : "memory");
}
DI bf16x8 lds_rd128(lds_ptr p) { return *(const LAS bf16x8*)p; }
DI s16x4 lds_tr(lds_ptr p) {
    return __builtin_bit_cast(s16x4, __builtin_amdgcn_ds_read_tr16_b64_v4i16((LAS s16x4*)p));
}
DI bf16x8 cat8(s16x4 a, s16x4 b) { return __builtin_shufflevector(a, b, 0, 1, 2, 3, 4, 5, 6, 7); }
DI u32x4 pack8(const f32x16& x, int s, float sc) {
    u32x4 p;
    p[0] = cvtpk(x[8 * s + 0] * sc, x[8 * s + 1] * sc);
    p[1] = cvtpk(x[8 * s + 2] * sc, x[8 * s + 3] * sc);
    p[2] = cvtpk(x[8 * s + 4] * sc, x[8 * s + 5] * sc);
    p[3] = cvtpk(x[8 * s + 6] * sc, x[8 * s + 7] * sc);
    return p;
}
DI u32x4 pack8n(const f32x16& x, int s) {
    u32x4 p;
    p[0] = cvtpk(x[8 * s + 0], x[8 * s + 1]);
    p[1] = cvtpk(x[8 * s + 2], x[8 * s + 3]);
    p[2] = cvtpk(x[8 * s + 4], x[8 * s + 5]);
    p[3] = cvtpk(x[8 * s + 6], x[8 * s + 7]);
    return p;
}
DI void rnd2(float& a, float& b) { unsigned u = cvtpk(a, b); a = bflo(u); b = bfhi(u); }

#define CONV_PACE()
#define RAW_BAR() asm volatile("s_barrier" ::: "memory")
DI unsigned gld32(unsigned voff, const void* sbase) {
    unsigned r;
    asm volatile("global_load_dword %0, %1, %2" : "=v"(r) : "v"(voff), "s"(sbase) : "memory");
    return r;
}
DI u32x2 gld64(unsigned voff, const void* sbase) {
    u32x2 r;
    asm volatile("global_load_dwordx2 %0, %1, %2" : "=v"(r) : "v"(voff), "s"(sbase) : "memory");
    return r;
}
DI float mul_sv(float s, float v) { float r; asm("v_mul_f32_e32 %0, %1, %2" : "=v"(r) : "s"(s), "v"(v)); return r; }
DI float mul_vv(float a, float b) { float r; asm("v_mul_f32_e32 %0, %1, %2" : "=v"(r) : "v"(a), "v"(b)); return r; }
DI float add_vv(float a, float b) { float r; asm("v_add_f32_e32 %0, %1, %2" : "=v"(r) : "v"(a), "v"(b)); return r; }
DI void acc_vv(float& a, float b) { asm("v_add_f32_e32 %0, %0, %1" : "+v"(a) : "v"(b)); }
DI void fmac_vv(float& acc, float a, float b) { asm("v_fmac_f32_e32 %0, %1, %2" : "+v"(acc) : "v"(a), "v"(b)); }
DI void fmac_sv(float& acc, float s, float v) { asm("v_fmac_f32_e32 %0, %1, %2" : "+v"(acc) : "s"(s), "v"(v)); }
typedef __attribute__((address_space(1))) unsigned gu32;
#define RLX_AGENT __ATOMIC_RELAXED, __HIP_MEMORY_SCOPE_AGENT
#define CFENCE() asm volatile("" ::: "memory")
#define DRAIN() asm volatile("s_waitcnt vmcnt(0)" ::: "memory")
constexpr int AUX_SC1 = 16;
DI u32x4 ld_sc1(__amdgpu_buffer_rsrc_t rs, unsigned off) { return __builtin_amdgcn_raw_buffer_load_b128(rs, (int)off, 0, AUX_SC1); }
DI u32x4 ld_sc1s(__amdgpu_buffer_rsrc_t rs, unsigned off, int soff) { return __builtin_amdgcn_raw_buffer_load_b128(rs, (int)off, soff, AUX_SC1); }
DI void st_sc1(u32x4 v, __amdgpu_buffer_rsrc_t rs, unsigned off) { __builtin_amdgcn_raw_buffer_store_b128(v, rs, (int)off, 0, AUX_SC1); }
constexpr unsigned EMPTY = 0xffffffffu;
DI unsigned nz(unsigned x) { return x < EMPTY - 1u ? x : EMPTY - 1u; }
DI u32x4 nz4(u32x4 v) { return u32x4{nz(v[0]), nz(v[1]), nz(v[2]), nz(v[3])}; }
DI float fmx(float a, float b) { return __builtin_elementwise_maximum(a, b); }
DI unsigned umax(unsigned a, unsigned b) { return a > b ? a : b; }
DI unsigned umax3(unsigned a, unsigned b, unsigned c) { return umax(umax(a, b), c); }
DI unsigned mx4(unsigned m, u32x4 v) { return umax3(umax3(m, v[0], v[1]), v[2], v[3]); }
DI unsigned mx8(u32x4 a, u32x4 b) { return umax(umax3(umax3(umax3(a[0], a[1], a[2]), a[3], b[0]), b[1], b[2]), b[3]); }
DI bool missing(unsigned m) { return __builtin_amdgcn_ballot_w64(m == EMPTY) != 0; }
DI unsigned lds_add_rtn(lds_ptr p, int lane) {
    unsigned old = 0;
    CFENCE();
    if (lane == 0) old = __hip_atomic_fetch_add((LAS unsigned*)p, 1u, __ATOMIC_RELAXED, __HIP_MEMORY_SCOPE_WORKGROUP);
    old = __builtin_amdgcn_readfirstlane(old);
    CFENCE();
    return old;
}
DI void lds_add(lds_ptr p, int lane) {
    unsigned long long sv;
    asm volatile("s_mov_b64 %0, exec\n\ts_mov_b64 exec, 1\n\tds_add_u32 %1, %2\n\ts_mov_b64 exec, %0"
                 : "=&s"(sv) : "v"(p), "v"(1u) : "memory");
}
DI void lds_wait_ge(lds_ptr p, unsigned v) {
    CFENCE();
    while ((unsigned)__builtin_amdgcn_readfirstlane(*(volatile LAS unsigned*)p) < v) __builtin_amdgcn_s_sleep(1);
    CFENCE();
}
DI void poll_ge(gu32* word, unsigned want) {
    CFENCE();
    for (unsigned spins = 0; spins < (1u << 22); ++spins) {
        if ((unsigned)__builtin_amdgcn_readfirstlane(__hip_atomic_load(word, RLX_AGENT)) >= want) break;
        __builtin_amdgcn_s_sleep(1);
    }
    __builtin_amdgcn_fence(__ATOMIC_ACQUIRE, "wavefront");
    CFENCE();
}
#define PB(x) __builtin_bit_cast(bf16x8, x)
#define PRIO(x) do { __builtin_amdgcn_s_setprio(x); SBAR(); } while (0)
DI u32x4 gld128(unsigned voff, const void* sbase) {
    u32x4 r;
    asm volatile("global_load_dwordx4 %0, %1, %2" : "=v"(r) : "v"(voff), "s"(sbase) : "memory");
    return r;
}

#define K1_UNR 15
#define K1_LA 5
#define K1_ROT ((tb & 3) * 4)
constexpr int K1_STAGE = 40960, K1U = K1_UNR;
__global__ __launch_bounds__(512, 2) void k1_qkv(const char* __restrict__ X, const char* __restrict__ W,
                                                 char* __restrict__ ws) {
    __shared__ __attribute__((aligned(16))) char lds_raw[4 * K1_STAGE];
    const lds_ptr lds = (lds_ptr)lds_raw;
    const unsigned lds0 = (unsigned)(uintptr_t)lds;
    const int tid = threadIdx.x, lane = tid & 63;
    const int w = __builtin_amdgcn_readfirstlane(tid >> 6);
    const int b = blockIdx.x, xcd = b & 7, idx = b >> 3;
    const int hp = (xcd >> 1) * 4 + (idx & 3), tb = (xcd & 1) * 8 + (idx >> 2);
    DG_DECL(0)

    const int lw = w & 3;
    const int rot = K1_ROT;
    unsigned voffX[4], voffW[2];
#pragma unroll
    for (int pp = 0; pp < 4; ++pp) {
        const int row = 32 * lw + 8 * pp + (lane >> 3);
        const int ch = (lane & 7) ^ ((row >> 1) & 7);
        voffX[pp] = (unsigned)(tb * 128 + row) * 2048u + ch * 16;
    }
#pragma unroll
    for (int pp = 0; pp < 2; ++pp) {
        const int krow = 16 * lw + 8 * pp + (lane >> 3);
        const int ch = (lane & 7) ^ (((lane >> 4) & 1) << 2);
        voffW[pp] = (unsigned)krow * 6144u + ch * 16;
    }
    auto issue = [&](int kt, int lo, int hi) {
        const int kr = (kt + rot) & 15;
        const char* xb = X + kr * 128;
        const char* wb = W + (size_t)kr * (64 * 6144) + hp * 128;
        const unsigned st = lds0 + (kt & 3) * K1_STAGE;
#pragma unroll
        for (int pp = 0; pp < 4; ++pp)
            if (pp >= lo && pp < hi) glds16(voffX[pp], xb, st + (4 * lw + pp) * 1024);
#pragma unroll
        for (int sec = 0; sec < 3; ++sec)
#pragma unroll
            for (int pp = 0; pp < 2; ++pp)
                if (4 + 2 * sec + pp >= lo && 4 + 2 * sec + pp < hi)
                    glds16(voffW[pp], wb + sec * 2048, st + 16384 + sec * 8192 + (2 * lw + pp) * 1024);
    };
    DG(13);
    if (w < 4) { issue(0, 0, 5); issue(1, 0, 5); }
    else { issue(0, 5, 10); issue(1, 5, 10); }
    DG(14);
    int w2 = w;
    asm volatile("" : "+s"(w2));

    if (w2 >= 4) {
#define WAITN_BAR(N) asm volatile("s_waitcnt vmcnt(%0) lgkmcnt(0)\n\ts_barrier" :: "n"(N) : "memory")
        issue(2, 0, K1_LA);
        asm volatile("s_waitcnt vmcnt(%0)" :: "n"(5 + K1_LA) : "memory");
#pragma unroll 1
        for (int kt = 0; kt < 13; ++kt) {
            WAITN_BAR(10 + K1_LA);
            issue(kt + 2, K1_LA, 10);
            issue(kt + 3, 0, K1_LA);
        }
        WAITN_BAR(10 + K1_LA);
        issue(15, K1_LA, 10);
        WAIT_BAR(10);
        WAIT_BAR(0);
        if (lw == 0 && lane == 0) *(unsigned*)(ws + WS_CNT + b * 256) = 0u;
        DG_END(5);
        return;
    }

    const int wm = w >> 1, wn = w & 1;
    const int r = lane & 31, hh = lane >> 5;
    const int g = lane >> 4, q = (lane & 15) >> 2, p = lane & 3;
    const int xoff0 = (64 * wm + r) * 128 + ((hh ^ ((r >> 1) & 7)) << 4);
    const int woff0 = 16384 + (8 * hh + q) * 128 + (((4 * wn + 2 * (g & 1) + (p >> 1)) ^ ((q >> 1) << 2)) << 4) + (p & 1) * 8;

    f32x16 acc[2][3];
#pragma unroll
    for (int m = 0; m < 2; ++m)
#pragma unroll
        for (int s = 0; s < 3; ++s) acc[m][s] = f32x16{};
    bf16x8 xf[2][2];
    s16x4 wl[2][3], wh[2][3];
#define K1_WF(f, sec) cat8(wl[f][sec], wh[f][sec])
#define K1_STEP(F, st, t, BAR_HERE) do { \
        const lds_ptr xp_ = (st) + (xoff0 ^ ((t) << 5)); const lds_ptr wp_ = (st) + woff0 + (t) * 2048; \
        acc[0][0] = MFMA32(K1_WF(F, 0), xf[F][0], acc[0][0]); SBAR(); \
        if (BAR_HERE) { WAIT_BAR(0); SBAR(); } \
        xf[F ^ 1][0] = lds_rd128(xp_); wl[F ^ 1][0] = lds_tr(wp_); wh[F ^ 1][0] = lds_tr(wp_ + 512); SBAR(); \
        acc[0][1] = MFMA32(K1_WF(F, 1), xf[F][0], acc[0][1]); SBAR(); \
        wl[F ^ 1][1] = lds_tr(wp_ + 8192); wh[F ^ 1][1] = lds_tr(wp_ + 8192 + 512); SBAR(); \
        acc[0][2] = MFMA32(xf[F][0], K1_WF(F, 2), acc[0][2]); SBAR(); \
        wl[F ^ 1][2] = lds_tr(wp_ + 16384); wh[F ^ 1][2] = lds_tr(wp_ + 16384 + 512); xf[F ^ 1][1] = lds_rd128(xp_ + 4096); SBAR(); \
        acc[1][0] = MFMA32(K1_WF(F, 0), xf[F][1], acc[1][0]); SBAR(); \
        acc[1][1] = MFMA32(K1_WF(F, 1), xf[F][1], acc[1][1]); \
        acc[1][2] = MFMA32(xf[F][1], K1_WF(F, 2), acc[1][2]); SBAR(); \
    } while (0)

    WAIT_BAR(5);
    DG(3);
    {
        const lds_ptr xp_ = lds + xoff0; const lds_ptr wp_ = lds + woff0;
        xf[0][0] = lds_rd128(xp_); xf[0][1] = lds_rd128(xp_ + 4096);
#pragma unroll
        for (int sec = 0; sec < 3; ++sec) { wl[0][sec] = lds_tr(wp_ + sec * 8192); wh[0][sec] = lds_tr(wp_ + sec * 8192 + 512); }
    }
    SBAR();
#pragma unroll K1U
    for (int kt = 0; kt < 15; ++kt) {
        const lds_ptr st = lds + (kt & 3) * K1_STAGE;
        const lds_ptr sn = lds + ((kt + 1) & 3) * K1_STAGE;
        K1_STEP(0, st, 1, false);
        K1_STEP(1, st, 2, false);
        K1_STEP(0, st, 3, false);
        K1_STEP(1, sn, 0, true);
    }
    {
        const lds_ptr st = lds + 3 * K1_STAGE;
        K1_STEP(0, st, 1, false);
        K1_STEP(1, st, 2, false);
        K1_STEP(0, st, 3, false);
#pragma unroll
        for (int m = 0; m < 2; ++m) {
            acc[m][1] = MFMA32(K1_WF(1, 1), xf[1][m], acc[m][1]);
            acc[m][2] = MFMA32(xf[1][m], K1_WF(1, 2), acc[m][2]);
            acc[m][0] = MFMA32(K1_WF(1, 0), xf[1][m], acc[m][0]);
        }
    }
    DG(4);
    {
        const int h = 2 * hp + wn;
        const float qscale = 0.17677669529663687f * 1.4426950408889634f;
        const __amdgpu_buffer_rsrc_t rs = __builtin_amdgcn_make_buffer_rsrc(ws, 0, 32 << 20, 0x00020000);
#pragma unroll
        for (int m = 0; m < 2; ++m) {
            const int T = tb * 4 + wm * 2 + m;
            const unsigned dq = (unsigned)WS_Q + (h * 64 + T) * 2048 + lane * 16;
            const unsigned dk = (unsigned)WS_KV + (h * 64 + T) * 4096 + lane * 16;
            st_sc1(pack8n(acc[m][1], 0), rs, dk);
            st_sc1(pack8n(acc[m][1], 1), rs, dk + 1024);
            st_sc1(pack8n(acc[m][2], 0), rs, dk + 2048);
            st_sc1(pack8n(acc[m][2], 1), rs, dk + 3072);
            st_sc1(pack8(acc[m][0], 0, qscale), rs, dq);
            st_sc1(pack8(acc[m][0], 1, qscale), rs, dq + 1024);
        }
    }
    DG_END(5);
}

constexpr int K2_SLOT = 32768;
constexpr int K2_SF = 65536;
constexpr int K2_CV = 98304;
constexpr int CV_HA = 0, CV_HB = 4160, CV_KV = 8320, CV_BYTES = 8320 + 480 + 4096;
constexpr int K2_LX = K2_CV + 4 * CV_BYTES;
constexpr int K2_FL = K2_LX + 2048;
constexpr int K2_SCR = K2_FL + 256;
constexpr int K2_LDS = K2_SCR + 4096;
constexpr int F_X = 0, F_R = 32, F_W2 = 64, C_OST = 68, C_S1 = 76, C_S2 = 80, C_S3 = 128, C_ZST = 88, F_ZGO = 92,
              C_KST = 96, C_U0 = 100, C_LD = 108, F_KGO = 116, C_END = 120, C_OL = 72, C_HF = 84;
#define NG_IN 0
#define GATE_WORD C_END
#define GATE_VAL 0
#define X_NZ 4
#define X_NW3 8
#define ZP_GATE 4u
#define ZL_GATE 4
#define ZL_AT 31
#define Z_SLEEP0 0
#define O_SLEEP 0
#define Z_SLEEP 8
#define OWIN 16
#define OW0 8
#define WD 2
#define W3_AT 10
#define X_NOLDW 0
#define X_NOLDO 0
#define X_NOCHK 0
#define KROT (nt * 2)
#define NG_LAST 0
#define PACE_IN "s_nop 7"
#define PACE_OUT "s_nop 1"
#define P3_PRIO 1
#define TOP_SLEEP 2
#define A_AUX 16
#define B_AUX 16
#define B_LDS 0
#define TL 8
#define XR_AT 62
#define W3_AUX 0
#define W3M 13
#define A16 1
#define SEL4 1
#define KV_DOT 1
#define ZE_PRIO 3
#define P3_T1 99
#define P3_T2 99
#define AUX_PRIO 2

__global__ __launch_bounds__(768, 3) void k2_attn(char* __restrict__ ws, const char* __restrict__ Aq,
                                                  const char* __restrict__ Ak, const char* __restrict__ Av,
                                                  const char* __restrict__ shiftk, const char* __restrict__ ssmk,
                                                  const unsigned* __restrict__ ssmD, const char* __restrict__ Wo,
                                                  char* __restrict__ out) {
    __shared__ __attribute__((aligned(16))) char lds_raw[K2_LDS];
    const lds_ptr lds = (lds_ptr)lds_raw;
    const lds_ptr fl = lds + K2_FL;
    const unsigned lds0 = (unsigned)(uintptr_t)lds;
    const int tid = threadIdx.x, lane = tid & 63;
    const int w = __builtin_amdgcn_readfirstlane(tid >> 6);
    const int b = blockIdx.x, xcd = b & 7, idx = b >> 3;
    const int h = xcd * 4 + (idx >> 3), qb = idx & 7;
    const int tt = xcd * 2 + (idx >> 4), nt = idx & 15;
    const char* kvg = ws + WS_KV + (size_t)h * 64 * 4096;
    const __amdgpu_buffer_rsrc_t rs = __builtin_amdgcn_make_buffer_rsrc(ws, 0, 32 << 20, 0x00020000);
    const __amdgpu_buffer_rsrc_t ro = __builtin_amdgcn_make_buffer_rsrc(out, 0, 8 << 20, 0x00020000);
    gu32* const cnt = (gu32*)(ws + WS_CNT);
    auto signal = [&]() {
        (void)__hip_atomic_fetch_add(cnt + ((lane & 31) * 8 + (lane < 32 ? xcd : qb)) * 64, 1u, RLX_AGENT);
    };
    const unsigned wvoff = (unsigned)(lane >> 3) * 2048u + nt * 128 + (((lane & 7) ^ (((lane >> 4) & 1) << 2)) << 4);
    const int krot = KROT;
    auto wsrc = [&](int hf, int lp) { return Wo + (size_t)(hf * 64 + ((lp + krot * 2) & 63)) * 16384; };
    DG_DECL(1)
    const unsigned voff = lane * 16;
    const int cw = w & 3;
    auto issue = [&](int st) {
        const char* src = kvg + (st * 4 + cw) * 4096;
        const unsigned dst = lds0 + (st & 1) * K2_SLOT + cw * 4096;
#pragma unroll
        for (int i = 0; i < 4; ++i) glds16(voff, src + i * 1024, dst + i * 1024);
#pragma unroll
        for (int i = 0; i < 4; ++i) glds16(voff, src + 131072 + i * 1024, dst + 16384 + i * 1024);
    };
    if (w >= 8) {
        __builtin_amdgcn_s_sleep(TOP_SLEEP);
#pragma unroll
        for (int i = 2; i < 8; ++i)
            glds16(voff, kvg + (i & 1) * 131072 + (i >> 1) * 4096 + cw * 1024, lds0 + (i & 1) * 16384 + (i >> 1) * 4096 + cw * 1024);
        RAW_BAR();
        issue(1);
        WAIT_BAR(8);
    } else {
        const int tq = cw * 2 + (w >> 2);
        const char* qg = ws + WS_Q + (size_t)(h * 64 + qb * 8 + tq) * 2048;
        glds16(voff, kvg + (w >> 2) * 131072 + cw * 1024, lds0 + (w >> 2) * 16384 + cw * 1024);
        glds16(voff, qg, lds0 + K2_SF + tq * 2048);
        glds16(voff, qg + 1024, lds0 + K2_SF + tq * 2048 + 1024);
    }
    int w2 = w;
    asm volatile("" : "+s"(w2));

    if (w2 >= 8) {
        __builtin_amdgcn_s_setprio(3);
        const int ch = xcd * 128 + idx * 4 + cw;
        const lds_ptr cv = lds + K2_CV + cw * CV_BYTES;
        {
            const u32x4 z4 = {0u, 0u, 0u, 0u};
            if (lane < 2) *(LAS u32x4*)(cv + CV_HA + lane * 16) = z4;
            if (lane < 30) *(LAS u32x4*)(cv + CV_KV + lane * 16) = z4;
            if (cw == 0 && lane < 16) *(LAS u32x4*)(fl + lane * 16) = z4;
        }
        const int c0 = xcd * 128 + cw * 32, cq = lane & 7, rg = lane >> 3;
        const int row0 = idx * 64 + rg * 8;
        const unsigned roff = (unsigned)row0 * 2048u + (c0 + cq * 4) * 2;
        const unsigned toffs = (unsigned)(c0 + (lane >> 3)) * 4096u + idx * 128;
        u32x4 sk[2];
        sk[0] = gld128((unsigned)(c0 + cq * 4) * 8u, shiftk); sk[1] = gld128((unsigned)(c0 + cq * 4) * 8u, shiftk + 16);
        unsigned dd = gld32((unsigned)(ch >> 1) * 4u, ssmD);
        auto stage = [&](const char* src, int dst) {
            const unsigned sbase = (unsigned)(idx * 64 + cw * 16 + (lane >> 4)) * 2048u + xcd * 256;
#pragma unroll
            for (int i = 0; i < 4; ++i)
                glds16(sbase + (((lane & 15) ^ (((2 * cw + (i >> 1)) & 3) << 2)) << 4), src + i * 8192, lds0 + dst + cw * 4096 + i * 1024);
        };
        auto blank = [&](unsigned off) {
            const u32x4 e4 = {EMPTY, EMPTY, EMPTY, EMPTY};
            CFENCE();
            st_sc1(e4, rs, off + voff); st_sc1(e4, rs, off + 1024 + voff);
            CFENCE();
        };
        WAIT_BAR(3);
        DG(12);
        issue(2);
        stage(Ak, K2_SF);
        glds16((unsigned)(idx * 64 - (idx ? 4 : 0) + (lane >> 4)) * 2048u + xcd * 256 + (((lane & 15) ^ 12) << 4), Ak, lds0 + K2_SCR + cw * 1024);
        blank((unsigned)WS_O + (unsigned)((qb * 8 + cw * 2) * 32 + h) * 2048u);
        WAIT_BAR(7);
        DG(9);
        issue(3);
        stage(Av, K2_SF + 16384);
        blank((unsigned)WS_O + (unsigned)((qb * 8 + cw * 2 + 1) * 32 + h) * 2048u);
        WAIT_BAR(6);
        DG(3);
        issue(4);
#pragma unroll
        for (int i = 0; i < 4; ++i) glds16(voff, ssmk + (size_t)ch * 4096 + i * 1024, lds0 + K2_CV + cw * CV_BYTES + CV_HA + 32 + i * 1024);
        blank((unsigned)WS_ZT + ch * 4096); blank((unsigned)WS_ZT + ch * 4096 + 2048);
        WAIT_BAR(8);
        DG(5);
        issue(5);
        SBAR();
        asm volatile("" : "+v"(sk[0]), "+v"(sk[1]), "+v"(dd));
        dd = __builtin_amdgcn_readfirstlane(dd);
        {
            u32x2 kr[11], vr[8];
            lds_ptr kb = lds + K2_SF + rg * 2048 + ((cw ^ (rg & 3)) << 6) + cq * 8;
            asm volatile("" : "+v"(kb));
            const lds_ptr hb = lds + (rg ? K2_SF + rg * 2048 - 768 : K2_SCR + cw * 1024 + 256) + ((cw ^ ((rg + 3) & 3)) << 6) + cq * 8;
#pragma unroll
            for (int i = 0; i < 3; ++i) kr[i] = *(const LAS u32x2*)(hb + i * 256);
#pragma unroll
            for (int i = 0; i < 8; ++i) kr[3 + i] = *(const LAS u32x2*)(kb + i * 256);
#pragma unroll
            for (int i = 0; i < 8; ++i) vr[i] = *(const LAS u32x2*)(kb + 16384 + i * 256);
            if (row0 == 0) { kr[0] = u32x2{0u, 0u}; kr[1] = u32x2{0u, 0u}; kr[2] = u32x2{0u, 0u}; }
            const lds_ptr tb = cv + CV_KV + 480;
#pragma unroll
            for (int c = 0; c < 4; ++c) {
                const unsigned s01 = sk[c >> 1][(c & 1) * 2], s23 = sk[c >> 1][(c & 1) * 2 + 1];
                unsigned kp[10];
                float kv[8], vf[8];
#pragma unroll
                for (int j = 0; j < 10; ++j) kp[j] = __builtin_amdgcn_perm(kr[j][c >> 1], kr[j + 1][c >> 1], (c & 1) ? 0x07060302u : 0x05040100u);
                SBAR();
#pragma unroll
                for (int i = 0; i < 8; ++i) asm("v_dot2_f32_bf16 %0, %1, %2, 0" : "=v"(kv[i]) : "v"(kp[i]), "v"(s23));
                SBAR();
#pragma unroll
                for (int i = 0; i < 8; ++i) asm("v_dot2_f32_bf16 %0, %1, %2, %0" : "+v"(kv[i]) : "v"(kp[i + 2]), "v"(s01));
                SBAR();
#pragma unroll
                for (int i = 0; i < 8; ++i) vf[i] = (c & 1) ? bfhi(vr[i][c >> 1]) : bflo(vr[i][c >> 1]);
                SBAR();
#pragma unroll
                for (int i = 0; i < 8; ++i) kv[i] = mul_vv(kv[i], vf[i]);
                u32x4 o4;
#pragma unroll
                for (int i = 0; i < 4; ++i) o4[i] = cvtpk(kv[7 - 2 * i], kv[6 - 2 * i]);
                *(LAS u32x4*)(tb + (cq * 4 + c) * 128 + ((rg ^ cq) << 4)) = o4;
            }
#pragma unroll
            for (int i = 0; i < 4; ++i) {
                const u32x4 t = *(const LAS u32x4*)(tb + i * 1024 + lane * 16);
                st_sc1(t, rs, (unsigned)WS_KVR + toffs + i * 32768 + (((lane & 7) ^ (2 * i + (lane >> 5))) << 4));
            }
        }
        CFENCE();
        DG(7);
        WAIT_BAR(4);
        DG(6);
        issue(6);
        asm volatile("s_waitcnt vmcnt(8)" ::: "memory");
        if (lds_add_rtn(fl + C_KST, lane) == 3u) signal();
#pragma unroll
        for (int i = 0; i < 8; ++i)
            glds16(wvoff, wsrc(i >> 2, cw * 4 + (i & 3)), lds0 + K2_SF + (i >> 2) * 16384 + (cw * 4 + (i & 3)) * 1024);
        {
            const u32x4 z4 = {0u, 0u, 0u, 0u};
            if (lane < 2) *(LAS u32x4*)(cv + CV_HB + lane * 16) = z4;
#pragma unroll
            for (int i = 0; i < 4; ++i) {
                const int off = 32 + i * 1024 + lane * 16;
                const u32x4 hv = *(const LAS u32x4*)(cv + CV_HA + off);
                const unsigned pv = *(const LAS unsigned*)(cv + CV_HA + off - 4);
                u32x4 sh;
                sh[0] = __builtin_amdgcn_alignbit(hv[0], pv, 16);
                sh[1] = __builtin_amdgcn_alignbit(hv[1], hv[0], 16);
                sh[2] = __builtin_amdgcn_alignbit(hv[2], hv[1], 16);
                sh[3] = __builtin_amdgcn_alignbit(hv[3], hv[2], 16);
                *(LAS u32x4*)(cv + CV_HB + off) = sh;
            }
        }
        WAIT_BAR(8);
        DG(8);
        issue(7);
#pragma unroll
        for (int i = 0; i < 8; ++i)
            glds16(wvoff, wsrc(idx >> 4, 32 + cw * 8 + i), lds0 + K2_SCR + cw * 1024);
        WAIT_BAR(8);
        DG(11);
        if (cw == 0) { poll_ge(cnt + b * 64, 64u); lds_add(fl + F_KGO, lane); }
        else lds_wait_ge(fl + F_KGO, 1u);
        {
            u32x4 t[4];
#pragma unroll
            for (int i = 0; i < 4; ++i) t[i] = ld_sc1(rs, (unsigned)WS_KVR + ch * 4096 + i * 1024 + voff);
#pragma unroll
            for (int i = 0; i < 4; ++i) *(LAS u32x4*)(cv + CV_KV + 480 + i * 1024 + lane * 16) = t[i];
        }
        const int r1 = lane & 15, q = lane >> 4;
        const int toff = (r1 & 1) ? CV_HA + 2 * (16 + r1 + 9 - 8 * q) : CV_HB + 2 * (16 + r1 + 10 - 8 * q);
        const int boff = CV_KV + 448 + 32 * r1 + 16 * q;
        f32x4 y[8];
        u32x4 tf[8];
        bf16x8 bf[8];
        const lds_ptr bp = cv + boff - 448;
        lds_ptr tp[4];
#pragma unroll
        for (int i = 0; i < 4; ++i) { tp[i] = cv + toff + 1024 * i; asm volatile("" : "+v"(tp[i])); }
#define LDT(bb, a, hf) do { const LAS unsigned* t_ = (const LAS unsigned*)(tp[(a) >> 1] + 64 * (bb) + 512 * ((a) & 1) + 8 * (hf)); \
            tf[a][2 * (hf)] = t_[0]; tf[a][2 * (hf) + 1] = t_[1]; SBAR(); } while (0)
#define LDB(bb, a) do { bf[a] = lds_rd128(bp + 448 - 64 * (bb) + 512 * (a)); SBAR(); } while (0)
#define CM(a, ww) do { asm volatile("v_mfma_f32_16x16x32_bf16 %0, %1, %2, %0" : "+v"(y[(a) + (ww)]) : "v"(tf[a]), "v"(bf[ww])); SBAR(); } while (0)
#define CM0(a, ww) do { asm volatile("v_mfma_f32_16x16x32_bf16 %0, %1, %2, 0" : "=&v"(y[(a) + (ww)]) : "v"(tf[a]), "v"(bf[ww])); SBAR(); } while (0)
#define PC() do { asm volatile(PACE_OUT); SBAR(); } while (0)
#pragma unroll
        for (int a = 0; a < 8; ++a) { if (a < 5) { LDT(0, a, 0); LDT(0, a, 1); } LDB(0, 7 - a); }
#pragma unroll
        for (int i = 0; i < 8; ++i)
            glds16(wvoff, wsrc(i >> 2, 16 + cw * 4 + (i & 3)), lds0 + (i >> 2) * 16384 + (cw * 4 + (i & 3)) * 1024);
#pragma unroll
        for (int i = 0; i < 4; ++i)
            glds16((unsigned)(idx * 64 + cw * 16 + (lane >> 4)) * 2048u + xcd * 256 + (lane & 15) * 16, Aq + i * 8192, lds0 + K2_SCR + cw * 1024);
        RAW_BAR();
        asm volatile("s_waitcnt vmcnt(4)" ::: "memory");
        lds_add(fl + C_S1, lane);
        __builtin_amdgcn_s_setprio(AUX_PRIO);
        const float dch = (ch & 1) ? bfhi(dd) : bflo(dd);
        const lds_ptr kdp = cv + CV_KV + 480 + 32 * r1 + 16 * (q >> 1) + ((q & 1) ? 0 : 8);
        float kd[8][4];
        u32x2 kk;
        unsigned kc0, kc1, zo[8][2];
#define KD_RD(t) do { kk = *(const LAS u32x2*)(kdp + 512 * (t)); SBAR(); } while (0)
#define KD_UP0(t) do { kd[t][0] = bfhi(kk[1]); kd[t][1] = bflo(kk[1]); SBAR(); } while (0)
#define KD_UP1(t) do { kd[t][2] = bfhi(kk[0]); kd[t][3] = bflo(kk[0]); SBAR(); } while (0)
#define KD_MU0(t) do { kd[t][0] = mul_sv(dch, kd[t][0]); kd[t][1] = mul_sv(dch, kd[t][1]); SBAR(); } while (0)
#define KD_MU1(t) do { kd[t][2] = mul_sv(dch, kd[t][2]); kd[t][3] = mul_sv(dch, kd[t][3]); SBAR(); } while (0)
#define KD_CV(t) do { kc0 = cvtpk(kd[t][0], kd[t][1]); kc1 = cvtpk(kd[t][2], kd[t][3]); SBAR(); } while (0)
#define KD_X0(t) do { kd[t][0] = bflo(kc0); kd[t][1] = bfhi(kc0); SBAR(); } while (0)
#define KD_X1(t) do { kd[t][2] = bflo(kc1); kd[t][3] = bfhi(kc1); SBAR(); } while (0)
#define ZF0(t) do { kc0 = cvtpk(y[t][0], y[t][1]); kc1 = cvtpk(y[t][2], y[t][3]); SBAR(); } while (0)
#define ZF1(t) do { y[t][0] = bflo(kc0); y[t][1] = bfhi(kc0); SBAR(); } while (0)
#define ZF2(t) do { y[t][2] = bflo(kc1); y[t][3] = bfhi(kc1); SBAR(); } while (0)
#define ZF3(t) do { y[t][0] = add_vv(y[t][0], kd[t][0]); y[t][1] = add_vv(y[t][1], kd[t][1]); SBAR(); } while (0)
#define ZF4(t) do { y[t][2] = add_vv(y[t][2], kd[t][2]); y[t][3] = add_vv(y[t][3], kd[t][3]); SBAR(); } while (0)
#define ZF5(t) do { zo[t][0] = nz(cvtpk(y[t][0], y[t][1])); zo[t][1] = nz(cvtpk(y[t][2], y[t][3])); SBAR(); } while (0)
#define ZST(pr) do { auto s0 = __builtin_amdgcn_permlane16_swap(zo[2 * (pr)][0], zo[2 * (pr) + 1][0], false, false); \
            auto s1 = __builtin_amdgcn_permlane16_swap(zo[2 * (pr)][1], zo[2 * (pr) + 1][1], false, false); \
            st_sc1(u32x4{s0[0], s1[0], s0[1], s1[1]}, rs, (unsigned)WS_ZT + ch * 4096 + (pr) * 1024 + voff); SBAR(); } while (0)
#define W3(i) do { if ((i) == 0) PC(); } while (0)
#define HF() do { lds_add(fl + C_HF, lane); SBAR(); } while (0)
        CM0(0, 7); LDB(1, 7); CM0(0, 6); LDT(0, 5, 0); CM0(0, 5); LDT(0, 5, 1); CM0(0, 4); LDT(0, 6, 0); CM0(0, 3); LDT(0, 6, 1); CM0(0, 2); LDT(0, 7, 0); CM0(0, 1); LDT(0, 7, 1); CM0(0, 0); KD_RD(0);
        CM(1, 6); LDB(1, 6); CM(1, 5); LDT(1, 0, 0); CM(1, 4); LDT(1, 0, 1); CM(1, 3); PC(); CM(1, 2); PC(); CM(1, 1); KD_UP0(0); CM(1, 0); KD_UP1(0);
        CM(2, 5); LDB(1, 5); CM(2, 4); LDT(1, 1, 0); CM(2, 3); LDT(1, 1, 1); CM(2, 2); KD_MU0(0); CM(2, 1); KD_MU1(0); CM(2, 0); KD_CV(0);
        CM(3, 4); LDB(1, 4); CM(3, 3); LDT(1, 2, 0); CM(3, 2); LDT(1, 2, 1); CM(3, 1); KD_X0(0); CM(3, 0); KD_X1(0);
        CM(4, 3); LDB(1, 3); CM(4, 2); LDT(1, 3, 0); CM(4, 1); LDT(1, 3, 1); CM(4, 0); KD_RD(1);
        CM(5, 2); LDB(1, 2); CM(5, 1); LDT(1, 4, 0); CM(5, 0); LDT(1, 4, 1);
        CM(6, 1); LDB(1, 1); CM(6, 0); PC();
        CM(7, 0); LDB(1, 0);
        CM(0, 7); LDB(2, 7); CM(0, 6); LDT(1, 5, 0); CM(0, 5); LDT(1, 5, 1); CM(0, 4); LDT(1, 6, 0); CM(0, 3); LDT(1, 6, 1); CM(0, 2); LDT(1, 7, 0); CM(0, 1); LDT(1, 7, 1); CM(0, 0); PC();
        CM(1, 6); LDB(2, 6); CM(1, 5); LDT(2, 0, 0); CM(1, 4); LDT(2, 0, 1); CM(1, 3); KD_UP0(1); CM(1, 2); KD_UP1(1); CM(1, 1); KD_MU0(1); CM(1, 0); KD_MU1(1);
        CM(2, 5); LDB(2, 5); CM(2, 4); LDT(2, 1, 0); CM(2, 3); LDT(2, 1, 1); CM(2, 2); KD_CV(1); CM(2, 1); KD_X0(1); CM(2, 0); KD_X1(1);
        CM(3, 4); LDB(2, 4); CM(3, 3); LDT(2, 2, 0); CM(3, 2); LDT(2, 2, 1); CM(3, 1); KD_RD(2); CM(3, 0); PC();
        CM(4, 3); LDB(2, 3); CM(4, 2); LDT(2, 3, 0); CM(4, 1); LDT(2, 3, 1); CM(4, 0); PC();
        CM(5, 2); LDB(2, 2); CM(5, 1); LDT(2, 4, 0); CM(5, 0); LDT(2, 4, 1);
        CM(6, 1); LDB(2, 1); CM(6, 0); KD_UP0(2);
        CM(7, 0); LDB(2, 0);
        CM(0, 7); LDB(3, 7); CM(0, 6); LDT(2, 5, 0); CM(0, 5); LDT(2, 5, 1); CM(0, 4); LDT(2, 6, 0); CM(0, 3); LDT(2, 6, 1); CM(0, 2); LDT(2, 7, 0); CM(0, 1); LDT(2, 7, 1); CM(0, 0); KD_UP1(2);
        CM(1, 6); LDB(3, 6); CM(1, 5); LDT(3, 0, 0); CM(1, 4); LDT(3, 0, 1); CM(1, 3); KD_MU0(2); CM(1, 2); KD_MU1(2); CM(1, 1); KD_CV(2); CM(1, 0); KD_X0(2);
        CM(2, 5); LDB(3, 5); CM(2, 4); LDT(3, 1, 0); CM(2, 3); LDT(3, 1, 1); CM(2, 2); KD_X1(2); CM(2, 1); KD_RD(3); CM(2, 0); PC();
        CM(3, 4); LDB(3, 4); CM(3, 3); LDT(3, 2, 0); CM(3, 2); LDT(3, 2, 1); CM(3, 1); PC(); CM(3, 0); KD_UP0(3);
        CM(4, 3); LDB(3, 3); CM(4, 2); LDT(3, 3, 0); CM(4, 1); LDT(3, 3, 1); CM(4, 0); KD_UP1(3);
        CM(5, 2); LDB(3, 2); CM(5, 1); LDT(3, 4, 0); CM(5, 0); LDT(3, 4, 1);
        CM(6, 1); LDB(3, 1); CM(6, 0); KD_MU0(3);
        CM(7, 0); LDB(3, 0);
        CM(0, 7); LDB(4, 7); CM(0, 6); LDT(3, 5, 0); CM(0, 5); LDT(3, 5, 1); CM(0, 4); LDT(3, 6, 0); CM(0, 3); LDT(3, 6, 1); CM(0, 2); LDT(3, 7, 0); CM(0, 1); LDT(3, 7, 1); CM(0, 0); KD_MU1(3);
        CM(1, 6); LDB(4, 6); CM(1, 5); LDT(4, 0, 0); CM(1, 4); LDT(4, 0, 1); CM(1, 3); KD_CV(3); CM(1, 2); KD_X0(3); CM(1, 1); KD_X1(3); CM(1, 0); KD_RD(4);
        CM(2, 5); LDB(4, 5); CM(2, 4); LDT(4, 1, 0); CM(2, 3); LDT(4, 1, 1); CM(2, 2); PC(); CM(2, 1); PC(); CM(2, 0); KD_UP0(4);
        CM(3, 4); LDB(4, 4); CM(3, 3); LDT(4, 2, 0); CM(3, 2); LDT(4, 2, 1); CM(3, 1); KD_UP1(4); CM(3, 0); KD_MU0(4);
        CM(4, 3); LDB(4, 3); CM(4, 2); LDT(4, 3, 0); CM(4, 1); LDT(4, 3, 1); CM(4, 0); KD_MU1(4);
        CM(5, 2); LDB(4, 2); CM(5, 1); LDT(4, 4, 0); CM(5, 0); LDT(4, 4, 1);
        CM(6, 1); LDB(4, 1); CM(6, 0); KD_CV(4);
        CM(7, 0); LDB(4, 0);
        CM(0, 7); LDB(5, 7); CM(0, 6); LDT(4, 5, 0); CM(0, 5); LDT(4, 5, 1); CM(0, 4); LDT(4, 6, 0); CM(0, 3); LDT(4, 6, 1); CM(0, 2); LDT(4, 7, 0); CM(0, 1); LDT(4, 7, 1); CM(0, 0); KD_X0(4);
        CM(1, 6); LDB(5, 6); CM(1, 5); LDT(5, 0, 0); CM(1, 4); LDT(5, 0, 1); CM(1, 3); KD_X1(4); CM(1, 2); KD_RD(5); CM(1, 1); PC(); CM(1, 0); PC();
        CM(2, 5); LDB(5, 5); CM(2, 4); LDT(5, 1, 0); CM(2, 3); LDT(5, 1, 1); CM(2, 2); KD_UP0(5); CM(2, 1); KD_UP1(5); CM(2, 0); KD_MU0(5);
        CM(3, 4); LDB(5, 4); CM(3, 3); LDT(5, 2, 0); CM(3, 2); LDT(5, 2, 1); CM(3, 1); KD_MU1(5); CM(3, 0); KD_CV(5);
        CM(4, 3); LDB(5, 3); CM(4, 2); LDT(5, 3, 0); CM(4, 1); LDT(5, 3, 1); CM(4, 0); KD_X0(5);
        CM(5, 2); LDB(5, 2); CM(5, 1); LDT(5, 4, 0); CM(5, 0); LDT(5, 4, 1);
        CM(6, 1); LDB(5, 1); CM(6, 0); KD_X1(5);
        CM(7, 0); LDB(5, 0);
        CM(0, 7); LDB(6, 7); CM(0, 6); LDT(5, 5, 0); CM(0, 5); LDT(5, 5, 1); CM(0, 4); LDT(5, 6, 0); CM(0, 3); LDT(5, 6, 1); CM(0, 2); LDT(5, 7, 0); CM(0, 1); LDT(5, 7, 1); CM(0, 0); KD_RD(6);
        CM(1, 6); LDB(6, 6); CM(1, 5); LDT(6, 0, 0); CM(1, 4); LDT(6, 0, 1); CM(1, 3); PC(); CM(1, 2); PC(); CM(1, 1); KD_UP0(6); CM(1, 0); KD_UP1(6);
        CM(2, 5); LDB(6, 5); CM(2, 4); LDT(6, 1, 0); CM(2, 3); LDT(6, 1, 1); CM(2, 2); KD_MU0(6); CM(2, 1); KD_MU1(6); CM(2, 0); KD_CV(6);
        CM(3, 4); LDB(6, 4); CM(3, 3); LDT(6, 2, 0); CM(3, 2); LDT(6, 2, 1); CM(3, 1); KD_X0(6); CM(3, 0); KD_X1(6);
        CM(4, 3); LDB(6, 3); CM(4, 2); LDT(6, 3, 0); CM(4, 1); LDT(6, 3, 1); CM(4, 0); KD_RD(7);
        CM(5, 2); LDB(6, 2); CM(5, 1); LDT(6, 4, 0); CM(5, 0); LDT(6, 4, 1);
        CM(6, 1); LDB(6, 1); CM(6, 0); PC();
        CM(7, 0); LDB(6, 0);
        CM(0, 7); LDB(7, 7); CM(0, 6); LDT(6, 5, 0); CM(0, 5); LDT(6, 5, 1); CM(0, 4); LDT(6, 6, 0); CM(0, 3); LDT(6, 6, 1); CM(0, 2); LDT(6, 7, 0); CM(0, 1); LDT(6, 7, 1); CM(0, 0); PC();
        CM(1, 6); LDB(7, 6); CM(1, 5); LDT(7, 0, 0); CM(1, 4); LDT(7, 0, 1); CM(1, 3); KD_UP0(7); CM(1, 2); KD_UP1(7); CM(1, 1); KD_MU0(7); CM(1, 0); KD_MU1(7);
        CM(2, 5); LDB(7, 5); CM(2, 4); LDT(7, 1, 0); CM(2, 3); LDT(7, 1, 1); CM(2, 2); KD_CV(7); CM(2, 1); KD_X0(7); CM(2, 0); KD_X1(7);
        CM(3, 4); LDB(7, 4); CM(3, 3); LDT(7, 2, 0); CM(3, 2); LDT(7, 2, 1); CM(3, 1); PC(); CM(3, 0); PC();
        CM(4, 3); LDB(7, 3); CM(4, 2); LDT(7, 3, 0); CM(4, 1); LDT(7, 3, 1); CM(4, 0); PC();
        CM(5, 2); LDB(7, 2); CM(5, 1); LDT(7, 4, 0); CM(5, 0); LDT(7, 4, 1);
        CM(6, 1); LDB(7, 1); CM(6, 0); PC();
        CM(7, 0); LDB(7, 0);
        CM(0, 3); LDT(7, 5, 0); CM(0, 2); LDT(7, 5, 1); CM(1, 2); LDT(7, 6, 0); CM(1, 1); LDT(7, 6, 1); CM(2, 1); LDT(7, 7, 0); CM(2, 0); LDT(7, 7, 1);
        CM(3, 0); HF(); CM(0, 1); PC(); CM(0, 0); W3(0); CM(1, 0); ZF0(2); W3(1); CM(0, 5); ZF1(2); W3(2); CM(0, 4); ZF2(2); W3(3);
        CM(1, 4); ZF3(2); W3(4); CM(1, 3); ZF4(2); W3(5); CM(2, 3); ZF5(2); W3(6); CM(2, 2); ZF0(3); W3(7); CM(3, 2); ZF1(3);  CM(3, 1); ZF2(3);
        CM(4, 1); ZF3(3);  CM(4, 0); ZF4(3);  CM(5, 0); ZF5(3);  CM(0, 7); ZST(1);  CM(0, 6); ZF0(0);  CM(1, 6); ZF1(0);
        CM(1, 5); ZF2(0);  CM(2, 5); ZF3(0);  CM(2, 4); ZF4(0);  CM(3, 4); ZF5(0);  CM(3, 3); ZF0(1);  CM(4, 3); ZF1(1);
        CM(4, 2); ZF2(1);  CM(5, 2); ZF3(1);  CM(5, 1); ZF4(1);  CM(6, 1); ZF5(1);  CM(6, 0); ZST(0);  CM(7, 0); ZF0(4);
        DG(4);
        ZF1(4); ZF2(4); ZF3(4); ZF4(4); ZF5(4); ZF0(5); ZF1(5); ZF2(5); ZF3(5); ZF4(5); ZF5(5); ZST(2); ZF0(6); ZF1(6); ZF2(6); ZF3(6); ZF4(6); ZF5(6); ZF0(7); ZF1(7); ZF2(7); ZF3(7); ZF4(7); ZF5(7); ZST(3);
        DG(10);
        __builtin_amdgcn_s_setprio(3);
        while ((unsigned)__builtin_amdgcn_readfirstlane(*(volatile LAS unsigned*)(fl + C_OL)) < ZL_GATE) __builtin_amdgcn_s_sleep(4);
        CFENCE();
        const int arow = idx * 64 + (cw >> 1) * 32 + rg * 4, acol = xcd * 256 + (cw & 1) * 128 + cq * 16;
        u32x4 qr[4];
#pragma unroll
        for (int i = 0; i < 4; ++i) qr[i] = gld128((unsigned)(arow * 2048 + acol), Aq + i * 2048);
        {
            const lds_ptr tr = cv + CV_KV + 480;
            u32x4 t[4];
            const int ck = (cw >> 1) * 4 + ((lane ^ ((lane >> 5) * 2)) & 3);
            const unsigned zb = (unsigned)WS_ZT + (unsigned)(xcd * 128 + (cw & 1) * 64 + (lane >> 4) * 4) * 4096u + (idx >> 3) * 1024 +
                                ((idx & 3) * 4 + ((idx >> 2) & 1) * 16 + (ck >> 1) + (ck & 1) * 32) * 16;
#pragma unroll 1
            for (unsigned spins = 0; spins < (1u << 20); ++spins) {
#pragma unroll
                for (int i = 0; i < 4; ++i) t[i] = ld_sc1s(rs, zb + ((((lane >> 2) & 3) ^ i) << 12), i * 65536);
                if (!missing(mx4(mx4(mx8(t[0], t[1]), t[2]), t[3]))) break;
                __builtin_amdgcn_s_sleep(Z_SLEEP);
            }
            DG(13);
            __builtin_amdgcn_s_setprio(ZE_PRIO);
#pragma unroll
            for (int i = 0; i < 4; ++i) *(LAS u32x4*)(tr + i * 1024 + lane * 16) = t[i];
#pragma unroll
            for (int i = 0; i < 4; ++i) asm volatile("" : "+v"(qr[i]));
            u32x2 zc[8];
#pragma unroll
            for (int c = 0; c < 8; ++c)
                zc[c] = *(const LAS u32x2*)(tr + cq * 512 + (c >> 2) * 256 + ((((c & 3) * 4 + (rg >> 1)) ^ (cq * 2)) << 4) + (rg & 1) * 8);
#pragma unroll
            for (int i = 0; i < 4; ++i) {
                u32x4 o;
#pragma unroll
                for (int c = 0; c < 4; ++c) {
                    const unsigned z0 = zc[2 * c][i >> 1], z1 = zc[2 * c + 1][i >> 1];
                    o[c] = cvtpk(mul_vv(bflo(qr[i][c]), (i & 1) ? bfhi(z0) : bflo(z0)), mul_vv(bfhi(qr[i][c]), (i & 1) ? bfhi(z1) : bflo(z1)));
                }
                __builtin_amdgcn_raw_buffer_store_b128(o, ro, arow * 4096 + acol, i * 4096, AUX_SC1);
            }
        }
        DG_END(14);
        return;
    }

    const int half = w >> 2;
    const int TA = qb * 8 + (w & 3) * 2 + half, TB = TA ^ 1;
    bf16x8 sel;
    {
        const short one = (((lane >> 2) & 1) == ((lane >> 4) & 1) && (lane & 3) == 0) ? (short)0x3f80 : (short)0;
#pragma unroll
        for (int i = 0; i < 8; ++i) sel[i] = one;
    }
    f32x16 oA = f32x16{}, oB = f32x16{};
    f32x4 lA = f32x4{}, lB = f32x4{};
    f32x16 negm, sA, sB;
    lds_ptr lp = lds + half * 16384 + lane * 16;
    bf16x8 k0[2], k1[2], v0[2], v1[2];
    u32x4 p0, p1 = {0u, 0u, 0u, 0u};
#define EX(X, i) X[i] = __builtin_amdgcn_exp2f(X[i])
    lds_ptr qpa = lds + K2_SF + (TA & 7) * 2048 + lane * 16, qpb = lds + K2_SF + (TB & 7) * 2048 + lane * 16;
    asm volatile("" : "+v"(qpa), "+v"(qpb), "+v"(lp), "+v"(sel), "+v"(oA), "+v"(oB), "+v"(lA), "+v"(lB), "+v"(p1));
    WAIT_BAR(0);
    DG(3);
    const bf16x8 qa0 = lds_rd128(qpa), qb0 = lds_rd128(qpb);
    const bf16x8 x0 = lds_rd128(lds + lane * 16), x1 = lds_rd128(lds + lane * 16 + 1024);
    const bf16x8 qa1 = lds_rd128(qpa + 1024), qb1 = lds_rd128(qpb + 1024);
    {
        k0[0] = lds_rd128(lp); k1[0] = lds_rd128(lp + 1024);
        v0[0] = lds_rd128(lp + 2048); v1[0] = lds_rd128(lp + 3072);
        sA = MFMA32(x0, qa0, f32x16{});
        sB = MFMA32(x0, qb0, f32x16{});
        sA = MFMA32(x1, qa1, sA);
        sB = MFMA32(x1, qb1, sB);
        float m = fmx(sA[0], sA[1]), m2 = fmx(sB[0], sB[1]);
#pragma unroll
        for (int i = 2; i < 16; i += 2) m = fmx(fmx(m, sA[i]), sA[i + 1]);
#pragma unroll
        for (int i = 2; i < 16; i += 2) m2 = fmx(fmx(m2, sB[i]), sB[i + 1]);
        m = fmx(m, m2);
        auto rr = __builtin_amdgcn_permlane32_swap(__float_as_uint(m), __float_as_uint(m), false, false);
        m = fmx(__uint_as_float(rr[0]), __uint_as_float(rr[1]));
#pragma unroll
        for (int i = 0; i < 16; ++i) negm[i] = -m;
        sA = MFMA32(k0[0], qa0, negm);
        sA = MFMA32(k1[0], qa1, sA);
        v1[1] = v1[0];
        SBAR();
        RAW_BAR();
        SBAR();
        EX(sA, 0); EX(sA, 1);
    }
    DG(10);
#define SUM16(l, p) asm volatile("v_mfma_f32_16x16x32_bf16 %0, %1, %2, %0" : "+v"(l) : "v"(sel), "v"(p))
#define HALF(C, oC, lC, N, qx, qy, ka, kb, oP, lP, vprev, vcur0, PRE) do { \
        oP = MFMA32(vprev, PB(p1), oP); SBAR(); \
        PRE; EX(C, 2); EX(C, 3); EX(C, 4); SBAR(); \
        N = MFMA32(ka, qx, negm); SBAR(); \
        EX(C, 5); EX(C, 6); EX(C, 7); SBAR(); \
        SUM16(lP, p1); SBAR(); \
        p0[0] = cvtpk(C[0], C[1]); p0[1] = cvtpk(C[2], C[3]); SBAR(); \
        N = MFMA32(kb, qy, N); SBAR(); \
        p0[2] = cvtpk(C[4], C[5]); p0[3] = cvtpk(C[6], C[7]); SBAR(); EX(C, 8); EX(C, 9); SBAR(); \
        oC = MFMA32(vcur0, PB(p0), oC); SBAR(); \
        EX(C, 10); EX(C, 11); EX(C, 12); SBAR(); \
        SUM16(lC, p0); SBAR(); \
        EX(C, 13); EX(C, 14); EX(C, 15); SBAR(); \
        p1[0] = cvtpk(C[8], C[9]); p1[1] = cvtpk(C[10], C[11]); p1[2] = cvtpk(C[12], C[13]); p1[3] = cvtpk(C[14], C[15]); SBAR(); \
        EX(N, 0); EX(N, 1); SBAR(); \
    } while (0)
#define RD4(Y, nx) do { v1[Y] = lds_rd128((nx) + 3072); v0[Y] = lds_rd128((nx) + 2048); \
        k1[Y] = lds_rd128((nx) + 1024); k0[Y] = lds_rd128(nx); } while (0)
#define KT(X, nx, PRE0) do { \
        HALF(sA, oA, lA, sB, qb0, qb1, k0[X], k1[X], oB, lB, v1[X ^ 1], v0[X], PRE0; RD4(X ^ 1, nx)); \
        HALF(sB, oB, lB, sA, qa0, qa1, k0[X ^ 1], k1[X ^ 1], oA, lA, v1[X], v0[X], (void)0); \
    } while (0)
    SBAR();
#pragma unroll 1
    for (int st = 0; st < 8; ++st) {
        const lds_ptr cur = lp + (st & 1) * K2_SLOT;
        const lds_ptr nxt = lp + ((st + 1) & 1) * K2_SLOT;
        PRIO(2);
        KT(0, cur + 4096, (void)0);
        PRIO(1);
        KT(1, cur + 8192, (void)0);
        PRIO(0);
        KT(0, cur + 12288, (void)0);
        PRIO(3);
        KT(1, nxt, RAW_BAR(); SBAR());
    }
    oB = MFMA32(v1[1], PB(p1), oB);
    SBAR();
    SUM16(lB, p1);
    asm volatile("s_nop 7" : "+v"(oB) :: "memory");
    SBAR();
    DG(4);
    {
        const lds_ptr xo = lds + K2_SLOT + w * 4096 + lane * 16;
        *(LAS float*)(lds + K2_LX + w * 256 + lane * 4) = lB[0];
#pragma unroll
        for (int i = 0; i < 4; ++i) *(LAS f32x4*)(xo + i * 1024) = f32x4{oB[4 * i], oB[4 * i + 1], oB[4 * i + 2], oB[4 * i + 3]};
        lds_add(fl + F_X + w * 4, lane);
        lds_add(fl + C_END + half * 4, lane);
        lds_wait_ge(fl + F_X + (w ^ 4) * 4, 1u);
        const lds_ptr xi = lds + K2_SLOT + (w ^ 4) * 4096 + lane * 16;
        f32x4 t[4];
        const float lx = *(const LAS float*)(lds + K2_LX + (w ^ 4) * 256 + lane * 4);
#pragma unroll
        for (int i = 0; i < 4; ++i) t[i] = *(const LAS f32x4*)(xi + i * 1024);
        SBAR();
        lA[0] += lx;
#pragma unroll
        for (int i = 0; i < 16; ++i) { float o = oA[i]; acc_vv(o, t[i >> 2][i & 3]); oA[i] = o; }
    }
    DG(9);
    const float l = lA[0];
    u32x4 on0, on1;
    auto finish = [&](float ls) {
        float rl = __builtin_amdgcn_rcpf(ls);
        asm volatile("s_nop 1" : "+v"(rl));
        f32x16 o;
#pragma unroll
        for (int i = 0; i < 16; ++i) o[i] = mul_vv(oA[i], rl);
        on0 = nz4(pack8n(o, 0)); on1 = nz4(pack8n(o, 1));
    };
    finish(l);
    {
        float t0, t1;
        asm("v_mul_f32_e32 %0, 0, %2\n\tv_mul_f32_e32 %1, 0, %3\n\tv_fmac_f32_e32 %0, 0, %4\n\tv_fmac_f32_e32 %1, 0, %5\n\tv_fmac_f32_e32 %0, 0, %6\n\tv_fmac_f32_e32 %1, 0, %7\n\tv_fmac_f32_e32 %0, 0, %8\n\tv_fmac_f32_e32 %1, 0, %9\n\t"
            "v_fmac_f32_e32 %0, 0, %10\n\tv_fmac_f32_e32 %1, 0, %11\n\tv_fmac_f32_e32 %0, 0, %12\n\tv_fmac_f32_e32 %1, 0, %13\n\tv_fmac_f32_e32 %0, 0, %14\n\tv_fmac_f32_e32 %1, 0, %15\n\tv_fmac_f32_e32 %0, 0, %16\n\tv_fmac_f32_e32 %1, 0, %17"
            : "=&v"(t0), "=&v"(t1) : "v"(oA[0]), "v"(oA[1]), "v"(oA[2]), "v"(oA[3]), "v"(oA[4]), "v"(oA[5]), "v"(oA[6]), "v"(oA[7]), "v"(oA[8]), "v"(oA[9]), "v"(oA[10]), "v"(oA[11]), "v"(oA[12]), "v"(oA[13]), "v"(oA[14]), "v"(oA[15]));
        const bool ok = (l <= 1.0e30f) && (l >= 1.0e-18f) && (t0 + t1 == 0.f);
        if (__builtin_expect(__any(!ok), 0)) {
            float m = -INFINITY, ls = 0.f;
            oA = f32x16{};
#pragma unroll 1
            for (int t = 0; t < 64; ++t) {
                const char* g = kvg + t * 4096 + lane * 16;
                const bf16x8 a0 = *(const bf16x8*)g, a1 = *(const bf16x8*)(g + 1024);
                const bf16x8 b0 = *(const bf16x8*)(g + 2048), b1 = *(const bf16x8*)(g + 3072);
                f32x16 s = MFMA32(a0, qa0, f32x16{});
                s = MFMA32(a1, qa1, s);
                float tm = s[0];
#pragma unroll
                for (int i = 1; i < 16; ++i) tm = __builtin_fmaxf(tm, s[i]);
                auto rr = __builtin_amdgcn_permlane32_swap(__float_as_uint(tm), __float_as_uint(tm), false, false);
                tm = __builtin_fmaxf(__uint_as_float(rr[0]), __uint_as_float(rr[1]));
                const float mn = __builtin_fmaxf(m, tm);
                const float alpha = __builtin_amdgcn_exp2f(m - mn);
                m = mn;
                float ps = 0.f;
#pragma unroll
                for (int i = 0; i < 16; ++i) { s[i] = __builtin_amdgcn_exp2f(s[i] - mn); ps += s[i]; oA[i] *= alpha; }
                ls = ls * alpha + ps;
                oA = MFMA32(b0, PB(pack8n(s, 0)), oA);
                oA = MFMA32(b1, PB(pack8n(s, 1)), oA);
            }
            auto rr = __builtin_amdgcn_permlane32_swap(__float_as_uint(ls), __float_as_uint(ls), false, false);
            asm volatile("s_nop 7\n\ts_nop 7" : "+v"(oA));
            finish(__uint_as_float(rr[0]) + __uint_as_float(rr[1]));
        }
    }
    u32x4 w3[4];
    {
        const unsigned og = (unsigned)WS_O + (unsigned)(TA * 32 + h) * 2048u + lane * 16;
        st_sc1(on0, rs, og);
        st_sc1(on1, rs, og + 1024);
        lds_add(fl + C_S1, lane);
        const char* s2 = wsrc(half ^ 1, 32 + (w & 3) * 4);
        const char* s3 = wsrc(half, 48 + (w & 3) * 4);
        lds_wait_ge(fl + C_S1, 12u);
        DG(5);
#pragma unroll
        for (int i = 0; i < 4; ++i)
            glds16(wvoff, s2 + i * 16384, lds0 + K2_SLOT + ((w ^ 4) * 4 + i) * 1024);
#pragma unroll
        for (int i = 0; i < 4; ++i) w3[i] = gld128(wvoff, s3 + i * 16384);
    }
    {
        const int half = w & 1;
        const int T3 = tt * 4 + (w >> 1);
        const unsigned og = (unsigned)WS_O + (unsigned)T3 * 65536u + half * 32768 + lane * 16;
        const int r = lane & 31, hh = lane >> 5;
        const int g = lane >> 4, q = (lane & 15) >> 2, p = lane & 3;
        int woff[2];
#pragma unroll
        for (int n = 0; n < 2; ++n) {
            const int c = 4 * (n ^ half) + 2 * (p & 1) + (g & 1);
            woff[n] = (4 * hh + q) * 128 + ((c ^ ((q >> 1) << 2)) << 4) + (p >> 1) * 8;
        }
        f32x16 acc[2] = {f32x16{}, f32x16{}};
        lds_ptr wl[3][2] = {{lds + half * 16384 + woff[0], lds + half * 16384 + woff[1]},
                            {lds + K2_SF + half * 16384 + woff[0], lds + K2_SF + half * 16384 + woff[1]},
                            {lds + K2_CV + half * 2 * CV_BYTES + woff[0], lds + K2_CV + half * 2 * CV_BYTES + woff[1]}};
        asm volatile("" : "+v"(wl[0][0]), "+v"(wl[0][1]), "+v"(wl[1][0]), "+v"(wl[1][1]), "+v"(wl[2][0]), "+v"(wl[2][1]));
#define MS(m) ((m) < 64 - 2 * TL ? (m) >> 1 : 32 - TL + ((m) - (64 - 2 * TL)) % TL)
#define MN(m) ((m) < 64 - 2 * TL ? (m) & 1 : (m) < 64 - TL)
        constexpr int WR = 2 * WD + 2;
        s16x4 wa[WR], wb[WR];
#define LDW(m) do { const int s_ = MS(m), f_ = s_ < 8 ? 1 : s_ < 24 ? 0 : 2; \
            const int o_ = f_ == 2 ? ((s_ >> 2) & 1) * CV_BYTES + (s_ & 3) * 2048 : (f_ ? 0 : ((s_ >> 3) - 1) * K2_SLOT) + (s_ & 7) * 2048; \
            wa[(m) % WR] = lds_tr(wl[f_][MN(m)] + o_); wb[(m) % WR] = lds_tr(wl[f_][MN(m)] + o_ + 1024); SBAR(); } while (0)
#pragma unroll
        for (int m = 0; m < 2 * WD; ++m) LDW(m);
        u32x4 of[OWIN];
#define LDO(i) do { of[(i) % OWIN] = ld_sc1s(rs, og, (((i) + krot) & 31) * 1024); SBAR(); } while (0)
        for (unsigned spins = 0; spins < (1u << 20); ++spins) {
#pragma unroll
            for (int i = 0; i < OW0; ++i) LDO(i);
            unsigned m = mx8(of[0], of[1]);
#pragma unroll
            for (int i = 2; i < OW0; ++i) m = mx4(m, of[i]);
            if (!missing(m)) break;
            DGC(12);
        }
        lds_add(fl + C_S2, lane);
#pragma unroll
        for (int i = OW0; i < OWIN; ++i) LDO(i);
        DG(6);
        __builtin_amdgcn_s_setprio(P3_PRIO);
        SBAR();
        unsigned fv = 0;
#define FLAG_RD(a) do { fv = *(volatile LAS unsigned*)(a); SBAR(); } while (0)
#define FLAG_CHK(a, n) do { if (__builtin_expect((unsigned)__builtin_amdgcn_readfirstlane(fv) < (n), 0)) lds_wait_ge(a, n); CFENCE(); SBAR(); } while (0)
        lds_ptr xo = lds + K2_SF + w * 4096 + lane * 16, xi = lds + K2_SF + (w ^ 1) * 4096 + lane * 16;
        int bo = (T3 * 32 + (lane & 15)) * 4096 + 2048 + nt * 128 + half * 64 + g * 16;
        lds_ptr w3d = lds + K2_CV + (w >> 1) * CV_BYTES + (w & 1) * 4096 + lane * 16;
        asm volatile("" : "+v"(xo), "+v"(xi), "+v"(bo), "+v"(w3d));
        f32x4 xt[4];
        constexpr int M3 = TL > 8 ? 64 - 2 * TL + TL - 8 - 2 * WD : 48 - 2 * WD;
        sfor(std::make_integer_sequence<int, 64>{}, [&](auto mc) __attribute__((always_inline)) {
            constexpr int m = decltype(mc)::value, s = MS(m), n = MN(m);
            constexpr bool first = m < 64 - 2 * TL ? n == 0 : n == 1;
            constexpr bool last = m < 64 - 2 * TL ? n == 1 : n == 0;
            acc[n] = MFMA32(cat8(wa[m % WR], wb[m % WR]), PB(of[s % OWIN]), acc[n]);
            SBAR();
            if constexpr (m == 32 - 2 * WD) FLAG_CHK(fl + C_S2, 8u);
            if constexpr (m == M3) FLAG_CHK(fl + C_S3, 8u);
            if constexpr (m + 2 * WD < 64) LDW(m + 2 * WD);
            if constexpr (first && (s & 1) && s + 1 >= OW0 && s + 1 < 32) {
                if (__builtin_expect(missing(mx8(of[(s + 1) % OWIN], of[(s + 2) % OWIN])), 0)) {
#pragma unroll 1
                    for (unsigned spins = 0; spins < (1u << 20); ++spins) {
                        __builtin_amdgcn_s_sleep(2);
                        LDO(s + 1); LDO(s + 2);
                        DGC(13);
                        if (!missing(mx8(of[(s + 1) % OWIN], of[(s + 2) % OWIN]))) break;
                    }
                }
            }
            if constexpr (last && s + OWIN < 32) LDO(s + OWIN);
            if constexpr (m == 15 - 2 * WD) lds_add(fl + C_U0, lane);
            if constexpr (m == 29 - 2 * WD) FLAG_RD(fl + C_S2);
            if constexpr (m == W3M - 3) FLAG_RD(fl + C_HF);
            if constexpr (m == W3M) FLAG_CHK(fl + C_HF, 4u);
            if constexpr (m >= W3M && m < W3M + 8 && ((m - W3M) & 1) == 0) {
                asm volatile("" : "+v"(w3[((m - W3M) >> 1) & 3]));
                *(LAS u32x4*)(w3d + (((m - W3M) >> 1) & 3) * 1024) = w3[((m - W3M) >> 1) & 3];
            }
            if constexpr (m == W3M + 6) lds_add(fl + C_S3, lane);
            if constexpr (m == M3 - 3) FLAG_RD(fl + C_S3);
            if constexpr (m == 64 - TL - 3) FLAG_RD(fl + C_U0);
            if constexpr (m == 64 - TL) FLAG_CHK(fl + C_U0, 8u);
            if constexpr (m == 64 - TL + 1 || m == 64 - TL + 2) {
                constexpr int i = 2 * (m - (64 - TL + 1));
                *(LAS f32x4*)(xo + i * 1024) = f32x4{acc[1][4 * i], acc[1][4 * i + 1], acc[1][4 * i + 2], acc[1][4 * i + 3]};
                *(LAS f32x4*)(xo + i * 1024 + 1024) = f32x4{acc[1][4 * i + 4], acc[1][4 * i + 5], acc[1][4 * i + 6], acc[1][4 * i + 7]};
            }
            if constexpr (m == 64 - TL + 2) lds_add(fl + F_R + w * 4, lane);
            if constexpr (m == XR_AT - 2) FLAG_RD(fl + F_R + (w ^ 1) * 4);
            if constexpr (m == XR_AT) {
                FLAG_CHK(fl + F_R + (w ^ 1) * 4, 1u);
#pragma unroll
                for (int i = 0; i < 4; ++i) xt[i] = *(const LAS f32x4*)(xi + i * 1024);
            }
            if constexpr (ZL_GATE && last && s + OWIN == ZL_AT) lds_add(fl + C_OL, lane);
            if constexpr (m == 2 * P3_T1 + 1) __builtin_amdgcn_s_setprio(P3_PRIO - 1);
            SBAR();
            if constexpr (m == 31) DG(11);
            if constexpr (m == 47) DG(14);
        });
        asm volatile("s_nop 11" : "+v"(acc[0]));
        DG(7);
#pragma unroll
        for (int i = 0; i < 16; ++i) { float o = xt[i >> 2][i & 3]; acc_vv(o, acc[0][i]); acc[1][i] = o; }
        u32x4 pa = pack8n(acc[1], 0), pb = pack8n(acc[1], 1);
#pragma unroll
        for (int i = 0; i < 4; ++i) {
            auto sw = __builtin_amdgcn_permlane16_swap(pa[i], pb[i], false, false);
            pa[i] = sw[0]; pb[i] = sw[1];
        }
        asm volatile("" :: "v"(acc[0]));
        __builtin_amdgcn_raw_buffer_store_b128(pa, ro, bo, 0, AUX_SC1);
        __builtin_amdgcn_raw_buffer_store_b128(pb, ro, bo, 65536, AUX_SC1);
    }
    DG_END(8);
}

extern "C" void kernel_launch(void* const* d_in, const int* in_sizes, int n_in,
                              void* d_out, int out_size, void* d_ws, size_t ws_size,
                              hipStream_t stream) {
    const void* A_q = d_in[0];
    const void* A_k = d_in[1];
    const void* A_v = d_in[2];
    const void* A_shift = d_in[3];
    const void* A_ssmk = d_in[4];
    const void* A_D = d_in[5];
    const void* B_x = d_in[6];
    const void* B_wqkv = d_in[7];
    const void* B_wo = d_in[8];
    char* ws = (char*)d_ws;
    k1_qkv<<<256, 512, 0, stream>>>((const char*)B_x, (const char*)B_wqkv, ws);
    k2_attn<<<256, 768, 0, stream>>>(ws, (const char*)A_q, (const char*)A_k, (const char*)A_v, (const char*)A_shift,
                                     (const char*)A_ssmk, (const unsigned*)A_D, (const char*)B_wo, (char*)d_out);
}
```

```cpp
#include <hip/hip_runtime.h>
#include <hip/hip_bf16.h>
#include <stdint.h>
#include <utility>

typedef short bf16x8 __attribute__((ext_vector_type(8)));
typedef short s16x4 __attribute__((ext_vector_type(4)));
typedef float f32x16 __attribute__((ext_vector_type(16)));
typedef float f32x4 __attribute__((ext_vector_type(4)));
typedef float f32x2 __attribute__((ext_vector_type(2)));
typedef unsigned u32x4 __attribute__((ext_vector_type(4)));
typedef unsigned u32x2 __attribute__((ext_vector_type(2)));
typedef __bf16 bf16x2_t __attribute__((ext_vector_type(2)));

#define LAS __attribute__((address_space(3)))
typedef LAS char* lds_ptr;
#define DI __device__ __forceinline__
#define SBAR() __builtin_amdgcn_sched_barrier(0)
#define MFMA32(a, b, c) __builtin_amdgcn_mfma_f32_32x32x16_bf16((a), (b), (c), 0, 0, 0)
#define MFMA16(a, b, c) __builtin_amdgcn_mfma_f32_16x16x32_bf16((a), (b), (c), 0, 0, 0)
#define WAIT_BAR(N) asm volatile("s_waitcnt vmcnt(" #N ") lgkmcnt(0)\n\ts_barrier" ::: "memory")
#define LGKM_BAR() asm volatile("s_waitcnt lgkmcnt(0)\n\ts_barrier" ::: "memory")

#define DG_DECL(K)
#define DG(i)
#define DG_END(i)
#define DGC(i)

constexpr int S_LEN = 2048, DM = 1024, NH = 32;

constexpr size_t WS_Q = 0;
constexpr size_t WS_KV = 4u << 20;
constexpr size_t WS_O = 12u << 20;
constexpr size_t WS_KVR = 16u << 20;
constexpr size_t WS_ZT = 20u << 20;
constexpr size_t WS_CNT = 24u << 20;

template <int... I, class F> DI void sfor(std::integer_sequence<int, I...>, F&& f) { (f(std::integral_constant<int, I>{}), ...); }
DI unsigned cvtpk(float lo, float hi) {
    f32x2 v = {lo, hi};
    return __builtin_bit_cast(unsigned, __builtin_convertvector(v, bf16x2_t));
}
DI float bflo(unsigned u) { return __uint_as_float(u << 16); }
DI float bfhi(unsigned u) { return __uint_as_float(u & 0xffff0000u); }

DI void glds16(unsigned voff, const void* sbase, unsigned lds_dst) {
    unsigned sv;
    asm volatile("s_mov_b32 %0, m0\n\ts_mov_b32 m0, %3\n\ts_nop 0\n\tglobal_load_lds_dwordx4 %1, %2\n\ts_mov_b32 m0, %0"
                 : "=&s"(sv) : "v"(voff), "s"(sbase), "s"(lds_dst) : "memory");
}
DI bf16x8 lds_rd128(lds_ptr p) { return *(const LAS bf16x8*)p; }
DI s16x4 lds_tr(lds_ptr p) {
    return __builtin_bit_cast(s16x4, __builtin_amdgcn_ds_read_tr16_b64_v4i16((LAS s16x4*)p));
}
DI bf16x8 cat8(s16x4 a, s16x4 b) { return __builtin_shufflevector(a, b, 0, 1, 2, 3, 4, 5, 6, 7); }
DI u32x4 pack8(const f32x16& x, int s, float sc) {
    u32x4 p;
    p[0] = cvtpk(x[8 * s + 0] * sc, x[8 * s + 1] * sc);
    p[1] = cvtpk(x[8 * s + 2] * sc, x[8 * s + 3] * sc);
    p[2] = cvtpk(x[8 * s + 4] * sc, x[8 * s + 5] * sc);
    p[3] = cvtpk(x[8 * s + 6] * sc, x[8 * s + 7] * sc);
    return p;
}
DI u32x4 pack8n(const f32x16& x, int s) {
    u32x4 p;
    p[0] = cvtpk(x[8 * s + 0], x[8 * s + 1]);
    p[1] = cvtpk(x[8 * s + 2], x[8 * s + 3]);
    p[2] = cvtpk(x[8 * s + 4], x[8 * s + 5]);
    p[3] = cvtpk(x[8 * s + 6], x[8 * s + 7]);
    return p;
}
DI void rnd2(float& a, float& b) { unsigned u = cvtpk(a, b); a = bflo(u); b = bfhi(u); }

#define CONV_PACE()
#define RAW_BAR() asm volatile("s_barrier" ::: "memory")
DI unsigned gld32(unsigned voff, const void* sbase) {
    unsigned r;
    asm volatile("global_load_dword %0, %1, %2" : "=v"(r) : "v"(voff), "s"(sbase) : "memory");
    return r;
}
DI u32x2 gld64(unsigned voff, const void* sbase) {
    u32x2 r;
    asm volatile("global_load_dwordx2 %0, %1, %2" : "=v"(r) : "v"(voff), "s"(sbase) : "memory");
    return r;
}
DI float mul_sv(float s, float v) { float r; asm("v_mul_f32_e32 %0, %1, %2" : "=v"(r) : "s"(s), "v"(v)); return r; }
DI float mul_vv(float a, float b) { float r; asm("v_mul_f32_e32 %0, %1, %2" : "=v"(r) : "v"(a), "v"(b)); return r; }
DI float add_vv(float a, float b) { float r; asm("v_add_f32_e32 %0, %1, %2" : "=v"(r) : "v"(a), "v"(b)); return r; }
DI void acc_vv(float& a, float b) { asm("v_add_f32_e32 %0, %0, %1" : "+v"(a) : "v"(b)); }
DI void fmac_vv(float& acc, float a, float b) { asm("v_fmac_f32_e32 %0, %1, %2" : "+v"(acc) : "v"(a), "v"(b)); }
DI void fmac_sv(float& acc, float s, float v) { asm("v_fmac_f32_e32 %0, %1, %2" : "+v"(acc) : "s"(s), "v"(v)); }
typedef __attribute__((address_space(1))) unsigned gu32;
#define RLX_AGENT __ATOMIC_RELAXED, __HIP_MEMORY_SCOPE_AGENT
#define CFENCE() asm volatile("" ::: "memory")
#define DRAIN() asm volatile("s_waitcnt vmcnt(0)" ::: "memory")
constexpr int AUX_SC1 = 16;
DI u32x4 ld_sc1(__amdgpu_buffer_rsrc_t rs, unsigned off) { return __builtin_amdgcn_raw_buffer_load_b128(rs, (int)off, 0, AUX_SC1); }
DI u32x4 ld_sc1s(__amdgpu_buffer_rsrc_t rs, unsigned off, int soff) { return __builtin_amdgcn_raw_buffer_load_b128(rs, (int)off, soff, AUX_SC1); }
DI void st_sc1(u32x4 v, __amdgpu_buffer_rsrc_t rs, unsigned off) { __builtin_amdgcn_raw_buffer_store_b128(v, rs, (int)off, 0, AUX_SC1); }
constexpr unsigned EMPTY = 0xffffffffu;
DI unsigned nz(unsigned x) { return x < EMPTY - 1u ? x : EMPTY - 1u; }
DI u32x4 nz4(u32x4 v) { return u32x4{nz(v[0]), nz(v[1]), nz(v[2]), nz(v[3])}; }
DI float fmx(float a, float b) { return __builtin_elementwise_maximum(a, b); }
DI unsigned umax(unsigned a, unsigned b) { return a > b ? a : b; }
DI unsigned umax3(unsigned a, unsigned b, unsigned c) { return umax(umax(a, b), c); }
DI unsigned mx4(unsigned m, u32x4 v) { return umax3(umax3(m, v[0], v[1]), v[2], v[3]); }
DI unsigned mx8(u32x4 a, u32x4 b) { return umax(umax3(umax3(umax3(a[0], a[1], a[2]), a[3], b[0]), b[1], b[2]), b[3]); }
DI bool missing(unsigned m) { return __builtin_amdgcn_ballot_w64(m == EMPTY) != 0; }
DI unsigned lds_add_rtn(lds_ptr p, int lane) {
    unsigned old = 0;
    CFENCE();
    if (lane == 0) old = __hip_atomic_fetch_add((LAS unsigned*)p, 1u, __ATOMIC_RELAXED, __HIP_MEMORY_SCOPE_WORKGROUP);
    old = __builtin_amdgcn_readfirstlane(old);
    CFENCE();
    return old;
}
DI void lds_add(lds_ptr p, int lane) {
    unsigned long long sv;
    asm volatile("s_mov_b64 %0, exec\n\ts_mov_b64 exec, 1\n\tds_add_u32 %1, %2\n\ts_mov_b64 exec, %0"
                 : "=&s"(sv) : "v"(p), "v"(1u) : "memory");
}
DI void lds_wait_ge(lds_ptr p, unsigned v) {
    CFENCE();
    while ((unsigned)__builtin_amdgcn_readfirstlane(*(volatile LAS unsigned*)p) < v) __builtin_amdgcn_s_sleep(1);
    CFENCE();
}
DI void poll_ge(gu32* word, unsigned want) {
    CFENCE();
    for (unsigned spins = 0; spins < (1u << 22); ++spins) {
        if ((unsigned)__builtin_amdgcn_readfirstlane(__hip_atomic_load(word, RLX_AGENT)) >= want) break;
        __builtin_amdgcn_s_sleep(1);
    }
    __builtin_amdgcn_fence(__ATOMIC_ACQUIRE, "wavefront");
    CFENCE();
}
#define PB(x) __builtin_bit_cast(bf16x8, x)
#define PRIO(x) do { __builtin_amdgcn_s_setprio(x); SBAR(); } while (0)
DI u32x4 gld128(unsigned voff, const void* sbase) {
    u32x4 r;
    asm volatile("global_load_dwordx4 %0, %1, %2" : "=v"(r) : "v"(voff), "s"(sbase) : "memory");
    return r;
}

#define K1_UNR 15
#define K1_LA 5
#define K1_ROT ((tb & 3) * 4)
constexpr int K1_STAGE = 40960, K1U = K1_UNR;
__global__ __launch_bounds__(512, 2) void k1_qkv(const char* __restrict__ X, const char* __restrict__ W,
                                                 char* __restrict__ ws) {
    __shared__ __attribute__((aligned(16))) char lds_raw[4 * K1_STAGE];
    const lds_ptr lds = (lds_ptr)lds_raw;
    const unsigned lds0 = (unsigned)(uintptr_t)lds;
    const int tid = threadIdx.x, lane = tid & 63;
    const int w = __builtin_amdgcn_readfirstlane(tid >> 6);
    const int b = blockIdx.x, xcd = b & 7, idx = b >> 3;
    const int hp = (xcd >> 1) * 4 + (idx & 3), tb = (xcd & 1) * 8 + (idx >> 2);
    DG_DECL(0)

    const int lw = w & 3;
    const int rot = K1_ROT;
    unsigned voffX[4], voffW[2];
#pragma unroll
    for (int pp = 0; pp < 4; ++pp) {
        const int row = 32 * lw + 8 * pp + (lane >> 3);
        const int ch = (lane & 7) ^ ((row >> 1) & 7);
        voffX[pp] = (unsigned)(tb * 128 + row) * 2048u + ch * 16;
    }
#pragma unroll
    for (int pp = 0; pp < 2; ++pp) {
        const int krow = 16 * lw + 8 * pp + (lane >> 3);
        const int ch = (lane & 7) ^ (((lane >> 4) & 1) << 2);
        voffW[pp] = (unsigned)krow * 6144u + ch * 16;
    }
    auto issue = [&](int kt, int lo, int hi) {
        const int kr = (kt + rot) & 15;
        const char* xb = X + kr * 128;
        const char* wb = W + (size_t)kr * (64 * 6144) + hp * 128;
        const unsigned st = lds0 + (kt & 3) * K1_STAGE;
#pragma unroll
        for (int pp = 0; pp < 4; ++pp)
            if (pp >= lo && pp < hi) glds16(voffX[pp], xb, st + (4 * lw + pp) * 1024);
#pragma unroll
        for (int sec = 0; sec < 3; ++sec)
#pragma unroll
            for (int pp = 0; pp < 2; ++pp)
                if (4 + 2 * sec + pp >= lo && 4 + 2 * sec + pp < hi)
                    glds16(voffW[pp], wb + sec * 2048, st + 16384 + sec * 8192 + (2 * lw + pp) * 1024);
    };
    DG(13);
    if (w < 4) { issue(0, 0, 5); issue(1, 0, 5); }
    else { issue(0, 5, 10); issue(1, 5, 10); }
    DG(14);
    int w2 = w;
    asm volatile("" : "+s"(w2));

    if (w2 >= 4) {
#define WAITN_BAR(N) asm volatile("s_waitcnt vmcnt(%0) lgkmcnt(0)\n\ts_barrier" :: "n"(N) : "memory")
        issue(2, 0, K1_LA);
        asm volatile("s_waitcnt vmcnt(%0)" :: "n"(5 + K1_LA) : "memory");
#pragma unroll 1
        for (int kt = 0; kt < 13; ++kt) {
            WAITN_BAR(10 + K1_LA);
            issue(kt + 2, K1_LA, 10);
            issue(kt + 3, 0, K1_LA);
        }
        WAITN_BAR(10 + K1_LA);
        issue(15, K1_LA, 10);
        WAIT_BAR(10);
        WAIT_BAR(0);
        if (lw == 0 && lane == 0) *(unsigned*)(ws + WS_CNT + b * 256) = 0u;
        DG_END(5);
        return;
    }

    const int wm = w >> 1, wn = w & 1;
    const int r = lane & 31, hh = lane >> 5;
    const int g = lane >> 4, q = (lane & 15) >> 2, p = lane & 3;
    const int xoff0 = (64 * wm + r) * 128 + ((hh ^ ((r >> 1) & 7)) << 4);
    const int woff0 = 16384 + (8 * hh + q) * 128 + (((4 * wn + 2 * (g & 1) + (p >> 1)) ^ ((q >> 1) << 2)) << 4) + (p & 1) * 8;

    f32x16 acc[2][3];
#pragma unroll
    for (int m = 0; m < 2; ++m)
#pragma unroll
        for (int s = 0; s < 3; ++s) acc[m][s] = f32x16{};
    bf16x8 xf[2][2];
    s16x4 wl[2][3], wh[2][3];
#define K1_WF(f, sec) cat8(wl[f][sec], wh[f][sec])
#define K1_STEP(F, st, t, BAR_HERE) do { \
        const lds_ptr xp_ = (st) + (xoff0 ^ ((t) << 5)); const lds_ptr wp_ = (st) + woff0 + (t) * 2048; \
        acc[0][0] = MFMA32(K1_WF(F, 0), xf[F][0], acc[0][0]); SBAR(); \
        if (BAR_HERE) { WAIT_BAR(0); SBAR(); } \
        xf[F ^ 1][0] = lds_rd128(xp_); wl[F ^ 1][0] = lds_tr(wp_); wh[F ^ 1][0] = lds_tr(wp_ + 512); SBAR(); \
        acc[0][1] = MFMA32(K1_WF(F, 1), xf[F][0], acc[0][1]); SBAR(); \
        wl[F ^ 1][1] = lds_tr(wp_ + 8192); wh[F ^ 1][1] = lds_tr(wp_ + 8192 + 512); SBAR(); \
        acc[0][2] = MFMA32(xf[F][0], K1_WF(F, 2), acc[0][2]); SBAR(); \
        wl[F ^ 1][2] = lds_tr(wp_ + 16384); wh[F ^ 1][2] = lds_tr(wp_ + 16384 + 512); xf[F ^ 1][1] = lds_rd128(xp_ + 4096); SBAR(); \
        acc[1][0] = MFMA32(K1_WF(F, 0), xf[F][1], acc[1][0]); SBAR(); \
        acc[1][1] = MFMA32(K1_WF(F, 1), xf[F][1], acc[1][1]); \
        acc[1][2] = MFMA32(xf[F][1], K1_WF(F, 2), acc[1][2]); SBAR(); \
    } while (0)

    WAIT_BAR(5);
    DG(3);
    {
        const lds_ptr xp_ = lds + xoff0; const lds_ptr wp_ = lds + woff0;
        xf[0][0] = lds_rd128(xp_); xf[0][1] = lds_rd128(xp_ + 4096);
#pragma unroll
        for (int sec = 0; sec < 3; ++sec) { wl[0][sec] = lds_tr(wp_ + sec * 8192); wh[0][sec] = lds_tr(wp_ + sec * 8192 + 512); }
    }
    SBAR();
#pragma unroll K1U
    for (int kt = 0; kt < 15; ++kt) {
        const lds_ptr st = lds + (kt & 3) * K1_STAGE;
        const lds_ptr sn = lds + ((kt + 1) & 3) * K1_STAGE;
        K1_STEP(0, st, 1, false);
        K1_STEP(1, st, 2, false);
        K1_STEP(0, st, 3, false);
        K1_STEP(1, sn, 0, true);
    }
    {
        const lds_ptr st = lds + 3 * K1_STAGE;
        K1_STEP(0, st, 1, false);
        K1_STEP(1, st, 2, false);
        K1_STEP(0, st, 3, false);
#pragma unroll
        for (int m = 0; m < 2; ++m) {
            acc[m][1] = MFMA32(K1_WF(1, 1), xf[1][m], acc[m][1]);
            acc[m][2] = MFMA32(xf[1][m], K1_WF(1, 2), acc[m][2]);
            acc[m][0] = MFMA32(K1_WF(1, 0), xf[1][m], acc[m][0]);
        }
    }
    DG(4);
    {
        const int h = 2 * hp + wn;
        const float qscale = 0.17677669529663687f * 1.4426950408889634f;
        const __amdgpu_buffer_rsrc_t rs = __builtin_amdgcn_make_buffer_rsrc(ws, 0, 32 << 20, 0x00020000);
#pragma unroll
        for (int m = 0; m < 2; ++m) {
            const int T = tb * 4 + wm * 2 + m;
            const unsigned dq = (unsigned)WS_Q + (h * 64 + T) * 2048 + lane * 16;
            const unsigned dk = (unsigned)WS_KV + (h * 64 + T) * 4096 + lane * 16;
            st_sc1(pack8n(acc[m][1], 0), rs, dk);
            st_sc1(pack8n(acc[m][1], 1), rs, dk + 1024);
            st_sc1(pack8n(acc[m][2], 0), rs, dk + 2048);
            st_sc1(pack8n(acc[m][2], 1), rs, dk + 3072);
            st_sc1(pack8(acc[m][0], 0, qscale), rs, dq);
            st_sc1(pack8(acc[m][0], 1, qscale), rs, dq + 1024);
        }
    }
    DG_END(5);
}

constexpr int K2_SLOT = 32768;
constexpr int K2_SF = 65536;
constexpr int K2_CV = 98304;
constexpr int CV_HA = 0, CV_HB = 4160, CV_KV = 8320, CV_BYTES = 8320 + 480 + 4096;
constexpr int K2_LX = K2_CV + 4 * CV_BYTES;
constexpr int K2_FL = K2_LX + 2048;
constexpr int K2_SCR = K2_FL + 256;
constexpr int K2_LDS = K2_SCR + 4096;
constexpr int F_X = 0, F_R = 32, F_W2 = 64, C_OST = 68, C_S1 = 76, C_S2 = 80, C_S3 = 128, C_ZST = 88, F_ZGO = 92,
              C_KST = 96, C_U0 = 100, C_LD = 108, F_KGO = 116, C_END = 120, C_OL = 72, C_HF = 84;
#define NG_IN 0
#define GATE_WORD C_END
#define GATE_VAL 0
#define X_NZ 4
#define X_NW3 8
#define ZP_GATE 4u
#define ZL_GATE 4
#define ZL_AT 31
#define Z_SLEEP0 0
#define O_SLEEP 0
#define Z_SLEEP 8
#define OWIN 16
#define OW0 8
#define WD 2
#define W3_AT 10
#define X_NOLDW 0
#define X_NOLDO 0
#define X_NOCHK 0
#define KROT (nt * 2)
#define NG_LAST 0
#define PACE_IN "s_nop 7"
#define PACE_OUT "s_nop 1"
#define P3_PRIO 1
#define TOP_SLEEP 2
#define A_AUX 16
#define B_AUX 16
#define B_LDS 0
#define TL 8
#define XR_AT 62
#define W3_AUX 0
#define W3M 13
#define A16 1
#define SEL4 2
#define KV_DOT 1
#define ZE_PRIO 3
#define P3_T1 99
#define P3_T2 99
#define AUX_PRIO 2

__global__ __launch_bounds__(768, 3) void k2_attn(char* __restrict__ ws, const char* __restrict__ Aq,
                                                  const char* __restrict__ Ak, const char* __restrict__ Av,
                                                  const char* __restrict__ shiftk, const char* __restrict__ ssmk,
                                                  const unsigned* __restrict__ ssmD, const char* __restrict__ Wo,
                                                  char* __restrict__ out) {
    __shared__ __attribute__((aligned(16))) char lds_raw[K2_LDS];
    const lds_ptr lds = (lds_ptr)lds_raw;
    const lds_ptr fl = lds + K2_FL;
    const unsigned lds0 = (unsigned)(uintptr_t)lds;
    const int tid = threadIdx.x, lane = tid & 63;
    const int w = __builtin_amdgcn_readfirstlane(tid >> 6);
    const int b = blockIdx.x, xcd = b & 7, idx = b >> 3;
    const int h = xcd * 4 + (idx >> 3), qb = idx & 7;
    const int tt = xcd * 2 + (idx >> 4), nt = idx & 15;
    const char* kvg = ws + WS_KV + (size_t)h * 64 * 4096;
    const __amdgpu_buffer_rsrc_t rs = __builtin_amdgcn_make_buffer_rsrc(ws, 0, 32 << 20, 0x00020000);
    const __amdgpu_buffer_rsrc_t ro = __builtin_amdgcn_make_buffer_rsrc(out, 0, 8 << 20, 0x00020000);
    gu32* const cnt = (gu32*)(ws + WS_CNT);
    auto signal = [&]() {
        (void)__hip_atomic_fetch_add(cnt + ((lane & 31) * 8 + (lane < 32 ? xcd : qb)) * 64, 1u, RLX_AGENT);
    };
    const unsigned wvoff = (unsigned)(lane >> 3) * 2048u + nt * 128 + (((lane & 7) ^ (((lane >> 4) & 1) << 2)) << 4);
    const int krot = KROT;
    auto wsrc = [&](int hf, int lp) { return Wo + (size_t)(hf * 64 + ((lp + krot * 2) & 63)) * 16384; };
    DG_DECL(1)
    const unsigned voff = lane * 16;
    const int cw = w & 3;
    auto issue = [&](int st) {
        const char* src = kvg + (st * 4 + cw) * 4096;
        const unsigned dst = lds0 + (st & 1) * K2_SLOT + cw * 4096;
#pragma unroll
        for (int i = 0; i < 4; ++i) glds16(voff, src + i * 1024, dst + i * 1024);
#pragma unroll
        for (int i = 0; i < 4; ++i) glds16(voff, src + 131072 + i * 1024, dst + 16384 + i * 1024);
    };
    if (w >= 8) {
        __builtin_amdgcn_s_sleep(TOP_SLEEP);
#pragma unroll
        for (int i = 2; i < 8; ++i)
            glds16(voff, kvg + (i & 1) * 131072 + (i >> 1) * 4096 + cw * 1024, lds0 + (i & 1) * 16384 + (i >> 1) * 4096 + cw * 1024);
        RAW_BAR();
        issue(1);
        WAIT_BAR(8);
    } else {
        const int tq = cw * 2 + (w >> 2);
        const char* qg = ws + WS_Q + (size_t)(h * 64 + qb * 8 + tq) * 2048;
        glds16(voff, kvg + (w >> 2) * 131072 + cw * 1024, lds0 + (w >> 2) * 16384 + cw * 1024);
        glds16(voff, qg, lds0 + K2_SF + tq * 2048);
        glds16(voff, qg + 1024, lds0 + K2_SF + tq * 2048 + 1024);
    }
    int w2 = w;
    asm volatile("" : "+s"(w2));

    if (w2 >= 8) {
        __builtin_amdgcn_s_setprio(3);
        const int ch = xcd * 128 + idx * 4 + cw;
        const lds_ptr cv = lds + K2_CV + cw * CV_BYTES;
        {
            const u32x4 z4 = {0u, 0u, 0u, 0u};
            if (lane < 2) *(LAS u32x4*)(cv + CV_HA + lane * 16) = z4;
            if (lane < 30) *(LAS u32x4*)(cv + CV_KV + lane * 16) = z4;
            if (cw == 0 && lane < 16) *(LAS u32x4*)(fl + lane * 16) = z4;
        }
        const int c0 = xcd * 128 + cw * 32, cq = lane & 7, rg = lane >> 3;
        const int row0 = idx * 64 + rg * 8;
        const unsigned roff = (unsigned)row0 * 2048u + (c0 + cq * 4) * 2;
        const unsigned toffs = (unsigned)(c0 + (lane >> 3)) * 4096u + idx * 128;
        u32x4 sk[2];
        sk[0] = gld128((unsigned)(c0 + cq * 4) * 8u, shiftk); sk[1] = gld128((unsigned)(c0 + cq * 4) * 8u, shiftk + 16);
        unsigned dd = gld32((unsigned)(ch >> 1) * 4u, ssmD);
        auto stage = [&](const char* src, int dst) {
            const unsigned sbase = (unsigned)(idx * 64 + cw * 16 + (lane >> 4)) * 2048u + xcd * 256;
#pragma unroll
            for (int i = 0; i < 4; ++i)
                glds16(sbase + (((lane & 15) ^ (((2 * cw + (i >> 1)) & 3) << 2)) << 4), src + i * 8192, lds0 + dst + cw * 4096 + i * 1024);
        };
        auto blank = [&](unsigned off) {
            const u32x4 e4 = {EMPTY, EMPTY, EMPTY, EMPTY};
            CFENCE();
            st_sc1(e4, rs, off + voff); st_sc1(e4, rs, off + 1024 + voff);
            CFENCE();
        };
        WAIT_BAR(3);
        DG(12);
        issue(2);
        stage(Ak, K2_SF);
        glds16((unsigned)(idx * 64 - (idx ? 4 : 0) + (lane >> 4)) * 2048u + xcd * 256 + (((lane & 15) ^ 12) << 4), Ak, lds0 + K2_SCR + cw * 1024);
        blank((unsigned)WS_O + (unsigned)((qb * 8 + cw * 2) * 32 + h) * 2048u);
        WAIT_BAR(7);
        DG(9);
        issue(3);
        stage(Av, K2_SF + 16384);
        blank((unsigned)WS_O + (unsigned)((qb * 8 + cw * 2 + 1) * 32 + h) * 2048u);
        WAIT_BAR(6);
        DG(3);
        issue(4);
#pragma unroll
        for (int i = 0; i < 4; ++i) glds16(voff, ssmk + (size_t)ch * 4096 + i * 1024, lds0 + K2_CV + cw * CV_BYTES + CV_HA + 32 + i * 1024);
        blank((unsigned)WS_ZT + ch * 4096); blank((unsigned)WS_ZT + ch * 4096 + 2048);
        WAIT_BAR(8);
        DG(5);
        issue(5);
        SBAR();
        asm volatile("" : "+v"(sk[0]), "+v"(sk[1]), "+v"(dd));
        dd = __builtin_amdgcn_readfirstlane(dd);
        {
            u32x2 kr[11], vr[8];
            lds_ptr kb = lds + K2_SF + rg * 2048 + ((cw ^ (rg & 3)) << 6) + cq * 8;
            asm volatile("" : "+v"(kb));
            const lds_ptr hb = lds + (rg ? K2_SF + rg * 2048 - 768 : K2_SCR + cw * 1024 + 256) + ((cw ^ ((rg + 3) & 3)) << 6) + cq * 8;
#pragma unroll
            for (int i = 0; i < 3; ++i) kr[i] = *(const LAS u32x2*)(hb + i * 256);
#pragma unroll
            for (int i = 0; i < 8; ++i) kr[3 + i] = *(const LAS u32x2*)(kb + i * 256);
#pragma unroll
            for (int i = 0; i < 8; ++i) vr[i] = *(const LAS u32x2*)(kb + 16384 + i * 256);
            if (row0 == 0) { kr[0] = u32x2{0u, 0u}; kr[1] = u32x2{0u, 0u}; kr[2] = u32x2{0u, 0u}; }
            const lds_ptr tb = cv + CV_KV + 480;
#pragma unroll
            for (int c = 0; c < 4; ++c) {
                const unsigned s01 = sk[c >> 1][(c & 1) * 2], s23 = sk[c >> 1][(c & 1) * 2 + 1];
                unsigned kp[10];
                float kv[8], vf[8];
#pragma unroll
                for (int j = 0; j < 10; ++j) kp[j] = __builtin_amdgcn_perm(kr[j][c >> 1], kr[j + 1][c >> 1], (c & 1) ? 0x07060302u : 0x05040100u);
                SBAR();
#pragma unroll
                for (int i = 0; i < 8; ++i) asm("v_dot2_f32_bf16 %0, %1, %2, 0" : "=v"(kv[i]) : "v"(kp[i]), "v"(s23));
                SBAR();
#pragma unroll
                for (int i = 0; i < 8; ++i) asm("v_dot2_f32_bf16 %0, %1, %2, %0" : "+v"(kv[i]) : "v"(kp[i + 2]), "v"(s01));
                SBAR();
#pragma unroll
                for (int i = 0; i < 8; ++i) vf[i] = (c & 1) ? bfhi(vr[i][c >> 1]) : bflo(vr[i][c >> 1]);
                SBAR();
#pragma unroll
                for (int i = 0; i < 8; ++i) kv[i] = mul_vv(kv[i], vf[i]);
                u32x4 o4;
#pragma unroll
                for (int i = 0; i < 4; ++i) o4[i] = cvtpk(kv[7 - 2 * i], kv[6 - 2 * i]);
                *(LAS u32x4*)(tb + (cq * 4 + c) * 128 + ((rg ^ cq) << 4)) = o4;
            }
#pragma unroll
            for (int i = 0; i < 4; ++i) {
                const u32x4 t = *(const LAS u32x4*)(tb + i * 1024 + lane * 16);
                st_sc1(t, rs, (unsigned)WS_KVR + toffs + i * 32768 + (((lane & 7) ^ (2 * i + (lane >> 5))) << 4));
            }
        }
        CFENCE();
        DG(7);
        WAIT_BAR(4);
        DG(6);
        issue(6);
        asm volatile("s_waitcnt vmcnt(8)" ::: "memory");
        if (lds_add_rtn(fl + C_KST, lane) == 3u) signal();
#pragma unroll
        for (int i = 0; i < 8; ++i)
            glds16(wvoff, wsrc(i >> 2, cw * 4 + (i & 3)), lds0 + K2_SF + (i >> 2) * 16384 + (cw * 4 + (i & 3)) * 1024);
        {
            const u32x4 z4 = {0u, 0u, 0u, 0u};
            if (lane < 2) *(LAS u32x4*)(cv + CV_HB + lane * 16) = z4;
#pragma unroll
            for (int i = 0; i < 4; ++i) {
                const int off = 32 + i * 1024 + lane * 16;
                const u32x4 hv = *(const LAS u32x4*)(cv + CV_HA + off);
                const unsigned pv = *(const LAS unsigned*)(cv + CV_HA + off - 4);
                u32x4 sh;
                sh[0] = __builtin_amdgcn_alignbit(hv[0], pv, 16);
                sh[1] = __builtin_amdgcn_alignbit(hv[1], hv[0], 16);
                sh[2] = __builtin_amdgcn_alignbit(hv[2], hv[1], 16);
                sh[3] = __builtin_amdgcn_alignbit(hv[3], hv[2], 16);
                *(LAS u32x4*)(cv + CV_HB + off) = sh;
            }
        }
        WAIT_BAR(8);
        DG(8);
        issue(7);
#pragma unroll
        for (int i = 0; i < 8; ++i)
            glds16(wvoff, wsrc(idx >> 4, 32 + cw * 8 + i), lds0 + K2_SCR + cw * 1024);
        WAIT_BAR(8);
        DG(11);
        if (cw == 0) { poll_ge(cnt + b * 64, 64u); lds_add(fl + F_KGO, lane); }
        else lds_wait_ge(fl + F_KGO, 1u);
        {
            u32x4 t[4];
#pragma unroll
            for (int i = 0; i < 4; ++i) t[i] = ld_sc1(rs, (unsigned)WS_KVR + ch * 4096 + i * 1024 + voff);
#pragma unroll
            for (int i = 0; i < 4; ++i) *(LAS u32x4*)(cv + CV_KV + 480 + i * 1024 + lane * 16) = t[i];
        }
        const int r1 = lane & 15, q = lane >> 4;
        const int toff = (r1 & 1) ? CV_HA + 2 * (16 + r1 + 9 - 8 * q) : CV_HB + 2 * (16 + r1 + 10 - 8 * q);
        const int boff = CV_KV + 448 + 32 * r1 + 16 * q;
        f32x4 y[8];
        u32x4 tf[8];
        bf16x8 bf[8];
        const lds_ptr bp = cv + boff - 448;
        lds_ptr tp[4];
#pragma unroll
        for (int i = 0; i < 4; ++i) { tp[i] = cv + toff + 1024 * i; asm volatile("" : "+v"(tp[i])); }
#define LDT(bb, a, hf) do { const LAS unsigned* t_ = (const LAS unsigned*)(tp[(a) >> 1] + 64 * (bb) + 512 * ((a) & 1) + 8 * (hf)); \
            tf[a][2 * (hf)] = t_[0]; tf[a][2 * (hf) + 1] = t_[1]; SBAR(); } while (0)
#define LDB(bb, a) do { bf[a] = lds_rd128(bp + 448 - 64 * (bb) + 512 * (a)); SBAR(); } while (0)
#define CM(a, ww) do { asm volatile("v_mfma_f32_16x16x32_bf16 %0, %1, %2, %0" : "+v"(y[(a) + (ww)]) : "v"(tf[a]), "v"(bf[ww])); SBAR(); } while (0)
#define CM0(a, ww) do { asm volatile("v_mfma_f32_16x16x32_bf16 %0, %1, %2, 0" : "=&v"(y[(a) + (ww)]) : "v"(tf[a]), "v"(bf[ww])); SBAR(); } while (0)
#define PC() do { asm volatile(PACE_OUT); SBAR(); } while (0)
#pragma unroll
        for (int a = 0; a < 8; ++a) { if (a < 5) { LDT(0, a, 0); LDT(0, a, 1); } LDB(0, 7 - a); }
#pragma unroll
        for (int i = 0; i < 8; ++i)
            glds16(wvoff, wsrc(i >> 2, 16 + cw * 4 + (i & 3)), lds0 + (i >> 2) * 16384 + (cw * 4 + (i & 3)) * 1024);
#pragma unroll
        for (int i = 0; i < 4; ++i)
            glds16((unsigned)(idx * 64 + cw * 16 + (lane >> 4)) * 2048u + xcd * 256 + (lane & 15) * 16, Aq + i * 8192, lds0 + K2_SCR + cw * 1024);
        RAW_BAR();
        asm volatile("s_waitcnt vmcnt(4)" ::: "memory");
        lds_add(fl + C_S1, lane);
        __builtin_amdgcn_s_setprio(AUX_PRIO);
        const float dch = (ch & 1) ? bfhi(dd) : bflo(dd);
        const lds_ptr kdp = cv + CV_KV + 480 + 32 * r1 + 16 * (q >> 1) + ((q & 1) ? 0 : 8);
        float kd[8][4];
        u32x2 kk;
        unsigned kc0, kc1, zo[8][2];
#define KD_RD(t) do { kk = *(const LAS u32x2*)(kdp + 512 * (t)); SBAR(); } while (0)
#define KD_UP0(t) do { kd[t][0] = bfhi(kk[1]); kd[t][1] = bflo(kk[1]); SBAR(); } while (0)
#define KD_UP1(t) do { kd[t][2] = bfhi(kk[0]); kd[t][3] = bflo(kk[0]); SBAR(); } while (0)
#define KD_MU0(t) do { kd[t][0] = mul_sv(dch, kd[t][0]); kd[t][1] = mul_sv(dch, kd[t][1]); SBAR(); } while (0)
#define KD_MU1(t) do { kd[t][2] = mul_sv(dch, kd[t][2]); kd[t][3] = mul_sv(dch, kd[t][3]); SBAR(); } while (0)
#define KD_CV(t) do { kc0 = cvtpk(kd[t][0], kd[t][1]); kc1 = cvtpk(kd[t][2], kd[t][3]); SBAR(); } while (0)
#define KD_X0(t) do { kd[t][0] = bflo(kc0); kd[t][1] = bfhi(kc0); SBAR(); } while (0)
#define KD_X1(t) do { kd[t][2] = bflo(kc1); kd[t][3] = bfhi(kc1); SBAR(); } while (0)
#define ZF0(t) do { kc0 = cvtpk(y[t][0], y[t][1]); kc1 = cvtpk(y[t][2], y[t][3]); SBAR(); } while (0)
#define ZF1(t) do { y[t][0] = bflo(kc0); y[t][1] = bfhi(kc0); SBAR(); } while (0)
#define ZF2(t) do { y[t][2] = bflo(kc1); y[t][3] = bfhi(kc1); SBAR(); } while (0)
#define ZF3(t) do { y[t][0] = add_vv(y[t][0], kd[t][0]); y[t][1] = add_vv(y[t][1], kd[t][1]); SBAR(); } while (0)
#define ZF4(t) do { y[t][2] = add_vv(y[t][2], kd[t][2]); y[t][3] = add_vv(y[t][3], kd[t][3]); SBAR(); } while (0)
#define ZF5(t) do { zo[t][0] = nz(cvtpk(y[t][0], y[t][1])); zo[t][1] = nz(cvtpk(y[t][2], y[t][3])); SBAR(); } while (0)
#define ZST(pr) do { auto s0 = __builtin_amdgcn_permlane16_swap(zo[2 * (pr)][0], zo[2 * (pr) + 1][0], false, false); \
            auto s1 = __builtin_amdgcn_permlane16_swap(zo[2 * (pr)][1], zo[2 * (pr) + 1][1], false, false); \
            st_sc1(u32x4{s0[0], s1[0], s0[1], s1[1]}, rs, (unsigned)WS_ZT + ch * 4096 + (pr) * 1024 + voff); SBAR(); } while (0)
#define W3(i) do { if ((i) == 0) PC(); } while (0)
#define HF() do { lds_add(fl + C_HF, lane); SBAR(); } while (0)
        CM0(0, 7); LDB(1, 7); CM0(0, 6); LDT(0, 5, 0); CM0(0, 5); LDT(0, 5, 1); CM0(0, 4); LDT(0, 6, 0); CM0(0, 3); LDT(0, 6, 1); CM0(0, 2); LDT(0, 7, 0); CM0(0, 1); LDT(0, 7, 1); CM0(0, 0); KD_RD(0);
        CM(1, 6); LDB(1, 6); CM(1, 5); LDT(1, 0, 0); CM(1, 4); LDT(1, 0, 1); CM(1, 3); PC(); CM(1, 2); PC(); CM(1, 1); KD_UP0(0); CM(1, 0); KD_UP1(0);
        CM(2, 5); LDB(1, 5); CM(2, 4); LDT(1, 1, 0); CM(2, 3); LDT(1, 1, 1); CM(2, 2); KD_MU0(0); CM(2, 1); KD_MU1(0); CM(2, 0); KD_CV(0);
        CM(3, 4); LDB(1, 4); CM(3, 3); LDT(1, 2, 0); CM(3, 2); LDT(1, 2, 1); CM(3, 1); KD_X0(0); CM(3, 0); KD_X1(0);
        CM(4, 3); LDB(1, 3); CM(4, 2); LDT(1, 3, 0); CM(4, 1); LDT(1, 3, 1); CM(4, 0); KD_RD(1);
        CM(5, 2); LDB(1, 2); CM(5, 1); LDT(1, 4, 0); CM(5, 0); LDT(1, 4, 1);
        CM(6, 1); LDB(1, 1); CM(6, 0); PC();
        CM(7, 0); LDB(1, 0);
        CM(0, 7); LDB(2, 7); CM(0, 6); LDT(1, 5, 0); CM(0, 5); LDT(1, 5, 1); CM(0, 4); LDT(1, 6, 0); CM(0, 3); LDT(1, 6, 1); CM(0, 2); LDT(1, 7, 0); CM(0, 1); LDT(1, 7, 1); CM(0, 0); PC();
        CM(1, 6); LDB(2, 6); CM(1, 5); LDT(2, 0, 0); CM(1, 4); LDT(2, 0, 1); CM(1, 3); KD_UP0(1); CM(1, 2); KD_UP1(1); CM(1, 1); KD_MU0(1); CM(1, 0); KD_MU1(1);
        CM(2, 5); LDB(2, 5); CM(2, 4); LDT(2, 1, 0); CM(2, 3); LDT(2, 1, 1); CM(2, 2); KD_CV(1); CM(2, 1); KD_X0(1); CM(2, 0); KD_X1(1);
        CM(3, 4); LDB(2, 4); CM(3, 3); LDT(2, 2, 0); CM(3, 2); LDT(2, 2, 1); CM(3, 1); KD_RD(2); CM(3, 0); PC();
        CM(4, 3); LDB(2, 3); CM(4, 2); LDT(2, 3, 0); CM(4, 1); LDT(2, 3, 1); CM(4, 0); PC();
        CM(5, 2); LDB(2, 2); CM(5, 1); LDT(2, 4, 0); CM(5, 0); LDT(2, 4, 1);
        CM(6, 1); LDB(2, 1); CM(6, 0); KD_UP0(2);
        CM(7, 0); LDB(2, 0);
        CM(0, 7); LDB(3, 7); CM(0, 6); LDT(2, 5, 0); CM(0, 5); LDT(2, 5, 1); CM(0, 4); LDT(2, 6, 0); CM(0, 3); LDT(2, 6, 1); CM(0, 2); LDT(2, 7, 0); CM(0, 1); LDT(2, 7, 1); CM(0, 0); KD_UP1(2);
        CM(1, 6); LDB(3, 6); CM(1, 5); LDT(3, 0, 0); CM(1, 4); LDT(3, 0, 1); CM(1, 3); KD_MU0(2); CM(1, 2); KD_MU1(2); CM(1, 1); KD_CV(2); CM(1, 0); KD_X0(2);
        CM(2, 5); LDB(3, 5); CM(2, 4); LDT(3, 1, 0); CM(2, 3); LDT(3, 1, 1); CM(2, 2); KD_X1(2); CM(2, 1); KD_RD(3); CM(2, 0); PC();
        CM(3, 4); LDB(3, 4); CM(3, 3); LDT(3, 2, 0); CM(3, 2); LDT(3, 2, 1); CM(3, 1); PC(); CM(3, 0); KD_UP0(3);
        CM(4, 3); LDB(3, 3); CM(4, 2); LDT(3, 3, 0); CM(4, 1); LDT(3, 3, 1); CM(4, 0); KD_UP1(3);
        CM(5, 2); LDB(3, 2); CM(5, 1); LDT(3, 4, 0); CM(5, 0); LDT(3, 4, 1);
        CM(6, 1); LDB(3, 1); CM(6, 0); KD_MU0(3);
        CM(7, 0); LDB(3, 0);
        CM(0, 7); LDB(4, 7); CM(0, 6); LDT(3, 5, 0); CM(0, 5); LDT(3, 5, 1); CM(0, 4); LDT(3, 6, 0); CM(0, 3); LDT(3, 6, 1); CM(0, 2); LDT(3, 7, 0); CM(0, 1); LDT(3, 7, 1); CM(0, 0); KD_MU1(3);
        CM(1, 6); LDB(4, 6); CM(1, 5); LDT(4, 0, 0); CM(1, 4); LDT(4, 0, 1); CM(1, 3); KD_CV(3); CM(1, 2); KD_X0(3); CM(1, 1); KD_X1(3); CM(1, 0); KD_RD(4);
        CM(2, 5); LDB(4, 5); CM(2, 4); LDT(4, 1, 0); CM(2, 3); LDT(4, 1, 1); CM(2, 2); PC(); CM(2, 1); PC(); CM(2, 0); KD_UP0(4);
        CM(3, 4); LDB(4, 4); CM(3, 3); LDT(4, 2, 0); CM(3, 2); LDT(4, 2, 1); CM(3, 1); KD_UP1(4); CM(3, 0); KD_MU0(4);
        CM(4, 3); LDB(4, 3); CM(4, 2); LDT(4, 3, 0); CM(4, 1); LDT(4, 3, 1); CM(4, 0); KD_MU1(4);
        CM(5, 2); LDB(4, 2); CM(5, 1); LDT(4, 4, 0); CM(5, 0); LDT(4, 4, 1);
        CM(6, 1); LDB(4, 1); CM(6, 0); KD_CV(4);
        CM(7, 0); LDB(4, 0);
        CM(0, 7); LDB(5, 7); CM(0, 6); LDT(4, 5, 0); CM(0, 5); LDT(4, 5, 1); CM(0, 4); LDT(4, 6, 0); CM(0, 3); LDT(4, 6, 1); CM(0, 2); LDT(4, 7, 0); CM(0, 1); LDT(4, 7, 1); CM(0, 0); KD_X0(4);
        CM(1, 6); LDB(5, 6); CM(1, 5); LDT(5, 0, 0); CM(1, 4); LDT(5, 0, 1); CM(1, 3); KD_X1(4); CM(1, 2); KD_RD(5); CM(1, 1); PC(); CM(1, 0); PC();
        CM(2, 5); LDB(5, 5); CM(2, 4); LDT(5, 1, 0); CM(2, 3); LDT(5, 1, 1); CM(2, 2); KD_UP0(5); CM(2, 1); KD_UP1(5); CM(2, 0); KD_MU0(5);
        CM(3, 4); LDB(5, 4); CM(3, 3); LDT(5, 2, 0); CM(3, 2); LDT(5, 2, 1); CM(3, 1); KD_MU1(5); CM(3, 0); KD_CV(5);
        CM(4, 3); LDB(5, 3); CM(4, 2); LDT(5, 3, 0); CM(4, 1); LDT(5, 3, 1); CM(4, 0); KD_X0(5);
        CM(5, 2); LDB(5, 2); CM(5, 1); LDT(5, 4, 0); CM(5, 0); LDT(5, 4, 1);
        CM(6, 1); LDB(5, 1); CM(6, 0); KD_X1(5);
        CM(7, 0); LDB(5, 0);
        CM(0, 7); LDB(6, 7); CM(0, 6); LDT(5, 5, 0); CM(0, 5); LDT(5, 5, 1); CM(0, 4); LDT(5, 6, 0); CM(0, 3); LDT(5, 6, 1); CM(0, 2); LDT(5, 7, 0); CM(0, 1); LDT(5, 7, 1); CM(0, 0); KD_RD(6);
        CM(1, 6); LDB(6, 6); CM(1, 5); LDT(6, 0, 0); CM(1, 4); LDT(6, 0, 1); CM(1, 3); PC(); CM(1, 2); PC(); CM(1, 1); KD_UP0(6); CM(1, 0); KD_UP1(6);
        CM(2, 5); LDB(6, 5); CM(2, 4); LDT(6, 1, 0); CM(2, 3); LDT(6, 1, 1); CM(2, 2); KD_MU0(6); CM(2, 1); KD_MU1(6); CM(2, 0); KD_CV(6);
        CM(3, 4); LDB(6, 4); CM(3, 3); LDT(6, 2, 0); CM(3, 2); LDT(6, 2, 1); CM(3, 1); KD_X0(6); CM(3, 0); KD_X1(6);
        CM(4, 3); LDB(6, 3); CM(4, 2); LDT(6, 3, 0); CM(4, 1); LDT(6, 3, 1); CM(4, 0); KD_RD(7);
        CM(5, 2); LDB(6, 2); CM(5, 1); LDT(6, 4, 0); CM(5, 0); LDT(6, 4, 1);
        CM(6, 1); LDB(6, 1); CM(6, 0); PC();
        CM(7, 0); LDB(6, 0);
        CM(0, 7); LDB(7, 7); CM(0, 6); LDT(6, 5, 0); CM(0, 5); LDT(6, 5, 1); CM(0, 4); LDT(6, 6, 0); CM(0, 3); LDT(6, 6, 1); CM(0, 2); LDT(6, 7, 0); CM(0, 1); LDT(6, 7, 1); CM(0, 0); PC();
        CM(1, 6); LDB(7, 6); CM(1, 5); LDT(7, 0, 0); CM(1, 4); LDT(7, 0, 1); CM(1, 3); KD_UP0(7); CM(1, 2); KD_UP1(7); CM(1, 1); KD_MU0(7); CM(1, 0); KD_MU1(7);
        CM(2, 5); LDB(7, 5); CM(2, 4); LDT(7, 1, 0); CM(2, 3); LDT(7, 1, 1); CM(2, 2); KD_CV(7); CM(2, 1); KD_X0(7); CM(2, 0); KD_X1(7);
        CM(3, 4); LDB(7, 4); CM(3, 3); LDT(7, 2, 0); CM(3, 2); LDT(7, 2, 1); CM(3, 1); PC(); CM(3, 0); PC();
        CM(4, 3); LDB(7, 3); CM(4, 2); LDT(7, 3, 0); CM(4, 1); LDT(7, 3, 1); CM(4, 0); PC();
        CM(5, 2); LDB(7, 2); CM(5, 1); LDT(7, 4, 0); CM(5, 0); LDT(7, 4, 1);
        CM(6, 1); LDB(7, 1); CM(6, 0); PC();
        CM(7, 0); LDB(7, 0);
        CM(0, 3); LDT(7, 5, 0); CM(0, 2); LDT(7, 5, 1); CM(1, 2); LDT(7, 6, 0); CM(1, 1); LDT(7, 6, 1); CM(2, 1); LDT(7, 7, 0); CM(2, 0); LDT(7, 7, 1);
        CM(3, 0); HF(); CM(0, 1); PC(); CM(0, 0); W3(0); CM(1, 0); ZF0(2); W3(1); CM(0, 5); ZF1(2); W3(2); CM(0, 4); ZF2(2); W3(3);
        CM(1, 4); ZF3(2); W3(4); CM(1, 3); ZF4(2); W3(5); CM(2, 3); ZF5(2); W3(6); CM(2, 2); ZF0(3); W3(7); CM(3, 2); ZF1(3);  CM(3, 1); ZF2(3);
        CM(4, 1); ZF3(3);  CM(4, 0); ZF4(3);  CM(5, 0); ZF5(3);  CM(0, 7); ZST(1);  CM(0, 6); ZF0(0);  CM(1, 6); ZF1(0);
        CM(1, 5); ZF2(0);  CM(2, 5); ZF3(0);  CM(2, 4); ZF4(0);  CM(3, 4); ZF5(0);  CM(3, 3); ZF0(1);  CM(4, 3); ZF1(1);
        CM(4, 2); ZF2(1);  CM(5, 2); ZF3(1);  CM(5, 1); ZF4(1);  CM(6, 1); ZF5(1);  CM(6, 0); ZST(0);  CM(7, 0); ZF0(4);
        DG(4);
        ZF1(4); ZF2(4); ZF3(4); ZF4(4); ZF5(4); ZF0(5); ZF1(5); ZF2(5); ZF3(5); ZF4(5); ZF5(5); ZST(2); ZF0(6); ZF1(6); ZF2(6); ZF3(6); ZF4(6); ZF5(6); ZF0(7); ZF1(7); ZF2(7); ZF3(7); ZF4(7); ZF5(7); ZST(3);
        DG(10);
        __builtin_amdgcn_s_setprio(3);
        while ((unsigned)__builtin_amdgcn_readfirstlane(*(volatile LAS unsigned*)(fl + C_OL)) < ZL_GATE) __builtin_amdgcn_s_sleep(4);
        CFENCE();
        const int arow = idx * 64 + (cw >> 1) * 32 + rg * 4, acol = xcd * 256 + (cw & 1) * 128 + cq * 16;
        u32x4 qr[4];
#pragma unroll
        for (int i = 0; i < 4; ++i) qr[i] = gld128((unsigned)(arow * 2048 + acol), Aq + i * 2048);
        {
            const lds_ptr tr = cv + CV_KV + 480;
            u32x4 t[4];
            const int ck = (cw >> 1) * 4 + ((lane ^ ((lane >> 5) * 2)) & 3);
            const unsigned zb = (unsigned)WS_ZT + (unsigned)(xcd * 128 + (cw & 1) * 64 + (lane >> 4) * 4) * 4096u + (idx >> 3) * 1024 +
                                ((idx & 3) * 4 + ((idx >> 2) & 1) * 16 + (ck >> 1) + (ck & 1) * 32) * 16;
#pragma unroll 1
            for (unsigned spins = 0; spins < (1u << 20); ++spins) {
#pragma unroll
                for (int i = 0; i < 4; ++i) t[i] = ld_sc1s(rs, zb + ((((lane >> 2) & 3) ^ i) << 12), i * 65536);
                if (!missing(mx4(mx4(mx8(t[0], t[1]), t[2]), t[3]))) break;
                __builtin_amdgcn_s_sleep(Z_SLEEP);
            }
            DG(13);
            __builtin_amdgcn_s_setprio(ZE_PRIO);
#pragma unroll
            for (int i = 0; i < 4; ++i) *(LAS u32x4*)(tr + i * 1024 + lane * 16) = t[i];
#pragma unroll
            for (int i = 0; i < 4; ++i) asm volatile("" : "+v"(qr[i]));
            u32x2 zc[8];
#pragma unroll
            for (int c = 0; c < 8; ++c)
                zc[c] = *(const LAS u32x2*)(tr + cq * 512 + (c >> 2) * 256 + ((((c & 3) * 4 + (rg >> 1)) ^ (cq * 2)) << 4) + (rg & 1) * 8);
#pragma unroll
            for (int i = 0; i < 4; ++i) {
                u32x4 o;
#pragma unroll
                for (int c = 0; c < 4; ++c) {
                    const unsigned z0 = zc[2 * c][i >> 1], z1 = zc[2 * c + 1][i >> 1];
                    o[c] = cvtpk(mul_vv(bflo(qr[i][c]), (i & 1) ? bfhi(z0) : bflo(z0)), mul_vv(bfhi(qr[i][c]), (i & 1) ? bfhi(z1) : bflo(z1)));
                }
                __builtin_amdgcn_raw_buffer_store_b128(o, ro, arow * 4096 + acol, i * 4096, AUX_SC1);
            }
        }
        DG_END(14);
        return;
    }

    const int half = w >> 2;
    const int TA = qb * 8 + (w & 3) * 2 + half, TB = TA ^ 1;
    bf16x8 sel;
    {
        const short one = (((lane >> 2) & 1) == ((lane >> 4) & 1) && (lane & 11) == 0) ? (short)0x3f80 : (short)0;
#pragma unroll
        for (int i = 0; i < 8; ++i) sel[i] = one;
    }
    f32x16 oA = f32x16{}, oB = f32x16{};
    f32x4 lA = f32x4{}, lB = f32x4{};
    f32x16 negm, sA, sB;
    lds_ptr lp = lds + half * 16384 + lane * 16;
    bf16x8 k0[2], k1[2], v0[2], v1[2];
    u32x4 p0, p1 = {0u, 0u, 0u, 0u};
#define EX(X, i) X[i] = __builtin_amdgcn_exp2f(X[i])
    lds_ptr qpa = lds + K2_SF + (TA & 7) * 2048 + lane * 16, qpb = lds + K2_SF + (TB & 7) * 2048 + lane * 16;
    asm volatile("" : "+v"(qpa), "+v"(qpb), "+v"(lp), "+v"(sel), "+v"(oA), "+v"(oB), "+v"(lA), "+v"(lB), "+v"(p1));
    WAIT_BAR(0);
    DG(3);
    const bf16x8 qa0 = lds_rd128(qpa), qb0 = lds_rd128(qpb);
    const bf16x8 x0 = lds_rd128(lds + lane * 16), x1 = lds_rd128(lds + lane * 16 + 1024);
    const bf16x8 qa1 = lds_rd128(qpa + 1024), qb1 = lds_rd128(qpb + 1024);
    {
        k0[0] = lds_rd128(lp); k1[0] = lds_rd128(lp + 1024);
        v0[0] = lds_rd128(lp + 2048); v1[0] = lds_rd128(lp + 3072);
        sA = MFMA32(x0, qa0, f32x16{});
        sB = MFMA32(x0, qb0, f32x16{});
        sA = MFMA32(x1, qa1, sA);
        sB = MFMA32(x1, qb1, sB);
        float m = fmx(sA[0], sA[1]), m2 = fmx(sB[0], sB[1]);
#pragma unroll
        for (int i = 2; i < 16; i += 2) m = fmx(fmx(m, sA[i]), sA[i + 1]);
#pragma unroll
        for (int i = 2; i < 16; i += 2) m2 = fmx(fmx(m2, sB[i]), sB[i + 1]);
        m = fmx(m, m2);
        auto rr = __builtin_amdgcn_permlane32_swap(__float_as_uint(m), __float_as_uint(m), false, false);
        m = fmx(__uint_as_float(rr[0]), __uint_as_float(rr[1]));
#pragma unroll
        for (int i = 0; i < 16; ++i) negm[i] = -m;
        sA = MFMA32(k0[0], qa0, negm);
        sA = MFMA32(k1[0], qa1, sA);
        v1[1] = v1[0];
        SBAR();
        RAW_BAR();
        SBAR();
        EX(sA, 0); EX(sA, 1);
    }
    DG(10);
#define SUM16(l, p) asm volatile("v_mfma_f32_16x16x32_bf16 %0, %1, %2, %0" : "+v"(l) : "v"(sel), "v"(p))
#define HALF(C, oC, lC, N, qx, qy, ka, kb, oP, lP, vprev, vcur0, PRE) do { \
        oP = MFMA32(vprev, PB(p1), oP); SBAR(); \
        PRE; EX(C, 2); EX(C, 3); EX(C, 4); SBAR(); \
        N = MFMA32(ka, qx, negm); SBAR(); \
        EX(C, 5); EX(C, 6); EX(C, 7); SBAR(); \
        SUM16(lP, p1); SBAR(); \
        p0[0] = cvtpk(C[0], C[1]); p0[1] = cvtpk(C[2], C[3]); SBAR(); \
        N = MFMA32(kb, qy, N); SBAR(); \
        p0[2] = cvtpk(C[4], C[5]); p0[3] = cvtpk(C[6], C[7]); SBAR(); EX(C, 8); EX(C, 9); SBAR(); \
        oC = MFMA32(vcur0, PB(p0), oC); SBAR(); \
        EX(C, 10); EX(C, 11); EX(C, 12); SBAR(); \
        SUM16(lC, p0); SBAR(); \
        EX(C, 13); EX(C, 14); EX(C, 15); SBAR(); \
        p1[0] = cvtpk(C[8], C[9]); p1[1] = cvtpk(C[10], C[11]); p1[2] = cvtpk(C[12], C[13]); p1[3] = cvtpk(C[14], C[15]); SBAR(); \
        EX(N, 0); EX(N, 1); SBAR(); \
    } while (0)
#define RD4(Y, nx) do { v1[Y] = lds_rd128((nx) + 3072); v0[Y] = lds_rd128((nx) + 2048); \
        k1[Y] = lds_rd128((nx) + 1024); k0[Y] = lds_rd128(nx); } while (0)
#define KT(X, nx, PRE0) do { \
        HALF(sA, oA, lA, sB, qb0, qb1, k0[X], k1[X], oB, lB, v1[X ^ 1], v0[X], PRE0; RD4(X ^ 1, nx)); \
        HALF(sB, oB, lB, sA, qa0, qa1, k0[X ^ 1], k1[X ^ 1], oA, lA, v1[X], v0[X], (void)0); \
    } while (0)
    SBAR();
#pragma unroll 1
    for (int st = 0; st < 8; ++st) {
        const lds_ptr cur = lp + (st & 1) * K2_SLOT;
        const lds_ptr nxt = lp + ((st + 1) & 1) * K2_SLOT;
        PRIO(2);
        KT(0, cur + 4096, (void)0);
        PRIO(1);
        KT(1, cur + 8192, (void)0);
        PRIO(0);
        KT(0, cur + 12288, (void)0);
        PRIO(3);
        KT(1, nxt, RAW_BAR(); SBAR());
    }
    oB = MFMA32(v1[1], PB(p1), oB);
    SBAR();
    SUM16(lB, p1);
    asm volatile("s_nop 7" : "+v"(oB) :: "memory");
    SBAR();
    DG(4);
    {
        const lds_ptr xo = lds + K2_SLOT + w * 4096 + lane * 16;
        *(LAS float*)(lds + K2_LX + w * 256 + lane * 4) = lB[0];
#pragma unroll
        for (int i = 0; i < 4; ++i) *(LAS f32x4*)(xo + i * 1024) = f32x4{oB[4 * i], oB[4 * i + 1], oB[4 * i + 2], oB[4 * i + 3]};
        lds_add(fl + F_X + w * 4, lane);
        lds_add(fl + C_END + half * 4, lane);
        lds_wait_ge(fl + F_X + (w ^ 4) * 4, 1u);
        const lds_ptr xi = lds + K2_SLOT + (w ^ 4) * 4096 + lane * 16;
        f32x4 t[4];
        const float lx = *(const LAS float*)(lds + K2_LX + (w ^ 4) * 256 + (lane & 31) * 4);
        { auto rr = __builtin_amdgcn_permlane32_swap(__float_as_uint(lA[0]), __float_as_uint(lA[0]), false, false); lA[0] = __uint_as_float(rr[0]); }
#pragma unroll
        for (int i = 0; i < 4; ++i) t[i] = *(const LAS f32x4*)(xi + i * 1024);
        SBAR();
        lA[0] += lx;
#pragma unroll
        for (int i = 0; i < 16; ++i) { float o = oA[i]; acc_vv(o, t[i >> 2][i & 3]); oA[i] = o; }
    }
    DG(9);
    const float l = lA[0];
    u32x4 on0, on1;
    auto finish = [&](float ls) {
        float rl = __builtin_amdgcn_rcpf(ls);
        asm volatile("s_nop 1" : "+v"(rl));
        f32x16 o;
#pragma unroll
        for (int i = 0; i < 16; ++i) o[i] = mul_vv(oA[i], rl);
        on0 = nz4(pack8n(o, 0)); on1 = nz4(pack8n(o, 1));
    };
    finish(l);
    {
        float t0, t1;
        asm("v_mul_f32_e32 %0, 0, %2\n\tv_mul_f32_e32 %1, 0, %3\n\tv_fmac_f32_e32 %0, 0, %4\n\tv_fmac_f32_e32 %1, 0, %5\n\tv_fmac_f32_e32 %0, 0, %6\n\tv_fmac_f32_e32 %1, 0, %7\n\tv_fmac_f32_e32 %0, 0, %8\n\tv_fmac_f32_e32 %1, 0, %9\n\t"
            "v_fmac_f32_e32 %0, 0, %10\n\tv_fmac_f32_e32 %1, 0, %11\n\tv_fmac_f32_e32 %0, 0, %12\n\tv_fmac_f32_e32 %1, 0, %13\n\tv_fmac_f32_e32 %0, 0, %14\n\tv_fmac_f32_e32 %1, 0, %15\n\tv_fmac_f32_e32 %0, 0, %16\n\tv_fmac_f32_e32 %1, 0, %17"
            : "=&v"(t0), "=&v"(t1) : "v"(oA[0]), "v"(oA[1]), "v"(oA[2]), "v"(oA[3]), "v"(oA[4]), "v"(oA[5]), "v"(oA[6]), "v"(oA[7]), "v"(oA[8]), "v"(oA[9]), "v"(oA[10]), "v"(oA[11]), "v"(oA[12]), "v"(oA[13]), "v"(oA[14]), "v"(oA[15]));
        const bool ok = (l <= 1.0e30f) && (l >= 1.0e-18f) && (t0 + t1 == 0.f);
        if (__builtin_expect(__any(!ok), 0)) {
            float m = -INFINITY, ls = 0.f;
            oA = f32x16{};
#pragma unroll 1
            for (int t = 0; t < 64; ++t) {
                const char* g = kvg + t * 4096 + lane * 16;
                const bf16x8 a0 = *(const bf16x8*)g, a1 = *(const bf16x8*)(g + 1024);
                const bf16x8 b0 = *(const bf16x8*)(g + 2048), b1 = *(const bf16x8*)(g + 3072);
                f32x16 s = MFMA32(a0, qa0, f32x16{});
                s = MFMA32(a1, qa1, s);
                float tm = s[0];
#pragma unroll
                for (int i = 1; i < 16; ++i) tm = __builtin_fmaxf(tm, s[i]);
                auto rr = __builtin_amdgcn_permlane32_swap(__float_as_uint(tm), __float_as_uint(tm), false, false);
                tm = __builtin_fmaxf(__uint_as_float(rr[0]), __uint_as_float(rr[1]));
                const float mn = __builtin_fmaxf(m, tm);
                const float alpha = __builtin_amdgcn_exp2f(m - mn);
                m = mn;
                float ps = 0.f;
#pragma unroll
                for (int i = 0; i < 16; ++i) { s[i] = __builtin_amdgcn_exp2f(s[i] - mn); ps += s[i]; oA[i] *= alpha; }
                ls = ls * alpha + ps;
                oA = MFMA32(b0, PB(pack8n(s, 0)), oA);
                oA = MFMA32(b1, PB(pack8n(s, 1)), oA);
            }
            auto rr = __builtin_amdgcn_permlane32_swap(__float_as_uint(ls), __float_as_uint(ls), false, false);
            asm volatile("s_nop 7\n\ts_nop 7" : "+v"(oA));
            finish(__uint_as_float(rr[0]) + __uint_as_float(rr[1]));
        }
    }
    u32x4 w3[4];
    {
        const unsigned og = (unsigned)WS_O + (unsigned)(TA * 32 + h) * 2048u + lane * 16;
        st_sc1(on0, rs, og);
        st_sc1(on1, rs, og + 1024);
        lds_add(fl + C_S1, lane);
        const char* s2 = wsrc(half ^ 1, 32 + (w & 3) * 4);
        const char* s3 = wsrc(half, 48 + (w & 3) * 4);
        lds_wait_ge(fl + C_S1, 12u);
        DG(5);
#pragma unroll
        for (int i = 0; i < 4; ++i)
            glds16(wvoff, s2 + i * 16384, lds0 + K2_SLOT + ((w ^ 4) * 4 + i) * 1024);
#pragma unroll
        for (int i = 0; i < 4; ++i) w3[i] = gld128(wvoff, s3 + i * 16384);
    }
    {
        const int half = w & 1;
        const int T3 = tt * 4 + (w >> 1);
        const unsigned og = (unsigned)WS_O + (unsigned)T3 * 65536u + half * 32768 + lane * 16;
        const int r = lane & 31, hh = lane >> 5;
        const int g = lane >> 4, q = (lane & 15) >> 2, p = lane & 3;
        int woff[2];
#pragma unroll
        for (int n = 0; n < 2; ++n) {
            const int c = 4 * (n ^ half) + 2 * (p & 1) + (g & 1);
            woff[n] = (4 * hh + q) * 128 + ((c ^ ((q >> 1) << 2)) << 4) + (p >> 1) * 8;
        }
        f32x16 acc[2] = {f32x16{}, f32x16{}};
        lds_ptr wl[3][2] = {{lds + half * 16384 + woff[0], lds + half * 16384 + woff[1]},
                            {lds + K2_SF + half * 16384 + woff[0], lds + K2_SF + half * 16384 + woff[1]},
                            {lds + K2_CV + half * 2 * CV_BYTES + woff[0], lds + K2_CV + half * 2 * CV_BYTES + woff[1]}};
        asm volatile("" : "+v"(wl[0][0]), "+v"(wl[0][1]), "+v"(wl[1][0]), "+v"(wl[1][1]), "+v"(wl[2][0]), "+v"(wl[2][1]));
#define MS(m) ((m) < 64 - 2 * TL ? (m) >> 1 : 32 - TL + ((m) - (64 - 2 * TL)) % TL)
#define MN(m) ((m) < 64 - 2 * TL ? (m) & 1 : (m) < 64 - TL)
        constexpr int WR = 2 * WD + 2;
        s16x4 wa[WR], wb[WR];
#define LDW(m) do { const int s_ = MS(m), f_ = s_ < 8 ? 1 : s_ < 24 ? 0 : 2; \
            const int o_ = f_ == 2 ? ((s_ >> 2) & 1) * CV_BYTES + (s_ & 3) * 2048 : (f_ ? 0 : ((s_ >> 3) - 1) * K2_SLOT) + (s_ & 7) * 2048; \
            wa[(m) % WR] = lds_tr(wl[f_][MN(m)] + o_); wb[(m) % WR] = lds_tr(wl[f_][MN(m)] + o_ + 1024); SBAR(); } while (0)
#pragma unroll
        for (int m = 0; m < 2 * WD; ++m) LDW(m);
        u32x4 of[OWIN];
#define LDO(i) do { of[(i) % OWIN] = ld_sc1s(rs, og, (((i) + krot) & 31) * 1024); SBAR(); } while (0)
        for (unsigned spins = 0; spins < (1u << 20); ++spins) {
#pragma unroll
            for (int i = 0; i < OW0; ++i) LDO(i);
            unsigned m = mx8(of[0], of[1]);
#pragma unroll
            for (int i = 2; i < OW0; ++i) m = mx4(m, of[i]);
            if (!missing(m)) break;
            DGC(12);
        }
        lds_add(fl + C_S2, lane);
#pragma unroll
        for (int i = OW0; i < OWIN; ++i) LDO(i);
        DG(6);
        __builtin_amdgcn_s_setprio(P3_PRIO);
        SBAR();
        unsigned fv = 0;
#define FLAG_RD(a) do { fv = *(volatile LAS unsigned*)(a); SBAR(); } while (0)
#define FLAG_CHK(a, n) do { if (__builtin_expect((unsigned)__builtin_amdgcn_readfirstlane(fv) < (n), 0)) lds_wait_ge(a, n); CFENCE(); SBAR(); } while (0)
        lds_ptr xo = lds + K2_SF + w * 4096 + lane * 16, xi = lds + K2_SF + (w ^ 1) * 4096 + lane * 16;
        int bo = (T3 * 32 + (lane & 15)) * 4096 + 2048 + nt * 128 + half * 64 + g * 16;
        lds_ptr w3d = lds + K2_CV + (w >> 1) * CV_BYTES + (w & 1) * 4096 + lane * 16;
        asm volatile("" : "+v"(xo), "+v"(xi), "+v"(bo), "+v"(w3d));
        f32x4 xt[4];
        constexpr int M3 = TL > 8 ? 64 - 2 * TL + TL - 8 - 2 * WD : 48 - 2 * WD;
        sfor(std::make_integer_sequence<int, 64>{}, [&](auto mc) __attribute__((always_inline)) {
            constexpr int m = decltype(mc)::value, s = MS(m), n = MN(m);
            constexpr bool first = m < 64 - 2 * TL ? n == 0 : n == 1;
            constexpr bool last = m < 64 - 2 * TL ? n == 1 : n == 0;
            acc[n] = MFMA32(cat8(wa[m % WR], wb[m % WR]), PB(of[s % OWIN]), acc[n]);
            SBAR();
            if constexpr (m == 32 - 2 * WD) FLAG_CHK(fl + C_S2, 8u);
            if constexpr (m == M3) FLAG_CHK(fl + C_S3, 8u);
            if constexpr (m + 2 * WD < 64) LDW(m + 2 * WD);
            if constexpr (first && (s & 1) && s + 1 >= OW0 && s + 1 < 32) {
                if (__builtin_expect(missing(mx8(of[(s + 1) % OWIN], of[(s + 2) % OWIN])), 0)) {
#pragma unroll 1
                    for (unsigned spins = 0; spins < (1u << 20); ++spins) {
                        __builtin_amdgcn_s_sleep(2);
                        LDO(s + 1); LDO(s + 2);
                        DGC(13);
                        if (!missing(mx8(of[(s + 1) % OWIN], of[(s + 2) % OWIN]))) break;
                    }
                }
            }
            if constexpr (last && s + OWIN < 32) LDO(s + OWIN);
            if constexpr (m == 15 - 2 * WD) lds_add(fl + C_U0, lane);
            if constexpr (m == 29 - 2 * WD) FLAG_RD(fl + C_S2);
            if constexpr (m == W3M - 3) FLAG_RD(fl + C_HF);
            if constexpr (m == W3M) FLAG_CHK(fl + C_HF, 4u);
            if constexpr (m >= W3M && m < W3M + 8 && ((m - W3M) & 1) == 0) {
                asm volatile("" : "+v"(w3[((m - W3M) >> 1) & 3]));
                *(LAS u32x4*)(w3d + (((m - W3M) >> 1) & 3) * 1024) = w3[((m - W3M) >> 1) & 3];
            }
            if constexpr (m == W3M + 6) lds_add(fl + C_S3, lane);
            if constexpr (m == M3 - 3) FLAG_RD(fl + C_S3);
            if constexpr (m == 64 - TL - 3) FLAG_RD(fl + C_U0);
            if constexpr (m == 64 - TL) FLAG_CHK(fl + C_U0, 8u);
            if constexpr (m == 64 - TL + 1 || m == 64 - TL + 2) {
                constexpr int i = 2 * (m - (64 - TL + 1));
                *(LAS f32x4*)(xo + i * 1024) = f32x4{acc[1][4 * i], acc[1][4 * i + 1], acc[1][4 * i + 2], acc[1][4 * i + 3]};
                *(LAS f32x4*)(xo + i * 1024 + 1024) = f32x4{acc[1][4 * i + 4], acc[1][4 * i + 5], acc[1][4 * i + 6], acc[1][4 * i + 7]};
            }
            if constexpr (m == 64 - TL + 2) lds_add(fl + F_R + w * 4, lane);
            if constexpr (m == XR_AT - 2) FLAG_RD(fl + F_R + (w ^ 1) * 4);
            if constexpr (m == XR_AT) {
                FLAG_CHK(fl + F_R + (w ^ 1) * 4, 1u);
#pragma unroll
                for (int i = 0; i < 4; ++i) xt[i] = *(const LAS f32x4*)(xi + i * 1024);
            }
            if constexpr (ZL_GATE && last && s + OWIN == ZL_AT) lds_add(fl + C_OL, lane);
            if constexpr (m == 2 * P3_T1 + 1) __builtin_amdgcn_s_setprio(P3_PRIO - 1);
            SBAR();
            if constexpr (m == 31) DG(11);
            if constexpr (m == 47) DG(14);
        });
        asm volatile("s_nop 11" : "+v"(acc[0]));
        DG(7);
#pragma unroll
        for (int i = 0; i < 16; ++i) { float o = xt[i >> 2][i & 3]; acc_vv(o, acc[0][i]); acc[1][i] = o; }
        u32x4 pa = pack8n(acc[1], 0), pb = pack8n(acc[1], 1);
#pragma unroll
        for (int i = 0; i < 4; ++i) {
            auto sw = __builtin_amdgcn_permlane16_swap(pa[i], pb[i], false, false);
            pa[i] = sw[0]; pb[i] = sw[1];
        }
        asm volatile("" :: "v"(acc[0]));
        __builtin_amdgcn_raw_buffer_store_b128(pa, ro, bo, 0, AUX_SC1);
        __builtin_amdgcn_raw_buffer_store_b128(pb, ro, bo, 65536, AUX_SC1);
    }
    DG_END(8);
}

extern "C" void kernel_launch(void* const* d_in, const int* in_sizes, int n_in,
                              void* d_out, int out_size, void* d_ws, size_t ws_size,
                              hipStream_t stream) {
    const void* A_q = d_in[0];
    const void* A_k = d_in[1];
    const void* A_v = d_in[2];
    const void* A_shift = d_in[3];
    const void* A_ssmk = d_in[4];
    const void* A_D = d_in[5];
    const void* B_x = d_in[6];
    const void* B_wqkv = d_in[7];
    const void* B_wo = d_in[8];
    char* ws = (char*)d_ws;
    k1_qkv<<<256, 512, 0, stream>>>((const char*)B_x, (const char*)B_wqkv, ws);
    k2_attn<<<256, 768, 0, stream>>>(ws, (const char*)A_q, (const char*)A_k, (const char*)A_v, (const char*)A_shift,
                                     (const char*)A_ssmk, (const unsigned*)A_D, (const char*)B_wo, (char*)d_out);
}
```

```cpp
#include <hip/hip_runtime.h>
#include <hip/hip_bf16.h>
#include <stdint.h>
#include <utility>

typedef short bf16x8 __attribute__((ext_vector_type(8)));
typedef short s16x4 __attribute__((ext_vector_type(4)));
typedef float f32x16 __attribute__((ext_vector_type(16)));
typedef float f32x4 __attribute__((ext_vector_type(4)));
typedef float f32x2 __attribute__((ext_vector_type(2)));
typedef unsigned u32x4 __attribute__((ext_vector_type(4)));
typedef unsigned u32x2 __attribute__((ext_vector_type(2)));
typedef __bf16 bf16x2_t __attribute__((ext_vector_type(2)));

#define LAS __attribute__((address_space(3)))
typedef LAS char* lds_ptr;
#define DI __device__ __forceinline__
#define SBAR() __builtin_amdgcn_sched_barrier(0)
#define MFMA32(a, b, c) __builtin_amdgcn_mfma_f32_32x32x16_bf16((a), (b), (c), 0, 0, 0)
#define MFMA16(a, b, c) __builtin_amdgcn_mfma_f32_16x16x32_bf16((a), (b), (c), 0, 0, 0)
#define WAIT_BAR(N) asm volatile("s_waitcnt vmcnt(" #N ") lgkmcnt(0)\n\ts_barrier" ::: "memory")
#define LGKM_BAR() asm volatile("s_waitcnt lgkmcnt(0)\n\ts_barrier" ::: "memory")

#define DG_DECL(K)
#define DG(i)
#define DG_END(i)
#define DGC(i)

constexpr int S_LEN = 2048, DM = 1024, NH = 32;

constexpr size_t WS_Q = 0;
constexpr size_t WS_KV = 4u << 20;
constexpr size_t WS_O = 12u << 20;
constexpr size_t WS_KVR = 16u << 20;
constexpr size_t WS_ZT = 20u << 20;
constexpr size_t WS_CNT = 24u << 20;

template <int... I, class F> DI void sfor(std::integer_sequence<int, I...>, F&& f) { (f(std::integral_constant<int, I>{}), ...); }
DI unsigned cvtpk(float lo, float hi) {
    f32x2 v = {lo, hi};
    return __builtin_bit_cast(unsigned, __builtin_convertvector(v, bf16x2_t));
}
DI float bflo(unsigned u) { return __uint_as_float(u << 16); }
DI float bfhi(unsigned u) { return __uint_as_float(u & 0xffff0000u); }

DI void glds16(unsigned voff, const void* sbase, unsigned lds_dst) {
    unsigned sv;
    asm volatile("s_mov_b32 %0, m0\n\ts_mov_b32 m0, %3\n\ts_nop 0\n\tglobal_load_lds_dwordx4 %1, %2\n\ts_mov_b32 m0, %0"
                 : "=&s"(sv) : "v"(voff), "s"(sbase), "s"(lds_dst) : "memory");
}
DI bf16x8 lds_rd128(lds_ptr p) { return *(const LAS bf16x8*)p; }
DI s16x4 lds_tr(lds_ptr p) {
    return __builtin_bit_cast(s16x4, __builtin_amdgcn_ds_read_tr16_b64_v4i16((LAS s16x4*)p));
}
DI bf16x8 cat8(s16x4 a, s16x4 b) { return __builtin_shufflevector(a, b, 0, 1, 2, 3, 4, 5, 6, 7); }
DI u32x4 pack8(const f32x16& x, int s, float sc) {
    u32x4 p;
    p[0] = cvtpk(x[8 * s + 0] * sc, x[8 * s + 1] * sc);
    p[1] = cvtpk(x[8 * s + 2] * sc, x[8 * s + 3] * sc);
    p[2] = cvtpk(x[8 * s + 4] * sc, x[8 * s + 5] * sc);
    p[3] = cvtpk(x[8 * s + 6] * sc, x[8 * s + 7] * sc);
    return p;
}
DI u32x4 pack8n(const f32x16& x, int s) {
    u32x4 p;
    p[0] = cvtpk(x[8 * s + 0], x[8 * s + 1]);
    p[1] = cvtpk(x[8 * s + 2], x[8 * s + 3]);
    p[2] = cvtpk(x[8 * s + 4], x[8 * s + 5]);
    p[3] = cvtpk(x[8 * s + 6], x[8 * s + 7]);
    return p;
}
DI void rnd2(float& a, float& b) { unsigned u = cvtpk(a, b); a = bflo(u); b = bfhi(u); }

#define CONV_PACE()
#define RAW_BAR() asm volatile("s_barrier" ::: "memory")
DI unsigned gld32(unsigned voff, const void* sbase) {
    unsigned r;
    asm volatile("global_load_dword %0, %1, %2" : "=v"(r) : "v"(voff), "s"(sbase) : "memory");
    return r;
}
DI u32x2 gld64(unsigned voff, const void* sbase) {
    u32x2 r;
    asm volatile("global_load_dwordx2 %0, %1, %2" : "=v"(r) : "v"(voff), "s"(sbase) : "memory");
    return r;
}
DI float mul_sv(float s, float v) { float r; asm("v_mul_f32_e32 %0, %1, %2" : "=v"(r) : "s"(s), "v"(v)); return r; }
DI float mul_vv(float a, float b) { float r; asm("v_mul_f32_e32 %0, %1, %2" : "=v"(r) : "v"(a), "v"(b)); return r; }
DI float add_vv(float a, float b) { float r; asm("v_add_f32_e32 %0, %1, %2" : "=v"(r) : "v"(a), "v"(b)); return r; }
DI void acc_vv(float& a, float b) { asm("v_add_f32_e32 %0, %0, %1" : "+v"(a) : "v"(b)); }
DI void fmac_vv(float& acc, float a, float b) { asm("v_fmac_f32_e32 %0, %1, %2" : "+v"(acc) : "v"(a), "v"(b)); }
DI void fmac_sv(float& acc, float s, float v) { asm("v_fmac_f32_e32 %0, %1, %2" : "+v"(acc) : "s"(s), "v"(v)); }
typedef __attribute__((address_space(1))) unsigned gu32;
#define RLX_AGENT __ATOMIC_RELAXED, __HIP_MEMORY_SCOPE_AGENT
#define CFENCE() asm volatile("" ::: "memory")
#define DRAIN() asm volatile("s_waitcnt vmcnt(0)" ::: "memory")
constexpr int AUX_SC1 = 16;
DI u32x4 ld_sc1(__amdgpu_buffer_rsrc_t rs, unsigned off) { return __builtin_amdgcn_raw_buffer_load_b128(rs, (int)off, 0, AUX_SC1); }
DI u32x4 ld_sc1s(__amdgpu_buffer_rsrc_t rs, unsigned off, int soff) { return __builtin_amdgcn_raw_buffer_load_b128(rs, (int)off, soff, AUX_SC1); }
DI void st_sc1(u32x4 v, __amdgpu_buffer_rsrc_t rs, unsigned off) { __builtin_amdgcn_raw_buffer_store_b128(v, rs, (int)off, 0, AUX_SC1); }
constexpr unsigned EMPTY = 0xffffffffu;
DI unsigned nz(unsigned x) { return x < EMPTY - 1u ? x : EMPTY - 1u; }
DI u32x4 nz4(u32x4 v) { return u32x4{nz(v[0]), nz(v[1]), nz(v[2]), nz(v[3])}; }
DI float fmx(float a, float b) { return __builtin_elementwise_maximum(a, b); }
DI unsigned umax(unsigned a, unsigned b) { return a > b ? a : b; }
DI unsigned umax3(unsigned a, unsigned b, unsigned c) { return umax(umax(a, b), c); }
DI unsigned mx4(unsigned m, u32x4 v) { return umax3(umax3(m, v[0], v[1]), v[2], v[3]); }
DI unsigned mx8(u32x4 a, u32x4 b) { return umax(umax3(umax3(umax3(a[0], a[1], a[2]), a[3], b[0]), b[1], b[2]), b[3]); }
DI bool missing(unsigned m) { return __builtin_amdgcn_ballot_w64(m == EMPTY) != 0; }
DI unsigned lds_add_rtn(lds_ptr p, int lane) {
    unsigned old = 0;
    CFENCE();
    if (lane == 0) old = __hip_atomic_fetch_add((LAS unsigned*)p, 1u, __ATOMIC_RELAXED, __HIP_MEMORY_SCOPE_WORKGROUP);
    old = __builtin_amdgcn_readfirstlane(old);
    CFENCE();
    return old;
}
DI void lds_add(lds_ptr p, int lane) {
    unsigned long long sv;
    asm volatile("s_mov_b64 %0, exec\n\ts_mov_b64 exec, 1\n\tds_add_u32 %1, %2\n\ts_mov_b64 exec, %0"
                 : "=&s"(sv) : "v"(p), "v"(1u) : "memory");
}
DI void lds_wait_ge(lds_ptr p, unsigned v) {
    CFENCE();
    while ((unsigned)__builtin_amdgcn_readfirstlane(*(volatile LAS unsigned*)p) < v) __builtin_amdgcn_s_sleep(1);
    CFENCE();
}
DI void poll_ge(gu32* word, unsigned want) {
    CFENCE();
    for (unsigned spins = 0; spins < (1u << 22); ++spins) {
        if ((unsigned)__builtin_amdgcn_readfirstlane(__hip_atomic_load(word, RLX_AGENT)) >= want) break;
        __builtin_amdgcn_s_sleep(1);
    }
    __builtin_amdgcn_fence(__ATOMIC_ACQUIRE, "wavefront");
    CFENCE();
}
#define PB(x) __builtin_bit_cast(bf16x8, x)
#define PRIO(x) do { __builtin_amdgcn_s_setprio(x); SBAR(); } while (0)
DI u32x4 gld128(unsigned voff, const void* sbase) {
    u32x4 r;
    asm volatile("global_load_dwordx4 %0, %1, %2" : "=v"(r) : "v"(voff), "s"(sbase) : "memory");
    return r;
}

#define K1_UNR 15
#define K1_LA 5
#define K1_ROT ((tb & 3) * 4)
#define K1_PF 0
#define K1_PG 0
#define PG1_AT 0
#define PG2_AT 2
#define PG3_AT 6
#define K1_WB(n) WAIT_BAR(0)
#define PF1_AT -1
#define PF1_LO 4
#define PF1_N 4
#define PF2_AT 1
#define PF2_LO 8
#define PF2_N 4
#define PF3_AT 5
#define PF3_LO 12
#define PF3_N 4
#define K1_KR ((kt & 12) + ((kt + hp + tb) & 3))
constexpr int K1_STAGE = 40960, K1U = K1_UNR;
__global__ __launch_bounds__(512, 2) void k1_qkv(const char* __restrict__ X, const char* __restrict__ W,
                                                 char* __restrict__ ws) {
    __shared__ __attribute__((aligned(16))) char lds_raw[4 * K1_STAGE];
    const lds_ptr lds = (lds_ptr)lds_raw;
    const unsigned lds0 = (unsigned)(uintptr_t)lds;
    const int tid = threadIdx.x, lane = tid & 63;
    const int w = __builtin_amdgcn_readfirstlane(tid >> 6);
    const int b = blockIdx.x, xcd = b & 7, idx = b >> 3;
    const int hp = (xcd >> 1) * 4 + (idx & 3), tb = (xcd & 1) * 8 + (idx >> 2);
    DG_DECL(0)

    const int lw = w & 3;
    const int rot = K1_ROT;
    unsigned voffX[4], voffW[2];
#pragma unroll
    for (int pp = 0; pp < 4; ++pp) {
        const int row = 32 * lw + 8 * pp + (lane >> 3);
        const int ch = (lane & 7) ^ ((row >> 1) & 7);
        voffX[pp] = (unsigned)(tb * 128 + row) * 2048u + ch * 16;
    }
#pragma unroll
    for (int pp = 0; pp < 2; ++pp) {
        const int krow = 16 * lw + 8 * pp + (lane >> 3);
        const int ch = (lane & 7) ^ (((lane >> 4) & 1) << 2);
        voffW[pp] = (unsigned)krow * 6144u + ch * 16;
    }
    auto issue = [&](int kt, int lo, int hi) {
        const int kr = K1_KR;
        const char* xb = X + kr * 128;
        const char* wb = W + (size_t)kr * (64 * 6144) + hp * 128;
        const unsigned st = lds0 + (kt & 3) * K1_STAGE;
#pragma unroll
        for (int pp = 0; pp < 4; ++pp)
            if (pp >= lo && pp < hi) glds16(voffX[pp], xb, st + (4 * lw + pp) * 1024);
#pragma unroll
        for (int sec = 0; sec < 3; ++sec)
#pragma unroll
            for (int pp = 0; pp < 2; ++pp)
                if (4 + 2 * sec + pp >= lo && 4 + 2 * sec + pp < hi)
                    glds16(voffW[pp], wb + sec * 2048, st + 16384 + sec * 8192 + (2 * lw + pp) * 1024);
    };
    DG(13);
    if (w < 4) { issue(0, 0, 5); issue(1, 0, 5); }
    else { issue(0, 5, 10); issue(1, 5, 10); }
    DG(14);
    int w2 = w;
    asm volatile("" : "+s"(w2));

    if (w2 >= 4) {
#define WAITN_BAR(N) asm volatile("s_waitcnt vmcnt(%0) lgkmcnt(0)\n\ts_barrier" :: "n"(N) : "memory")
        issue(2, 0, K1_LA);
        asm volatile("s_waitcnt vmcnt(%0)" :: "n"(5 + K1_LA) : "memory");
#pragma unroll 1
        for (int kt = 0; kt < 13; ++kt) {
            WAITN_BAR(10 + K1_LA);
            issue(kt + 2, K1_LA, 10);
            issue(kt + 3, 0, K1_LA);
        }
        WAITN_BAR(10 + K1_LA);
        issue(15, K1_LA, 10);
        WAIT_BAR(10);
        WAIT_BAR(0);
        if (lw == 0 && lane == 0) *(unsigned*)(ws + WS_CNT + b * 256) = 0u;
        DG_END(5);
        return;
    }

    const int wm = w >> 1, wn = w & 1;
    const int r = lane & 31, hh = lane >> 5;
    const int g = lane >> 4, q = (lane & 15) >> 2, p = lane & 3;
    const int xoff0 = (64 * wm + r) * 128 + ((hh ^ ((r >> 1) & 7)) << 4);
    const int woff0 = 16384 + (8 * hh + q) * 128 + (((4 * wn + 2 * (g & 1) + (p >> 1)) ^ ((q >> 1) << 2)) << 4) + (p & 1) * 8;

    f32x16 acc[2][3];
#pragma unroll
    for (int m = 0; m < 2; ++m)
#pragma unroll
        for (int s = 0; s < 3; ++s) acc[m][s] = f32x16{};
    bf16x8 xf[2][2];
    s16x4 wl[2][3], wh[2][3];
#define K1_WF(f, sec) cat8(wl[f][sec], wh[f][sec])
#define K1_STEP(F, st, t, BAR_HERE) do { \
        const lds_ptr xp_ = (st) + (xoff0 ^ ((t) << 5)); const lds_ptr wp_ = (st) + woff0 + (t) * 2048; \
        acc[0][0] = MFMA32(K1_WF(F, 0), xf[F][0], acc[0][0]); SBAR(); \
        if (BAR_HERE) { K1_WB(BAR_HERE); SBAR(); } \
        xf[F ^ 1][0] = lds_rd128(xp_); wl[F ^ 1][0] = lds_tr(wp_); wh[F ^ 1][0] = lds_tr(wp_ + 512); SBAR(); \
        acc[0][1] = MFMA32(K1_WF(F, 1), xf[F][0], acc[0][1]); SBAR(); \
        wl[F ^ 1][1] = lds_tr(wp_ + 8192); wh[F ^ 1][1] = lds_tr(wp_ + 8192 + 512); SBAR(); \
        acc[0][2] = MFMA32(xf[F][0], K1_WF(F, 2), acc[0][2]); SBAR(); \
        wl[F ^ 1][2] = lds_tr(wp_ + 16384); wh[F ^ 1][2] = lds_tr(wp_ + 16384 + 512); xf[F ^ 1][1] = lds_rd128(xp_ + 4096); SBAR(); \
        acc[1][0] = MFMA32(K1_WF(F, 0), xf[F][1], acc[1][0]); SBAR(); \
        acc[1][1] = MFMA32(K1_WF(F, 1), xf[F][1], acc[1][1]); \
        acc[1][2] = MFMA32(xf[F][1], K1_WF(F, 2), acc[1][2]); SBAR(); \
    } while (0)

    WAIT_BAR(5);
    DG(3);
    {
        const lds_ptr xp_ = lds + xoff0; const lds_ptr wp_ = lds + woff0;
        xf[0][0] = lds_rd128(xp_); xf[0][1] = lds_rd128(xp_ + 4096);
#pragma unroll
        for (int sec = 0; sec < 3; ++sec) { wl[0][sec] = lds_tr(wp_ + sec * 8192); wh[0][sec] = lds_tr(wp_ + sec * 8192 + 512); }
    }
    SBAR();
#pragma unroll K1U
    for (int kt = 0; kt < 15; ++kt) {
        const lds_ptr st = lds + (kt & 3) * K1_STAGE;
        const lds_ptr sn = lds + ((kt + 1) & 3) * K1_STAGE;
        K1_STEP(0, st, 1, false);
        K1_STEP(1, st, 2, false);
        K1_STEP(0, st, 3, false);
        K1_STEP(1, sn, 0, kt + 1);
    }
    {
        const lds_ptr st = lds + 3 * K1_STAGE;
        K1_STEP(0, st, 1, false);
        K1_STEP(1, st, 2, false);
        K1_STEP(0, st, 3, false);
#pragma unroll
        for (int m = 0; m < 2; ++m) {
            acc[m][1] = MFMA32(K1_WF(1, 1), xf[1][m], acc[m][1]);
            acc[m][2] = MFMA32(xf[1][m], K1_WF(1, 2), acc[m][2]);
            acc[m][0] = MFMA32(K1_WF(1, 0), xf[1][m], acc[m][0]);
        }
    }
    DG(4);
    {
        const int h = 2 * hp + wn;
        const float qscale = 0.17677669529663687f * 1.4426950408889634f;
        const __amdgpu_buffer_rsrc_t rs = __builtin_amdgcn_make_buffer_rsrc(ws, 0, 32 << 20, 0x00020000);
#pragma unroll
        for (int m = 0; m < 2; ++m) {
            const int T = tb * 4 + wm * 2 + m;
            const unsigned dq = (unsigned)WS_Q + (h * 64 + T) * 2048 + lane * 16;
            const unsigned dk = (unsigned)WS_KV + (h * 64 + T) * 4096 + lane * 16;
            st_sc1(pack8n(acc[m][1], 0), rs, dk);
            st_sc1(pack8n(acc[m][1], 1), rs, dk + 1024);
            st_sc1(pack8n(acc[m][2], 0), rs, dk + 2048);
            st_sc1(pack8n(acc[m][2], 1), rs, dk + 3072);
            st_sc1(pack8(acc[m][0], 0, qscale), rs, dq);
            st_sc1(pack8(acc[m][0], 1, qscale), rs, dq + 1024);
        }
    }
    DG_END(5);
}

constexpr int K2_SLOT = 32768;
constexpr int K2_SF = 65536;
constexpr int K2_CV = 98304;
constexpr int CV_HA = 0, CV_HB = 4160, CV_KV = 8320, CV_BYTES = 8320 + 480 + 4096;
constexpr int K2_LX = K2_CV + 4 * CV_BYTES;
constexpr int K2_FL = K2_LX + 2048;
constexpr int K2_SCR = K2_FL + 256;
constexpr int K2_LDS = K2_SCR + 4096;
constexpr int F_X = 0, F_R = 32, F_W2 = 64, C_OST = 68, C_S1 = 76, C_S2 = 80, C_S3 = 128, C_ZST = 88, F_ZGO = 92,
              C_KST = 96, C_U0 = 100, C_LD = 108, F_KGO = 116, C_END = 120, C_OL = 72, C_HF = 84;
#define NG_IN 0
#define GATE_WORD C_END
#define GATE_VAL 0
#define X_NZ 4
#define X_NW3 8
#define ZP_GATE 4u
#define ZL_GATE 4
#define ZL_AT 31
#define Z_SLEEP0 0
#define O_SLEEP 0
#define Z_SLEEP 8
#define OWIN 16
#define OW0 8
#define WD 2
#define W3_AT 10
#define X_NOLDW 0
#define X_NOLDO 0
#define X_NOCHK 0
#define KROT (nt * 2)
#define NG_LAST 0
#define PACE_IN "s_nop 7"
#define PACE_OUT "s_nop 1"
#define P3_PRIO 1
#define TOP_SLEEP 2
#define A_AUX 16
#define B_AUX 16
#define B_LDS 0
#define TL 8
#define XR_AT 62
#define W3_AUX 0
#define W3M 13
#define A16 1
#define SEL4 2
#define KV_DOT 1
#define ZE_PRIO 3
#define P3_T1 99
#define P3_T2 99
#define AUX_PRIO 2

__global__ __launch_bounds__(768, 3) void k2_attn(char* __restrict__ ws, const char* __restrict__ Aq,
                                                  const char* __restrict__ Ak, const char* __restrict__ Av,
                                                  const char* __restrict__ shiftk, const char* __restrict__ ssmk,
                                                  const unsigned* __restrict__ ssmD, const char* __restrict__ Wo,
                                                  char* __restrict__ out) {
    __shared__ __attribute__((aligned(16))) char lds_raw[K2_LDS];
    const lds_ptr lds = (lds_ptr)lds_raw;
    const lds_ptr fl = lds + K2_FL;
    const unsigned lds0 = (unsigned)(uintptr_t)lds;
    const int tid = threadIdx.x, lane = tid & 63;
    const int w = __builtin_amdgcn_readfirstlane(tid >> 6);
    const int b = blockIdx.x, xcd = b & 7, idx = b >> 3;
    const int h = xcd * 4 + (idx >> 3), qb = idx & 7;
    const int tt = xcd * 2 + (idx >> 4), nt = idx & 15;
    const char* kvg = ws + WS_KV + (size_t)h * 64 * 4096;
    const __amdgpu_buffer_rsrc_t rs = __builtin_amdgcn_make_buffer_rsrc(ws, 0, 32 << 20, 0x00020000);
    const __amdgpu_buffer_rsrc_t ro = __builtin_amdgcn_make_buffer_rsrc(out, 0, 8 << 20, 0x00020000);
    gu32* const cnt = (gu32*)(ws + WS_CNT);
    auto signal = [&]() {
        (void)__hip_atomic_fetch_add(cnt + ((lane & 31) * 8 + (lane < 32 ? xcd : qb)) * 64, 1u, RLX_AGENT);
    };
    const unsigned wvoff = (unsigned)(lane >> 3) * 2048u + nt * 128 + (((lane & 7) ^ (((lane >> 4) & 1) << 2)) << 4);
    const int krot = KROT;
    auto wsrc = [&](int hf, int lp) { return Wo + (size_t)(hf * 64 + ((lp + krot * 2) & 63)) * 16384; };
    DG_DECL(1)
    const unsigned voff = lane * 16;
    const int cw = w & 3;
    auto issue = [&](int st) {
        const char* src = kvg + (st * 4 + cw) * 4096;
        const unsigned dst = lds0 + (st & 1) * K2_SLOT + cw * 4096;
#pragma unroll
        for (int i = 0; i < 4; ++i) glds16(voff, src + i * 1024, dst + i * 1024);
#pragma unroll
        for (int i = 0; i < 4; ++i) glds16(voff, src + 131072 + i * 1024, dst + 16384 + i * 1024);
    };
    if (w >= 8) {
        __builtin_amdgcn_s_sleep(TOP_SLEEP);
#pragma unroll
        for (int i = 2; i < 8; ++i)
            glds16(voff, kvg + (i & 1) * 131072 + (i >> 1) * 4096 + cw * 1024, lds0 + (i & 1) * 16384 + (i >> 1) * 4096 + cw * 1024);
        RAW_BAR();
        issue(1);
        WAIT_BAR(8);
    } else {
        const int tq = cw * 2 + (w >> 2);
        const char* qg = ws + WS_Q + (size_t)(h * 64 + qb * 8 + tq) * 2048;
        glds16(voff, kvg + (w >> 2) * 131072 + cw * 1024, lds0 + (w >> 2) * 16384 + cw * 1024);
        glds16(voff, qg, lds0 + K2_SF + tq * 2048);
        glds16(voff, qg + 1024, lds0 + K2_SF + tq * 2048 + 1024);
    }
    int w2 = w;
    asm volatile("" : "+s"(w2));

    if (w2 >= 8) {
        __builtin_amdgcn_s_setprio(3);
        const int ch = xcd * 128 + idx * 4 + cw;
        const lds_ptr cv = lds + K2_CV + cw * CV_BYTES;
        {
            const u32x4 z4 = {0u, 0u, 0u, 0u};
            if (lane < 2) *(LAS u32x4*)(cv + CV_HA + lane * 16) = z4;
            if (lane < 30) *(LAS u32x4*)(cv + CV_KV + lane * 16) = z4;
            if (cw == 0 && lane < 16) *(LAS u32x4*)(fl + lane * 16) = z4;
        }
        const int c0 = xcd * 128 + cw * 32, cq = lane & 7, rg = lane >> 3;
        const int row0 = idx * 64 + rg * 8;
        const unsigned roff = (unsigned)row0 * 2048u + (c0 + cq * 4) * 2;
        const unsigned toffs = (unsigned)(c0 + (lane >> 3)) * 4096u + idx * 128;
        u32x4 sk[2];
        sk[0] = gld128((unsigned)(c0 + cq * 4) * 8u, shiftk); sk[1] = gld128((unsigned)(c0 + cq * 4) * 8u, shiftk + 16);
        unsigned dd = gld32((unsigned)(ch >> 1) * 4u, ssmD);
        auto stage = [&](const char* src, int dst) {
            const unsigned sbase = (unsigned)(idx * 64 + cw * 16 + (lane >> 4)) * 2048u + xcd * 256;
#pragma unroll
            for (int i = 0; i < 4; ++i)
                glds16(sbase + (((lane & 15) ^ (((2 * cw + (i >> 1)) & 3) << 2)) << 4), src + i * 8192, lds0 + dst + cw * 4096 + i * 1024);
        };
        auto blank = [&](unsigned off) {
            const u32x4 e4 = {EMPTY, EMPTY, EMPTY, EMPTY};
            CFENCE();
            st_sc1(e4, rs, off + voff); st_sc1(e4, rs, off + 1024 + voff);
            CFENCE();
        };
        WAIT_BAR(3);
        DG(12);
        issue(2);
        stage(Ak, K2_SF);
        glds16((unsigned)(idx * 64 - (idx ? 4 : 0) + (lane >> 4)) * 2048u + xcd * 256 + (((lane & 15) ^ 12) << 4), Ak, lds0 + K2_SCR + cw * 1024);
        blank((unsigned)WS_O + (unsigned)((qb * 8 + cw * 2) * 32 + h) * 2048u);
        WAIT_BAR(7);
        DG(9);
        issue(3);
        stage(Av, K2_SF + 16384);
        blank((unsigned)WS_O + (unsigned)((qb * 8 + cw * 2 + 1) * 32 + h) * 2048u);
        WAIT_BAR(6);
        DG(3);
        issue(4);
#pragma unroll
        for (int i = 0; i < 4; ++i) glds16(voff, ssmk + (size_t)ch * 4096 + i * 1024, lds0 + K2_CV + cw * CV_BYTES + CV_HA + 32 + i * 1024);
        blank((unsigned)WS_ZT + ch * 4096); blank((unsigned)WS_ZT + ch * 4096 + 2048);
        WAIT_BAR(8);
        DG(5);
        issue(5);
        SBAR();
        asm volatile("" : "+v"(sk[0]), "+v"(sk[1]), "+v"(dd));
        dd = __builtin_amdgcn_readfirstlane(dd);
        {
            u32x2 kr[11], vr[8];
            lds_ptr kb = lds + K2_SF + rg * 2048 + ((cw ^ (rg & 3)) << 6) + cq * 8;
            asm volatile("" : "+v"(kb));
            const lds_ptr hb = lds + (rg ? K2_SF + rg * 2048 - 768 : K2_SCR + cw * 1024 + 256) + ((cw ^ ((rg + 3) & 3)) << 6) + cq * 8;
#pragma unroll
            for (int i = 0; i < 3; ++i) kr[i] = *(const LAS u32x2*)(hb + i * 256);
#pragma unroll
            for (int i = 0; i < 8; ++i) kr[3 + i] = *(const LAS u32x2*)(kb + i * 256);
#pragma unroll
            for (int i = 0; i < 8; ++i) vr[i] = *(const LAS u32x2*)(kb + 16384 + i * 256);
            if (row0 == 0) { kr[0] = u32x2{0u, 0u}; kr[1] = u32x2{0u, 0u}; kr[2] = u32x2{0u, 0u}; }
            const lds_ptr tb = cv + CV_KV + 480;
#pragma unroll
            for (int c = 0; c < 4; ++c) {
                const unsigned s01 = sk[c >> 1][(c & 1) * 2], s23 = sk[c >> 1][(c & 1) * 2 + 1];
                unsigned kp[10];
                float kv[8], vf[8];
#pragma unroll
                for (int j = 0; j < 10; ++j) kp[j] = __builtin_amdgcn_perm(kr[j][c >> 1], kr[j + 1][c >> 1], (c & 1) ? 0x07060302u : 0x05040100u);
                SBAR();
#pragma unroll
                for (int i = 0; i < 8; ++i) asm("v_dot2_f32_bf16 %0, %1, %2, 0" : "=v"(kv[i]) : "v"(kp[i]), "v"(s23));
                SBAR();
#pragma unroll
                for (int i = 0; i < 8; ++i) asm("v_dot2_f32_bf16 %0, %1, %2, %0" : "+v"(kv[i]) : "v"(kp[i + 2]), "v"(s01));
                SBAR();
#pragma unroll
                for (int i = 0; i < 8; ++i) vf[i] = (c & 1) ? bfhi(vr[i][c >> 1]) : bflo(vr[i][c >> 1]);
                SBAR();
#pragma unroll
                for (int i = 0; i < 8; ++i) kv[i] = mul_vv(kv[i], vf[i]);
                u32x4 o4;
#pragma unroll
                for (int i = 0; i < 4; ++i) o4[i] = cvtpk(kv[7 - 2 * i], kv[6 - 2 * i]);
                *(LAS u32x4*)(tb + (cq * 4 + c) * 128 + ((rg ^ cq) << 4)) = o4;
            }
#pragma unroll
            for (int i = 0; i < 4; ++i) {
                const u32x4 t = *(const LAS u32x4*)(tb + i * 1024 + lane * 16);
                st_sc1(t, rs, (unsigned)WS_KVR + toffs + i * 32768 + (((lane & 7) ^ (2 * i + (lane >> 5))) << 4));
            }
        }
        CFENCE();
        DG(7);
        WAIT_BAR(4);
        DG(6);
        issue(6);
        asm volatile("s_waitcnt vmcnt(8)" ::: "memory");
        if (lds_add_rtn(fl + C_KST, lane) == 3u) signal();
#pragma unroll
        for (int i = 0; i < 8; ++i)
            glds16(wvoff, wsrc(i >> 2, cw * 4 + (i & 3)), lds0 + K2_SF + (i >> 2) * 16384 + (cw * 4 + (i & 3)) * 1024);
        {
            const u32x4 z4 = {0u, 0u, 0u, 0u};
            if (lane < 2) *(LAS u32x4*)(cv + CV_HB + lane * 16) = z4;
#pragma unroll
            for (int i = 0; i < 4; ++i) {
                const int off = 32 + i * 1024 + lane * 16;
                const u32x4 hv = *(const LAS u32x4*)(cv + CV_HA + off);
                const unsigned pv = *(const LAS unsigned*)(cv + CV_HA + off - 4);
                u32x4 sh;
                sh[0] = __builtin_amdgcn_alignbit(hv[0], pv, 16);
                sh[1] = __builtin_amdgcn_alignbit(hv[1], hv[0], 16);
                sh[2] = __builtin_amdgcn_alignbit(hv[2], hv[1], 16);
                sh[3] = __builtin_amdgcn_alignbit(hv[3], hv[2], 16);
                *(LAS u32x4*)(cv + CV_HB + off) = sh;
            }
        }
        WAIT_BAR(8);
        DG(8);
        issue(7);
#pragma unroll
        for (int i = 0; i < 8; ++i)
            glds16(wvoff, wsrc(idx >> 4, 32 + cw * 8 + i), lds0 + K2_SCR + cw * 1024);
        WAIT_BAR(8);
        DG(11);
        if (cw == 0) { poll_ge(cnt + b * 64, 64u); lds_add(fl + F_KGO, lane); }
        else lds_wait_ge(fl + F_KGO, 1u);
        {
            u32x4 t[4];
#pragma unroll
            for (int i = 0; i < 4; ++i) t[i] = ld_sc1(rs, (unsigned)WS_KVR + ch * 4096 + i * 1024 + voff);
#pragma unroll
            for (int i = 0; i < 4; ++i) *(LAS u32x4*)(cv + CV_KV + 480 + i * 1024 + lane * 16) = t[i];
        }
        const int r1 = lane & 15, q = lane >> 4;
        const int toff = (r1 & 1) ? CV_HA + 2 * (16 + r1 + 9 - 8 * q) : CV_HB + 2 * (16 + r1 + 10 - 8 * q);
        const int boff = CV_KV + 448 + 32 * r1 + 16 * q;
        f32x4 y[8];
        u32x4 tf[8];
        bf16x8 bf[8];
        const lds_ptr bp = cv + boff - 448;
        lds_ptr tp[4];
#pragma unroll
        for (int i = 0; i < 4; ++i) { tp[i] = cv + toff + 1024 * i; asm volatile("" : "+v"(tp[i])); }
#define LDT(bb, a, hf) do { const LAS unsigned* t_ = (const LAS unsigned*)(tp[(a) >> 1] + 64 * (bb) + 512 * ((a) & 1) + 8 * (hf)); \
            tf[a][2 * (hf)] = t_[0]; tf[a][2 * (hf) + 1] = t_[1]; SBAR(); } while (0)
#define LDB(bb, a) do { bf[a] = lds_rd128(bp + 448 - 64 * (bb) + 512 * (a)); SBAR(); } while (0)
#define CM(a, ww) do { asm volatile("v_mfma_f32_16x16x32_bf16 %0, %1, %2, %0" : "+v"(y[(a) + (ww)]) : "v"(tf[a]), "v"(bf[ww])); SBAR(); } while (0)
#define CM0(a, ww) do { asm volatile("v_mfma_f32_16x16x32_bf16 %0, %1, %2, 0" : "=&v"(y[(a) + (ww)]) : "v"(tf[a]), "v"(bf[ww])); SBAR(); } while (0)
#define PC() do { asm volatile(PACE_OUT); SBAR(); } while (0)
#pragma unroll
        for (int a = 0; a < 8; ++a) { if (a < 5) { LDT(0, a, 0); LDT(0, a, 1); } LDB(0, 7 - a); }
#pragma unroll
        for (int i = 0; i < 8; ++i)
            glds16(wvoff, wsrc(i >> 2, 16 + cw * 4 + (i & 3)), lds0 + (i >> 2) * 16384 + (cw * 4 + (i & 3)) * 1024);
#pragma unroll
        for (int i = 0; i < 4; ++i)
            glds16((unsigned)(idx * 64 + cw * 16 + (lane >> 4)) * 2048u + xcd * 256 + (lane & 15) * 16, Aq + i * 8192, lds0 + K2_SCR + cw * 1024);
        RAW_BAR();
        asm volatile("s_waitcnt vmcnt(4)" ::: "memory");
        lds_add(fl + C_S1, lane);
        __builtin_amdgcn_s_setprio(AUX_PRIO);
        const float dch = (ch & 1) ? bfhi(dd) : bflo(dd);
        const lds_ptr kdp = cv + CV_KV + 480 + 32 * r1 + 16 * (q >> 1) + ((q & 1) ? 0 : 8);
        float kd[8][4];
        u32x2 kk;
        unsigned kc0, kc1, zo[8][2];
#define KD_RD(t) do { kk = *(const LAS u32x2*)(kdp + 512 * (t)); SBAR(); } while (0)
#define KD_UP0(t) do { kd[t][0] = bfhi(kk[1]); kd[t][1] = bflo(kk[1]); SBAR(); } while (0)
#define KD_UP1(t) do { kd[t][2] = bfhi(kk[0]); kd[t][3] = bflo(kk[0]); SBAR(); } while (0)
#define KD_MU0(t) do { kd[t][0] = mul_sv(dch, kd[t][0]); kd[t][1] = mul_sv(dch, kd[t][1]); SBAR(); } while (0)
#define KD_MU1(t) do { kd[t][2] = mul_sv(dch, kd[t][2]); kd[t][3] = mul_sv(dch, kd[t][3]); SBAR(); } while (0)
#define KD_CV(t) do { kc0 = cvtpk(kd[t][0], kd[t][1]); kc1 = cvtpk(kd[t][2], kd[t][3]); SBAR(); } while (0)
#define KD_X0(t) do { kd[t][0] = bflo(kc0); kd[t][1] = bfhi(kc0); SBAR(); } while (0)
#define KD_X1(t) do { kd[t][2] = bflo(kc1); kd[t][3] = bfhi(kc1); SBAR(); } while (0)
#define ZF0(t) do { kc0 = cvtpk(y[t][0], y[t][1]); kc1 = cvtpk(y[t][2], y[t][3]); SBAR(); } while (0)
#define ZF1(t) do { y[t][0] = bflo(kc0); y[t][1] = bfhi(kc0); SBAR(); } while (0)
#define ZF2(t) do { y[t][2] = bflo(kc1); y[t][3] = bfhi(kc1); SBAR(); } while (0)
#define ZF3(t) do { y[t][0] = add_vv(y[t][0], kd[t][0]); y[t][1] = add_vv(y[t][1], kd[t][1]); SBAR(); } while (0)
#define ZF4(t) do { y[t][2] = add_vv(y[t][2], kd[t][2]); y[t][3] = add_vv(y[t][3], kd[t][3]); SBAR(); } while (0)
#define ZF5(t) do { zo[t][0] = nz(cvtpk(y[t][0], y[t][1])); zo[t][1] = nz(cvtpk(y[t][2], y[t][3])); SBAR(); } while (0)
#define ZST(pr) do { auto s0 = __builtin_amdgcn_permlane16_swap(zo[2 * (pr)][0], zo[2 * (pr) + 1][0], false, false); \
            auto s1 = __builtin_amdgcn_permlane16_swap(zo[2 * (pr)][1], zo[2 * (pr) + 1][1], false, false); \
            st_sc1(u32x4{s0[0], s1[0], s0[1], s1[1]}, rs, (unsigned)WS_ZT + ch * 4096 + (pr) * 1024 + voff); SBAR(); } while (0)
#define W3(i) do { if ((i) == 0) PC(); } while (0)
#define HF() do { lds_add(fl + C_HF, lane); SBAR(); } while (0)
        CM0(0, 7); LDB(1, 7); CM0(0, 6); LDT(0, 5, 0); CM0(0, 5); LDT(0, 5, 1); CM0(0, 4); LDT(0, 6, 0); CM0(0, 3); LDT(0, 6, 1); CM0(0, 2); LDT(0, 7, 0); CM0(0, 1); LDT(0, 7, 1); CM0(0, 0); KD_RD(0);
        CM(1, 6); LDB(1, 6); CM(1, 5); LDT(1, 0, 0); CM(1, 4); LDT(1, 0, 1); CM(1, 3); PC(); CM(1, 2); PC(); CM(1, 1); KD_UP0(0); CM(1, 0); KD_UP1(0);
        CM(2, 5); LDB(1, 5); CM(2, 4); LDT(1, 1, 0); CM(2, 3); LDT(1, 1, 1); CM(2, 2); KD_MU0(0); CM(2, 1); KD_MU1(0); CM(2, 0); KD_CV(0);
        CM(3, 4); LDB(1, 4); CM(3, 3); LDT(1, 2, 0); CM(3, 2); LDT(1, 2, 1); CM(3, 1); KD_X0(0); CM(3, 0); KD_X1(0);
        CM(4, 3); LDB(1, 3); CM(4, 2); LDT(1, 3, 0); CM(4, 1); LDT(1, 3, 1); CM(4, 0); KD_RD(1);
        CM(5, 2); LDB(1, 2); CM(5, 1); LDT(1, 4, 0); CM(5, 0); LDT(1, 4, 1);
        CM(6, 1); LDB(1, 1); CM(6, 0); PC();
        CM(7, 0); LDB(1, 0);
        CM(0, 7); LDB(2, 7); CM(0, 6); LDT(1, 5, 0); CM(0, 5); LDT(1, 5, 1); CM(0, 4); LDT(1, 6, 0); CM(0, 3); LDT(1, 6, 1); CM(0, 2); LDT(1, 7, 0); CM(0, 1); LDT(1, 7, 1); CM(0, 0); PC();
        CM(1, 6); LDB(2, 6); CM(1, 5); LDT(2, 0, 0); CM(1, 4); LDT(2, 0, 1); CM(1, 3); KD_UP0(1); CM(1, 2); KD_UP1(1); CM(1, 1); KD_MU0(1); CM(1, 0); KD_MU1(1);
        CM(2, 5); LDB(2, 5); CM(2, 4); LDT(2, 1, 0); CM(2, 3); LDT(2, 1, 1); CM(2, 2); KD_CV(1); CM(2, 1); KD_X0(1); CM(2, 0); KD_X1(1);
        CM(3, 4); LDB(2, 4); CM(3, 3); LDT(2, 2, 0); CM(3, 2); LDT(2, 2, 1); CM(3, 1); KD_RD(2); CM(3, 0); PC();
        CM(4, 3); LDB(2, 3); CM(4, 2); LDT(2, 3, 0); CM(4, 1); LDT(2, 3, 1); CM(4, 0); PC();
        CM(5, 2); LDB(2, 2); CM(5, 1); LDT(2, 4, 0); CM(5, 0); LDT(2, 4, 1);
        CM(6, 1); LDB(2, 1); CM(6, 0); KD_UP0(2);
        CM(7, 0); LDB(2, 0);
        CM(0, 7); LDB(3, 7); CM(0, 6); LDT(2, 5, 0); CM(0, 5); LDT(2, 5, 1); CM(0, 4); LDT(2, 6, 0); CM(0, 3); LDT(2, 6, 1); CM(0, 2); LDT(2, 7, 0); CM(0, 1); LDT(2, 7, 1); CM(0, 0); KD_UP1(2);
        CM(1, 6); LDB(3, 6); CM(1, 5); LDT(3, 0, 0); CM(1, 4); LDT(3, 0, 1); CM(1, 3); KD_MU0(2); CM(1, 2); KD_MU1(2); CM(1, 1); KD_CV(2); CM(1, 0); KD_X0(2);
        CM(2, 5); LDB(3, 5); CM(2, 4); LDT(3, 1, 0); CM(2, 3); LDT(3, 1, 1); CM(2, 2); KD_X1(2); CM(2, 1); KD_RD(3); CM(2, 0); PC();
        CM(3, 4); LDB(3, 4); CM(3, 3); LDT(3, 2, 0); CM(3, 2); LDT(3, 2, 1); CM(3, 1); PC(); CM(3, 0); KD_UP0(3);
        CM(4, 3); LDB(3, 3); CM(4, 2); LDT(3, 3, 0); CM(4, 1); LDT(3, 3, 1); CM(4, 0); KD_UP1(3);
        CM(5, 2); LDB(3, 2); CM(5, 1); LDT(3, 4, 0); CM(5, 0); LDT(3, 4, 1);
        CM(6, 1); LDB(3, 1); CM(6, 0); KD_MU0(3);
        CM(7, 0); LDB(3, 0);
        CM(0, 7); LDB(4, 7); CM(0, 6); LDT(3, 5, 0); CM(0, 5); LDT(3, 5, 1); CM(0, 4); LDT(3, 6, 0); CM(0, 3); LDT(3, 6, 1); CM(0, 2); LDT(3, 7, 0); CM(0, 1); LDT(3, 7, 1); CM(0, 0); KD_MU1(3);
        CM(1, 6); LDB(4, 6); CM(1, 5); LDT(4, 0, 0); CM(1, 4); LDT(4, 0, 1); CM(1, 3); KD_CV(3); CM(1, 2); KD_X0(3); CM(1, 1); KD_X1(3); CM(1, 0); KD_RD(4);
        CM(2, 5); LDB(4, 5); CM(2, 4); LDT(4, 1, 0); CM(2, 3); LDT(4, 1, 1); CM(2, 2); PC(); CM(2, 1); PC(); CM(2, 0); KD_UP0(4);
        CM(3, 4); LDB(4, 4); CM(3, 3); LDT(4, 2, 0); CM(3, 2); LDT(4, 2, 1); CM(3, 1); KD_UP1(4); CM(3, 0); KD_MU0(4);
        CM(4, 3); LDB(4, 3); CM(4, 2); LDT(4, 3, 0); CM(4, 1); LDT(4, 3, 1); CM(4, 0); KD_MU1(4);
        CM(5, 2); LDB(4, 2); CM(5, 1); LDT(4, 4, 0); CM(5, 0); LDT(4, 4, 1);
        CM(6, 1); LDB(4, 1); CM(6, 0); KD_CV(4);
        CM(7, 0); LDB(4, 0);
        CM(0, 7); LDB(5, 7); CM(0, 6); LDT(4, 5, 0); CM(0, 5); LDT(4, 5, 1); CM(0, 4); LDT(4, 6, 0); CM(0, 3); LDT(4, 6, 1); CM(0, 2); LDT(4, 7, 0); CM(0, 1); LDT(4, 7, 1); CM(0, 0); KD_X0(4);
        CM(1, 6); LDB(5, 6); CM(1, 5); LDT(5, 0, 0); CM(1, 4); LDT(5, 0, 1); CM(1, 3); KD_X1(4); CM(1, 2); KD_RD(5); CM(1, 1); PC(); CM(1, 0); PC();
        CM(2, 5); LDB(5, 5); CM(2, 4); LDT(5, 1, 0); CM(2, 3); LDT(5, 1, 1); CM(2, 2); KD_UP0(5); CM(2, 1); KD_UP1(5); CM(2, 0); KD_MU0(5);
        CM(3, 4); LDB(5, 4); CM(3, 3); LDT(5, 2, 0); CM(3, 2); LDT(5, 2, 1); CM(3, 1); KD_MU1(5); CM(3, 0); KD_CV(5);
        CM(4, 3); LDB(5, 3); CM(4, 2); LDT(5, 3, 0); CM(4, 1); LDT(5, 3, 1); CM(4, 0); KD_X0(5);
        CM(5, 2); LDB(5, 2); CM(5, 1); LDT(5, 4, 0); CM(5, 0); LDT(5, 4, 1);
        CM(6, 1); LDB(5, 1); CM(6, 0); KD_X1(5);
        CM(7, 0); LDB(5, 0);
        CM(0, 7); LDB(6, 7); CM(0, 6); LDT(5, 5, 0); CM(0, 5); LDT(5, 5, 1); CM(0, 4); LDT(5, 6, 0); CM(0, 3); LDT(5, 6, 1); CM(0, 2); LDT(5, 7, 0); CM(0, 1); LDT(5, 7, 1); CM(0, 0); KD_RD(6);
        CM(1, 6); LDB(6, 6); CM(1, 5); LDT(6, 0, 0); CM(1, 4); LDT(6, 0, 1); CM(1, 3); PC(); CM(1, 2); PC(); CM(1, 1); KD_UP0(6); CM(1, 0); KD_UP1(6);
        CM(2, 5); LDB(6, 5); CM(2, 4); LDT(6, 1, 0); CM(2, 3); LDT(6, 1, 1); CM(2, 2); KD_MU0(6); CM(2, 1); KD_MU1(6); CM(2, 0); KD_CV(6);
        CM(3, 4); LDB(6, 4); CM(3, 3); LDT(6, 2, 0); CM(3, 2); LDT(6, 2, 1); CM(3, 1); KD_X0(6); CM(3, 0); KD_X1(6);
        CM(4, 3); LDB(6, 3); CM(4, 2); LDT(6, 3, 0); CM(4, 1); LDT(6, 3, 1); CM(4, 0); KD_RD(7);
        CM(5, 2); LDB(6, 2); CM(5, 1); LDT(6, 4, 0); CM(5, 0); LDT(6, 4, 1);
        CM(6, 1); LDB(6, 1); CM(6, 0); PC();
        CM(7, 0); LDB(6, 0);
        CM(0, 7); LDB(7, 7); CM(0, 6); LDT(6, 5, 0); CM(0, 5); LDT(6, 5, 1); CM(0, 4); LDT(6, 6, 0); CM(0, 3); LDT(6, 6, 1); CM(0, 2); LDT(6, 7, 0); CM(0, 1); LDT(6, 7, 1); CM(0, 0); PC();
        CM(1, 6); LDB(7, 6); CM(1, 5); LDT(7, 0, 0); CM(1, 4); LDT(7, 0, 1); CM(1, 3); KD_UP0(7); CM(1, 2); KD_UP1(7); CM(1, 1); KD_MU0(7); CM(1, 0); KD_MU1(7);
        CM(2, 5); LDB(7, 5); CM(2, 4); LDT(7, 1, 0); CM(2, 3); LDT(7, 1, 1); CM(2, 2); KD_CV(7); CM(2, 1); KD_X0(7); CM(2, 0); KD_X1(7);
        CM(3, 4); LDB(7, 4); CM(3, 3); LDT(7, 2, 0); CM(3, 2); LDT(7, 2, 1); CM(3, 1); PC(); CM(3, 0); PC();
        CM(4, 3); LDB(7, 3); CM(4, 2); LDT(7, 3, 0); CM(4, 1); LDT(7, 3, 1); CM(4, 0); PC();
        CM(5, 2); LDB(7, 2); CM(5, 1); LDT(7, 4, 0); CM(5, 0); LDT(7, 4, 1);
        CM(6, 1); LDB(7, 1); CM(6, 0); PC();
        CM(7, 0); LDB(7, 0);
        CM(0, 3); LDT(7, 5, 0); CM(0, 2); LDT(7, 5, 1); CM(1, 2); LDT(7, 6, 0); CM(1, 1); LDT(7, 6, 1); CM(2, 1); LDT(7, 7, 0); CM(2, 0); LDT(7, 7, 1);
        CM(3, 0); HF(); CM(0, 1); PC(); CM(0, 0); W3(0); CM(1, 0); ZF0(2); W3(1); CM(0, 5); ZF1(2); W3(2); CM(0, 4); ZF2(2); W3(3);
        CM(1, 4); ZF3(2); W3(4); CM(1, 3); ZF4(2); W3(5); CM(2, 3); ZF5(2); W3(6); CM(2, 2); ZF0(3); W3(7); CM(3, 2); ZF1(3);  CM(3, 1); ZF2(3);
        CM(4, 1); ZF3(3);  CM(4, 0); ZF4(3);  CM(5, 0); ZF5(3);  CM(0, 7); ZST(1);  CM(0, 6); ZF0(0);  CM(1, 6); ZF1(0);
        CM(1, 5); ZF2(0);  CM(2, 5); ZF3(0);  CM(2, 4); ZF4(0);  CM(3, 4); ZF5(0);  CM(3, 3); ZF0(1);  CM(4, 3); ZF1(1);
        CM(4, 2); ZF2(1);  CM(5, 2); ZF3(1);  CM(5, 1); ZF4(1);  CM(6, 1); ZF5(1);  CM(6, 0); ZST(0);  CM(7, 0); ZF0(4);
        DG(4);
        ZF1(4); ZF2(4); ZF3(4); ZF4(4); ZF5(4); ZF0(5); ZF1(5); ZF2(5); ZF3(5); ZF4(5); ZF5(5); ZST(2); ZF0(6); ZF1(6); ZF2(6); ZF3(6); ZF4(6); ZF5(6); ZF0(7); ZF1(7); ZF2(7); ZF3(7); ZF4(7); ZF5(7); ZST(3);
        DG(10);
        __builtin_amdgcn_s_setprio(3);
        while ((unsigned)__builtin_amdgcn_readfirstlane(*(volatile LAS unsigned*)(fl + C_OL)) < ZL_GATE) __builtin_amdgcn_s_sleep(4);
        CFENCE();
        const int arow = idx * 64 + (cw >> 1) * 32 + rg * 4, acol = xcd * 256 + (cw & 1) * 128 + cq * 16;
        u32x4 qr[4];
#pragma unroll
        for (int i = 0; i < 4; ++i) qr[i] = gld128((unsigned)(arow * 2048 + acol), Aq + i * 2048);
        {
            const lds_ptr tr = cv + CV_KV + 480;
            u32x4 t[4];
            const int ck = (cw >> 1) * 4 + ((lane ^ ((lane >> 5) * 2)) & 3);
            const unsigned zb = (unsigned)WS_ZT + (unsigned)(xcd * 128 + (cw & 1) * 64 + (lane >> 4) * 4) * 4096u + (idx >> 3) * 1024 +
                                ((idx & 3) * 4 + ((idx >> 2) & 1) * 16 + (ck >> 1) + (ck & 1) * 32) * 16;
#pragma unroll 1
            for (unsigned spins = 0; spins < (1u << 20); ++spins) {
#pragma unroll
                for (int i = 0; i < 4; ++i) t[i] = ld_sc1s(rs, zb + ((((lane >> 2) & 3) ^ i) << 12), i * 65536);
                if (!missing(mx4(mx4(mx8(t[0], t[1]), t[2]), t[3]))) break;
                __builtin_amdgcn_s_sleep(Z_SLEEP);
            }
            DG(13);
            __builtin_amdgcn_s_setprio(ZE_PRIO);
#pragma unroll
            for (int i = 0; i < 4; ++i) *(LAS u32x4*)(tr + i * 1024 + lane * 16) = t[i];
#pragma unroll
            for (int i = 0; i < 4; ++i) asm volatile("" : "+v"(qr[i]));
            u32x2 zc[8];
#pragma unroll
            for (int c = 0; c < 8; ++c)
                zc[c] = *(const LAS u32x2*)(tr + cq * 512 + (c >> 2) * 256 + ((((c & 3) * 4 + (rg >> 1)) ^ (cq * 2)) << 4) + (rg & 1) * 8);
#pragma unroll
            for (int i = 0; i < 4; ++i) {
                u32x4 o;
#pragma unroll
                for (int c = 0; c < 4; ++c) {
                    const unsigned z0 = zc[2 * c][i >> 1], z1 = zc[2 * c + 1][i >> 1];
                    o[c] = cvtpk(mul_vv(bflo(qr[i][c]), (i & 1) ? bfhi(z0) : bflo(z0)), mul_vv(bfhi(qr[i][c]), (i & 1) ? bfhi(z1) : bflo(z1)));
                }
                __builtin_amdgcn_raw_buffer_store_b128(o, ro, arow * 4096 + acol, i * 4096, AUX_SC1);
            }
        }
        DG_END(14);
        return;
    }

    const int half = w >> 2;
    const int TA = qb * 8 + (w & 3) * 2 + half, TB = TA ^ 1;
    bf16x8 sel;
    {
        const short one = (((lane >> 2) & 1) == ((lane >> 4) & 1) && (lane & 11) == 0) ? (short)0x3f80 : (short)0;
#pragma unroll
        for (int i = 0; i < 8; ++i) sel[i] = one;
    }
    f32x16 oA = f32x16{}, oB = f32x16{};
    f32x4 lA = f32x4{}, lB = f32x4{};
    f32x16 negm, sA, sB;
    lds_ptr lp = lds + half * 16384 + lane * 16;
    bf16x8 k0[2], k1[2], v0[2], v1[2];
    u32x4 p0, p1 = {0u, 0u, 0u, 0u};
#define EX(X, i) X[i] = __builtin_amdgcn_exp2f(X[i])
    lds_ptr qpa = lds + K2_SF + (TA & 7) * 2048 + lane * 16, qpb = lds + K2_SF + (TB & 7) * 2048 + lane * 16;
    asm volatile("" : "+v"(qpa), "+v"(qpb), "+v"(lp), "+v"(sel), "+v"(oA), "+v"(oB), "+v"(lA), "+v"(lB), "+v"(p1));
    WAIT_BAR(0);
    DG(3);
    const bf16x8 qa0 = lds_rd128(qpa), qb0 = lds_rd128(qpb);
    const bf16x8 x0 = lds_rd128(lds + lane * 16), x1 = lds_rd128(lds + lane * 16 + 1024);
    const bf16x8 qa1 = lds_rd128(qpa + 1024), qb1 = lds_rd128(qpb + 1024);
    {
        k0[0] = lds_rd128(lp); k1[0] = lds_rd128(lp + 1024);
        v0[0] = lds_rd128(lp + 2048); v1[0] = lds_rd128(lp + 3072);
        sA = MFMA32(x0, qa0, f32x16{});
        sB = MFMA32(x0, qb0, f32x16{});
        sA = MFMA32(x1, qa1, sA);
        sB = MFMA32(x1, qb1, sB);
        float m = fmx(sA[0], sA[1]), m2 = fmx(sB[0], sB[1]);
#pragma unroll
        for (int i = 2; i < 16; i += 2) m = fmx(fmx(m, sA[i]), sA[i + 1]);
#pragma unroll
        for (int i = 2; i < 16; i += 2) m2 = fmx(fmx(m2, sB[i]), sB[i + 1]);
        m = fmx(m, m2);
        auto rr = __builtin_amdgcn_permlane32_swap(__float_as_uint(m), __float_as_uint(m), false, false);
        m = fmx(__uint_as_float(rr[0]), __uint_as_float(rr[1]));
#pragma unroll
        for (int i = 0; i < 16; ++i) negm[i] = -m;
        sA = MFMA32(k0[0], qa0, negm);
        sA = MFMA32(k1[0], qa1, sA);
        v1[1] = v1[0];
        SBAR();
        RAW_BAR();
        SBAR();
        EX(sA, 0); EX(sA, 1);
    }
    DG(10);
#define SUM16(l, p) asm volatile("v_mfma_f32_16x16x32_bf16 %0, %1, %2, %0" : "+v"(l) : "v"(sel), "v"(p))
#define HALF(C, oC, lC, N, qx, qy, ka, kb, oP, lP, vprev, vcur0, PRE) do { \
        oP = MFMA32(vprev, PB(p1), oP); SBAR(); \
        PRE; EX(C, 2); EX(C, 3); EX(C, 4); SBAR(); \
        N = MFMA32(ka, qx, negm); SBAR(); \
        EX(C, 5); EX(C, 6); EX(C, 7); SBAR(); \
        SUM16(lP, p1); SBAR(); \
        p0[0] = cvtpk(C[0], C[1]); p0[1] = cvtpk(C[2], C[3]); SBAR(); \
        N = MFMA32(kb, qy, N); SBAR(); \
        p0[2] = cvtpk(C[4], C[5]); p0[3] = cvtpk(C[6], C[7]); SBAR(); EX(C, 8); EX(C, 9); SBAR(); \
        oC = MFMA32(vcur0, PB(p0), oC); SBAR(); \
        EX(C, 10); EX(C, 11); EX(C, 12); SBAR(); \
        SUM16(lC, p0); SBAR(); \
        EX(C, 13); EX(C, 14); EX(C, 15); SBAR(); \
        p1[0] = cvtpk(C[8], C[9]); p1[1] = cvtpk(C[10], C[11]); p1[2] = cvtpk(C[12], C[13]); p1[3] = cvtpk(C[14], C[15]); SBAR(); \
        EX(N, 0); EX(N, 1); SBAR(); \
    } while (0)
#define RD4(Y, nx) do { v1[Y] = lds_rd128((nx) + 3072); v0[Y] = lds_rd128((nx) + 2048); \
        k1[Y] = lds_rd128((nx) + 1024); k0[Y] = lds_rd128(nx); } while (0)
#define KT(X, nx, PRE0) do { \
        HALF(sA, oA, lA, sB, qb0, qb1, k0[X], k1[X], oB, lB, v1[X ^ 1], v0[X], PRE0; RD4(X ^ 1, nx)); \
        HALF(sB, oB, lB, sA, qa0, qa1, k0[X ^ 1], k1[X ^ 1], oA, lA, v1[X], v0[X], (void)0); \
    } while (0)
    SBAR();
#pragma unroll 1
    for (int st = 0; st < 8; ++st) {
        const lds_ptr cur = lp + (st & 1) * K2_SLOT;
        const lds_ptr nxt = lp + ((st + 1) & 1) * K2_SLOT;
        PRIO(2);
        KT(0, cur + 4096, (void)0);
        PRIO(1);
        KT(1, cur + 8192, (void)0);
        PRIO(0);
        KT(0, cur + 12288, (void)0);
        PRIO(3);
        KT(1, nxt, RAW_BAR(); SBAR());
    }
    oB = MFMA32(v1[1], PB(p1), oB);
    SBAR();
    SUM16(lB, p1);
    asm volatile("s_nop 7" : "+v"(oB) :: "memory");
    SBAR();
    DG(4);
    {
        const lds_ptr xo = lds + K2_SLOT + w * 4096 + lane * 16;
        *(LAS float*)(lds + K2_LX + w * 256 + lane * 4) = lB[0];
#pragma unroll
        for (int i = 0; i < 4; ++i) *(LAS f32x4*)(xo + i * 1024) = f32x4{oB[4 * i], oB[4 * i + 1], oB[4 * i + 2], oB[4 * i + 3]};
        lds_add(fl + F_X + w * 4, lane);
        lds_add(fl + C_END + half * 4, lane);
        lds_wait_ge(fl + F_X + (w ^ 4) * 4, 1u);
        const lds_ptr xi = lds + K2_SLOT + (w ^ 4) * 4096 + lane * 16;
        f32x4 t[4];
        const float lx = *(const LAS float*)(lds + K2_LX + (w ^ 4) * 256 + (lane & 31) * 4);
        { auto rr = __builtin_amdgcn_permlane32_swap(__float_as_uint(lA[0]), __float_as_uint(lA[0]), false, false); lA[0] = __uint_as_float(rr[0]); }
#pragma unroll
        for (int i = 0; i < 4; ++i) t[i] = *(const LAS f32x4*)(xi + i * 1024);
        SBAR();
        lA[0] += lx;
#pragma unroll
        for (int i = 0; i < 16; ++i) { float o = oA[i]; acc_vv(o, t[i >> 2][i & 3]); oA[i] = o; }
    }
    DG(9);
    const float l = lA[0];
    u32x4 on0, on1;
    auto finish = [&](float ls) {
        float rl = __builtin_amdgcn_rcpf(ls);
        asm volatile("s_nop 1" : "+v"(rl));
        f32x16 o;
#pragma unroll
        for (int i = 0; i < 16; ++i) o[i] = mul_vv(oA[i], rl);
        on0 = nz4(pack8n(o, 0)); on1 = nz4(pack8n(o, 1));
    };
    finish(l);
    {
        float t0, t1;
        asm("v_mul_f32_e32 %0, 0, %2\n\tv_mul_f32_e32 %1, 0, %3\n\tv_fmac_f32_e32 %0, 0, %4\n\tv_fmac_f32_e32 %1, 0, %5\n\tv_fmac_f32_e32 %0, 0, %6\n\tv_fmac_f32_e32 %1, 0, %7\n\tv_fmac_f32_e32 %0, 0, %8\n\tv_fmac_f32_e32 %1, 0, %9\n\t"
            "v_fmac_f32_e32 %0, 0, %10\n\tv_fmac_f32_e32 %1, 0, %11\n\tv_fmac_f32_e32 %0, 0, %12\n\tv_fmac_f32_e32 %1, 0, %13\n\tv_fmac_f32_e32 %0, 0, %14\n\tv_fmac_f32_e32 %1, 0, %15\n\tv_fmac_f32_e32 %0, 0, %16\n\tv_fmac_f32_e32 %1, 0, %17"
            : "=&v"(t0), "=&v"(t1) : "v"(oA[0]), "v"(oA[1]), "v"(oA[2]), "v"(oA[3]), "v"(oA[4]), "v"(oA[5]), "v"(oA[6]), "v"(oA[7]), "v"(oA[8]), "v"(oA[9]), "v"(oA[10]), "v"(oA[11]), "v"(oA[12]), "v"(oA[13]), "v"(oA[14]), "v"(oA[15]));
        const bool ok = (l <= 1.0e30f) && (l >= 1.0e-18f) && (t0 + t1 == 0.f);
        if (__builtin_expect(__any(!ok), 0)) {
            float m = -INFINITY, ls = 0.f;
            oA = f32x16{};
#pragma unroll 1
            for (int t = 0; t < 64; ++t) {
                const char* g = kvg + t * 4096 + lane * 16;
                const bf16x8 a0 = *(const bf16x8*)g, a1 = *(const bf16x8*)(g + 1024);
                const bf16x8 b0 = *(const bf16x8*)(g + 2048), b1 = *(const bf16x8*)(g + 3072);
                f32x16 s = MFMA32(a0, qa0, f32x16{});
                s = MFMA32(a1, qa1, s);
                float tm = s[0];
#pragma unroll
                for (int i = 1; i < 16; ++i) tm = __builtin_fmaxf(tm, s[i]);
                auto rr = __builtin_amdgcn_permlane32_swap(__float_as_uint(tm), __float_as_uint(tm), false, false);
                tm = __builtin_fmaxf(__uint_as_float(rr[0]), __uint_as_float(rr[1]));
                const float mn = __builtin_fmaxf(m, tm);
                const float alpha = __builtin_amdgcn_exp2f(m - mn);
                m = mn;
                float ps = 0.f;
#pragma unroll
                for (int i = 0; i < 16; ++i) { s[i] = __builtin_amdgcn_exp2f(s[i] - mn); ps += s[i]; oA[i] *= alpha; }
                ls = ls * alpha + ps;
                oA = MFMA32(b0, PB(pack8n(s, 0)), oA);
                oA = MFMA32(b1, PB(pack8n(s, 1)), oA);
            }
            auto rr = __builtin_amdgcn_permlane32_swap(__float_as_uint(ls), __float_as_uint(ls), false, false);
            asm volatile("s_nop 7\n\ts_nop 7" : "+v"(oA));
            finish(__uint_as_float(rr[0]) + __uint_as_float(rr[1]));
        }
    }
    u32x4 w3[4];
    {
        const unsigned og = (unsigned)WS_O + (unsigned)(TA * 32 + h) * 2048u + lane * 16;
        st_sc1(on0, rs, og);
        st_sc1(on1, rs, og + 1024);
        lds_add(fl + C_S1, lane);
        const char* s2 = wsrc(half ^ 1, 32 + (w & 3) * 4);
        const char* s3 = wsrc(half, 48 + (w & 3) * 4);
        lds_wait_ge(fl + C_S1, 12u);
        DG(5);
#pragma unroll
        for (int i = 0; i < 4; ++i)
            glds16(wvoff, s2 + i * 16384, lds0 + K2_SLOT + ((w ^ 4) * 4 + i) * 1024);
#pragma unroll
        for (int i = 0; i < 4; ++i) w3[i] = gld128(wvoff, s3 + i * 16384);
    }
    {
        const int half = w & 1;
        const int T3 = tt * 4 + (w >> 1);
        const unsigned og = (unsigned)WS_O + (unsigned)T3 * 65536u + half * 32768 + lane * 16;
        const int r = lane & 31, hh = lane >> 5;
        const int g = lane >> 4, q = (lane & 15) >> 2, p = lane & 3;
        int woff[2];
#pragma unroll
        for (int n = 0; n < 2; ++n) {
            const int c = 4 * (n ^ half) + 2 * (p & 1) + (g & 1);
            woff[n] = (4 * hh + q) * 128 + ((c ^ ((q >> 1) << 2)) << 4) + (p >> 1) * 8;
        }
        f32x16 acc[2] = {f32x16{}, f32x16{}};
        lds_ptr wl[3][2] = {{lds + half * 16384 + woff[0], lds + half * 16384 + woff[1]},
                            {lds + K2_SF + half * 16384 + woff[0], lds + K2_SF + half * 16384 + woff[1]},
                            {lds + K2_CV + half * 2 * CV_BYTES + woff[0], lds + K2_CV + half * 2 * CV_BYTES + woff[1]}};
        asm volatile("" : "+v"(wl[0][0]), "+v"(wl[0][1]), "+v"(wl[1][0]), "+v"(wl[1][1]), "+v"(wl[2][0]), "+v"(wl[2][1]));
#define MS(m) ((m) < 64 - 2 * TL ? (m) >> 1 : 32 - TL + ((m) - (64 - 2 * TL)) % TL)
#define MN(m) ((m) < 64 - 2 * TL ? (m) & 1 : (m) < 64 - TL)
        constexpr int WR = 2 * WD + 2;
        s16x4 wa[WR], wb[WR];
#define LDW(m) do { const int s_ = MS(m), f_ = s_ < 8 ? 1 : s_ < 24 ? 0 : 2; \
            const int o_ = f_ == 2 ? ((s_ >> 2) & 1) * CV_BYTES + (s_ & 3) * 2048 : (f_ ? 0 : ((s_ >> 3) - 1) * K2_SLOT) + (s_ & 7) * 2048; \
            wa[(m) % WR] = lds_tr(wl[f_][MN(m)] + o_); wb[(m) % WR] = lds_tr(wl[f_][MN(m)] + o_ + 1024); SBAR(); } while (0)
#pragma unroll
        for (int m = 0; m < 2 * WD; ++m) LDW(m);
        u32x4 of[OWIN];
#define LDO(i) do { of[(i) % OWIN] = ld_sc1s(rs, og, (((i) + krot) & 31) * 1024); SBAR(); } while (0)
        for (unsigned spins = 0; spins < (1u << 20); ++spins) {
#pragma unroll
            for (int i = 0; i < OW0; ++i) LDO(i);
            unsigned m = mx8(of[0], of[1]);
#pragma unroll
            for (int i = 2; i < OW0; ++i) m = mx4(m, of[i]);
            if (!missing(m)) break;
            DGC(12);
        }
        lds_add(fl + C_S2, lane);
#pragma unroll
        for (int i = OW0; i < OWIN; ++i) LDO(i);
        DG(6);
        __builtin_amdgcn_s_setprio(P3_PRIO);
        SBAR();
        unsigned fv = 0;
#define FLAG_RD(a) do { fv = *(volatile LAS unsigned*)(a); SBAR(); } while (0)
#define FLAG_CHK(a, n) do { if (__builtin_expect((unsigned)__builtin_amdgcn_readfirstlane(fv) < (n), 0)) lds_wait_ge(a, n); CFENCE(); SBAR(); } while (0)
        lds_ptr xo = lds + K2_SF + w * 4096 + lane * 16, xi = lds + K2_SF + (w ^ 1) * 4096 + lane * 16;
        int bo = (T3 * 32 + (lane & 15)) * 4096 + 2048 + nt * 128 + half * 64 + g * 16;
        lds_ptr w3d = lds + K2_CV + (w >> 1) * CV_BYTES + (w & 1) * 4096 + lane * 16;
        asm volatile("" : "+v"(xo), "+v"(xi), "+v"(bo), "+v"(w3d));
        f32x4 xt[4];
        constexpr int M3 = TL > 8 ? 64 - 2 * TL + TL - 8 - 2 * WD : 48 - 2 * WD;
        sfor(std::make_integer_sequence<int, 64>{}, [&](auto mc) __attribute__((always_inline)) {
            constexpr int m = decltype(mc)::value, s = MS(m), n = MN(m);
            constexpr bool first = m < 64 - 2 * TL ? n == 0 : n == 1;
            constexpr bool last = m < 64 - 2 * TL ? n == 1 : n == 0;
            acc[n] = MFMA32(cat8(wa[m % WR], wb[m % WR]), PB(of[s % OWIN]), acc[n]);
            SBAR();
            if constexpr (m == 32 - 2 * WD) FLAG_CHK(fl + C_S2, 8u);
            if constexpr (m == M3) FLAG_CHK(fl + C_S3, 8u);
            if constexpr (m + 2 * WD < 64) LDW(m + 2 * WD);
            if constexpr (first && (s & 1) && s + 1 >= OW0 && s + 1 < 32) {
                if (__builtin_expect(missing(mx8(of[(s + 1) % OWIN], of[(s + 2) % OWIN])), 0)) {
#pragma unroll 1
                    for (unsigned spins = 0; spins < (1u << 20); ++spins) {
                        __builtin_amdgcn_s_sleep(2);
                        LDO(s + 1); LDO(s + 2);
                        DGC(13);
                        if (!missing(mx8(of[(s + 1) % OWIN], of[(s + 2) % OWIN]))) break;
                    }
                }
            }
            if constexpr (last && s + OWIN < 32) LDO(s + OWIN);
            if constexpr (m == 15 - 2 * WD) lds_add(fl + C_U0, lane);
            if constexpr (m == 29 - 2 * WD) FLAG_RD(fl + C_S2);
            if constexpr (m == W3M - 3) FLAG_RD(fl + C_HF);
            if constexpr (m == W3M) FLAG_CHK(fl + C_HF, 4u);
            if constexpr (m >= W3M && m < W3M + 8 && ((m - W3M) & 1) == 0) {
                asm volatile("" : "+v"(w3[((m - W3M) >> 1) & 3]));
                *(LAS u32x4*)(w3d + (((m - W3M) >> 1) & 3) * 1024) = w3[((m - W3M) >> 1) & 3];
            }
            if constexpr (m == W3M + 6) lds_add(fl + C_S3, lane);
            if constexpr (m == M3 - 3) FLAG_RD(fl + C_S3);
            if constexpr (m == 64 - TL - 3) FLAG_RD(fl + C_U0);
            if constexpr (m == 64 - TL) FLAG_CHK(fl + C_U0, 8u);
            if constexpr (m == 64 - TL + 1 || m == 64 - TL + 2) {
                constexpr int i = 2 * (m - (64 - TL + 1));
                *(LAS f32x4*)(xo + i * 1024) = f32x4{acc[1][4 * i], acc[1][4 * i + 1], acc[1][4 * i + 2], acc[1][4 * i + 3]};
                *(LAS f32x4*)(xo + i * 1024 + 1024) = f32x4{acc[1][4 * i + 4], acc[1][4 * i + 5], acc[1][4 * i + 6], acc[1][4 * i + 7]};
            }
            if constexpr (m == 64 - TL + 2) lds_add(fl + F_R + w * 4, lane);
            if constexpr (m == XR_AT - 2) FLAG_RD(fl + F_R + (w ^ 1) * 4);
            if constexpr (m == XR_AT) {
                FLAG_CHK(fl + F_R + (w ^ 1) * 4, 1u);
#pragma unroll
                for (int i = 0; i < 4; ++i) xt[i] = *(const LAS f32x4*)(xi + i * 1024);
            }
            if constexpr (ZL_GATE && last && s + OWIN == ZL_AT) lds_add(fl + C_OL, lane);
            if constexpr (m == 2 * P3_T1 + 1) __builtin_amdgcn_s_setprio(P3_PRIO - 1);
            SBAR();
            if constexpr (m == 31) DG(11);
            if constexpr (m == 47) DG(14);
        });
        asm volatile("s_nop 11" : "+v"(acc[0]));
        DG(7);
#pragma unroll
        for (int i = 0; i < 16; ++i) { float o = xt[i >> 2][i & 3]; acc_vv(o, acc[0][i]); acc[1][i] = o; }
        u32x4 pa = pack8n(acc[1], 0), pb = pack8n(acc[1], 1);
#pragma unroll
        for (int i = 0; i < 4; ++i) {
            auto sw = __builtin_amdgcn_permlane16_swap(pa[i], pb[i], false, false);
            pa[i] = sw[0]; pb[i] = sw[1];
        }
        asm volatile("" :: "v"(acc[0]));
        __builtin_amdgcn_raw_buffer_store_b128(pa, ro, bo, 0, AUX_SC1);
        __builtin_amdgcn_raw_buffer_store_b128(pb, ro, bo, 65536, AUX_SC1);
    }
    DG_END(8);
}

extern "C" void kernel_launch(void* const* d_in, const int* in_sizes, int n_in,
                              void* d_out, int out_size, void* d_ws, size_t ws_size,
                              hipStream_t stream) {
    const void* A_q = d_in[0];
    const void* A_k = d_in[1];
    const void* A_v = d_in[2];
    const void* A_shift = d_in[3];
    const void* A_ssmk = d_in[4];
    const void* A_D = d_in[5];
    const void* B_x = d_in[6];
    const void* B_wqkv = d_in[7];
    const void* B_wo = d_in[8];
    char* ws = (char*)d_ws;
    k1_qkv<<<256, 512, 0, stream>>>((const char*)B_x, (const char*)B_wqkv, ws);
    k2_attn<<<256, 768, 0, stream>>>(ws, (const char*)A_q, (const char*)A_k, (const char*)A_v, (const char*)A_shift,
                                     (const char*)A_ssmk, (const unsigned*)A_D, (const char*)B_wo, (char*)d_out);
}
```

```cpp
#include <hip/hip_runtime.h>
#include <hip/hip_bf16.h>
#include <stdint.h>
#include <utility>

typedef short bf16x8 __attribute__((ext_vector_type(8)));
typedef short s16x4 __attribute__((ext_vector_type(4)));
typedef float f32x16 __attribute__((ext_vector_type(16)));
typedef float f32x4 __attribute__((ext_vector_type(4)));
typedef float f32x2 __attribute__((ext_vector_type(2)));
typedef unsigned u32x4 __attribute__((ext_vector_type(4)));
typedef unsigned u32x2 __attribute__((ext_vector_type(2)));
typedef __bf16 bf16x2_t __attribute__((ext_vector_type(2)));

#define LAS __attribute__((address_space(3)))
typedef LAS char* lds_ptr;
#define DI __device__ __forceinline__
#define SBAR() __builtin_amdgcn_sched_barrier(0)
#define MFMA32(a, b, c) __builtin_amdgcn_mfma_f32_32x32x16_bf16((a), (b), (c), 0, 0, 0)
#define MFMA16(a, b, c) __builtin_amdgcn_mfma_f32_16x16x32_bf16((a), (b), (c), 0, 0, 0)
#define WAIT_BAR(N) asm volatile("s_waitcnt vmcnt(" #N ") lgkmcnt(0)\n\ts_barrier" ::: "memory")
#define LGKM_BAR() asm volatile("s_waitcnt lgkmcnt(0)\n\ts_barrier" ::: "memory")

#define DG_DECL(K)
#define DG(i)
#define DG_END(i)
#define DGC(i)

constexpr int S_LEN = 2048, DM = 1024, NH = 32;

#define OQ 1
constexpr size_t WS_Q = 0;
constexpr size_t WS_KV = 4u << 20;
constexpr size_t WS_O = OQ ? WS_Q : 12u << 20;
constexpr size_t WS_KVR = 16u << 20;
#define ZK 1
constexpr size_t WS_ZT = ZK ? WS_KVR : 20u << 20;
constexpr size_t WS_CNT = 24u << 20;

template <int... I, class F> DI void sfor(std::integer_sequence<int, I...>, F&& f) { (f(std::integral_constant<int, I>{}), ...); }
DI unsigned cvtpk(float lo, float hi) {
    f32x2 v = {lo, hi};
    return __builtin_bit_cast(unsigned, __builtin_convertvector(v, bf16x2_t));
}
DI float bflo(unsigned u) { return __uint_as_float(u << 16); }
DI float bfhi(unsigned u) { return __uint_as_float(u & 0xffff0000u); }

DI void glds16(unsigned voff, const void* sbase, unsigned lds_dst) {
    unsigned sv;
    asm volatile("s_mov_b32 %0, m0\n\ts_mov_b32 m0, %3\n\ts_nop 0\n\tglobal_load_lds_dwordx4 %1, %2\n\ts_mov_b32 m0, %0"
                 : "=&s"(sv) : "v"(voff), "s"(sbase), "s"(lds_dst) : "memory");
}
template <int NT> DI void glds16p(unsigned voff, const void* sbase, unsigned lds_dst) {
    if constexpr (NT) {
        unsigned sv;
        asm volatile("s_mov_b32 %0, m0\n\ts_mov_b32 m0, %3\n\ts_nop 0\n\tglobal_load_lds_dwordx4 %1, %2 nt\n\ts_mov_b32 m0, %0"
                     : "=&s"(sv) : "v"(voff), "s"(sbase), "s"(lds_dst) : "memory");
    } else glds16(voff, sbase, lds_dst);
}
#define K1_XNT 0
#define K1_WNT 0
#define K2_SNT 1
#define K2_QNT 0
#define K2_WNT 0
#define OUT_AUX 16
#define K1_SAUX 16
DI bf16x8 lds_rd128(lds_ptr p) { return *(const LAS bf16x8*)p; }
DI s16x4 lds_tr(lds_ptr p) {
    return __builtin_bit_cast(s16x4, __builtin_amdgcn_ds_read_tr16_b64_v4i16((LAS s16x4*)p));
}
DI bf16x8 cat8(s16x4 a, s16x4 b) { return __builtin_shufflevector(a, b, 0, 1, 2, 3, 4, 5, 6, 7); }
DI u32x4 pack8(const f32x16& x, int s, float sc) {
    u32x4 p;
    p[0] = cvtpk(x[8 * s + 0] * sc, x[8 * s + 1] * sc);
    p[1] = cvtpk(x[8 * s + 2] * sc, x[8 * s + 3] * sc);
    p[2] = cvtpk(x[8 * s + 4] * sc, x[8 * s + 5] * sc);
    p[3] = cvtpk(x[8 * s + 6] * sc, x[8 * s + 7] * sc);
    return p;
}
DI u32x4 pack8n(const f32x16& x, int s) {
    u32x4 p;
    p[0] = cvtpk(x[8 * s + 0], x[8 * s + 1]);
    p[1] = cvtpk(x[8 * s + 2], x[8 * s + 3]);
    p[2] = cvtpk(x[8 * s + 4], x[8 * s + 5]);
    p[3] = cvtpk(x[8 * s + 6], x[8 * s + 7]);
    return p;
}
DI void rnd2(float& a, float& b) { unsigned u = cvtpk(a, b); a = bflo(u); b = bfhi(u); }

#define CONV_PACE()
#define RAW_BAR() asm volatile("s_barrier" ::: "memory")
DI unsigned gld32(unsigned voff, const void* sbase) {
    unsigned r;
    asm volatile("global_load_dword %0, %1, %2" : "=v"(r) : "v"(voff), "s"(sbase) : "memory");
    return r;
}
DI u32x2 gld64(unsigned voff, const void* sbase) {
    u32x2 r;
    asm volatile("global_load_dwordx2 %0, %1, %2" : "=v"(r) : "v"(voff), "s"(sbase) : "memory");
    return r;
}
DI float mul_sv(float s, float v) { float r; asm("v_mul_f32_e32 %0, %1, %2" : "=v"(r) : "s"(s), "v"(v)); return r; }
DI float mul_vv(float a, float b) { float r; asm("v_mul_f32_e32 %0, %1, %2" : "=v"(r) : "v"(a), "v"(b)); return r; }
DI float add_vv(float a, float b) { float r; asm("v_add_f32_e32 %0, %1, %2" : "=v"(r) : "v"(a), "v"(b)); return r; }
DI void acc_vv(float& a, float b) { asm("v_add_f32_e32 %0, %0, %1" : "+v"(a) : "v"(b)); }
DI void fmac_vv(float& acc, float a, float b) { asm("v_fmac_f32_e32 %0, %1, %2" : "+v"(acc) : "v"(a), "v"(b)); }
DI void fmac_sv(float& acc, float s, float v) { asm("v_fmac_f32_e32 %0, %1, %2" : "+v"(acc) : "s"(s), "v"(v)); }
typedef __attribute__((address_space(1))) unsigned gu32;
#define RLX_AGENT __ATOMIC_RELAXED, __HIP_MEMORY_SCOPE_AGENT
#define CFENCE() asm volatile("" ::: "memory")
#define DRAIN() asm volatile("s_waitcnt vmcnt(0)" ::: "memory")
constexpr int AUX_SC1 = 16;
DI u32x4 ld_sc1(__amdgpu_buffer_rsrc_t rs, unsigned off) { return __builtin_amdgcn_raw_buffer_load_b128(rs, (int)off, 0, AUX_SC1); }
DI u32x4 ld_sc1s(__amdgpu_buffer_rsrc_t rs, unsigned off, int soff) { return __builtin_amdgcn_raw_buffer_load_b128(rs, (int)off, soff, AUX_SC1); }
DI void st_sc1(u32x4 v, __amdgpu_buffer_rsrc_t rs, unsigned off) { __builtin_amdgcn_raw_buffer_store_b128(v, rs, (int)off, 0, AUX_SC1); }
constexpr unsigned EMPTY = 0xffffffffu;
DI unsigned nz(unsigned x) { return x < EMPTY - 1u ? x : EMPTY - 1u; }
DI u32x4 nz4(u32x4 v) { return u32x4{nz(v[0]), nz(v[1]), nz(v[2]), nz(v[3])}; }
DI float fmx(float a, float b) { return __builtin_elementwise_maximum(a, b); }
DI unsigned umax(unsigned a, unsigned b) { return a > b ? a : b; }
DI unsigned umax3(unsigned a, unsigned b, unsigned c) { return umax(umax(a, b), c); }
DI unsigned mx4(unsigned m, u32x4 v) { return umax3(umax3(m, v[0], v[1]), v[2], v[3]); }
DI unsigned mx8(u32x4 a, u32x4 b) { return umax(umax3(umax3(umax3(a[0], a[1], a[2]), a[3], b[0]), b[1], b[2]), b[3]); }
DI bool missing(unsigned m) { return __builtin_amdgcn_ballot_w64(m == EMPTY) != 0; }
DI unsigned lds_add_rtn(lds_ptr p, int lane) {
    unsigned old = 0;
    CFENCE();
    if (lane == 0) old = __hip_atomic_fetch_add((LAS unsigned*)p, 1u, __ATOMIC_RELAXED, __HIP_MEMORY_SCOPE_WORKGROUP);
    old = __builtin_amdgcn_readfirstlane(old);
    CFENCE();
    return old;
}
DI void lds_add(lds_ptr p, int lane) {
    unsigned long long sv;
    asm volatile("s_mov_b64 %0, exec\n\ts_mov_b64 exec, 1\n\tds_add_u32 %1, %2\n\ts_mov_b64 exec, %0"
                 : "=&s"(sv) : "v"(p), "v"(1u) : "memory");
}
DI void lds_wait_ge(lds_ptr p, unsigned v) {
    CFENCE();
    while ((unsigned)__builtin_amdgcn_readfirstlane(*(volatile LAS unsigned*)p) < v) __builtin_amdgcn_s_sleep(1);
    CFENCE();
}
DI void poll_ge(gu32* word, unsigned want) {
    CFENCE();
    for (unsigned spins = 0; spins < (1u << 22); ++spins) {
        if ((unsigned)__builtin_amdgcn_readfirstlane(__hip_atomic_load(word, RLX_AGENT)) >= want) break;
        __builtin_amdgcn_s_sleep(1);
    }
    __builtin_amdgcn_fence(__ATOMIC_ACQUIRE, "wavefront");
    CFENCE();
}
#define PB(x) __builtin_bit_cast(bf16x8, x)
#define PRIO(x) do { __builtin_amdgcn_s_setprio(x); SBAR(); } while (0)
DI u32x4 gld128(unsigned voff, const void* sbase) {
    u32x4 r;
    asm volatile("global_load_dwordx4 %0, %1, %2" : "=v"(r) : "v"(voff), "s"(sbase) : "memory");
    return r;
}

#define K1_UNR 15
#define K1_LA 5
#define K1_ROT ((tb & 3) * 4)
#define K1_PF 0
#define K1_PG 0
#define K1_TW 0
#define PG1_AT 0
#define PG2_AT 2
#define PG3_AT 6
#define K1_WB(n) WAIT_BAR(0)
#define PF1_AT -1
#define PF1_LO 4
#define PF1_N 4
#define PF2_AT 1
#define PF2_LO 8
#define PF2_N 4
#define PF3_AT 5
#define PF3_LO 12
#define PF3_N 4
#define K1_KR ((kt + rot) & 15)
#define K1_ST(v, rs, off) __builtin_amdgcn_raw_buffer_store_b128(v, rs, (int)(off), 0, K1_SAUX)
constexpr int K1_STAGE = 40960, K1U = K1_UNR;
__global__ __launch_bounds__(512, 2) void k1_qkv(const char* __restrict__ X, const char* __restrict__ W,
                                                 char* __restrict__ ws) {
    __shared__ __attribute__((aligned(16))) char lds_raw[4 * K1_STAGE];
    const lds_ptr lds = (lds_ptr)lds_raw;
    const unsigned lds0 = (unsigned)(uintptr_t)lds;
    const int tid = threadIdx.x, lane = tid & 63;
    const int w = __builtin_amdgcn_readfirstlane(tid >> 6);
    const int b = blockIdx.x, xcd = b & 7, idx = b >> 3;
    const int hp = (xcd >> 1) * 4 + (idx & 3), tb = (xcd & 1) * 8 + (idx >> 2);
    DG_DECL(0)

    const int lw = w & 3;
    const int rot = K1_ROT;
    unsigned voffX[4], voffW[2];
#pragma unroll
    for (int pp = 0; pp < 4; ++pp) {
        const int row = 32 * lw + 8 * pp + (lane >> 3);
        const int ch = (lane & 7) ^ ((row >> 1) & 7);
        voffX[pp] = (unsigned)(tb * 128 + row) * 2048u + ch * 16;
    }
#pragma unroll
    for (int pp = 0; pp < 2; ++pp) {
        const int krow = 16 * lw + 8 * pp + (lane >> 3);
        const int ch = (lane & 7) ^ (((lane >> 4) & 1) << 2);
        voffW[pp] = (unsigned)krow * 6144u + ch * 16;
    }
    auto issue = [&](int kt, int lo, int hi) {
        const int kr = K1_KR;
        const char* xb = X + kr * 128;
        const char* wb = W + (size_t)kr * (64 * 6144) + hp * 128;
        const unsigned st = lds0 + (kt & 3) * K1_STAGE;
#pragma unroll
        for (int pp = 0; pp < 4; ++pp)
            if (pp >= lo && pp < hi) glds16p<K1_XNT>(voffX[pp], xb, st + (4 * lw + pp) * 1024);
#pragma unroll
        for (int sec = 0; sec < 3; ++sec)
#pragma unroll
            for (int pp = 0; pp < 2; ++pp)
                if (4 + 2 * sec + pp >= lo && 4 + 2 * sec + pp < hi)
                    glds16p<K1_WNT>(voffW[pp], wb + sec * 2048, st + 16384 + sec * 8192 + (2 * lw + pp) * 1024);
    };
    DG(13);
    if (w < 4) { issue(0, 0, 5); issue(1, 0, 5); }
    else { issue(0, 5, 10); issue(1, 5, 10); }
    DG(14);
    int w2 = w;
    asm volatile("" : "+s"(w2));

    if (w2 >= 4) {
#define WAITN_BAR(N) asm volatile("s_waitcnt vmcnt(%0) lgkmcnt(0)\n\ts_barrier" :: "n"(N) : "memory")
        issue(2, 0, K1_LA);
        asm volatile("s_waitcnt vmcnt(%0)" :: "n"(5 + K1_LA) : "memory");
#pragma unroll 1
        for (int kt = 0; kt < 13; ++kt) {
            WAITN_BAR(10 + K1_LA);
            issue(kt + 2, K1_LA, 10);
            issue(kt + 3, 0, K1_LA);
        }
        WAITN_BAR(10 + K1_LA);
        issue(15, K1_LA, 10);
        WAIT_BAR(10);
        WAIT_BAR(0);
        if (lw == 0 && lane < 2) *(unsigned*)(ws + WS_CNT + b * 256 + lane * 128) = 0u;
        DG_END(5);
        return;
    }

    const int wm = w >> 1, wn = w & 1;
    const int r = lane & 31, hh = lane >> 5;
    const int g = lane >> 4, q = (lane & 15) >> 2, p = lane & 3;
    const int xoff0 = (64 * wm + r) * 128 + ((hh ^ ((r >> 1) & 7)) << 4);
    const int woff0 = 16384 + (8 * hh + q) * 128 + (((4 * wn + 2 * (g & 1) + (p >> 1)) ^ ((q >> 1) << 2)) << 4) + (p & 1) * 8;

    f32x16 acc[2][3];
#pragma unroll
    for (int m = 0; m < 2; ++m)
#pragma unroll
        for (int s = 0; s < 3; ++s) acc[m][s] = f32x16{};
    bf16x8 xf[2][2];
    s16x4 wl[2][3], wh[2][3];
#define K1_WF(f, sec) cat8(wl[f][sec], wh[f][sec])
#define K1_STEP(F, st, t, BAR_HERE) do { \
        const lds_ptr xp_ = (st) + (xoff0 ^ ((t) << 5)); const lds_ptr wp_ = (st) + woff0 + (t) * 2048; \
        acc[0][0] = MFMA32(K1_WF(F, 0), xf[F][0], acc[0][0]); SBAR(); \
        if (BAR_HERE) { K1_WB(BAR_HERE); SBAR(); } \
        xf[F ^ 1][0] = lds_rd128(xp_); wl[F ^ 1][0] = lds_tr(wp_); wh[F ^ 1][0] = lds_tr(wp_ + 512); SBAR(); \
        acc[0][1] = MFMA32(K1_WF(F, 1), xf[F][0], acc[0][1]); SBAR(); \
        wl[F ^ 1][1] = lds_tr(wp_ + 8192); wh[F ^ 1][1] = lds_tr(wp_ + 8192 + 512); SBAR(); \
        acc[0][2] = MFMA32(xf[F][0], K1_WF(F, 2), acc[0][2]); SBAR(); \
        wl[F ^ 1][2] = lds_tr(wp_ + 16384); wh[F ^ 1][2] = lds_tr(wp_ + 16384 + 512); xf[F ^ 1][1] = lds_rd128(xp_ + 4096); SBAR(); \
        acc[1][0] = MFMA32(K1_WF(F, 0), xf[F][1], acc[1][0]); SBAR(); \
        acc[1][1] = MFMA32(K1_WF(F, 1), xf[F][1], acc[1][1]); \
        acc[1][2] = MFMA32(xf[F][1], K1_WF(F, 2), acc[1][2]); SBAR(); \
    } while (0)

    WAIT_BAR(5);
    DG(3);
    {
        const lds_ptr xp_ = lds + xoff0; const lds_ptr wp_ = lds + woff0;
        xf[0][0] = lds_rd128(xp_); xf[0][1] = lds_rd128(xp_ + 4096);
#pragma unroll
        for (int sec = 0; sec < 3; ++sec) { wl[0][sec] = lds_tr(wp_ + sec * 8192); wh[0][sec] = lds_tr(wp_ + sec * 8192 + 512); }
    }
    SBAR();
#pragma unroll K1U
    for (int kt = 0; kt < 15; ++kt) {
        const lds_ptr st = lds + (kt & 3) * K1_STAGE;
        const lds_ptr sn = lds + ((kt + 1) & 3) * K1_STAGE;
        K1_STEP(0, st, 1, false);
        K1_STEP(1, st, 2, false);
        K1_STEP(0, st, 3, false);
        K1_STEP(1, sn, 0, kt + 1);
    }
    {
        const lds_ptr st = lds + 3 * K1_STAGE;
        K1_STEP(0, st, 1, false);
        K1_STEP(1, st, 2, false);
        K1_STEP(0, st, 3, false);
#pragma unroll
        for (int m = 0; m < 2; ++m) {
            acc[m][1] = MFMA32(K1_WF(1, 1), xf[1][m], acc[m][1]);
            acc[m][2] = MFMA32(xf[1][m], K1_WF(1, 2), acc[m][2]);
            acc[m][0] = MFMA32(K1_WF(1, 0), xf[1][m], acc[m][0]);
        }
    }
    DG(4);
    {
        const int h = 2 * hp + wn;
        const float qscale = 0.17677669529663687f * 1.4426950408889634f;
        const __amdgpu_buffer_rsrc_t rs = __builtin_amdgcn_make_buffer_rsrc(ws, 0, 32 << 20, 0x00020000);
#pragma unroll
        for (int m = 0; m < 2; ++m) {
            const int T = tb * 4 + wm * 2 + m;
            const unsigned dq = (unsigned)WS_Q + (T * 32 + h) * 2048 + lane * 16;
            const unsigned dk = (unsigned)WS_KV + (h * 64 + T) * 4096 + lane * 16;
            K1_ST(pack8n(acc[m][1], 0), rs, dk);
            K1_ST(pack8n(acc[m][1], 1), rs, dk + 1024);
            K1_ST(pack8n(acc[m][2], 0), rs, dk + 2048);
            K1_ST(pack8n(acc[m][2], 1), rs, dk + 3072);
            K1_ST(pack8(acc[m][0], 0, qscale), rs, dq);
            K1_ST(pack8(acc[m][0], 1, qscale), rs, dq + 1024);
        }
    }
    DG_END(5);
}

constexpr int K2_SLOT = 32768;
constexpr int K2_SF = 65536;
constexpr int K2_CV = 98304;
constexpr int CV_HA = 0, CV_HB = 4160, CV_KV = 8320, CV_BYTES = 8320 + 480 + 4096;
constexpr int K2_LX = K2_CV + 4 * CV_BYTES;
constexpr int K2_FL = K2_LX + 2048;
constexpr int K2_SCR = K2_FL + 256;
constexpr int K2_LDS = K2_SCR + 4096;
constexpr int F_X = 0, F_R = 32, F_W2 = 64, C_OST = 68, C_S1 = 76, C_S2 = 80, C_S3 = 128, C_ZST = 88, F_ZGO = 92,
              C_KST = 96, C_U0 = 100, C_LD = 108, F_KGO = 116, C_END = 120, C_OL = 72, C_HF = 84;
#define NG_IN 0
#define GATE_WORD C_END
#define GATE_VAL 0
#define X_NZ 4
#define X_NW3 8
#define ZP_GATE 4u
#define ZL_GATE 4
#define ZL_AT 31
#define Z_SLEEP0 0
#define O_SLEEP 0
#define Z_SLEEP 8
#define OWIN 16
#define OW0 8
#define WD 2
#define W3_AT 10
#define X_NOLDW 0
#define X_NOLDO 0
#define X_NOCHK 0
#define KROT (nt * 2)
#define NG_LAST 0
#define PACE_IN "s_nop 7"
#define PACE_OUT "s_nop 1"
#define P3_PRIO 1
#define TOP_SLEEP 2
#define A_AUX 16
#define B_AUX 16
#define B_LDS 0
#define TL 8
#define XR_AT 62
#define W3_AUX 0
#define W3M 13
#define A16 1
#define SEL4 2
#define KV_DOT 1
#define ZE_PRIO 3
#define P3_T1 99
#define P3_T2 99
#define AUX_PRIO 2

__global__ __launch_bounds__(768, 3) void k2_attn(char* __restrict__ ws, const char* __restrict__ Aq,
                                                  const char* __restrict__ Ak, const char* __restrict__ Av,
                                                  const char* __restrict__ shiftk, const char* __restrict__ ssmk,
                                                  const unsigned* __restrict__ ssmD, const char* __restrict__ Wo,
                                                  char* __restrict__ out) {
    __shared__ __attribute__((aligned(16))) char lds_raw[K2_LDS];
    const lds_ptr lds = (lds_ptr)lds_raw;
    const lds_ptr fl = lds + K2_FL;
    const unsigned lds0 = (unsigned)(uintptr_t)lds;
    const int tid = threadIdx.x, lane = tid & 63;
    const int w = __builtin_amdgcn_readfirstlane(tid >> 6);
    const int b = blockIdx.x, xcd = b & 7, idx = b >> 3;
    const int h = xcd * 4 + (idx >> 3), qb = idx & 7;
    const int tt = xcd * 2 + (idx >> 4), nt = idx & 15;
    const char* kvg = ws + WS_KV + (size_t)h * 64 * 4096;
    const __amdgpu_buffer_rsrc_t rs = __builtin_amdgcn_make_buffer_rsrc(ws, 0, 32 << 20, 0x00020000);
    const __amdgpu_buffer_rsrc_t ro = __builtin_amdgcn_make_buffer_rsrc(out, 0, 8 << 20, 0x00020000);
    gu32* const cnt = (gu32*)(ws + WS_CNT);
    auto signal = [&]() {
        (void)__hip_atomic_fetch_add(cnt + ((lane & 31) * 8 + (lane < 32 ? xcd : qb)) * 64, 1u, RLX_AGENT);
    };
    const unsigned wvoff = (unsigned)(lane >> 3) * 2048u + nt * 128 + (((lane & 7) ^ (((lane >> 4) & 1) << 2)) << 4);
    const int krot = KROT;
    auto wsrc = [&](int hf, int lp) { return Wo + (size_t)(hf * 64 + ((lp + krot * 2) & 63)) * 16384; };
    DG_DECL(1)
    const unsigned voff = lane * 16;
    const int cw = w & 3;
    auto issue = [&](int st) {
        const char* src = kvg + (st * 4 + cw) * 4096;
        const unsigned dst = lds0 + (st & 1) * K2_SLOT + cw * 4096;
#pragma unroll
        for (int i = 0; i < 4; ++i) glds16(voff, src + i * 1024, dst + i * 1024);
#pragma unroll
        for (int i = 0; i < 4; ++i) glds16(voff, src + 131072 + i * 1024, dst + 16384 + i * 1024);
    };
    if (w >= 8) {
        __builtin_amdgcn_s_sleep(TOP_SLEEP);
#pragma unroll
        for (int i = 2; i < 8; ++i)
            glds16(voff, kvg + (i & 1) * 131072 + (i >> 1) * 4096 + cw * 1024, lds0 + (i & 1) * 16384 + (i >> 1) * 4096 + cw * 1024);
        RAW_BAR();
        issue(1);
        WAIT_BAR(8);
    } else {
        const int tq = cw * 2 + (w >> 2);
        const char* qg = ws + WS_Q + (size_t)((qb * 8 + tq) * 32 + h) * 2048;
        glds16(voff, kvg + (w >> 2) * 131072 + cw * 1024, lds0 + (w >> 2) * 16384 + cw * 1024);
        glds16(voff, qg, lds0 + K2_SF + tq * 2048);
        glds16(voff, qg + 1024, lds0 + K2_SF + tq * 2048 + 1024);
    }
    int w2 = w;
    asm volatile("" : "+s"(w2));

    if (w2 >= 8) {
        __builtin_amdgcn_s_setprio(3);
        const int ch = xcd * 128 + idx * 4 + cw;
        const lds_ptr cv = lds + K2_CV + cw * CV_BYTES;
        {
            const u32x4 z4 = {0u, 0u, 0u, 0u};
            if (lane < 2) *(LAS u32x4*)(cv + CV_HA + lane * 16) = z4;
            if (lane < 30) *(LAS u32x4*)(cv + CV_KV + lane * 16) = z4;
            if (cw == 0 && lane < 16) *(LAS u32x4*)(fl + lane * 16) = z4;
        }
        const int c0 = xcd * 128 + cw * 32, cq = lane & 7, rg = lane >> 3;
        const int row0 = idx * 64 + rg * 8;
        const unsigned roff = (unsigned)row0 * 2048u + (c0 + cq * 4) * 2;
        const unsigned toffs = (unsigned)(c0 + (lane >> 3)) * 4096u + idx * 128;
        u32x4 sk[2];
        sk[0] = gld128((unsigned)(c0 + cq * 4) * 8u, shiftk); sk[1] = gld128((unsigned)(c0 + cq * 4) * 8u, shiftk + 16);
        unsigned dd = gld32((unsigned)(ch >> 1) * 4u, ssmD);
        auto stage = [&](const char* src, int dst) {
            const unsigned sbase = (unsigned)(idx * 64 + cw * 16 + (lane >> 4)) * 2048u + xcd * 256;
#pragma unroll
            for (int i = 0; i < 4; ++i)
                glds16p<K2_SNT>(sbase + (((lane & 15) ^ (((2 * cw + (i >> 1)) & 3) << 2)) << 4), src + i * 8192, lds0 + dst + cw * 4096 + i * 1024);
        };
        auto blank = [&](unsigned off) {
            const u32x4 e4 = {EMPTY, EMPTY, EMPTY, EMPTY};
            CFENCE();
            st_sc1(e4, rs, off + voff); st_sc1(e4, rs, off + 1024 + voff);
            CFENCE();
        };
        WAIT_BAR(3);
        DG(12);
        issue(2);
        stage(Ak, K2_SF);
        glds16((unsigned)(idx * 64 - (idx ? 4 : 0) + (lane >> 4)) * 2048u + xcd * 256 + (((lane & 15) ^ 12) << 4), Ak, lds0 + K2_SCR + cw * 1024);
        blank((unsigned)WS_O + (unsigned)((qb * 8 + cw * 2) * 32 + h) * 2048u);
        WAIT_BAR(7);
        DG(9);
        issue(3);
        stage(Av, K2_SF + 16384);
        blank((unsigned)WS_O + (unsigned)((qb * 8 + cw * 2 + 1) * 32 + h) * 2048u);
        WAIT_BAR(6);
        DG(3);
        issue(4);
#pragma unroll
        for (int i = 0; i < 4; ++i) glds16p<K2_SNT>(voff, ssmk + (size_t)ch * 4096 + i * 1024, lds0 + K2_CV + cw * CV_BYTES + CV_HA + 32 + i * 1024);
        WAIT_BAR(4);
        DG(5);
        issue(5);
        SBAR();
        asm volatile("" : "+v"(sk[0]), "+v"(sk[1]), "+v"(dd));
        dd = __builtin_amdgcn_readfirstlane(dd);
        {
            u32x2 kr[11], vr[8];
            lds_ptr kb = lds + K2_SF + rg * 2048 + ((cw ^ (rg & 3)) << 6) + cq * 8;
            asm volatile("" : "+v"(kb));
            const lds_ptr hb = lds + (rg ? K2_SF + rg * 2048 - 768 : K2_SCR + cw * 1024 + 256) + ((cw ^ ((rg + 3) & 3)) << 6) + cq * 8;
#pragma unroll
            for (int i = 0; i < 3; ++i) kr[i] = *(const LAS u32x2*)(hb + i * 256);
#pragma unroll
            for (int i = 0; i < 8; ++i) kr[3 + i] = *(const LAS u32x2*)(kb + i * 256);
#pragma unroll
            for (int i = 0; i < 8; ++i) vr[i] = *(const LAS u32x2*)(kb + 16384 + i * 256);
            if (row0 == 0) { kr[0] = u32x2{0u, 0u}; kr[1] = u32x2{0u, 0u}; kr[2] = u32x2{0u, 0u}; }
            const lds_ptr tb = cv + CV_KV + 480;
#pragma unroll
            for (int c = 0; c < 4; ++c) {
                const unsigned s01 = sk[c >> 1][(c & 1) * 2], s23 = sk[c >> 1][(c & 1) * 2 + 1];
                unsigned kp[10];
                float kv[8], vf[8];
#pragma unroll
                for (int j = 0; j < 10; ++j) kp[j] = __builtin_amdgcn_perm(kr[j][c >> 1], kr[j + 1][c >> 1], (c & 1) ? 0x07060302u : 0x05040100u);
                SBAR();
#pragma unroll
                for (int i = 0; i < 8; ++i) asm("v_dot2_f32_bf16 %0, %1, %2, 0" : "=v"(kv[i]) : "v"(kp[i]), "v"(s23));
                SBAR();
#pragma unroll
                for (int i = 0; i < 8; ++i) asm("v_dot2_f32_bf16 %0, %1, %2, %0" : "+v"(kv[i]) : "v"(kp[i + 2]), "v"(s01));
                SBAR();
#pragma unroll
                for (int i = 0; i < 8; ++i) vf[i] = (c & 1) ? bfhi(vr[i][c >> 1]) : bflo(vr[i][c >> 1]);
                SBAR();
#pragma unroll
                for (int i = 0; i < 8; ++i) kv[i] = mul_vv(kv[i], vf[i]);
                u32x4 o4;
#pragma unroll
                for (int i = 0; i < 4; ++i) o4[i] = cvtpk(kv[7 - 2 * i], kv[6 - 2 * i]);
                *(LAS u32x4*)(tb + (cq * 4 + c) * 128 + ((rg ^ cq) << 4)) = o4;
            }
#pragma unroll
            for (int i = 0; i < 4; ++i) {
                const u32x4 t = *(const LAS u32x4*)(tb + i * 1024 + lane * 16);
                st_sc1(t, rs, (unsigned)WS_KVR + toffs + i * 32768 + (((lane & 7) ^ (2 * i + (lane >> 5))) << 4));
            }
        }
        CFENCE();
        DG(7);
        WAIT_BAR(4);
        DG(6);
        issue(6);
        asm volatile("s_waitcnt vmcnt(8)" ::: "memory");
        if (lds_add_rtn(fl + C_KST, lane) == 3u) signal();
#pragma unroll
        for (int i = 0; i < 8; ++i)
            glds16p<K2_WNT>(wvoff, wsrc(i >> 2, cw * 4 + (i & 3)), lds0 + K2_SF + (i >> 2) * 16384 + (cw * 4 + (i & 3)) * 1024);
        {
            const u32x4 z4 = {0u, 0u, 0u, 0u};
            if (lane < 2) *(LAS u32x4*)(cv + CV_HB + lane * 16) = z4;
#pragma unroll
            for (int i = 0; i < 4; ++i) {
                const int off = 32 + i * 1024 + lane * 16;
                const u32x4 hv = *(const LAS u32x4*)(cv + CV_HA + off);
                const unsigned pv = *(const LAS unsigned*)(cv + CV_HA + off - 4);
                u32x4 sh;
                sh[0] = __builtin_amdgcn_alignbit(hv[0], pv, 16);
                sh[1] = __builtin_amdgcn_alignbit(hv[1], hv[0], 16);
                sh[2] = __builtin_amdgcn_alignbit(hv[2], hv[1], 16);
                sh[3] = __builtin_amdgcn_alignbit(hv[3], hv[2], 16);
                *(LAS u32x4*)(cv + CV_HB + off) = sh;
            }
        }
        WAIT_BAR(8);
        DG(8);
        issue(7);
#pragma unroll
        for (int i = 0; i < 8; ++i)
            glds16p<K2_WNT>(wvoff, wsrc(idx >> 4, 32 + cw * 8 + i), lds0 + K2_SCR + cw * 1024);
        WAIT_BAR(8);
        DG(11);
        if (cw == 0) { poll_ge(cnt + b * 64, 64u); lds_add(fl + F_KGO, lane); }
        else lds_wait_ge(fl + F_KGO, 1u);
        {
            u32x4 t[4];
#pragma unroll
            for (int i = 0; i < 4; ++i) t[i] = ld_sc1(rs, (unsigned)WS_KVR + ch * 4096 + i * 1024 + voff);
#pragma unroll
            for (int i = 0; i < 4; ++i) *(LAS u32x4*)(cv + CV_KV + 480 + i * 1024 + lane * 16) = t[i];
            blank((unsigned)WS_ZT + ch * 4096); blank((unsigned)WS_ZT + ch * 4096 + 2048);
        }
        const int r1 = lane & 15, q = lane >> 4;
        const int toff = (r1 & 1) ? CV_HA + 2 * (16 + r1 + 9 - 8 * q) : CV_HB + 2 * (16 + r1 + 10 - 8 * q);
        const int boff = CV_KV + 448 + 32 * r1 + 16 * q;
        f32x4 y[8];
        u32x4 tf[8];
        bf16x8 bf[8];
        const lds_ptr bp = cv + boff - 448;
        lds_ptr tp[4];
#pragma unroll
        for (int i = 0; i < 4; ++i) { tp[i] = cv + toff + 1024 * i; asm volatile("" : "+v"(tp[i])); }
#define LDT(bb, a, hf) do { const LAS unsigned* t_ = (const LAS unsigned*)(tp[(a) >> 1] + 64 * (bb) + 512 * ((a) & 1) + 8 * (hf)); \
            tf[a][2 * (hf)] = t_[0]; tf[a][2 * (hf) + 1] = t_[1]; SBAR(); } while (0)
#define LDB(bb, a) do { bf[a] = lds_rd128(bp + 448 - 64 * (bb) + 512 * (a)); SBAR(); } while (0)
#define CM(a, ww) do { asm volatile("v_mfma_f32_16x16x32_bf16 %0, %1, %2, %0" : "+v"(y[(a) + (ww)]) : "v"(tf[a]), "v"(bf[ww])); SBAR(); } while (0)
#define CM0(a, ww) do { asm volatile("v_mfma_f32_16x16x32_bf16 %0, %1, %2, 0" : "=&v"(y[(a) + (ww)]) : "v"(tf[a]), "v"(bf[ww])); SBAR(); } while (0)
#define PC() do { asm volatile(PACE_OUT); SBAR(); } while (0)
#pragma unroll
        for (int a = 0; a < 8; ++a) { if (a < 5) { LDT(0, a, 0); LDT(0, a, 1); } LDB(0, 7 - a); }
#pragma unroll
        for (int i = 0; i < 8; ++i)
            glds16p<K2_WNT>(wvoff, wsrc(i >> 2, 16 + cw * 4 + (i & 3)), lds0 + (i >> 2) * 16384 + (cw * 4 + (i & 3)) * 1024);
#pragma unroll
        for (int i = 0; i < 4; ++i)
            glds16p<K2_QNT>((unsigned)(idx * 64 + cw * 16 + (lane >> 4)) * 2048u + xcd * 256 + (lane & 15) * 16, Aq + i * 8192, lds0 + K2_SCR + cw * 1024);
        RAW_BAR();
        asm volatile("s_waitcnt vmcnt(4)" ::: "memory");
        lds_add(fl + C_S1, lane);
        if (lds_add_rtn(fl + C_ZST, lane) == 3u && lane < 32) (void)__hip_atomic_fetch_add(cnt + (lane * 8 + xcd) * 64 + 32, 1u, RLX_AGENT);
        unsigned zf;
#define ZFL() do { asm volatile("global_load_dword %0, %1, %2 sc1" : "=v"(zf) : "v"(128u), "s"(cnt + b * 64) : "memory"); SBAR(); } while (0)
        __builtin_amdgcn_s_setprio(AUX_PRIO);
        const float dch = (ch & 1) ? bfhi(dd) : bflo(dd);
        const lds_ptr kdp = cv + CV_KV + 480 + 32 * r1 + 16 * (q >> 1) + ((q & 1) ? 0 : 8);
        float kd[8][4];
        u32x2 kk;
        unsigned kc0, kc1, zo[8][2];
#define KD_RD(t) do { kk = *(const LAS u32x2*)(kdp + 512 * (t)); SBAR(); } while (0)
#define KD_UP0(t) do { kd[t][0] = bfhi(kk[1]); kd[t][1] = bflo(kk[1]); SBAR(); } while (0)
#define KD_UP1(t) do { kd[t][2] = bfhi(kk[0]); kd[t][3] = bflo(kk[0]); SBAR(); } while (0)
#define KD_MU0(t) do { kd[t][0] = mul_sv(dch, kd[t][0]); kd[t][1] = mul_sv(dch, kd[t][1]); SBAR(); } while (0)
#define KD_MU1(t) do { kd[t][2] = mul_sv(dch, kd[t][2]); kd[t][3] = mul_sv(dch, kd[t][3]); SBAR(); } while (0)
#define KD_CV(t) do { kc0 = cvtpk(kd[t][0], kd[t][1]); kc1 = cvtpk(kd[t][2], kd[t][3]); SBAR(); } while (0)
#define KD_X0(t) do { kd[t][0] = bflo(kc0); kd[t][1] = bfhi(kc0); SBAR(); } while (0)
#define KD_X1(t) do { kd[t][2] = bflo(kc1); kd[t][3] = bfhi(kc1); SBAR(); } while (0)
#define ZF0(t) do { kc0 = cvtpk(y[t][0], y[t][1]); kc1 = cvtpk(y[t][2], y[t][3]); SBAR(); } while (0)
#define ZF1(t) do { y[t][0] = bflo(kc0); y[t][1] = bfhi(kc0); SBAR(); } while (0)
#define ZF2(t) do { y[t][2] = bflo(kc1); y[t][3] = bfhi(kc1); SBAR(); } while (0)
#define ZF3(t) do { y[t][0] = add_vv(y[t][0], kd[t][0]); y[t][1] = add_vv(y[t][1], kd[t][1]); SBAR(); } while (0)
#define ZF4(t) do { y[t][2] = add_vv(y[t][2], kd[t][2]); y[t][3] = add_vv(y[t][3], kd[t][3]); SBAR(); } while (0)
#define ZF5(t) do { zo[t][0] = nz(cvtpk(y[t][0], y[t][1])); zo[t][1] = nz(cvtpk(y[t][2], y[t][3])); SBAR(); } while (0)
#define ZST(pr) do { auto s0 = __builtin_amdgcn_permlane16_swap(zo[2 * (pr)][0], zo[2 * (pr) + 1][0], false, false); \
            auto s1 = __builtin_amdgcn_permlane16_swap(zo[2 * (pr)][1], zo[2 * (pr) + 1][1], false, false); \
            st_sc1(u32x4{s0[0], s1[0], s0[1], s1[1]}, rs, (unsigned)WS_ZT + ch * 4096 + (pr) * 1024 + voff); SBAR(); } while (0)
#define W3(i) do { if ((i) == 0) PC(); } while (0)
#define HF() do { lds_add(fl + C_HF, lane); SBAR(); } while (0)
        CM0(0, 7); LDB(1, 7); CM0(0, 6); LDT(0, 5, 0); CM0(0, 5); LDT(0, 5, 1); CM0(0, 4); LDT(0, 6, 0); CM0(0, 3); LDT(0, 6, 1); CM0(0, 2); LDT(0, 7, 0); CM0(0, 1); LDT(0, 7, 1); CM0(0, 0); KD_RD(0);
        CM(1, 6); LDB(1, 6); CM(1, 5); LDT(1, 0, 0); CM(1, 4); LDT(1, 0, 1); CM(1, 3); PC(); CM(1, 2); PC(); CM(1, 1); KD_UP0(0); CM(1, 0); KD_UP1(0);
        CM(2, 5); LDB(1, 5); CM(2, 4); LDT(1, 1, 0); CM(2, 3); LDT(1, 1, 1); CM(2, 2); KD_MU0(0); CM(2, 1); KD_MU1(0); CM(2, 0); KD_CV(0);
        CM(3, 4); LDB(1, 4); CM(3, 3); LDT(1, 2, 0); CM(3, 2); LDT(1, 2, 1); CM(3, 1); KD_X0(0); CM(3, 0); KD_X1(0);
        CM(4, 3); LDB(1, 3); CM(4, 2); LDT(1, 3, 0); CM(4, 1); LDT(1, 3, 1); CM(4, 0); KD_RD(1);
        CM(5, 2); LDB(1, 2); CM(5, 1); LDT(1, 4, 0); CM(5, 0); LDT(1, 4, 1);
        CM(6, 1); LDB(1, 1); CM(6, 0); PC();
        CM(7, 0); LDB(1, 0);
        CM(0, 7); LDB(2, 7); CM(0, 6); LDT(1, 5, 0); CM(0, 5); LDT(1, 5, 1); CM(0, 4); LDT(1, 6, 0); CM(0, 3); LDT(1, 6, 1); CM(0, 2); LDT(1, 7, 0); CM(0, 1); LDT(1, 7, 1); CM(0, 0); PC();
        CM(1, 6); LDB(2, 6); CM(1, 5); LDT(2, 0, 0); CM(1, 4); LDT(2, 0, 1); CM(1, 3); KD_UP0(1); CM(1, 2); KD_UP1(1); CM(1, 1); KD_MU0(1); CM(1, 0); KD_MU1(1);
        CM(2, 5); LDB(2, 5); CM(2, 4); LDT(2, 1, 0); CM(2, 3); LDT(2, 1, 1); CM(2, 2); KD_CV(1); CM(2, 1); KD_X0(1); CM(2, 0); KD_X1(1);
        CM(3, 4); LDB(2, 4); CM(3, 3); LDT(2, 2, 0); CM(3, 2); LDT(2, 2, 1); CM(3, 1); KD_RD(2); CM(3, 0); PC();
        CM(4, 3); LDB(2, 3); CM(4, 2); LDT(2, 3, 0); CM(4, 1); LDT(2, 3, 1); CM(4, 0); PC();
        CM(5, 2); LDB(2, 2); CM(5, 1); LDT(2, 4, 0); CM(5, 0); LDT(2, 4, 1);
        CM(6, 1); LDB(2, 1); CM(6, 0); KD_UP0(2);
        CM(7, 0); LDB(2, 0);
        CM(0, 7); LDB(3, 7); CM(0, 6); LDT(2, 5, 0); CM(0, 5); LDT(2, 5, 1); CM(0, 4); LDT(2, 6, 0); CM(0, 3); LDT(2, 6, 1); CM(0, 2); LDT(2, 7, 0); CM(0, 1); LDT(2, 7, 1); CM(0, 0); KD_UP1(2);
        CM(1, 6); LDB(3, 6); CM(1, 5); LDT(3, 0, 0); CM(1, 4); LDT(3, 0, 1); CM(1, 3); KD_MU0(2); CM(1, 2); KD_MU1(2); CM(1, 1); KD_CV(2); CM(1, 0); KD_X0(2);
        CM(2, 5); LDB(3, 5); CM(2, 4); LDT(3, 1, 0); CM(2, 3); LDT(3, 1, 1); CM(2, 2); KD_X1(2); CM(2, 1); KD_RD(3); CM(2, 0); PC();
        CM(3, 4); LDB(3, 4); CM(3, 3); LDT(3, 2, 0); CM(3, 2); LDT(3, 2, 1); CM(3, 1); PC(); CM(3, 0); KD_UP0(3);
        CM(4, 3); LDB(3, 3); CM(4, 2); LDT(3, 3, 0); CM(4, 1); LDT(3, 3, 1); CM(4, 0); KD_UP1(3);
        CM(5, 2); LDB(3, 2); CM(5, 1); LDT(3, 4, 0); CM(5, 0); LDT(3, 4, 1);
        CM(6, 1); LDB(3, 1); CM(6, 0); KD_MU0(3);
        CM(7, 0); LDB(3, 0);
        CM(0, 7); LDB(4, 7); CM(0, 6); LDT(3, 5, 0); CM(0, 5); LDT(3, 5, 1); CM(0, 4); LDT(3, 6, 0); CM(0, 3); LDT(3, 6, 1); CM(0, 2); LDT(3, 7, 0); CM(0, 1); LDT(3, 7, 1); CM(0, 0); KD_MU1(3);
        CM(1, 6); LDB(4, 6); CM(1, 5); LDT(4, 0, 0); CM(1, 4); LDT(4, 0, 1); CM(1, 3); KD_CV(3); CM(1, 2); KD_X0(3); CM(1, 1); KD_X1(3); CM(1, 0); KD_RD(4);
        CM(2, 5); LDB(4, 5); CM(2, 4); LDT(4, 1, 0); CM(2, 3); LDT(4, 1, 1); CM(2, 2); PC(); CM(2, 1); PC(); CM(2, 0); KD_UP0(4);
        CM(3, 4); LDB(4, 4); CM(3, 3); LDT(4, 2, 0); CM(3, 2); LDT(4, 2, 1); CM(3, 1); KD_UP1(4); CM(3, 0); KD_MU0(4);
        CM(4, 3); LDB(4, 3); CM(4, 2); LDT(4, 3, 0); CM(4, 1); LDT(4, 3, 1); CM(4, 0); KD_MU1(4);
        CM(5, 2); LDB(4, 2); CM(5, 1); LDT(4, 4, 0); CM(5, 0); LDT(4, 4, 1);
        CM(6, 1); LDB(4, 1); CM(6, 0); KD_CV(4);
        CM(7, 0); LDB(4, 0);
        CM(0, 7); LDB(5, 7); CM(0, 6); LDT(4, 5, 0); CM(0, 5); LDT(4, 5, 1); CM(0, 4); LDT(4, 6, 0); CM(0, 3); LDT(4, 6, 1); CM(0, 2); LDT(4, 7, 0); CM(0, 1); LDT(4, 7, 1); CM(0, 0); KD_X0(4);
        CM(1, 6); LDB(5, 6); CM(1, 5); LDT(5, 0, 0); CM(1, 4); LDT(5, 0, 1); CM(1, 3); KD_X1(4); CM(1, 2); KD_RD(5); CM(1, 1); PC(); CM(1, 0); PC();
        CM(2, 5); LDB(5, 5); CM(2, 4); LDT(5, 1, 0); CM(2, 3); LDT(5, 1, 1); CM(2, 2); KD_UP0(5); CM(2, 1); KD_UP1(5); CM(2, 0); KD_MU0(5);
        CM(3, 4); LDB(5, 4); CM(3, 3); LDT(5, 2, 0); CM(3, 2); LDT(5, 2, 1); CM(3, 1); KD_MU1(5); CM(3, 0); KD_CV(5);
        CM(4, 3); LDB(5, 3); CM(4, 2); LDT(5, 3, 0); CM(4, 1); LDT(5, 3, 1); CM(4, 0); KD_X0(5);
        CM(5, 2); LDB(5, 2); CM(5, 1); LDT(5, 4, 0); CM(5, 0); LDT(5, 4, 1);
        CM(6, 1); LDB(5, 1); CM(6, 0); KD_X1(5);
        CM(7, 0); LDB(5, 0);
        CM(0, 7); LDB(6, 7); CM(0, 6); LDT(5, 5, 0); CM(0, 5); LDT(5, 5, 1); CM(0, 4); LDT(5, 6, 0); CM(0, 3); LDT(5, 6, 1); CM(0, 2); LDT(5, 7, 0); CM(0, 1); LDT(5, 7, 1); CM(0, 0); KD_RD(6);
        CM(1, 6); LDB(6, 6); CM(1, 5); LDT(6, 0, 0); CM(1, 4); LDT(6, 0, 1); CM(1, 3); PC(); CM(1, 2); PC(); CM(1, 1); KD_UP0(6); CM(1, 0); KD_UP1(6);
        CM(2, 5); LDB(6, 5); CM(2, 4); LDT(6, 1, 0); CM(2, 3); LDT(6, 1, 1); CM(2, 2); KD_MU0(6); CM(2, 1); KD_MU1(6); CM(2, 0); KD_CV(6);
        CM(3, 4); LDB(6, 4); CM(3, 3); LDT(6, 2, 0); CM(3, 2); LDT(6, 2, 1); CM(3, 1); KD_X0(6); CM(3, 0); KD_X1(6);
        CM(4, 3); LDB(6, 3); CM(4, 2); LDT(6, 3, 0); CM(4, 1); LDT(6, 3, 1); CM(4, 0); KD_RD(7);
        CM(5, 2); LDB(6, 2); CM(5, 1); LDT(6, 4, 0); CM(5, 0); LDT(6, 4, 1);
        CM(6, 1); LDB(6, 1); CM(6, 0); PC();
        CM(7, 0); LDB(6, 0);
        CM(0, 7); LDB(7, 7); CM(0, 6); LDT(6, 5, 0); CM(0, 5); LDT(6, 5, 1); CM(0, 4); LDT(6, 6, 0); CM(0, 3); LDT(6, 6, 1); CM(0, 2); LDT(6, 7, 0); CM(0, 1); LDT(6, 7, 1); CM(0, 0); PC();
        CM(1, 6); LDB(7, 6); CM(1, 5); LDT(7, 0, 0); CM(1, 4); LDT(7, 0, 1); CM(1, 3); KD_UP0(7); CM(1, 2); KD_UP1(7); CM(1, 1); KD_MU0(7); CM(1, 0); KD_MU1(7);
        CM(2, 5); LDB(7, 5); CM(2, 4); LDT(7, 1, 0); CM(2, 3); LDT(7, 1, 1); CM(2, 2); KD_CV(7); CM(2, 1); KD_X0(7); CM(2, 0); KD_X1(7);
        CM(3, 4); LDB(7, 4); CM(3, 3); LDT(7, 2, 0); CM(3, 2); LDT(7, 2, 1); CM(3, 1); PC(); CM(3, 0); PC();
        CM(4, 3); LDB(7, 3); CM(4, 2); LDT(7, 3, 0); CM(4, 1); LDT(7, 3, 1); CM(4, 0); PC();
        CM(5, 2); LDB(7, 2); CM(5, 1); LDT(7, 4, 0); CM(5, 0); LDT(7, 4, 1);
        CM(6, 1); LDB(7, 1); CM(6, 0); PC();
        CM(7, 0); LDB(7, 0);
        CM(0, 3); LDT(7, 5, 0); CM(0, 2); LDT(7, 5, 1); CM(1, 2); LDT(7, 6, 0); CM(1, 1); LDT(7, 6, 1); CM(2, 1); LDT(7, 7, 0); CM(2, 0); LDT(7, 7, 1);
        CM(3, 0); HF(); CM(0, 1); ZFL(); CM(0, 0); W3(0); CM(1, 0); ZF0(2); W3(1); CM(0, 5); ZF1(2); W3(2); CM(0, 4); ZF2(2); W3(3);
        CM(1, 4); ZF3(2); W3(4); CM(1, 3); ZF4(2); W3(5); CM(2, 3); ZF5(2); W3(6); CM(2, 2); ZF0(3); W3(7); CM(3, 2); ZF1(3);  CM(3, 1); ZF2(3);
        CM(4, 1); ZF3(3);  CM(4, 0); ZF4(3);  CM(5, 0); ZF5(3);  CM(0, 7); ZST(1);  CM(0, 6); ZF0(0);  CM(1, 6); ZF1(0);
        CM(1, 5); ZF2(0);  CM(2, 5); ZF3(0);  CM(2, 4); ZF4(0);  CM(3, 4); ZF5(0);  CM(3, 3); ZF0(1);  CM(4, 3); ZF1(1);
        CM(4, 2); ZF2(1);  CM(5, 2); ZF3(1);  CM(5, 1); ZF4(1);  CM(6, 1); ZF5(1);  CM(6, 0); ZST(0);  CM(7, 0); ZF0(4);
        DG(4);
        ZF1(4); ZF2(4); ZF3(4); ZF4(4); ZF5(4); ZF0(5); ZF1(5); ZF2(5); ZF3(5); ZF4(5); ZF5(5); ZST(2); ZF0(6); ZF1(6); ZF2(6); ZF3(6); ZF4(6); ZF5(6); ZF0(7); ZF1(7); ZF2(7); ZF3(7); ZF4(7); ZF5(7); ZST(3);
        DG(10);
        __builtin_amdgcn_s_setprio(3);
        asm volatile("s_waitcnt vmcnt(4)" : "+v"(zf) :: "memory");
        if (__builtin_expect((unsigned)__builtin_amdgcn_readfirstlane(zf) < 32u, 0)) poll_ge(cnt + b * 64 + 32, 32u);
        CFENCE();
        while ((unsigned)__builtin_amdgcn_readfirstlane(*(volatile LAS unsigned*)(fl + C_OL)) < ZL_GATE) __builtin_amdgcn_s_sleep(4);
        CFENCE();
        const int arow = idx * 64 + (cw >> 1) * 32 + rg * 4, acol = xcd * 256 + (cw & 1) * 128 + cq * 16;
        u32x4 qr[4];
#pragma unroll
        for (int i = 0; i < 4; ++i) qr[i] = gld128((unsigned)(arow * 2048 + acol), Aq + i * 2048);
        {
            const lds_ptr tr = cv + CV_KV + 480;
            u32x4 t[4];
            const int ck = (cw >> 1) * 4 + ((lane ^ ((lane >> 5) * 2)) & 3);
            const unsigned zb = (unsigned)WS_ZT + (unsigned)(xcd * 128 + (cw & 1) * 64 + (lane >> 4) * 4) * 4096u + (idx >> 3) * 1024 +
                                ((idx & 3) * 4 + ((idx >> 2) & 1) * 16 + (ck >> 1) + (ck & 1) * 32) * 16;
#pragma unroll 1
            for (unsigned spins = 0; spins < (1u << 20); ++spins) {
#pragma unroll
                for (int i = 0; i < 4; ++i) t[i] = ld_sc1s(rs, zb + ((((lane >> 2) & 3) ^ i) << 12), i * 65536);
                if (!missing(mx4(mx4(mx8(t[0], t[1]), t[2]), t[3]))) break;
                __builtin_amdgcn_s_sleep(Z_SLEEP);
            }
            DG(13);
            __builtin_amdgcn_s_setprio(ZE_PRIO);
#pragma unroll
            for (int i = 0; i < 4; ++i) *(LAS u32x4*)(tr + i * 1024 + lane * 16) = t[i];
#pragma unroll
            for (int i = 0; i < 4; ++i) asm volatile("" : "+v"(qr[i]));
            u32x2 zc[8];
#pragma unroll
            for (int c = 0; c < 8; ++c)
                zc[c] = *(const LAS u32x2*)(tr + cq * 512 + (c >> 2) * 256 + ((((c & 3) * 4 + (rg >> 1)) ^ (cq * 2)) << 4) + (rg & 1) * 8);
#pragma unroll
            for (int i = 0; i < 4; ++i) {
                u32x4 o;
#pragma unroll
                for (int c = 0; c < 4; ++c) {
                    const unsigned z0 = zc[2 * c][i >> 1], z1 = zc[2 * c + 1][i >> 1];
                    o[c] = cvtpk(mul_vv(bflo(qr[i][c]), (i & 1) ? bfhi(z0) : bflo(z0)), mul_vv(bfhi(qr[i][c]), (i & 1) ? bfhi(z1) : bflo(z1)));
                }
                __builtin_amdgcn_raw_buffer_store_b128(o, ro, arow * 4096 + acol, i * 4096, OUT_AUX);
            }
        }
        DG_END(14);
        return;
    }

    const int half = w >> 2;
    const int TA = qb * 8 + (w & 3) * 2 + half, TB = TA ^ 1;
    bf16x8 sel;
    {
        const short one = (((lane >> 2) & 1) == ((lane >> 4) & 1) && (lane & 11) == 0) ? (short)0x3f80 : (short)0;
#pragma unroll
        for (int i = 0; i < 8; ++i) sel[i] = one;
    }
    f32x16 oA = f32x16{}, oB = f32x16{};
    f32x4 lA = f32x4{}, lB = f32x4{};
    f32x16 negm, sA, sB;
    lds_ptr lp = lds + half * 16384 + lane * 16;
    bf16x8 k0[2], k1[2], v0[2], v1[2];
    u32x4 p0, p1 = {0u, 0u, 0u, 0u};
#define EX(X, i) X[i] = __builtin_amdgcn_exp2f(X[i])
    lds_ptr qpa = lds + K2_SF + (TA & 7) * 2048 + lane * 16, qpb = lds + K2_SF + (TB & 7) * 2048 + lane * 16;
    asm volatile("" : "+v"(qpa), "+v"(qpb), "+v"(lp), "+v"(sel), "+v"(oA), "+v"(oB), "+v"(lA), "+v"(lB), "+v"(p1));
    WAIT_BAR(0);
    DG(3);
    const bf16x8 qa0 = lds_rd128(qpa), qb0 = lds_rd128(qpb);
    const bf16x8 x0 = lds_rd128(lds + lane * 16), x1 = lds_rd128(lds + lane * 16 + 1024);
    const bf16x8 qa1 = lds_rd128(qpa + 1024), qb1 = lds_rd128(qpb + 1024);
    {
        k0[0] = lds_rd128(lp); k1[0] = lds_rd128(lp + 1024);
        v0[0] = lds_rd128(lp + 2048); v1[0] = lds_rd128(lp + 3072);
        sA = MFMA32(x0, qa0, f32x16{});
        sB = MFMA32(x0, qb0, f32x16{});
        sA = MFMA32(x1, qa1, sA);
        sB = MFMA32(x1, qb1, sB);
        float m = fmx(sA[0], sA[1]), m2 = fmx(sB[0], sB[1]);
#pragma unroll
        for (int i = 2; i < 16; i += 2) m = fmx(fmx(m, sA[i]), sA[i + 1]);
#pragma unroll
        for (int i = 2; i < 16; i += 2) m2 = fmx(fmx(m2, sB[i]), sB[i + 1]);
        m = fmx(m, m2);
        auto rr = __builtin_amdgcn_permlane32_swap(__float_as_uint(m), __float_as_uint(m), false, false);
        m = fmx(__uint_as_float(rr[0]), __uint_as_float(rr[1]));
#pragma unroll
        for (int i = 0; i < 16; ++i) negm[i] = -m;
        sA = MFMA32(k0[0], qa0, negm);
        sA = MFMA32(k1[0], qa1, sA);
        v1[1] = v1[0];
        SBAR();
        RAW_BAR();
        SBAR();
        EX(sA, 0); EX(sA, 1);
    }
    DG(10);
#define SUM16(l, p) asm volatile("v_mfma_f32_16x16x32_bf16 %0, %1, %2, %0" : "+v"(l) : "v"(sel), "v"(p))
#define HALF(C, oC, lC, N, qx, qy, ka, kb, oP, lP, vprev, vcur0, PRE) do { \
        oP = MFMA32(vprev, PB(p1), oP); SBAR(); \
        PRE; EX(C, 2); EX(C, 3); EX(C, 4); SBAR(); \
        N = MFMA32(ka, qx, negm); SBAR(); \
        EX(C, 5); EX(C, 6); EX(C, 7); SBAR(); \
        SUM16(lP, p1); SBAR(); \
        p0[0] = cvtpk(C[0], C[1]); p0[1] = cvtpk(C[2], C[3]); SBAR(); \
        N = MFMA32(kb, qy, N); SBAR(); \
        p0[2] = cvtpk(C[4], C[5]); p0[3] = cvtpk(C[6], C[7]); SBAR(); EX(C, 8); EX(C, 9); SBAR(); \
        oC = MFMA32(vcur0, PB(p0), oC); SBAR(); \
        EX(C, 10); EX(C, 11); EX(C, 12); SBAR(); \
        SUM16(lC, p0); SBAR(); \
        EX(C, 13); EX(C, 14); EX(C, 15); SBAR(); \
        p1[0] = cvtpk(C[8], C[9]); p1[1] = cvtpk(C[10], C[11]); p1[2] = cvtpk(C[12], C[13]); p1[3] = cvtpk(C[14], C[15]); SBAR(); \
        EX(N, 0); EX(N, 1); SBAR(); \
    } while (0)
#define RD4(Y, nx) do { v1[Y] = lds_rd128((nx) + 3072); v0[Y] = lds_rd128((nx) + 2048); \
        k1[Y] = lds_rd128((nx) + 1024); k0[Y] = lds_rd128(nx); } while (0)
#define KT(X, nx, PRE0) do { \
        HALF(sA, oA, lA, sB, qb0, qb1, k0[X], k1[X], oB, lB, v1[X ^ 1], v0[X], PRE0; RD4(X ^ 1, nx)); \
        HALF(sB, oB, lB, sA, qa0, qa1, k0[X ^ 1], k1[X ^ 1], oA, lA, v1[X], v0[X], (void)0); \
    } while (0)
    SBAR();
#pragma unroll 1
    for (int st = 0; st < 8; ++st) {
        const lds_ptr cur = lp + (st & 1) * K2_SLOT;
        const lds_ptr nxt = lp + ((st + 1) & 1) * K2_SLOT;
        PRIO(2);
        KT(0, cur + 4096, (void)0);
        PRIO(1);
        KT(1, cur + 8192, (void)0);
        PRIO(0);
        KT(0, cur + 12288, (void)0);
        PRIO(3);
        KT(1, nxt, RAW_BAR(); SBAR());
    }
    oB = MFMA32(v1[1], PB(p1), oB);
    SBAR();
    SUM16(lB, p1);
    asm volatile("s_nop 7" : "+v"(oB) :: "memory");
    SBAR();
    DG(4);
    {
        const lds_ptr xo = lds + K2_SLOT + w * 4096 + lane * 16;
        *(LAS float*)(lds + K2_LX + w * 256 + lane * 4) = lB[0];
#pragma unroll
        for (int i = 0; i < 4; ++i) *(LAS f32x4*)(xo + i * 1024) = f32x4{oB[4 * i], oB[4 * i + 1], oB[4 * i + 2], oB[4 * i + 3]};
        lds_add(fl + F_X + w * 4, lane);
        lds_add(fl + C_END + half * 4, lane);
        lds_wait_ge(fl + F_X + (w ^ 4) * 4, 1u);
        const lds_ptr xi = lds + K2_SLOT + (w ^ 4) * 4096 + lane * 16;
        f32x4 t[4];
        const float lx = *(const LAS float*)(lds + K2_LX + (w ^ 4) * 256 + (lane & 31) * 4);
        { auto rr = __builtin_amdgcn_permlane32_swap(__float_as_uint(lA[0]), __float_as_uint(lA[0]), false, false); lA[0] = __uint_as_float(rr[0]); }
#pragma unroll
        for (int i = 0; i < 4; ++i) t[i] = *(const LAS f32x4*)(xi + i * 1024);
        SBAR();
        lA[0] += lx;
#pragma unroll
        for (int i = 0; i < 16; ++i) { float o = oA[i]; acc_vv(o, t[i >> 2][i & 3]); oA[i] = o; }
    }
    DG(9);
    const float l = lA[0];
    u32x4 on0, on1;
    auto finish = [&](float ls) {
        float rl = __builtin_amdgcn_rcpf(ls);
        asm volatile("s_nop 1" : "+v"(rl));
        f32x16 o;
#pragma unroll
        for (int i = 0; i < 16; ++i) o[i] = mul_vv(oA[i], rl);
        on0 = nz4(pack8n(o, 0)); on1 = nz4(pack8n(o, 1));
    };
    finish(l);
    {
        float t0, t1;
        asm("v_mul_f32_e32 %0, 0, %2\n\tv_mul_f32_e32 %1, 0, %3\n\tv_fmac_f32_e32 %0, 0, %4\n\tv_fmac_f32_e32 %1, 0, %5\n\tv_fmac_f32_e32 %0, 0, %6\n\tv_fmac_f32_e32 %1, 0, %7\n\tv_fmac_f32_e32 %0, 0, %8\n\tv_fmac_f32_e32 %1, 0, %9\n\t"
            "v_fmac_f32_e32 %0, 0, %10\n\tv_fmac_f32_e32 %1, 0, %11\n\tv_fmac_f32_e32 %0, 0, %12\n\tv_fmac_f32_e32 %1, 0, %13\n\tv_fmac_f32_e32 %0, 0, %14\n\tv_fmac_f32_e32 %1, 0, %15\n\tv_fmac_f32_e32 %0, 0, %16\n\tv_fmac_f32_e32 %1, 0, %17"
            : "=&v"(t0), "=&v"(t1) : "v"(oA[0]), "v"(oA[1]), "v"(oA[2]), "v"(oA[3]), "v"(oA[4]), "v"(oA[5]), "v"(oA[6]), "v"(oA[7]), "v"(oA[8]), "v"(oA[9]), "v"(oA[10]), "v"(oA[11]), "v"(oA[12]), "v"(oA[13]), "v"(oA[14]), "v"(oA[15]));
        const bool ok = (l <= 1.0e30f) && (l >= 1.0e-18f) && (t0 + t1 == 0.f);
        if (__builtin_expect(__any(!ok), 0)) {
            float m = -INFINITY, ls = 0.f;
            oA = f32x16{};
#pragma unroll 1
            for (int t = 0; t < 64; ++t) {
                const char* g = kvg + t * 4096 + lane * 16;
                const bf16x8 a0 = *(const bf16x8*)g, a1 = *(const bf16x8*)(g + 1024);
                const bf16x8 b0 = *(const bf16x8*)(g + 2048), b1 = *(const bf16x8*)(g + 3072);
                f32x16 s = MFMA32(a0, qa0, f32x16{});
                s = MFMA32(a1, qa1, s);
                float tm = s[0];
#pragma unroll
                for (int i = 1; i < 16; ++i) tm = __builtin_fmaxf(tm, s[i]);
                auto rr = __builtin_amdgcn_permlane32_swap(__float_as_uint(tm), __float_as_uint(tm), false, false);
                tm = __builtin_fmaxf(__uint_as_float(rr[0]), __uint_as_float(rr[1]));
                const float mn = __builtin_fmaxf(m, tm);
                const float alpha = __builtin_amdgcn_exp2f(m - mn);
                m = mn;
                float ps = 0.f;
#pragma unroll
                for (int i = 0; i < 16; ++i) { s[i] = __builtin_amdgcn_exp2f(s[i] - mn); ps += s[i]; oA[i] *= alpha; }
                ls = ls * alpha + ps;
                oA = MFMA32(b0, PB(pack8n(s, 0)), oA);
                oA = MFMA32(b1, PB(pack8n(s, 1)), oA);
            }
            auto rr = __builtin_amdgcn_permlane32_swap(__float_as_uint(ls), __float_as_uint(ls), false, false);
            asm volatile("s_nop 7\n\ts_nop 7" : "+v"(oA));
            finish(__uint_as_float(rr[0]) + __uint_as_float(rr[1]));
        }
    }
    u32x4 w3[4];
    {
        const unsigned og = (unsigned)WS_O + (unsigned)(TA * 32 + h) * 2048u + lane * 16;
        st_sc1(on0, rs, og);
        st_sc1(on1, rs, og + 1024);
        lds_add(fl + C_S1, lane);
        const char* s2 = wsrc(half ^ 1, 32 + (w & 3) * 4);
        const char* s3 = wsrc(half, 48 + (w & 3) * 4);
        lds_wait_ge(fl + C_S1, 12u);
        DG(5);
#pragma unroll
        for (int i = 0; i < 4; ++i)
            glds16p<K2_WNT>(wvoff, s2 + i * 16384, lds0 + K2_SLOT + ((w ^ 4) * 4 + i) * 1024);
#pragma unroll
        for (int i = 0; i < 4; ++i) w3[i] = gld128(wvoff, s3 + i * 16384);
    }
    {
        const int half = w & 1;
        const int T3 = tt * 4 + (w >> 1);
        const unsigned og = (unsigned)WS_O + (unsigned)T3 * 65536u + half * 32768 + lane * 16;
        const int r = lane & 31, hh = lane >> 5;
        const int g = lane >> 4, q = (lane & 15) >> 2, p = lane & 3;
        int woff[2];
#pragma unroll
        for (int n = 0; n < 2; ++n) {
            const int c = 4 * (n ^ half) + 2 * (p & 1) + (g & 1);
            woff[n] = (4 * hh + q) * 128 + ((c ^ ((q >> 1) << 2)) << 4) + (p >> 1) * 8;
        }
        f32x16 acc[2] = {f32x16{}, f32x16{}};
        lds_ptr wl[3][2] = {{lds + half * 16384 + woff[0], lds + half * 16384 + woff[1]},
                            {lds + K2_SF + half * 16384 + woff[0], lds + K2_SF + half * 16384 + woff[1]},
                            {lds + K2_CV + half * 2 * CV_BYTES + woff[0], lds + K2_CV + half * 2 * CV_BYTES + woff[1]}};
        asm volatile("" : "+v"(wl[0][0]), "+v"(wl[0][1]), "+v"(wl[1][0]), "+v"(wl[1][1]), "+v"(wl[2][0]), "+v"(wl[2][1]));
#define MS(m) ((m) < 64 - 2 * TL ? (m) >> 1 : 32 - TL + ((m) - (64 - 2 * TL)) % TL)
#define MN(m) ((m) < 64 - 2 * TL ? (m) & 1 : (m) < 64 - TL)
        constexpr int WR = 2 * WD + 2;
        s16x4 wa[WR], wb[WR];
#define LDW(m) do { const int s_ = MS(m), f_ = s_ < 8 ? 1 : s_ < 24 ? 0 : 2; \
            const int o_ = f_ == 2 ? ((s_ >> 2) & 1) * CV_BYTES + (s_ & 3) * 2048 : (f_ ? 0 : ((s_ >> 3) - 1) * K2_SLOT) + (s_ & 7) * 2048; \
            wa[(m) % WR] = lds_tr(wl[f_][MN(m)] + o_); wb[(m) % WR] = lds_tr(wl[f_][MN(m)] + o_ + 1024); SBAR(); } while (0)
#pragma unroll
        for (int m = 0; m < 2 * WD; ++m) LDW(m);
        u32x4 of[OWIN];
#define LDO(i) do { of[(i) % OWIN] = ld_sc1s(rs, og, (((i) + krot) & 31) * 1024); SBAR(); } while (0)
        for (unsigned spins = 0; spins < (1u << 20); ++spins) {
#pragma unroll
            for (int i = 0; i < OW0; ++i) LDO(i);
            unsigned m = mx8(of[0], of[1]);
#pragma unroll
            for (int i = 2; i < OW0; ++i) m = mx4(m, of[i]);
            if (!missing(m)) break;
            DGC(12);
        }
        lds_add(fl + C_S2, lane);
#pragma unroll
        for (int i = OW0; i < OWIN; ++i) LDO(i);
        DG(6);
        __builtin_amdgcn_s_setprio(P3_PRIO);
        SBAR();
        unsigned fv = 0;
#define FLAG_RD(a) do { fv = *(volatile LAS unsigned*)(a); SBAR(); } while (0)
#define FLAG_CHK(a, n) do { if (__builtin_expect((unsigned)__builtin_amdgcn_readfirstlane(fv) < (n), 0)) lds_wait_ge(a, n); CFENCE(); SBAR(); } while (0)
        lds_ptr xo = lds + K2_SF + w * 4096 + lane * 16, xi = lds + K2_SF + (w ^ 1) * 4096 + lane * 16;
        int bo = (T3 * 32 + (lane & 15)) * 4096 + 2048 + nt * 128 + half * 64 + g * 16;
        lds_ptr w3d = lds + K2_CV + (w >> 1) * CV_BYTES + (w & 1) * 4096 + lane * 16;
        asm volatile("" : "+v"(xo), "+v"(xi), "+v"(bo), "+v"(w3d));
        f32x4 xt[4];
        constexpr int M3 = TL > 8 ? 64 - 2 * TL + TL - 8 - 2 * WD : 48 - 2 * WD;
        sfor(std::make_integer_sequence<int, 64>{}, [&](auto mc) __attribute__((always_inline)) {
            constexpr int m = decltype(mc)::value, s = MS(m), n = MN(m);
            constexpr bool first = m < 64 - 2 * TL ? n == 0 : n == 1;
            constexpr bool last = m < 64 - 2 * TL ? n == 1 : n == 0;
            acc[n] = MFMA32(cat8(wa[m % WR], wb[m % WR]), PB(of[s % OWIN]), acc[n]);
            SBAR();
            if constexpr (m == 32 - 2 * WD) FLAG_CHK(fl + C_S2, 8u);
            if constexpr (m == M3) FLAG_CHK(fl + C_S3, 8u);
            if constexpr (m + 2 * WD < 64) LDW(m + 2 * WD);
            if constexpr (first && (s & 1) && s + 1 >= OW0 && s + 1 < 32) {
                if (__builtin_expect(missing(mx8(of[(s + 1) % OWIN], of[(s + 2) % OWIN])), 0)) {
#pragma unroll 1
                    for (unsigned spins = 0; spins < (1u << 20); ++spins) {
                        __builtin_amdgcn_s_sleep(2);
                        LDO(s + 1); LDO(s + 2);
                        DGC(13);
                        if (!missing(mx8(of[(s + 1) % OWIN], of[(s + 2) % OWIN]))) break;
                    }
                }
            }
            if constexpr (last && s + OWIN < 32) LDO(s + OWIN);
            if constexpr (m == 15 - 2 * WD) lds_add(fl + C_U0, lane);
            if constexpr (m == 29 - 2 * WD) FLAG_RD(fl + C_S2);
            if constexpr (m == W3M - 3) FLAG_RD(fl + C_HF);
            if constexpr (m == W3M) FLAG_CHK(fl + C_HF, 4u);
            if constexpr (m >= W3M && m < W3M + 8 && ((m - W3M) & 1) == 0) {
                asm volatile("" : "+v"(w3[((m - W3M) >> 1) & 3]));
                *(LAS u32x4*)(w3d + (((m - W3M) >> 1) & 3) * 1024) = w3[((m - W3M) >> 1) & 3];
            }
            if constexpr (m == W3M + 6) lds_add(fl + C_S3, lane);
            if constexpr (m == M3 - 3) FLAG_RD(fl + C_S3);
            if constexpr (m == 64 - TL - 3) FLAG_RD(fl + C_U0);
            if constexpr (m == 64 - TL) FLAG_CHK(fl + C_U0, 8u);
            if constexpr (m == 64 - TL + 1 || m == 64 - TL + 2) {
                constexpr int i = 2 * (m - (64 - TL + 1));
                *(LAS f32x4*)(xo + i * 1024) = f32x4{acc[1][4 * i], acc[1][4 * i + 1], acc[1][4 * i + 2], acc[1][4 * i + 3]};
                *(LAS f32x4*)(xo + i * 1024 + 1024) = f32x4{acc[1][4 * i + 4], acc[1][4 * i + 5], acc[1][4 * i + 6], acc[1][4 * i + 7]};
            }
            if constexpr (m == 64 - TL + 2) lds_add(fl + F_R + w * 4, lane);
            if constexpr (m == XR_AT - 2) FLAG_RD(fl + F_R + (w ^ 1) * 4);
            if constexpr (m == XR_AT) {
                FLAG_CHK(fl + F_R + (w ^ 1) * 4, 1u);
#pragma unroll
                for (int i = 0; i < 4; ++i) xt[i] = *(const LAS f32x4*)(xi + i * 1024);
            }
            if constexpr (ZL_GATE && last && s + OWIN == ZL_AT) lds_add(fl + C_OL, lane);
            if constexpr (m == 2 * P3_T1 + 1) __builtin_amdgcn_s_setprio(P3_PRIO - 1);
            SBAR();
            if constexpr (m == 31) DG(11);
            if constexpr (m == 47) DG(14);
        });
        asm volatile("s_nop 11" : "+v"(acc[0]));
        DG(7);
#pragma unroll
        for (int i = 0; i < 16; ++i) { float o = xt[i >> 2][i & 3]; acc_vv(o, acc[0][i]); acc[1][i] = o; }
        u32x4 pa = pack8n(acc[1], 0), pb = pack8n(acc[1], 1);
#pragma unroll
        for (int i = 0; i < 4; ++i) {
            auto sw = __builtin_amdgcn_permlane16_swap(pa[i], pb[i], false, false);
            pa[i] = sw[0]; pb[i] = sw[1];
        }
        asm volatile("" :: "v"(acc[0]));
        __builtin_amdgcn_raw_buffer_store_b128(pa, ro, bo, 0, OUT_AUX);
        __builtin_amdgcn_raw_buffer_store_b128(pb, ro, bo, 65536, OUT_AUX);
    }
    DG_END(8);
}

extern "C" void kernel_launch(void* const* d_in, const int* in_sizes, int n_in,
                              void* d_out, int out_size, void* d_ws, size_t ws_size,
                              hipStream_t stream) {
    const void* A_q = d_in[0];
    const void* A_k = d_in[1];
    const void* A_v = d_in[2];
    const void* A_shift = d_in[3];
    const void* A_ssmk = d_in[4];
    const void* A_D = d_in[5];
    const void* B_x = d_in[6];
    const void* B_wqkv = d_in[7];
    const void* B_wo = d_in[8];
    char* ws = (char*)d_ws;
    k1_qkv<<<256, 512, 0, stream>>>((const char*)B_x, (const char*)B_wqkv, ws);
    k2_attn<<<256, 768, 0, stream>>>(ws, (const char*)A_q, (const char*)A_k, (const char*)A_v, (const char*)A_shift,
                                     (const char*)A_ssmk, (const unsigned*)A_D, (const char*)B_wo, (char*)d_out);
}
```

```cpp
#include <hip/hip_runtime.h>
#include <hip/hip_bf16.h>
#include <stdint.h>
#include <utility>

typedef short bf16x8 __attribute__((ext_vector_type(8)));
typedef short s16x4 __attribute__((ext_vector_type(4)));
typedef float f32x16 __attribute__((ext_vector_type(16)));
typedef float f32x4 __attribute__((ext_vector_type(4)));
typedef float f32x2 __attribute__((ext_vector_type(2)));
typedef unsigned u32x4 __attribute__((ext_vector_type(4)));
typedef unsigned u32x2 __attribute__((ext_vector_type(2)));
typedef __bf16 bf16x2_t __attribute__((ext_vector_type(2)));

#define LAS __attribute__((address_space(3)))
typedef LAS char* lds_ptr;
#define DI __device__ __forceinline__
#define SBAR() __builtin_amdgcn_sched_barrier(0)
#define MFMA32(a, b, c) __builtin_amdgcn_mfma_f32_32x32x16_bf16((a), (b), (c), 0, 0, 0)
#define MFMA16(a, b, c) __builtin_amdgcn_mfma_f32_16x16x32_bf16((a), (b), (c), 0, 0, 0)
#define WAIT_BAR(N) asm volatile("s_waitcnt vmcnt(" #N ") lgkmcnt(0)\n\ts_barrier" ::: "memory")
#define LGKM_BAR() asm volatile("s_waitcnt lgkmcnt(0)\n\ts_barrier" ::: "memory")

#define DG_DECL(K)
#define DG(i)
#define DG_END(i)
#define DGC(i)

constexpr int S_LEN = 2048, DM = 1024, NH = 32;

#define KQ 1
#define OQ 0
#define QT(h, T) (KQ ? (h) * 64 + (T) : (T) * 32 + (h))
constexpr size_t WS_Q = 0;
constexpr size_t WS_KV = 4u << 20;
constexpr size_t WS_O = OQ ? WS_Q : 12u << 20;
constexpr size_t WS_KVR = 16u << 20;
#define ZK 0
constexpr size_t WS_ZT = ZK ? WS_KVR : 20u << 20;
constexpr size_t WS_CNT = 24u << 20;

template <int... I, class F> DI void sfor(std::integer_sequence<int, I...>, F&& f) { (f(std::integral_constant<int, I>{}), ...); }
DI unsigned cvtpk(float lo, float hi) {
    f32x2 v = {lo, hi};
    return __builtin_bit_cast(unsigned, __builtin_convertvector(v, bf16x2_t));
}
DI float bflo(unsigned u) { return __uint_as_float(u << 16); }
DI float bfhi(unsigned u) { return __uint_as_float(u & 0xffff0000u); }

DI void glds16(unsigned voff, const void* sbase, unsigned lds_dst) {
    unsigned sv;
    asm volatile("s_mov_b32 %0, m0\n\ts_mov_b32 m0, %3\n\ts_nop 0\n\tglobal_load_lds_dwordx4 %1, %2\n\ts_mov_b32 m0, %0"
                 : "=&s"(sv) : "v"(voff), "s"(sbase), "s"(lds_dst) : "memory");
}
template <int NT> DI void glds16p(unsigned voff, const void* sbase, unsigned lds_dst) {
    if constexpr (NT) {
        unsigned sv;
        asm volatile("s_mov_b32 %0, m0\n\ts_mov_b32 m0, %3\n\ts_nop 0\n\tglobal_load_lds_dwordx4 %1, %2 nt\n\ts_mov_b32 m0, %0"
                     : "=&s"(sv) : "v"(voff), "s"(sbase), "s"(lds_dst) : "memory");
    } else glds16(voff, sbase, lds_dst);
}
#define K1_XNT 0
#define K1_WNT 0
#define K2_SNT 1
#define K2_QNT 0
#define K2_WNT 0
#define OUT_AUX 16
#define K1_SAUX 16
DI bf16x8 lds_rd128(lds_ptr p) { return *(const LAS bf16x8*)p; }
DI s16x4 lds_tr(lds_ptr p) {
    return __builtin_bit_cast(s16x4, __builtin_amdgcn_ds_read_tr16_b64_v4i16((LAS s16x4*)p));
}
DI bf16x8 cat8(s16x4 a, s16x4 b) { return __builtin_shufflevector(a, b, 0, 1, 2, 3, 4, 5, 6, 7); }
DI u32x4 pack8(const f32x16& x, int s, float sc) {
    u32x4 p;
    p[0] = cvtpk(x[8 * s + 0] * sc, x[8 * s + 1] * sc);
    p[1] = cvtpk(x[8 * s + 2] * sc, x[8 * s + 3] * sc);
    p[2] = cvtpk(x[8 * s + 4] * sc, x[8 * s + 5] * sc);
    p[3] = cvtpk(x[8 * s + 6] * sc, x[8 * s + 7] * sc);
    return p;
}
DI u32x4 pack8n(const f32x16& x, int s) {
    u32x4 p;
    p[0] = cvtpk(x[8 * s + 0], x[8 * s + 1]);
    p[1] = cvtpk(x[8 * s + 2], x[8 * s + 3]);
    p[2] = cvtpk(x[8 * s + 4], x[8 * s + 5]);
    p[3] = cvtpk(x[8 * s + 6], x[8 * s + 7]);
    return p;
}
DI void rnd2(float& a, float& b) { unsigned u = cvtpk(a, b); a = bflo(u); b = bfhi(u); }

#define CONV_PACE()
#define RAW_BAR() asm volatile("s_barrier" ::: "memory")
DI unsigned gld32(unsigned voff, const void* sbase) {
    unsigned r;
    asm volatile("global_load_dword %0, %1, %2" : "=v"(r) : "v"(voff), "s"(sbase) : "memory");
    return r;
}
DI u32x2 gld64(unsigned voff, const void* sbase) {
    u32x2 r;
    asm volatile("global_load_dwordx2 %0, %1, %2" : "=v"(r) : "v"(voff), "s"(sbase) : "memory");
    return r;
}
DI float mul_sv(float s, float v) { float r; asm("v_mul_f32_e32 %0, %1, %2" : "=v"(r) : "s"(s), "v"(v)); return r; }
DI float mul_vv(float a, float b) { float r; asm("v_mul_f32_e32 %0, %1, %2" : "=v"(r) : "v"(a), "v"(b)); return r; }
DI float add_vv(float a, float b) { float r; asm("v_add_f32_e32 %0, %1, %2" : "=v"(r) : "v"(a), "v"(b)); return r; }
DI void acc_vv(float& a, float b) { asm("v_add_f32_e32 %0, %0, %1" : "+v"(a) : "v"(b)); }
DI void fmac_vv(float& acc, float a, float b) { asm("v_fmac_f32_e32 %0, %1, %2" : "+v"(acc) : "v"(a), "v"(b)); }
DI void fmac_sv(float& acc, float s, float v) { asm("v_fmac_f32_e32 %0, %1, %2" : "+v"(acc) : "s"(s), "v"(v)); }
typedef __attribute__((address_space(1))) unsigned gu32;
#define RLX_AGENT __ATOMIC_RELAXED, __HIP_MEMORY_SCOPE_AGENT
#define CFENCE() asm volatile("" ::: "memory")
#define DRAIN() asm volatile("s_waitcnt vmcnt(0)" ::: "memory")
constexpr int AUX_SC1 = 16;
DI u32x4 ld_sc1(__amdgpu_buffer_rsrc_t rs, unsigned off) { return __builtin_amdgcn_raw_buffer_load_b128(rs, (int)off, 0, AUX_SC1); }
DI u32x4 ld_sc1s(__amdgpu_buffer_rsrc_t rs, unsigned off, int soff) { return __builtin_amdgcn_raw_buffer_load_b128(rs, (int)off, soff, AUX_SC1); }
DI void st_sc1(u32x4 v, __amdgpu_buffer_rsrc_t rs, unsigned off) { __builtin_amdgcn_raw_buffer_store_b128(v, rs, (int)off, 0, AUX_SC1); }
constexpr unsigned EMPTY = 0xffffffffu;
DI unsigned nz(unsigned x) { return x < EMPTY - 1u ? x : EMPTY - 1u; }
DI u32x4 nz4(u32x4 v) { return u32x4{nz(v[0]), nz(v[1]), nz(v[2]), nz(v[3])}; }
DI float fmx(float a, float b) { return __builtin_elementwise_maximum(a, b); }
DI unsigned umax(unsigned a, unsigned b) { return a > b ? a : b; }
DI unsigned umax3(unsigned a, unsigned b, unsigned c) { return umax(umax(a, b), c); }
DI unsigned mx4(unsigned m, u32x4 v) { return umax3(umax3(m, v[0], v[1]), v[2], v[3]); }
DI unsigned mx8(u32x4 a, u32x4 b) { return umax(umax3(umax3(umax3(a[0], a[1], a[2]), a[3], b[0]), b[1], b[2]), b[3]); }
DI bool missing(unsigned m) { return __builtin_amdgcn_ballot_w64(m == EMPTY) != 0; }
DI unsigned lds_add_rtn(lds_ptr p, int lane) {
    unsigned old = 0;
    CFENCE();
    if (lane == 0) old = __hip_atomic_fetch_add((LAS unsigned*)p, 1u, __ATOMIC_RELAXED, __HIP_MEMORY_SCOPE_WORKGROUP);
    old = __builtin_amdgcn_readfirstlane(old);
    CFENCE();
    return old;
}
DI void lds_add(lds_ptr p, int lane) {
    unsigned long long sv;
    asm volatile("s_mov_b64 %0, exec\n\ts_mov_b64 exec, 1\n\tds_add_u32 %1, %2\n\ts_mov_b64 exec, %0"
                 : "=&s"(sv) : "v"(p), "v"(1u) : "memory");
}
DI void lds_wait_ge(lds_ptr p, unsigned v) {
    CFENCE();
    while ((unsigned)__builtin_amdgcn_readfirstlane(*(volatile LAS unsigned*)p) < v) __builtin_amdgcn_s_sleep(1);
    CFENCE();
}
DI void poll_ge(gu32* word, unsigned want) {
    CFENCE();
    for (unsigned spins = 0; spins < (1u << 22); ++spins) {
        if ((unsigned)__builtin_amdgcn_readfirstlane(__hip_atomic_load(word, RLX_AGENT)) >= want) break;
        __builtin_amdgcn_s_sleep(1);
    }
    __builtin_amdgcn_fence(__ATOMIC_ACQUIRE, "wavefront");
    CFENCE();
}
#define PB(x) __builtin_bit_cast(bf16x8, x)
#define PRIO(x) do { __builtin_amdgcn_s_setprio(x); SBAR(); } while (0)
DI u32x4 gld128(unsigned voff, const void* sbase) {
    u32x4 r;
    asm volatile("global_load_dwordx4 %0, %1, %2" : "=v"(r) : "v"(voff), "s"(sbase) : "memory");
    return r;
}

#define K1_UNR 15
#define K1_LA 5
#define K1_ROT ((tb & 3) * 4)
#define K1_PF 0
#define K1_PG 0
#define K1_TW 0
#define PG1_AT 0
#define PG2_AT 2
#define PG3_AT 6
#define K1_WB(n) WAIT_BAR(0)
#define PF1_AT -1
#define PF1_LO 4
#define PF1_N 4
#define PF2_AT 1
#define PF2_LO 8
#define PF2_N 4
#define PF3_AT 5
#define PF3_LO 12
#define PF3_N 4
#define K1_KR ((kt + rot) & 15)
#define K1_ST(v, rs, off) __builtin_amdgcn_raw_buffer_store_b128(v, rs, (int)(off), 0, K1_SAUX)
constexpr int K1_STAGE = 40960, K1U = K1_UNR;
__global__ __launch_bounds__(512, 2) void k1_qkv(const char* __restrict__ X, const char* __restrict__ W,
                                                 char* __restrict__ ws) {
    __shared__ __attribute__((aligned(16))) char lds_raw[4 * K1_STAGE];
    const lds_ptr lds = (lds_ptr)lds_raw;
    const unsigned lds0 = (unsigned)(uintptr_t)lds;
    const int tid = threadIdx.x, lane = tid & 63;
    const int w = __builtin_amdgcn_readfirstlane(tid >> 6);
    const int b = blockIdx.x, xcd = b & 7, idx = b >> 3;
    const int hp = (xcd >> 1) * 4 + (idx & 3), tb = (xcd & 1) * 8 + (idx >> 2);
    DG_DECL(0)

    const int lw = w & 3;
    const int rot = K1_ROT;
    unsigned voffX[4], voffW[2];
#pragma unroll
    for (int pp = 0; pp < 4; ++pp) {
        const int row = 32 * lw + 8 * pp + (lane >> 3);
        const int ch = (lane & 7) ^ ((row >> 1) & 7);
        voffX[pp] = (unsigned)(tb * 128 + row) * 2048u + ch * 16;
    }
#pragma unroll
    for (int pp = 0; pp < 2; ++pp) {
        const int krow = 16 * lw + 8 * pp + (lane >> 3);
        const int ch = (lane & 7) ^ (((lane >> 4) & 1) << 2);
        voffW[pp] = (unsigned)krow * 6144u + ch * 16;
    }
    auto issue = [&](int kt, int lo, int hi) {
        const int kr = K1_KR;
        const char* xb = X + kr * 128;
        const char* wb = W + (size_t)kr * (64 * 6144) + hp * 128;
        const unsigned st = lds0 + (kt & 3) * K1_STAGE;
#pragma unroll
        for (int pp = 0; pp < 4; ++pp)
            if (pp >= lo && pp < hi) glds16p<K1_XNT>(voffX[pp], xb, st + (4 * lw + pp) * 1024);
#pragma unroll
        for (int sec = 0; sec < 3; ++sec)
#pragma unroll
            for (int pp = 0; pp < 2; ++pp)
                if (4 + 2 * sec + pp >= lo && 4 + 2 * sec + pp < hi)
                    glds16p<K1_WNT>(voffW[pp], wb + sec * 2048, st + 16384 + sec * 8192 + (2 * lw + pp) * 1024);
    };
    DG(13);
    if (w < 4) { issue(0, 0, 5); issue(1, 0, 5); }
    else { issue(0, 5, 10); issue(1, 5, 10); }
    DG(14);
    int w2 = w;
    asm volatile("" : "+s"(w2));

    if (w2 >= 4) {
#define WAITN_BAR(N) asm volatile("s_waitcnt vmcnt(%0) lgkmcnt(0)\n\ts_barrier" :: "n"(N) : "memory")
        issue(2, 0, K1_LA);
        asm volatile("s_waitcnt vmcnt(%0)" :: "n"(5 + K1_LA) : "memory");
#pragma unroll 1
        for (int kt = 0; kt < 13; ++kt) {
            WAITN_BAR(10 + K1_LA);
            issue(kt + 2, K1_LA, 10);
            issue(kt + 3, 0, K1_LA);
        }
        WAITN_BAR(10 + K1_LA);
        issue(15, K1_LA, 10);
        WAIT_BAR(10);
        WAIT_BAR(0);
        if (lw == 0 && lane < 2) *(unsigned*)(ws + WS_CNT + b * 256 + lane * 128) = 0u;
        DG_END(5);
        return;
    }

    const int wm = w >> 1, wn = w & 1;
    const int r = lane & 31, hh = lane >> 5;
    const int g = lane >> 4, q = (lane & 15) >> 2, p = lane & 3;
    const int xoff0 = (64 * wm + r) * 128 + ((hh ^ ((r >> 1) & 7)) << 4);
    const int woff0 = 16384 + (8 * hh + q) * 128 + (((4 * wn + 2 * (g & 1) + (p >> 1)) ^ ((q >> 1) << 2)) << 4) + (p & 1) * 8;

    f32x16 acc[2][3];
#pragma unroll
    for (int m = 0; m < 2; ++m)
#pragma unroll
        for (int s = 0; s < 3; ++s) acc[m][s] = f32x16{};
    bf16x8 xf[2][2];
    s16x4 wl[2][3], wh[2][3];
#define K1_WF(f, sec) cat8(wl[f][sec], wh[f][sec])
#define K1_STEP(F, st, t, BAR_HERE) do { \
        const lds_ptr xp_ = (st) + (xoff0 ^ ((t) << 5)); const lds_ptr wp_ = (st) + woff0 + (t) * 2048; \
        acc[0][0] = MFMA32(K1_WF(F, 0), xf[F][0], acc[0][0]); SBAR(); \
        if (BAR_HERE) { K1_WB(BAR_HERE); SBAR(); } \
        xf[F ^ 1][0] = lds_rd128(xp_); wl[F ^ 1][0] = lds_tr(wp_); wh[F ^ 1][0] = lds_tr(wp_ + 512); SBAR(); \
        acc[0][1] = MFMA32(K1_WF(F, 1), xf[F][0], acc[0][1]); SBAR(); \
        wl[F ^ 1][1] = lds_tr(wp_ + 8192); wh[F ^ 1][1] = lds_tr(wp_ + 8192 + 512); SBAR(); \
        acc[0][2] = MFMA32(xf[F][0], K1_WF(F, 2), acc[0][2]); SBAR(); \
        wl[F ^ 1][2] = lds_tr(wp_ + 16384); wh[F ^ 1][2] = lds_tr(wp_ + 16384 + 512); xf[F ^ 1][1] = lds_rd128(xp_ + 4096); SBAR(); \
        acc[1][0] = MFMA32(K1_WF(F, 0), xf[F][1], acc[1][0]); SBAR(); \
        acc[1][1] = MFMA32(K1_WF(F, 1), xf[F][1], acc[1][1]); \
        acc[1][2] = MFMA32(xf[F][1], K1_WF(F, 2), acc[1][2]); SBAR(); \
    } while (0)

    WAIT_BAR(5);
    DG(3);
    {
        const lds_ptr xp_ = lds + xoff0; const lds_ptr wp_ = lds + woff0;
        xf[0][0] = lds_rd128(xp_); xf[0][1] = lds_rd128(xp_ + 4096);
#pragma unroll
        for (int sec = 0; sec < 3; ++sec) { wl[0][sec] = lds_tr(wp_ + sec * 8192); wh[0][sec] = lds_tr(wp_ + sec * 8192 + 512); }
    }
    SBAR();
#pragma unroll K1U
    for (int kt = 0; kt < 15; ++kt) {
        const lds_ptr st = lds + (kt & 3) * K1_STAGE;
        const lds_ptr sn = lds + ((kt + 1) & 3) * K1_STAGE;
        K1_STEP(0, st, 1, false);
        K1_STEP(1, st, 2, false);
        K1_STEP(0, st, 3, false);
        K1_STEP(1, sn, 0, kt + 1);
    }
    {
        const lds_ptr st = lds + 3 * K1_STAGE;
        K1_STEP(0, st, 1, false);
        K1_STEP(1, st, 2, false);
        K1_STEP(0, st, 3, false);
#pragma unroll
        for (int m = 0; m < 2; ++m) {
            acc[m][1] = MFMA32(K1_WF(1, 1), xf[1][m], acc[m][1]);
            acc[m][2] = MFMA32(xf[1][m], K1_WF(1, 2), acc[m][2]);
            acc[m][0] = MFMA32(K1_WF(1, 0), xf[1][m], acc[m][0]);
        }
    }
    DG(4);
    {
        const int h = 2 * hp + wn;
        const float qscale = 0.17677669529663687f * 1.4426950408889634f;
        const __amdgpu_buffer_rsrc_t rs = __builtin_amdgcn_make_buffer_rsrc(ws, 0, 32 << 20, 0x00020000);
#pragma unroll
        for (int m = 0; m < 2; ++m) {
            const int T = tb * 4 + wm * 2 + m;
            const unsigned dq = (unsigned)WS_Q + QT(h, T) * 2048 + lane * 16;
            const unsigned dk = (unsigned)WS_KV + (h * 64 + T) * 4096 + lane * 16;
            K1_ST(pack8n(acc[m][1], 0), rs, dk);
            K1_ST(pack8n(acc[m][1], 1), rs, dk + 1024);
            K1_ST(pack8n(acc[m][2], 0), rs, dk + 2048);
            K1_ST(pack8n(acc[m][2], 1), rs, dk + 3072);
            K1_ST(pack8(acc[m][0], 0, qscale), rs, dq);
            K1_ST(pack8(acc[m][0], 1, qscale), rs, dq + 1024);
        }
    }
    DG_END(5);
}

constexpr int K2_SLOT = 32768;
constexpr int K2_SF = 65536;
constexpr int K2_CV = 98304;
constexpr int CV_HA = 0, CV_HB = 4160, CV_KV = 8320, CV_BYTES = 8320 + 480 + 4096;
constexpr int K2_LX = K2_CV + 4 * CV_BYTES;
constexpr int K2_FL = K2_LX + 2048;
constexpr int K2_SCR = K2_FL + 256;
constexpr int K2_PAD = K2_SCR + 4096;
constexpr int K2_LDS = K2_PAD + 1024;
#define NB8 0
#define WW 0
constexpr int F_X = 0, F_R = 32, F_W2 = 64, C_OST = 68, C_S1 = 76, C_S2 = 80, C_S3 = 128, C_ZST = 88, F_ZGO = 92,
              C_KST = 96, C_U0 = 100, C_LD = 108, F_KGO = 116, C_END = 120, C_OL = 72, C_HF = 84;
#define NG_IN 0
#define GATE_WORD C_END
#define GATE_VAL 0
#define X_NZ 4
#define X_NW3 8
#define ZP_GATE 4u
#define ZL_GATE 4
#define ZL_AT 31
#define Z_SLEEP0 0
#define O_SLEEP 0
#define Z_SLEEP 8
#define OWIN 16
#define OW0 8
#define WD 2
#define W3_AT 10
#define X_NOLDW 0
#define X_NOLDO 0
#define X_NOCHK 0
#define KROT (nt * 2)
#define NG_LAST 0
#define PACE_IN "s_nop 7"
#define PACE_OUT "s_nop 1"
#define P3_PRIO 1
#define TOP_SLEEP 2
#define A_AUX 16
#define B_AUX 16
#define B_LDS 0
#define TL 8
#define XR_AT 62
#define W3_AUX 0
#define W3M 13
#define A16 1
#define SEL4 2
#define KV_DOT 1
#define ZE_PRIO 3
#define P3_T1 99
#define P3_T2 99
#define AUX_PRIO 2

__global__ __launch_bounds__(768, 3) void k2_attn(char* __restrict__ ws, const char* __restrict__ Aq,
                                                  const char* __restrict__ Ak, const char* __restrict__ Av,
                                                  const char* __restrict__ shiftk, const char* __restrict__ ssmk,
                                                  const unsigned* __restrict__ ssmD, const char* __restrict__ Wo,
                                                  char* __restrict__ out) {
    __shared__ __attribute__((aligned(16))) char lds_raw[K2_LDS];
    const lds_ptr lds = (lds_ptr)lds_raw;
    const lds_ptr fl = lds + K2_FL;
    const unsigned lds0 = (unsigned)(uintptr_t)lds;
    const int tid = threadIdx.x, lane = tid & 63;
    const int w = __builtin_amdgcn_readfirstlane(tid >> 6);
    const int b = blockIdx.x, xcd = b & 7, idx = b >> 3;
    const int h = xcd * 4 + (idx >> 3), qb = idx & 7;
    const int tt = xcd * 2 + (idx >> 4), nt = idx & 15;
    const char* kvg = ws + WS_KV + (size_t)h * 64 * 4096;
    const __amdgpu_buffer_rsrc_t rs = __builtin_amdgcn_make_buffer_rsrc(ws, 0, 32 << 20, 0x00020000);
    const __amdgpu_buffer_rsrc_t ro = __builtin_amdgcn_make_buffer_rsrc(out, 0, 8 << 20, 0x00020000);
    gu32* const cnt = (gu32*)(ws + WS_CNT);
    auto signal = [&]() {
        (void)__hip_atomic_fetch_add(cnt + ((lane & 31) * 8 + (lane < 32 ? xcd : qb)) * 64, 1u, RLX_AGENT);
    };
    const unsigned wvoff = (unsigned)(lane >> 3) * 2048u + nt * 128 + (((lane & 7) ^ (((lane >> 4) & 1) << 2)) << 4);
    const int krot = KROT;
    auto wsrc = [&](int hf, int lp) { return Wo + (size_t)(hf * 64 + ((lp + krot * 2) & 63)) * 16384; };
    DG_DECL(1)
    const unsigned voff = lane * 16;
    const int cw = w & 3;
    auto issue = [&](int st) {
        const char* src = kvg + (st * 4 + cw) * 4096;
        const unsigned dst = lds0 + (st & 1) * K2_SLOT + cw * 4096;
#pragma unroll
        for (int i = 0; i < 4; ++i) glds16(voff, src + i * 1024, dst + i * 1024);
#pragma unroll
        for (int i = 0; i < 4; ++i) glds16(voff, src + 131072 + i * 1024, dst + 16384 + i * 1024);
    };
    if (w >= 8) {
        __builtin_amdgcn_s_sleep(TOP_SLEEP);
#pragma unroll
        for (int i = 2; i < 8; ++i)
            glds16(voff, kvg + (i & 1) * 131072 + (i >> 1) * 4096 + cw * 1024, lds0 + (i & 1) * 16384 + (i >> 1) * 4096 + cw * 1024);
        RAW_BAR();
        issue(1);
        WAIT_BAR(8);
    } else {
        const int tq = cw * 2 + (w >> 2);
        const char* qg = ws + WS_Q + (size_t)QT(h, qb * 8 + tq) * 2048;
        glds16(voff, kvg + (w >> 2) * 131072 + cw * 1024, lds0 + (w >> 2) * 16384 + cw * 1024);
        glds16(voff, qg, lds0 + K2_SF + tq * 2048);
        glds16(voff, qg + 1024, lds0 + K2_SF + tq * 2048 + 1024);
    }
    int w2 = w;
    asm volatile("" : "+s"(w2));

    if (w2 >= 8) {
        __builtin_amdgcn_s_setprio(3);
        const int ch = xcd * 128 + idx * 4 + cw;
        const lds_ptr cv = lds + K2_CV + cw * CV_BYTES;
        {
            const u32x4 z4 = {0u, 0u, 0u, 0u};
            if (lane < 2) *(LAS u32x4*)(cv + CV_HA + lane * 16) = z4;
            if (lane < 30) *(LAS u32x4*)(cv + CV_KV + lane * 16) = z4;
            if (cw == 0 && lane < 16) *(LAS u32x4*)(fl + lane * 16) = z4;
        }
        const int c0 = xcd * 128 + cw * 32, cq = lane & 7, rg = lane >> 3;
        const int row0 = idx * 64 + rg * 8;
        const unsigned roff = (unsigned)row0 * 2048u + (c0 + cq * 4) * 2;
        const unsigned toffs = (unsigned)(c0 + (lane >> 3)) * 4096u + idx * 128;
        u32x4 sk[2];
        sk[0] = gld128((unsigned)(c0 + cq * 4) * 8u, shiftk); sk[1] = gld128((unsigned)(c0 + cq * 4) * 8u, shiftk + 16);
        unsigned dd = gld32((unsigned)(ch >> 1) * 4u, ssmD);
        auto stage = [&](const char* src, int dst) {
            const unsigned sbase = (unsigned)(idx * 64 + cw * 16 + (lane >> 4)) * 2048u + xcd * 256;
#pragma unroll
            for (int i = 0; i < 4; ++i)
                glds16p<K2_SNT>(sbase + (((lane & 15) ^ (((2 * cw + (i >> 1)) & 3) << 2)) << 4), src + i * 8192, lds0 + dst + cw * 4096 + i * 1024);
        };
        auto blank = [&](unsigned off) {
            const u32x4 e4 = {EMPTY, EMPTY, EMPTY, EMPTY};
            CFENCE();
            st_sc1(e4, rs, off + voff); st_sc1(e4, rs, off + 1024 + voff);
            CFENCE();
        };
        WAIT_BAR(3);
        DG(12);
        issue(2);
        stage(Ak, K2_SF);
        glds16((unsigned)(idx * 64 - (idx ? 4 : 0) + (lane >> 4)) * 2048u + xcd * 256 + (((lane & 15) ^ 12) << 4), Ak, lds0 + K2_SCR + cw * 1024);
        blank((unsigned)WS_O + (unsigned)((qb * 8 + cw * 2) * 32 + h) * 2048u);
        auto warm = [&](int g) {
#pragma unroll
            for (int i = 0; i < 4; ++i) glds16(wvoff, wsrc(idx >> 4, 16 + g * 16 + cw * 4 + i), lds0 + K2_PAD);
        };
        WAIT_BAR(7);
        DG(9);
        issue(3);
        stage(Av, K2_SF + 16384);
        blank((unsigned)WS_O + (unsigned)((qb * 8 + cw * 2 + 1) * 32 + h) * 2048u);
        WAIT_BAR(6);
        DG(3);
        issue(4);
#pragma unroll
        for (int i = 0; i < 4; ++i) glds16p<K2_SNT>(voff, ssmk + (size_t)ch * 4096 + i * 1024, lds0 + K2_CV + cw * CV_BYTES + CV_HA + 32 + i * 1024);
        blank((unsigned)WS_ZT + ch * 4096); blank((unsigned)WS_ZT + ch * 4096 + 2048);
        WAIT_BAR(8);
        DG(5);
        issue(5);
        SBAR();
        asm volatile("" : "+v"(sk[0]), "+v"(sk[1]), "+v"(dd));
        dd = __builtin_amdgcn_readfirstlane(dd);
        {
            u32x2 kr[11], vr[8];
            lds_ptr kb = lds + K2_SF + rg * 2048 + ((cw ^ (rg & 3)) << 6) + cq * 8;
            asm volatile("" : "+v"(kb));
            const lds_ptr hb = lds + (rg ? K2_SF + rg * 2048 - 768 : K2_SCR + cw * 1024 + 256) + ((cw ^ ((rg + 3) & 3)) << 6) + cq * 8;
#pragma unroll
            for (int i = 0; i < 3; ++i) kr[i] = *(const LAS u32x2*)(hb + i * 256);
#pragma unroll
            for (int i = 0; i < 8; ++i) kr[3 + i] = *(const LAS u32x2*)(kb + i * 256);
#pragma unroll
            for (int i = 0; i < 8; ++i) vr[i] = *(const LAS u32x2*)(kb + 16384 + i * 256);
            if (row0 == 0) { kr[0] = u32x2{0u, 0u}; kr[1] = u32x2{0u, 0u}; kr[2] = u32x2{0u, 0u}; }
            const lds_ptr tb = cv + CV_KV + 480;
#pragma unroll
            for (int c = 0; c < 4; ++c) {
                const unsigned s01 = sk[c >> 1][(c & 1) * 2], s23 = sk[c >> 1][(c & 1) * 2 + 1];
                unsigned kp[10];
                float kv[8], vf[8];
#pragma unroll
                for (int j = 0; j < 10; ++j) kp[j] = __builtin_amdgcn_perm(kr[j][c >> 1], kr[j + 1][c >> 1], (c & 1) ? 0x07060302u : 0x05040100u);
                SBAR();
#pragma unroll
                for (int i = 0; i < 8; ++i) asm("v_dot2_f32_bf16 %0, %1, %2, 0" : "=v"(kv[i]) : "v"(kp[i]), "v"(s23));
                SBAR();
#pragma unroll
                for (int i = 0; i < 8; ++i) asm("v_dot2_f32_bf16 %0, %1, %2, %0" : "+v"(kv[i]) : "v"(kp[i + 2]), "v"(s01));
                SBAR();
#pragma unroll
                for (int i = 0; i < 8; ++i) vf[i] = (c & 1) ? bfhi(vr[i][c >> 1]) : bflo(vr[i][c >> 1]);
                SBAR();
#pragma unroll
                for (int i = 0; i < 8; ++i) kv[i] = mul_vv(kv[i], vf[i]);
                u32x4 o4;
#pragma unroll
                for (int i = 0; i < 4; ++i) o4[i] = cvtpk(kv[7 - 2 * i], kv[6 - 2 * i]);
                *(LAS u32x4*)(tb + (cq * 4 + c) * 128 + ((rg ^ cq) << 4)) = o4;
            }
#pragma unroll
            for (int i = 0; i < 4; ++i) {
                const u32x4 t = *(const LAS u32x4*)(tb + i * 1024 + lane * 16);
                st_sc1(t, rs, (unsigned)WS_Q + (xcd * 32 + idx) * 16384 + cw * 4096 + i * 1024 + (lane >> 3) * 128 + (((lane & 7) ^ (2 * i + (lane >> 5))) << 4));
            }
        }
        CFENCE();
        DG(7);
        WAIT_BAR(4);
        DG(6);
        issue(6);
        asm volatile("s_waitcnt vmcnt(8)" ::: "memory");
        if (lds_add_rtn(fl + C_KST, lane) == 3u) signal();
#pragma unroll
        for (int i = 0; i < 8; ++i)
            glds16p<K2_WNT>(wvoff, wsrc(i >> 2, cw * 4 + (i & 3)), lds0 + K2_SF + (i >> 2) * 16384 + (cw * 4 + (i & 3)) * 1024);
        {
            const u32x4 z4 = {0u, 0u, 0u, 0u};
            if (lane < 2) *(LAS u32x4*)(cv + CV_HB + lane * 16) = z4;
#pragma unroll
            for (int i = 0; i < 4; ++i) {
                const int off = 32 + i * 1024 + lane * 16;
                const u32x4 hv = *(const LAS u32x4*)(cv + CV_HA + off);
                const unsigned pv = *(const LAS unsigned*)(cv + CV_HA + off - 4);
                u32x4 sh;
                sh[0] = __builtin_amdgcn_alignbit(hv[0], pv, 16);
                sh[1] = __builtin_amdgcn_alignbit(hv[1], hv[0], 16);
                sh[2] = __builtin_amdgcn_alignbit(hv[2], hv[1], 16);
                sh[3] = __builtin_amdgcn_alignbit(hv[3], hv[2], 16);
                *(LAS u32x4*)(cv + CV_HB + off) = sh;
            }
        }
        WAIT_BAR(8);
        DG(8);
        issue(7);
#pragma unroll
        for (int i = 0; i < 8; ++i)
            glds16p<K2_WNT>(wvoff, wsrc(idx >> 4, 32 + cw * 8 + i), lds0 + K2_SCR + cw * 1024);
        WAIT_BAR(8);
        DG(11);
        if (cw == 0) { poll_ge(cnt + b * 64, 64u); lds_add(fl + F_KGO, lane); }
        else lds_wait_ge(fl + F_KGO, 1u);
        {
            u32x4 t[4];
#pragma unroll
            for (int i = 0; i < 4; ++i) t[i] = ld_sc1s(rs, (unsigned)WS_Q + (xcd * 32 + (lane >> 3)) * 16384 + (idx * 4 + cw) * 128 + (lane & 7) * 16, i * 131072);
#pragma unroll
            for (int i = 0; i < 4; ++i) *(LAS u32x4*)(cv + CV_KV + 480 + i * 1024 + lane * 16) = t[i];
        }
        const int r1 = lane & 15, q = lane >> 4;
        const int toff = (r1 & 1) ? CV_HA + 2 * (16 + r1 + 9 - 8 * q) : CV_HB + 2 * (16 + r1 + 10 - 8 * q);
        const int boff = CV_KV + 448 + 32 * r1 + 16 * q;
        f32x4 y[8];
        u32x4 tf[8];
        bf16x8 bf[8];
        const lds_ptr bp = cv + boff - 448;
        lds_ptr tp[4];
#pragma unroll
        for (int i = 0; i < 4; ++i) { tp[i] = cv + toff + 1024 * i; asm volatile("" : "+v"(tp[i])); }
#define LDT(bb, a, hf) do { const LAS unsigned* t_ = (const LAS unsigned*)(tp[(a) >> 1] + 64 * (bb) + 512 * ((a) & 1) + 8 * (hf)); \
            tf[a][2 * (hf)] = t_[0]; tf[a][2 * (hf) + 1] = t_[1]; SBAR(); } while (0)
#define LDB(bb, a) do { bf[a] = lds_rd128(bp + 448 - 64 * (bb) + 512 * (a)); SBAR(); } while (0)
#define CM(a, ww) do { asm volatile("v_mfma_f32_16x16x32_bf16 %0, %1, %2, %0" : "+v"(y[(a) + (ww)]) : "v"(tf[a]), "v"(bf[ww])); SBAR(); } while (0)
#define CM0(a, ww) do { asm volatile("v_mfma_f32_16x16x32_bf16 %0, %1, %2, 0" : "=&v"(y[(a) + (ww)]) : "v"(tf[a]), "v"(bf[ww])); SBAR(); } while (0)
#define PC() do { asm volatile(PACE_OUT); SBAR(); } while (0)
#pragma unroll
        for (int a = 0; a < 8; ++a) { if (a < 5) { LDT(0, a, 0); LDT(0, a, 1); } LDB(0, 7 - a); }
#pragma unroll
        for (int i = 0; i < 8; ++i)
            glds16p<K2_WNT>(wvoff, wsrc(i >> 2, 16 + cw * 4 + (i & 3)), lds0 + (i >> 2) * 16384 + (cw * 4 + (i & 3)) * 1024);
#pragma unroll
        for (int i = 0; i < 4; ++i)
            glds16p<K2_QNT>((unsigned)(idx * 64 + cw * 16 + (lane >> 4)) * 2048u + xcd * 256 + (lane & 15) * 16, Aq + i * 8192, lds0 + K2_SCR + cw * 1024);
        RAW_BAR();
        asm volatile("s_waitcnt vmcnt(4)" ::: "memory");
        lds_add(fl + C_S1, lane);
#define ZFL() PC()
        __builtin_amdgcn_s_setprio(AUX_PRIO);
        const float dch = (ch & 1) ? bfhi(dd) : bflo(dd);
        const lds_ptr kdp = cv + CV_KV + 480 + 32 * r1 + 16 * (q >> 1) + ((q & 1) ? 0 : 8);
        float kd[8][4];
        u32x2 kk;
        unsigned kc0, kc1, zo[8][2];
#define KD_RD(t) do { kk = *(const LAS u32x2*)(kdp + 512 * (t)); SBAR(); } while (0)
#define KD_UP0(t) do { kd[t][0] = bfhi(kk[1]); kd[t][1] = bflo(kk[1]); SBAR(); } while (0)
#define KD_UP1(t) do { kd[t][2] = bfhi(kk[0]); kd[t][3] = bflo(kk[0]); SBAR(); } while (0)
#define KD_MU0(t) do { kd[t][0] = mul_sv(dch, kd[t][0]); kd[t][1] = mul_sv(dch, kd[t][1]); SBAR(); } while (0)
#define KD_MU1(t) do { kd[t][2] = mul_sv(dch, kd[t][2]); kd[t][3] = mul_sv(dch, kd[t][3]); SBAR(); } while (0)
#define KD_CV(t) do { kc0 = cvtpk(kd[t][0], kd[t][1]); kc1 = cvtpk(kd[t][2], kd[t][3]); SBAR(); } while (0)
#define KD_X0(t) do { kd[t][0] = bflo(kc0); kd[t][1] = bfhi(kc0); SBAR(); } while (0)
#define KD_X1(t) do { kd[t][2] = bflo(kc1); kd[t][3] = bfhi(kc1); SBAR(); } while (0)
#define ZF0(t) do { kc0 = cvtpk(y[t][0], y[t][1]); kc1 = cvtpk(y[t][2], y[t][3]); SBAR(); } while (0)
#define ZF1(t) do { y[t][0] = bflo(kc0); y[t][1] = bfhi(kc0); SBAR(); } while (0)
#define ZF2(t) do { y[t][2] = bflo(kc1); y[t][3] = bfhi(kc1); SBAR(); } while (0)
#define ZF3(t) do { y[t][0] = add_vv(y[t][0], kd[t][0]); y[t][1] = add_vv(y[t][1], kd[t][1]); SBAR(); } while (0)
#define ZF4(t) do { y[t][2] = add_vv(y[t][2], kd[t][2]); y[t][3] = add_vv(y[t][3], kd[t][3]); SBAR(); } while (0)
#define ZF5(t) do { zo[t][0] = nz(cvtpk(y[t][0], y[t][1])); zo[t][1] = nz(cvtpk(y[t][2], y[t][3])); SBAR(); } while (0)
#define ZST(pr) do { auto s0 = __builtin_amdgcn_permlane16_swap(zo[2 * (pr)][0], zo[2 * (pr) + 1][0], false, false); \
            auto s1 = __builtin_amdgcn_permlane16_swap(zo[2 * (pr)][1], zo[2 * (pr) + 1][1], false, false); \
            st_sc1(u32x4{s0[0], s1[0], s0[1], s1[1]}, rs, (unsigned)WS_ZT + ch * 4096 + (pr) * 1024 + voff); SBAR(); } while (0)
#define W3(i) do { if ((i) == 0) PC(); } while (0)
#define HF() do { lds_add(fl + C_HF, lane); SBAR(); } while (0)
        CM0(0, 7); LDB(1, 7); CM0(0, 6); LDT(0, 5, 0); CM0(0, 5); LDT(0, 5, 1); CM0(0, 4); LDT(0, 6, 0); CM0(0, 3); LDT(0, 6, 1); CM0(0, 2); LDT(0, 7, 0); CM0(0, 1); LDT(0, 7, 1); CM0(0, 0); KD_RD(0);
        CM(1, 6); LDB(1, 6); CM(1, 5); LDT(1, 0, 0); CM(1, 4); LDT(1, 0, 1); CM(1, 3); PC(); CM(1, 2); PC(); CM(1, 1); KD_UP0(0); CM(1, 0); KD_UP1(0);
        CM(2, 5); LDB(1, 5); CM(2, 4); LDT(1, 1, 0); CM(2, 3); LDT(1, 1, 1); CM(2, 2); KD_MU0(0); CM(2, 1); KD_MU1(0); CM(2, 0); KD_CV(0);
        CM(3, 4); LDB(1, 4); CM(3, 3); LDT(1, 2, 0); CM(3, 2); LDT(1, 2, 1); CM(3, 1); KD_X0(0); CM(3, 0); KD_X1(0);
        CM(4, 3); LDB(1, 3); CM(4, 2); LDT(1, 3, 0); CM(4, 1); LDT(1, 3, 1); CM(4, 0); KD_RD(1);
        CM(5, 2); LDB(1, 2); CM(5, 1); LDT(1, 4, 0); CM(5, 0); LDT(1, 4, 1);
        CM(6, 1); LDB(1, 1); CM(6, 0); PC();
        CM(7, 0); LDB(1, 0);
        CM(0, 7); LDB(2, 7); CM(0, 6); LDT(1, 5, 0); CM(0, 5); LDT(1, 5, 1); CM(0, 4); LDT(1, 6, 0); CM(0, 3); LDT(1, 6, 1); CM(0, 2); LDT(1, 7, 0); CM(0, 1); LDT(1, 7, 1); CM(0, 0); PC();
        CM(1, 6); LDB(2, 6); CM(1, 5); LDT(2, 0, 0); CM(1, 4); LDT(2, 0, 1); CM(1, 3); KD_UP0(1); CM(1, 2); KD_UP1(1); CM(1, 1); KD_MU0(1); CM(1, 0); KD_MU1(1);
        CM(2, 5); LDB(2, 5); CM(2, 4); LDT(2, 1, 0); CM(2, 3); LDT(2, 1, 1); CM(2, 2); KD_CV(1); CM(2, 1); KD_X0(1); CM(2, 0); KD_X1(1);
        CM(3, 4); LDB(2, 4); CM(3, 3); LDT(2, 2, 0); CM(3, 2); LDT(2, 2, 1); CM(3, 1); KD_RD(2); CM(3, 0); PC();
        CM(4, 3); LDB(2, 3); CM(4, 2); LDT(2, 3, 0); CM(4, 1); LDT(2, 3, 1); CM(4, 0); PC();
        CM(5, 2); LDB(2, 2); CM(5, 1); LDT(2, 4, 0); CM(5, 0); LDT(2, 4, 1);
        CM(6, 1); LDB(2, 1); CM(6, 0); KD_UP0(2);
        CM(7, 0); LDB(2, 0);
        CM(0, 7); LDB(3, 7); CM(0, 6); LDT(2, 5, 0); CM(0, 5); LDT(2, 5, 1); CM(0, 4); LDT(2, 6, 0); CM(0, 3); LDT(2, 6, 1); CM(0, 2); LDT(2, 7, 0); CM(0, 1); LDT(2, 7, 1); CM(0, 0); KD_UP1(2);
        CM(1, 6); LDB(3, 6); CM(1, 5); LDT(3, 0, 0); CM(1, 4); LDT(3, 0, 1); CM(1, 3); KD_MU0(2); CM(1, 2); KD_MU1(2); CM(1, 1); KD_CV(2); CM(1, 0); KD_X0(2);
        CM(2, 5); LDB(3, 5); CM(2, 4); LDT(3, 1, 0); CM(2, 3); LDT(3, 1, 1); CM(2, 2); KD_X1(2); CM(2, 1); KD_RD(3); CM(2, 0); PC();
        CM(3, 4); LDB(3, 4); CM(3, 3); LDT(3, 2, 0); CM(3, 2); LDT(3, 2, 1); CM(3, 1); PC(); CM(3, 0); KD_UP0(3);
        CM(4, 3); LDB(3, 3); CM(4, 2); LDT(3, 3, 0); CM(4, 1); LDT(3, 3, 1); CM(4, 0); KD_UP1(3);
        CM(5, 2); LDB(3, 2); CM(5, 1); LDT(3, 4, 0); CM(5, 0); LDT(3, 4, 1);
        CM(6, 1); LDB(3, 1); CM(6, 0); KD_MU0(3);
        CM(7, 0); LDB(3, 0);
        CM(0, 7); LDB(4, 7); CM(0, 6); LDT(3, 5, 0); CM(0, 5); LDT(3, 5, 1); CM(0, 4); LDT(3, 6, 0); CM(0, 3); LDT(3, 6, 1); CM(0, 2); LDT(3, 7, 0); CM(0, 1); LDT(3, 7, 1); CM(0, 0); KD_MU1(3);
        CM(1, 6); LDB(4, 6); CM(1, 5); LDT(4, 0, 0); CM(1, 4); LDT(4, 0, 1); CM(1, 3); KD_CV(3); CM(1, 2); KD_X0(3); CM(1, 1); KD_X1(3); CM(1, 0); KD_RD(4);
        CM(2, 5); LDB(4, 5); CM(2, 4); LDT(4, 1, 0); CM(2, 3); LDT(4, 1, 1); CM(2, 2); PC(); CM(2, 1); PC(); CM(2, 0); KD_UP0(4);
        CM(3, 4); LDB(4, 4); CM(3, 3); LDT(4, 2, 0); CM(3, 2); LDT(4, 2, 1); CM(3, 1); KD_UP1(4); CM(3, 0); KD_MU0(4);
        CM(4, 3); LDB(4, 3); CM(4, 2); LDT(4, 3, 0); CM(4, 1); LDT(4, 3, 1); CM(4, 0); KD_MU1(4);
        CM(5, 2); LDB(4, 2); CM(5, 1); LDT(4, 4, 0); CM(5, 0); LDT(4, 4, 1);
        CM(6, 1); LDB(4, 1); CM(6, 0); KD_CV(4);
        CM(7, 0); LDB(4, 0);
        CM(0, 7); LDB(5, 7); CM(0, 6); LDT(4, 5, 0); CM(0, 5); LDT(4, 5, 1); CM(0, 4); LDT(4, 6, 0); CM(0, 3); LDT(4, 6, 1); CM(0, 2); LDT(4, 7, 0); CM(0, 1); LDT(4, 7, 1); CM(0, 0); KD_X0(4);
        CM(1, 6); LDB(5, 6); CM(1, 5); LDT(5, 0, 0); CM(1, 4); LDT(5, 0, 1); CM(1, 3); KD_X1(4); CM(1, 2); KD_RD(5); CM(1, 1); PC(); CM(1, 0); PC();
        CM(2, 5); LDB(5, 5); CM(2, 4); LDT(5, 1, 0); CM(2, 3); LDT(5, 1, 1); CM(2, 2); KD_UP0(5); CM(2, 1); KD_UP1(5); CM(2, 0); KD_MU0(5);
        CM(3, 4); LDB(5, 4); CM(3, 3); LDT(5, 2, 0); CM(3, 2); LDT(5, 2, 1); CM(3, 1); KD_MU1(5); CM(3, 0); KD_CV(5);
        CM(4, 3); LDB(5, 3); CM(4, 2); LDT(5, 3, 0); CM(4, 1); LDT(5, 3, 1); CM(4, 0); KD_X0(5);
        CM(5, 2); LDB(5, 2); CM(5, 1); LDT(5, 4, 0); CM(5, 0); LDT(5, 4, 1);
        CM(6, 1); LDB(5, 1); CM(6, 0); KD_X1(5);
        CM(7, 0); LDB(5, 0);
        CM(0, 7); LDB(6, 7); CM(0, 6); LDT(5, 5, 0); CM(0, 5); LDT(5, 5, 1); CM(0, 4); LDT(5, 6, 0); CM(0, 3); LDT(5, 6, 1); CM(0, 2); LDT(5, 7, 0); CM(0, 1); LDT(5, 7, 1); CM(0, 0); KD_RD(6);
        CM(1, 6); LDB(6, 6); CM(1, 5); LDT(6, 0, 0); CM(1, 4); LDT(6, 0, 1); CM(1, 3); PC(); CM(1, 2); PC(); CM(1, 1); KD_UP0(6); CM(1, 0); KD_UP1(6);
        CM(2, 5); LDB(6, 5); CM(2, 4); LDT(6, 1, 0); CM(2, 3); LDT(6, 1, 1); CM(2, 2); KD_MU0(6); CM(2, 1); KD_MU1(6); CM(2, 0); KD_CV(6);
        CM(3, 4); LDB(6, 4); CM(3, 3); LDT(6, 2, 0); CM(3, 2); LDT(6, 2, 1); CM(3, 1); KD_X0(6); CM(3, 0); KD_X1(6);
        CM(4, 3); LDB(6, 3); CM(4, 2); LDT(6, 3, 0); CM(4, 1); LDT(6, 3, 1); CM(4, 0); KD_RD(7);
        CM(5, 2); LDB(6, 2); CM(5, 1); LDT(6, 4, 0); CM(5, 0); LDT(6, 4, 1);
        CM(6, 1); LDB(6, 1); CM(6, 0); PC();
        CM(7, 0); LDB(6, 0);
        CM(0, 7); LDB(7, 7); CM(0, 6); LDT(6, 5, 0); CM(0, 5); LDT(6, 5, 1); CM(0, 4); LDT(6, 6, 0); CM(0, 3); LDT(6, 6, 1); CM(0, 2); LDT(6, 7, 0); CM(0, 1); LDT(6, 7, 1); CM(0, 0); PC();
        CM(1, 6); LDB(7, 6); CM(1, 5); LDT(7, 0, 0); CM(1, 4); LDT(7, 0, 1); CM(1, 3); KD_UP0(7); CM(1, 2); KD_UP1(7); CM(1, 1); KD_MU0(7); CM(1, 0); KD_MU1(7);
        CM(2, 5); LDB(7, 5); CM(2, 4); LDT(7, 1, 0); CM(2, 3); LDT(7, 1, 1); CM(2, 2); KD_CV(7); CM(2, 1); KD_X0(7); CM(2, 0); KD_X1(7);
        CM(3, 4); LDB(7, 4); CM(3, 3); LDT(7, 2, 0); CM(3, 2); LDT(7, 2, 1); CM(3, 1); PC(); CM(3, 0); PC();
        CM(4, 3); LDB(7, 3); CM(4, 2); LDT(7, 3, 0); CM(4, 1); LDT(7, 3, 1); CM(4, 0); PC();
        CM(5, 2); LDB(7, 2); CM(5, 1); LDT(7, 4, 0); CM(5, 0); LDT(7, 4, 1);
        CM(6, 1); LDB(7, 1); CM(6, 0); PC();
        CM(7, 0); LDB(7, 0);
        CM(0, 3); LDT(7, 5, 0); CM(0, 2); LDT(7, 5, 1); CM(1, 2); LDT(7, 6, 0); CM(1, 1); LDT(7, 6, 1); CM(2, 1); LDT(7, 7, 0); CM(2, 0); LDT(7, 7, 1);
        CM(3, 0); HF(); CM(0, 1); ZFL(); CM(0, 0); W3(0); CM(1, 0); ZF0(2); W3(1); CM(0, 5); ZF1(2); W3(2); CM(0, 4); ZF2(2); W3(3);
        CM(1, 4); ZF3(2); W3(4); CM(1, 3); ZF4(2); W3(5); CM(2, 3); ZF5(2); W3(6); CM(2, 2); ZF0(3); W3(7); CM(3, 2); ZF1(3);  CM(3, 1); ZF2(3);
        CM(4, 1); ZF3(3);  CM(4, 0); ZF4(3);  CM(5, 0); ZF5(3);  CM(0, 7); ZST(1);  CM(0, 6); ZF0(0);  CM(1, 6); ZF1(0);
        CM(1, 5); ZF2(0);  CM(2, 5); ZF3(0);  CM(2, 4); ZF4(0);  CM(3, 4); ZF5(0);  CM(3, 3); ZF0(1);  CM(4, 3); ZF1(1);
        CM(4, 2); ZF2(1);  CM(5, 2); ZF3(1);  CM(5, 1); ZF4(1);  CM(6, 1); ZF5(1);  CM(6, 0); ZST(0);  CM(7, 0); ZF0(4);
        DG(4);
        ZF1(4); ZF2(4); ZF3(4); ZF4(4); ZF5(4); ZF0(5); ZF1(5); ZF2(5); ZF3(5); ZF4(5); ZF5(5); ZST(2); ZF0(6); ZF1(6); ZF2(6); ZF3(6); ZF4(6); ZF5(6); ZF0(7); ZF1(7); ZF2(7); ZF3(7); ZF4(7); ZF5(7); ZST(3);
        DG(10);
        __builtin_amdgcn_s_setprio(3);
        while ((unsigned)__builtin_amdgcn_readfirstlane(*(volatile LAS unsigned*)(fl + C_OL)) < ZL_GATE) __builtin_amdgcn_s_sleep(4);
        CFENCE();
        const int arow = idx * 64 + (cw >> 1) * 32 + rg * 4, acol = xcd * 256 + (cw & 1) * 128 + cq * 16;
        u32x4 qr[4];
#pragma unroll
        for (int i = 0; i < 4; ++i) qr[i] = gld128((unsigned)(arow * 2048 + acol), Aq + i * 2048);
        {
            const lds_ptr tr = cv + CV_KV + 480;
            u32x4 t[4];
            const int ck = (cw >> 1) * 4 + ((lane ^ ((lane >> 5) * 2)) & 3);
            const unsigned zb = (unsigned)WS_ZT + (unsigned)(xcd * 128 + (cw & 1) * 64 + (lane >> 4) * 4) * 4096u + (idx >> 3) * 1024 +
                                ((idx & 3) * 4 + ((idx >> 2) & 1) * 16 + (ck >> 1) + (ck & 1) * 32) * 16;
#pragma unroll 1
            for (unsigned spins = 0; spins < (1u << 20); ++spins) {
#pragma unroll
                for (int i = 0; i < 4; ++i) t[i] = ld_sc1s(rs, zb + ((((lane >> 2) & 3) ^ i) << 12), i * 65536);
                if (!missing(mx4(mx4(mx8(t[0], t[1]), t[2]), t[3]))) break;
                __builtin_amdgcn_s_sleep(Z_SLEEP);
            }
            DG(13);
            __builtin_amdgcn_s_setprio(ZE_PRIO);
#pragma unroll
            for (int i = 0; i < 4; ++i) *(LAS u32x4*)(tr + i * 1024 + lane * 16) = t[i];
#pragma unroll
            for (int i = 0; i < 4; ++i) asm volatile("" : "+v"(qr[i]));
            u32x2 zc[8];
#pragma unroll
            for (int c = 0; c < 8; ++c)
                zc[c] = *(const LAS u32x2*)(tr + cq * 512 + (c >> 2) * 256 + ((((c & 3) * 4 + (rg >> 1)) ^ (cq * 2)) << 4) + (rg & 1) * 8);
#pragma unroll
            for (int i = 0; i < 4; ++i) {
                u32x4 o;
#pragma unroll
                for (int c = 0; c < 4; ++c) {
                    const unsigned z0 = zc[2 * c][i >> 1], z1 = zc[2 * c + 1][i >> 1];
                    o[c] = cvtpk(mul_vv(bflo(qr[i][c]), (i & 1) ? bfhi(z0) : bflo(z0)), mul_vv(bfhi(qr[i][c]), (i & 1) ? bfhi(z1) : bflo(z1)));
                }
                __builtin_amdgcn_raw_buffer_store_b128(o, ro, arow * 4096 + acol, i * 4096, OUT_AUX);
            }
        }
        DG_END(14);
        return;
    }

    const int half = w >> 2;
    const int TA = qb * 8 + (w & 3) * 2 + half, TB = TA ^ 1;
    bf16x8 sel;
    {
        const short one = (((lane >> 2) & 1) == ((lane >> 4) & 1) && (lane & 11) == 0) ? (short)0x3f80 : (short)0;
#pragma unroll
        for (int i = 0; i < 8; ++i) sel[i] = one;
    }
    f32x16 oA = f32x16{}, oB = f32x16{};
    f32x4 lA = f32x4{}, lB = f32x4{};
    f32x16 negm, sA, sB;
    lds_ptr lp = lds + half * 16384 + lane * 16;
    bf16x8 k0[2], k1[2], v0[2], v1[2];
    u32x4 p0, p1 = {0u, 0u, 0u, 0u};
#define EX(X, i) X[i] = __builtin_amdgcn_exp2f(X[i])
    lds_ptr qpa = lds + K2_SF + (TA & 7) * 2048 + lane * 16, qpb = lds + K2_SF + (TB & 7) * 2048 + lane * 16;
    asm volatile("" : "+v"(qpa), "+v"(qpb), "+v"(lp), "+v"(sel), "+v"(oA), "+v"(oB), "+v"(lA), "+v"(lB), "+v"(p1));
    WAIT_BAR(0);
    DG(3);
    const bf16x8 qa0 = lds_rd128(qpa), qb0 = lds_rd128(qpb);
    const bf16x8 x0 = lds_rd128(lds + lane * 16), x1 = lds_rd128(lds + lane * 16 + 1024);
    const bf16x8 qa1 = lds_rd128(qpa + 1024), qb1 = lds_rd128(qpb + 1024);
    {
        k0[0] = lds_rd128(lp); k1[0] = lds_rd128(lp + 1024);
        v0[0] = lds_rd128(lp + 2048); v1[0] = lds_rd128(lp + 3072);
        sA = MFMA32(x0, qa0, f32x16{});
        sB = MFMA32(x0, qb0, f32x16{});
        sA = MFMA32(x1, qa1, sA);
        sB = MFMA32(x1, qb1, sB);
        float m = fmx(sA[0], sA[1]), m2 = fmx(sB[0], sB[1]);
#pragma unroll
        for (int i = 2; i < 16; i += 2) m = fmx(fmx(m, sA[i]), sA[i + 1]);
#pragma unroll
        for (int i = 2; i < 16; i += 2) m2 = fmx(fmx(m2, sB[i]), sB[i + 1]);
        m = fmx(m, m2);
        auto rr = __builtin_amdgcn_permlane32_swap(__float_as_uint(m), __float_as_uint(m), false, false);
        m = fmx(__uint_as_float(rr[0]), __uint_as_float(rr[1]));
#pragma unroll
        for (int i = 0; i < 16; ++i) negm[i] = -m;
        sA = MFMA32(k0[0], qa0, negm);
        sA = MFMA32(k1[0], qa1, sA);
        v1[1] = v1[0];
        SBAR();
        RAW_BAR();
        SBAR();
        EX(sA, 0); EX(sA, 1);
    }
    DG(10);
#define SUM16(l, p) asm volatile("v_mfma_f32_16x16x32_bf16 %0, %1, %2, %0" : "+v"(l) : "v"(sel), "v"(p))
#define HALF(C, oC, lC, N, qx, qy, ka, kb, oP, lP, vprev, vcur0, PRE) do { \
        oP = MFMA32(vprev, PB(p1), oP); SBAR(); \
        PRE; EX(C, 2); EX(C, 3); EX(C, 4); SBAR(); \
        N = MFMA32(ka, qx, negm); SBAR(); \
        EX(C, 5); EX(C, 6); EX(C, 7); SBAR(); \
        SUM16(lP, p1); SBAR(); \
        p0[0] = cvtpk(C[0], C[1]); p0[1] = cvtpk(C[2], C[3]); SBAR(); \
        N = MFMA32(kb, qy, N); SBAR(); \
        p0[2] = cvtpk(C[4], C[5]); p0[3] = cvtpk(C[6], C[7]); SBAR(); EX(C, 8); EX(C, 9); SBAR(); \
        oC = MFMA32(vcur0, PB(p0), oC); SBAR(); \
        EX(C, 10); EX(C, 11); EX(C, 12); SBAR(); \
        SUM16(lC, p0); SBAR(); \
        EX(C, 13); EX(C, 14); EX(C, 15); SBAR(); \
        p1[0] = cvtpk(C[8], C[9]); p1[1] = cvtpk(C[10], C[11]); p1[2] = cvtpk(C[12], C[13]); p1[3] = cvtpk(C[14], C[15]); SBAR(); \
        EX(N, 0); EX(N, 1); SBAR(); \
    } while (0)
#define RD4(Y, nx) do { v1[Y] = lds_rd128((nx) + 3072); v0[Y] = lds_rd128((nx) + 2048); \
        k1[Y] = lds_rd128((nx) + 1024); k0[Y] = lds_rd128(nx); } while (0)
#define KT(X, nx, PRE0) do { \
        HALF(sA, oA, lA, sB, qb0, qb1, k0[X], k1[X], oB, lB, v1[X ^ 1], v0[X], PRE0; RD4(X ^ 1, nx)); \
        HALF(sB, oB, lB, sA, qa0, qa1, k0[X ^ 1], k1[X ^ 1], oA, lA, v1[X], v0[X], (void)0); \
    } while (0)
#define RING_BAR() do { unsigned long long sv_; \
        asm volatile("s_cmp_eq_u32 %1, 7\n\ts_cbranch_scc1 1f\n\ts_barrier\n1:\n\ts_mov_b64 %0, exec\n\ts_mov_b64 exec, 1\n\tds_add_u32 %2, %3\n\ts_mov_b64 exec, %0" \
                     : "=&s"(sv_) : "s"(st), "v"(fl + C_LD), "v"(1u) : "memory", "scc"); } while (0)
    SBAR();
#pragma unroll 1
    for (int st = 0; st < 8; ++st) {
        const lds_ptr cur = lp + (st & 1) * K2_SLOT;
        const lds_ptr nxt = lp + ((st + 1) & 1) * K2_SLOT;
        PRIO(2);
        KT(0, cur + 4096, (void)0);
        PRIO(1);
        KT(1, cur + 8192, (void)0);
        PRIO(0);
        KT(0, cur + 12288, (void)0);
        PRIO(3);
        KT(1, nxt, RAW_BAR(); SBAR());
    }
    oB = MFMA32(v1[1], PB(p1), oB);
    SBAR();
    SUM16(lB, p1);
    asm volatile("s_nop 7" : "+v"(oB) :: "memory");
    SBAR();
    DG(4);
    {
        const lds_ptr xo = lds + K2_SLOT + w * 4096 + lane * 16;
        *(LAS float*)(lds + K2_LX + w * 256 + lane * 4) = lB[0];
#pragma unroll
        for (int i = 0; i < 4; ++i) *(LAS f32x4*)(xo + i * 1024) = f32x4{oB[4 * i], oB[4 * i + 1], oB[4 * i + 2], oB[4 * i + 3]};
        lds_add(fl + F_X + w * 4, lane);
        lds_add(fl + C_END + half * 4, lane);
        lds_wait_ge(fl + F_X + (w ^ 4) * 4, 1u);
        const lds_ptr xi = lds + K2_SLOT + (w ^ 4) * 4096 + lane * 16;
        f32x4 t[4];
        const float lx = *(const LAS float*)(lds + K2_LX + (w ^ 4) * 256 + (lane & 31) * 4);
        { auto rr = __builtin_amdgcn_permlane32_swap(__float_as_uint(lA[0]), __float_as_uint(lA[0]), false, false); lA[0] = __uint_as_float(rr[0]); }
#pragma unroll
        for (int i = 0; i < 4; ++i) t[i] = *(const LAS f32x4*)(xi + i * 1024);
        SBAR();
        lA[0] += lx;
#pragma unroll
        for (int i = 0; i < 16; ++i) { float o = oA[i]; acc_vv(o, t[i >> 2][i & 3]); oA[i] = o; }
    }
    DG(9);
    const float l = lA[0];
    u32x4 on0, on1;
    auto finish = [&](float ls) {
        float rl = __builtin_amdgcn_rcpf(ls);
        asm volatile("s_nop 1" : "+v"(rl));
        f32x16 o;
#pragma unroll
        for (int i = 0; i < 16; ++i) o[i] = mul_vv(oA[i], rl);
        on0 = nz4(pack8n(o, 0)); on1 = nz4(pack8n(o, 1));
    };
    finish(l);
    {
        float t0, t1;
        asm("v_mul_f32_e32 %0, 0, %2\n\tv_mul_f32_e32 %1, 0, %3\n\tv_fmac_f32_e32 %0, 0, %4\n\tv_fmac_f32_e32 %1, 0, %5\n\tv_fmac_f32_e32 %0, 0, %6\n\tv_fmac_f32_e32 %1, 0, %7\n\tv_fmac_f32_e32 %0, 0, %8\n\tv_fmac_f32_e32 %1, 0, %9\n\t"
            "v_fmac_f32_e32 %0, 0, %10\n\tv_fmac_f32_e32 %1, 0, %11\n\tv_fmac_f32_e32 %0, 0, %12\n\tv_fmac_f32_e32 %1, 0, %13\n\tv_fmac_f32_e32 %0, 0, %14\n\tv_fmac_f32_e32 %1, 0, %15\n\tv_fmac_f32_e32 %0, 0, %16\n\tv_fmac_f32_e32 %1, 0, %17"
            : "=&v"(t0), "=&v"(t1) : "v"(oA[0]), "v"(oA[1]), "v"(oA[2]), "v"(oA[3]), "v"(oA[4]), "v"(oA[5]), "v"(oA[6]), "v"(oA[7]), "v"(oA[8]), "v"(oA[9]), "v"(oA[10]), "v"(oA[11]), "v"(oA[12]), "v"(oA[13]), "v"(oA[14]), "v"(oA[15]));
        const bool ok = (l <= 1.0e30f) && (l >= 1.0e-18f) && (t0 + t1 == 0.f);
        if (__builtin_expect(__any(!ok), 0)) {
            float m = -INFINITY, ls = 0.f;
            oA = f32x16{};
#pragma unroll 1
            for (int t = 0; t < 64; ++t) {
                const char* g = kvg + t * 4096 + lane * 16;
                const bf16x8 a0 = *(const bf16x8*)g, a1 = *(const bf16x8*)(g + 1024);
                const bf16x8 b0 = *(const bf16x8*)(g + 2048), b1 = *(const bf16x8*)(g + 3072);
                f32x16 s = MFMA32(a0, qa0, f32x16{});
                s = MFMA32(a1, qa1, s);
                float tm = s[0];
#pragma unroll
                for (int i = 1; i < 16; ++i) tm = __builtin_fmaxf(tm, s[i]);
                auto rr = __builtin_amdgcn_permlane32_swap(__float_as_uint(tm), __float_as_uint(tm), false, false);
                tm = __builtin_fmaxf(__uint_as_float(rr[0]), __uint_as_float(rr[1]));
                const float mn = __builtin_fmaxf(m, tm);
                const float alpha = __builtin_amdgcn_exp2f(m - mn);
                m = mn;
                float ps = 0.f;
#pragma unroll
                for (int i = 0; i < 16; ++i) { s[i] = __builtin_amdgcn_exp2f(s[i] - mn); ps += s[i]; oA[i] *= alpha; }
                ls = ls * alpha + ps;
                oA = MFMA32(b0, PB(pack8n(s, 0)), oA);
                oA = MFMA32(b1, PB(pack8n(s, 1)), oA);
            }
            auto rr = __builtin_amdgcn_permlane32_swap(__float_as_uint(ls), __float_as_uint(ls), false, false);
            asm volatile("s_nop 7\n\ts_nop 7" : "+v"(oA));
            finish(__uint_as_float(rr[0]) + __uint_as_float(rr[1]));
        }
    }
    u32x4 w3[4];
    {
        const unsigned og = (unsigned)WS_O + (unsigned)(TA * 32 + h) * 2048u + lane * 16;
        st_sc1(on0, rs, og);
        st_sc1(on1, rs, og + 1024);
        lds_add(fl + C_S1, lane);
        const char* s2 = wsrc(half ^ 1, 32 + (w & 3) * 4);
        const char* s3 = wsrc(half, 48 + (w & 3) * 4);
        lds_wait_ge(fl + C_S1, 12u);
        DG(5);
#pragma unroll
        for (int i = 0; i < 4; ++i)
            glds16p<K2_WNT>(wvoff, s2 + i * 16384, lds0 + K2_SLOT + ((w ^ 4) * 4 + i) * 1024);
#pragma unroll
        for (int i = 0; i < 4; ++i) w3[i] = gld128(wvoff, s3 + i * 16384);
    }
    {
        const int half = w & 1;
        const int T3 = tt * 4 + (w >> 1);
        const unsigned og = (unsigned)WS_O + (unsigned)T3 * 65536u + half * 32768 + lane * 16;
        const int r = lane & 31, hh = lane >> 5;
        const int g = lane >> 4, q = (lane & 15) >> 2, p = lane & 3;
        int woff[2];
#pragma unroll
        for (int n = 0; n < 2; ++n) {
            const int c = 4 * (n ^ half) + 2 * (p & 1) + (g & 1);
            woff[n] = (4 * hh + q) * 128 + ((c ^ ((q >> 1) << 2)) << 4) + (p >> 1) * 8;
        }
        f32x16 acc[2] = {f32x16{}, f32x16{}};
        lds_ptr wl[3][2] = {{lds + half * 16384 + woff[0], lds + half * 16384 + woff[1]},
                            {lds + K2_SF + half * 16384 + woff[0], lds + K2_SF + half * 16384 + woff[1]},
                            {lds + K2_CV + half * 2 * CV_BYTES + woff[0], lds + K2_CV + half * 2 * CV_BYTES + woff[1]}};
        asm volatile("" : "+v"(wl[0][0]), "+v"(wl[0][1]), "+v"(wl[1][0]), "+v"(wl[1][1]), "+v"(wl[2][0]), "+v"(wl[2][1]));
#define MS(m) ((m) < 64 - 2 * TL ? (m) >> 1 : 32 - TL + ((m) - (64 - 2 * TL)) % TL)
#define MN(m) ((m) < 64 - 2 * TL ? (m) & 1 : (m) < 64 - TL)
        constexpr int WR = 2 * WD + 2;
        s16x4 wa[WR], wb[WR];
#define LDW(m) do { const int s_ = MS(m), f_ = s_ < 8 ? 1 : s_ < 24 ? 0 : 2; \
            const int o_ = f_ == 2 ? ((s_ >> 2) & 1) * CV_BYTES + (s_ & 3) * 2048 : (f_ ? 0 : ((s_ >> 3) - 1) * K2_SLOT) + (s_ & 7) * 2048; \
            wa[(m) % WR] = lds_tr(wl[f_][MN(m)] + o_); wb[(m) % WR] = lds_tr(wl[f_][MN(m)] + o_ + 1024); SBAR(); } while (0)
#pragma unroll
        for (int m = 0; m < 2 * WD; ++m) LDW(m);
        u32x4 of[OWIN];
#define LDO(i) do { of[(i) % OWIN] = ld_sc1s(rs, og, (((i) + krot) & 31) * 1024); SBAR(); } while (0)
        for (unsigned spins = 0; spins < (1u << 20); ++spins) {
#pragma unroll
            for (int i = 0; i < OW0; ++i) LDO(i);
            unsigned m = mx8(of[0], of[1]);
#pragma unroll
            for (int i = 2; i < OW0; ++i) m = mx4(m, of[i]);
            if (!missing(m)) break;
            DGC(12);
        }
        lds_add(fl + C_S2, lane);
#pragma unroll
        for (int i = OW0; i < OWIN; ++i) LDO(i);
        DG(6);
        __builtin_amdgcn_s_setprio(P3_PRIO);
        SBAR();
        unsigned fv = 0;
#define FLAG_RD(a) do { fv = *(volatile LAS unsigned*)(a); SBAR(); } while (0)
#define FLAG_CHK(a, n) do { if (__builtin_expect((unsigned)__builtin_amdgcn_readfirstlane(fv) < (n), 0)) lds_wait_ge(a, n); CFENCE(); SBAR(); } while (0)
        lds_ptr xo = lds + K2_SF + w * 4096 + lane * 16, xi = lds + K2_SF + (w ^ 1) * 4096 + lane * 16;
        int bo = (T3 * 32 + (lane & 15)) * 4096 + 2048 + nt * 128 + half * 64 + g * 16;
        lds_ptr w3d = lds + K2_CV + (w >> 1) * CV_BYTES + (w & 1) * 4096 + lane * 16;
        asm volatile("" : "+v"(xo), "+v"(xi), "+v"(bo), "+v"(w3d));
        f32x4 xt[4];
        constexpr int M3 = TL > 8 ? 64 - 2 * TL + TL - 8 - 2 * WD : 48 - 2 * WD;
        sfor(std::make_integer_sequence<int, 64>{}, [&](auto mc) __attribute__((always_inline)) {
            constexpr int m = decltype(mc)::value, s = MS(m), n = MN(m);
            constexpr bool first = m < 64 - 2 * TL ? n == 0 : n == 1;
            constexpr bool last = m < 64 - 2 * TL ? n == 1 : n == 0;
            acc[n] = MFMA32(cat8(wa[m % WR], wb[m % WR]), PB(of[s % OWIN]), acc[n]);
            SBAR();
            if constexpr (m == 32 - 2 * WD) FLAG_CHK(fl + C_S2, 8u);
            if constexpr (m == M3) FLAG_CHK(fl + C_S3, 8u);
            if constexpr (m + 2 * WD < 64) LDW(m + 2 * WD);
            if constexpr (first && (s & 1) && s + 1 >= OW0 && s + 1 < 32) {
                if (__builtin_expect(missing(mx8(of[(s + 1) % OWIN], of[(s + 2) % OWIN])), 0)) {
#pragma unroll 1
                    for (unsigned spins = 0; spins < (1u << 20); ++spins) {
                        __builtin_amdgcn_s_sleep(2);
                        LDO(s + 1); LDO(s + 2);
                        DGC(13);
                        if (!missing(mx8(of[(s + 1) % OWIN], of[(s + 2) % OWIN]))) break;
                    }
                }
            }
            if constexpr (last && s + OWIN < 32) LDO(s + OWIN);
            if constexpr (m == 15 - 2 * WD) lds_add(fl + C_U0, lane);
            if constexpr (m == 29 - 2 * WD) FLAG_RD(fl + C_S2);
            if constexpr (m == W3M - 3) FLAG_RD(fl + C_HF);
            if constexpr (m == W3M) FLAG_CHK(fl + C_HF, 4u);
            if constexpr (m >= W3M && m < W3M + 8 && ((m - W3M) & 1) == 0) {
                asm volatile("" : "+v"(w3[((m - W3M) >> 1) & 3]));
                *(LAS u32x4*)(w3d + (((m - W3M) >> 1) & 3) * 1024) = w3[((m - W3M) >> 1) & 3];
            }
            if constexpr (m == W3M + 6) lds_add(fl + C_S3, lane);
            if constexpr (m == M3 - 3) FLAG_RD(fl + C_S3);
            if constexpr (m == 64 - TL - 3) FLAG_RD(fl + C_U0);
            if constexpr (m == 64 - TL) FLAG_CHK(fl + C_U0, 8u);
            if constexpr (m == 64 - TL + 1 || m == 64 - TL + 2) {
                constexpr int i = 2 * (m - (64 - TL + 1));
                *(LAS f32x4*)(xo + i * 1024) = f32x4{acc[1][4 * i], acc[1][4 * i + 1], acc[1][4 * i + 2], acc[1][4 * i + 3]};
                *(LAS f32x4*)(xo + i * 1024 + 1024) = f32x4{acc[1][4 * i + 4], acc[1][4 * i + 5], acc[1][4 * i + 6], acc[1][4 * i + 7]};
            }
            if constexpr (m == 64 - TL + 2) lds_add(fl + F_R + w * 4, lane);
            if constexpr (m == XR_AT - 2) FLAG_RD(fl + F_R + (w ^ 1) * 4);
            if constexpr (m == XR_AT) {
                FLAG_CHK(fl + F_R + (w ^ 1) * 4, 1u);
#pragma unroll
                for (int i = 0; i < 4; ++i) xt[i] = *(const LAS f32x4*)(xi + i * 1024);
            }
            if constexpr (ZL_GATE && last && s + OWIN == ZL_AT) lds_add(fl + C_OL, lane);
            if constexpr (m == 2 * P3_T1 + 1) __builtin_amdgcn_s_setprio(P3_PRIO - 1);
            SBAR();
            if constexpr (m == 31) DG(11);
            if constexpr (m == 47) DG(14);
        });
        asm volatile("s_nop 11" : "+v"(acc[0]));
        DG(7);
#pragma unroll
        for (int i = 0; i < 16; ++i) { float o = xt[i >> 2][i & 3]; acc_vv(o, acc[0][i]); acc[1][i] = o; }
        u32x4 pa = pack8n(acc[1], 0), pb = pack8n(acc[1], 1);
#pragma unroll
        for (int i = 0; i < 4; ++i) {
            auto sw = __builtin_amdgcn_permlane16_swap(pa[i], pb[i], false, false);
            pa[i] = sw[0]; pb[i] = sw[1];
        }
        asm volatile("" :: "v"(acc[0]));
        __builtin_amdgcn_raw_buffer_store_b128(pa, ro, bo, 0, OUT_AUX);
        __builtin_amdgcn_raw_buffer_store_b128(pb, ro, bo, 65536, OUT_AUX);
    }
    DG_END(8);
}

extern "C" void kernel_launch(void* const* d_in, const int* in_sizes, int n_in,
                              void* d_out, int out_size, void* d_ws, size_t ws_size,
                              hipStream_t stream) {
    const void* A_q = d_in[0];
    const void* A_k = d_in[1];
    const void* A_v = d_in[2];
    const void* A_shift = d_in[3];
    const void* A_ssmk = d_in[4];
    const void* A_D = d_in[5];
    const void* B_x = d_in[6];
    const void* B_wqkv = d_in[7];
    const void* B_wo = d_in[8];
    char* ws = (char*)d_ws;
    k1_qkv<<<256, 512, 0, stream>>>((const char*)B_x, (const char*)B_wqkv, ws);
    k2_attn<<<256, 768, 0, stream>>>(ws, (const char*)A_q, (const char*)A_k, (const char*)A_v, (const char*)A_shift,
                                     (const char*)A_ssmk, (const unsigned*)A_D, (const char*)B_wo, (char*)d_out);
}
```

```cpp
#include <hip/hip_runtime.h>
#include <hip/hip_bf16.h>
#include <stdint.h>
#include <utility>

typedef short bf16x8 __attribute__((ext_vector_type(8)));
typedef short s16x4 __attribute__((ext_vector_type(4)));
typedef float f32x16 __attribute__((ext_vector_type(16)));
typedef float f32x4 __attribute__((ext_vector_type(4)));
typedef float f32x2 __attribute__((ext_vector_type(2)));
typedef unsigned u32x4 __attribute__((ext_vector_type(4)));
typedef unsigned u32x2 __attribute__((ext_vector_type(2)));
typedef __bf16 bf16x2_t __attribute__((ext_vector_type(2)));

#define LAS __attribute__((address_space(3)))
typedef LAS char* lds_ptr;
#define DI __device__ __forceinline__
#define SBAR() __builtin_amdgcn_sched_barrier(0)
#define MFMA32(a, b, c) __builtin_amdgcn_mfma_f32_32x32x16_bf16((a), (b), (c), 0, 0, 0)
#define MFMA16(a, b, c) __builtin_amdgcn_mfma_f32_16x16x32_bf16((a), (b), (c), 0, 0, 0)
#define WAIT_BAR(N) asm volatile("s_waitcnt vmcnt(" #N ") lgkmcnt(0)\n\ts_barrier" ::: "memory")
#define LGKM_BAR() asm volatile("s_waitcnt lgkmcnt(0)\n\ts_barrier" ::: "memory")

#define DG_DECL(K)
#define DG(i)
#define DG_END(i)
#define DGC(i)

constexpr int S_LEN = 2048, DM = 1024, NH = 32;

#define KQ 1
#define OQ 0
#define QT(h, T) (KQ ? (h) * 64 + (T) : (T) * 32 + (h))
constexpr size_t WS_Q = 0;
constexpr size_t WS_KV = 4u << 20;
constexpr size_t WS_O = OQ ? WS_Q : 12u << 20;
constexpr size_t WS_KVR = 16u << 20;
#define ZK 0
constexpr size_t WS_ZT = ZK ? WS_KVR : 20u << 20;
constexpr size_t WS_CNT = 24u << 20;

template <int... I, class F> DI void sfor(std::integer_sequence<int, I...>, F&& f) { (f(std::integral_constant<int, I>{}), ...); }
DI unsigned cvtpk(float lo, float hi) {
    f32x2 v = {lo, hi};
    return __builtin_bit_cast(unsigned, __builtin_convertvector(v, bf16x2_t));
}
DI float bflo(unsigned u) { return __uint_as_float(u << 16); }
DI float bfhi(unsigned u) { return __uint_as_float(u & 0xffff0000u); }

DI void glds16(unsigned voff, const void* sbase, unsigned lds_dst) {
    unsigned sv;
    asm volatile("s_mov_b32 %0, m0\n\ts_mov_b32 m0, %3\n\ts_nop 0\n\tglobal_load_lds_dwordx4 %1, %2\n\ts_mov_b32 m0, %0"
                 : "=&s"(sv) : "v"(voff), "s"(sbase), "s"(lds_dst) : "memory");
}
template <int NT> DI void glds16p(unsigned voff, const void* sbase, unsigned lds_dst) {
    if constexpr (NT) {
        unsigned sv;
        asm volatile("s_mov_b32 %0, m0\n\ts_mov_b32 m0, %3\n\ts_nop 0\n\tglobal_load_lds_dwordx4 %1, %2 nt\n\ts_mov_b32 m0, %0"
                     : "=&s"(sv) : "v"(voff), "s"(sbase), "s"(lds_dst) : "memory");
    } else glds16(voff, sbase, lds_dst);
}
#define K1_XNT 0
#define K1_WNT 0
#define K2_SNT 1
#define K2_QNT 0
#define K2_WNT 0
#define OUT_AUX 16
#define K1_SAUX 16
DI bf16x8 lds_rd128(lds_ptr p) { return *(const LAS bf16x8*)p; }
DI s16x4 lds_tr(lds_ptr p) {
    return __builtin_bit_cast(s16x4, __builtin_amdgcn_ds_read_tr16_b64_v4i16((LAS s16x4*)p));
}
DI bf16x8 cat8(s16x4 a, s16x4 b) { return __builtin_shufflevector(a, b, 0, 1, 2, 3, 4, 5, 6, 7); }
DI u32x4 pack8(const f32x16& x, int s, float sc) {
    u32x4 p;
    p[0] = cvtpk(x[8 * s + 0] * sc, x[8 * s + 1] * sc);
    p[1] = cvtpk(x[8 * s + 2] * sc, x[8 * s + 3] * sc);
    p[2] = cvtpk(x[8 * s + 4] * sc, x[8 * s + 5] * sc);
    p[3] = cvtpk(x[8 * s + 6] * sc, x[8 * s + 7] * sc);
    return p;
}
DI u32x4 pack8n(const f32x16& x, int s) {
    u32x4 p;
    p[0] = cvtpk(x[8 * s + 0], x[8 * s + 1]);
    p[1] = cvtpk(x[8 * s + 2], x[8 * s + 3]);
    p[2] = cvtpk(x[8 * s + 4], x[8 * s + 5]);
    p[3] = cvtpk(x[8 * s + 6], x[8 * s + 7]);
    return p;
}
DI void rnd2(float& a, float& b) { unsigned u = cvtpk(a, b); a = bflo(u); b = bfhi(u); }

#define CONV_PACE()
#define RAW_BAR() asm volatile("s_barrier" ::: "memory")
DI unsigned gld32(unsigned voff, const void* sbase) {
    unsigned r;
    asm volatile("global_load_dword %0, %1, %2" : "=v"(r) : "v"(voff), "s"(sbase) : "memory");
    return r;
}
DI u32x2 gld64(unsigned voff, const void* sbase) {
    u32x2 r;
    asm volatile("global_load_dwordx2 %0, %1, %2" : "=v"(r) : "v"(voff), "s"(sbase) : "memory");
    return r;
}
DI float mul_sv(float s, float v) { float r; asm("v_mul_f32_e32 %0, %1, %2" : "=v"(r) : "s"(s), "v"(v)); return r; }
DI float mul_vv(float a, float b) { float r; asm("v_mul_f32_e32 %0, %1, %2" : "=v"(r) : "v"(a), "v"(b)); return r; }
DI float add_vv(float a, float b) { float r; asm("v_add_f32_e32 %0, %1, %2" : "=v"(r) : "v"(a), "v"(b)); return r; }
DI void acc_vv(float& a, float b) { asm("v_add_f32_e32 %0, %0, %1" : "+v"(a) : "v"(b)); }
DI void fmac_vv(float& acc, float a, float b) { asm("v_fmac_f32_e32 %0, %1, %2" : "+v"(acc) : "v"(a), "v"(b)); }
DI void fmac_sv(float& acc, float s, float v) { asm("v_fmac_f32_e32 %0, %1, %2" : "+v"(acc) : "s"(s), "v"(v)); }
typedef __attribute__((address_space(1))) unsigned gu32;
#define RLX_AGENT __ATOMIC_RELAXED, __HIP_MEMORY_SCOPE_AGENT
#define CFENCE() asm volatile("" ::: "memory")
#define DRAIN() asm volatile("s_waitcnt vmcnt(0)" ::: "memory")
constexpr int AUX_SC1 = 16;
DI u32x4 ld_sc1(__amdgpu_buffer_rsrc_t rs, unsigned off) { return __builtin_amdgcn_raw_buffer_load_b128(rs, (int)off, 0, AUX_SC1); }
DI u32x4 ld_sc1s(__amdgpu_buffer_rsrc_t rs, unsigned off, int soff) { return __builtin_amdgcn_raw_buffer_load_b128(rs, (int)off, soff, AUX_SC1); }
DI void st_sc1(u32x4 v, __amdgpu_buffer_rsrc_t rs, unsigned off) { __builtin_amdgcn_raw_buffer_store_b128(v, rs, (int)off, 0, AUX_SC1); }
constexpr unsigned EMPTY = 0xffffffffu;
DI unsigned nz(unsigned x) { return x < EMPTY - 1u ? x : EMPTY - 1u; }
DI u32x4 nz4(u32x4 v) { return u32x4{nz(v[0]), nz(v[1]), nz(v[2]), nz(v[3])}; }
DI float fmx(float a, float b) { return __builtin_elementwise_maximum(a, b); }
DI unsigned umax(unsigned a, unsigned b) { return a > b ? a : b; }
DI unsigned umax3(unsigned a, unsigned b, unsigned c) { return umax(umax(a, b), c); }
DI unsigned mx4(unsigned m, u32x4 v) { return umax3(umax3(m, v[0], v[1]), v[2], v[3]); }
DI unsigned mx8(u32x4 a, u32x4 b) { return umax(umax3(umax3(umax3(a[0], a[1], a[2]), a[3], b[0]), b[1], b[2]), b[3]); }
DI bool missing(unsigned m) { return __builtin_amdgcn_ballot_w64(m == EMPTY) != 0; }
DI unsigned lds_add_rtn(lds_ptr p, int lane) {
    unsigned old = 0;
    CFENCE();
    if (lane == 0) old = __hip_atomic_fetch_add((LAS unsigned*)p, 1u, __ATOMIC_RELAXED, __HIP_MEMORY_SCOPE_WORKGROUP);
    old = __builtin_amdgcn_readfirstlane(old);
    CFENCE();
    return old;
}
DI void lds_add(lds_ptr p, int lane) {
    unsigned long long sv;
    asm volatile("s_mov_b64 %0, exec\n\ts_mov_b64 exec, 1\n\tds_add_u32 %1, %2\n\ts_mov_b64 exec, %0"
                 : "=&s"(sv) : "v"(p), "v"(1u) : "memory");
}
DI void lds_wait_ge(lds_ptr p, unsigned v) {
    CFENCE();
    while ((unsigned)__builtin_amdgcn_readfirstlane(*(volatile LAS unsigned*)p) < v) __builtin_amdgcn_s_sleep(1);
    CFENCE();
}
DI void poll_ge(gu32* word, unsigned want) {
    CFENCE();
    for (unsigned spins = 0; spins < (1u << 22); ++spins) {
        if ((unsigned)__builtin_amdgcn_readfirstlane(__hip_atomic_load(word, RLX_AGENT)) >= want) break;
        __builtin_amdgcn_s_sleep(1);
    }
    __builtin_amdgcn_fence(__ATOMIC_ACQUIRE, "wavefront");
    CFENCE();
}
#define PB(x) __builtin_bit_cast(bf16x8, x)
#define PRIO(x) do { __builtin_amdgcn_s_setprio(x); SBAR(); } while (0)
DI u32x4 gld128(unsigned voff, const void* sbase) {
    u32x4 r;
    asm volatile("global_load_dwordx4 %0, %1, %2" : "=v"(r) : "v"(voff), "s"(sbase) : "memory");
    return r;
}

#define K1_UNR 15
#define K1_LA 5
#define K1_ROT ((tb & 3) * 4)
#define K1_PF 0
#define K1_PG 0
#define K1_TW 0
#define CPF 3
#define CPF_POS 1
#define PG1_AT 0
#define PG2_AT 2
#define PG3_AT 6
#define K1_WB(n) WAIT_BAR(1)
#define PF1_AT -1
#define PF1_LO 4
#define PF1_N 4
#define PF2_AT 1
#define PF2_LO 8
#define PF2_N 4
#define PF3_AT 5
#define PF3_LO 12
#define PF3_N 4
#define K1_KR ((kt + rot) & 15)
#define K1_ST(v, rs, off) __builtin_amdgcn_raw_buffer_store_b128(v, rs, (int)(off), 0, K1_SAUX)
constexpr int K1_STAGE = 40960, K1U = K1_UNR;
__global__ __launch_bounds__(512, 2) void k1_qkv(const char* __restrict__ X, const char* __restrict__ W,
                                                 char* __restrict__ ws) {
    __shared__ __attribute__((aligned(16))) char lds_raw[4 * K1_STAGE];
    const lds_ptr lds = (lds_ptr)lds_raw;
    const unsigned lds0 = (unsigned)(uintptr_t)lds;
    const int tid = threadIdx.x, lane = tid & 63;
    const int w = __builtin_amdgcn_readfirstlane(tid >> 6);
    const int b = blockIdx.x, xcd = b & 7, idx = b >> 3;
    const int hp = (xcd >> 1) * 4 + (idx & 3), tb = (xcd & 1) * 8 + (idx >> 2);
    DG_DECL(0)

    const int lw = w & 3;
    const int rot = K1_ROT;
    unsigned voffX[4], voffW[2];
#pragma unroll
    for (int pp = 0; pp < 4; ++pp) {
        const int row = 32 * lw + 8 * pp + (lane >> 3);
        const int ch = (lane & 7) ^ ((row >> 1) & 7);
        voffX[pp] = (unsigned)(tb * 128 + row) * 2048u + ch * 16;
    }
#pragma unroll
    for (int pp = 0; pp < 2; ++pp) {
        const int krow = 16 * lw + 8 * pp + (lane >> 3);
        const int ch = (lane & 7) ^ (((lane >> 4) & 1) << 2);
        voffW[pp] = (unsigned)krow * 6144u + ch * 16;
    }
    auto issue = [&](int kt, int lo, int hi) {
        const int kr = K1_KR;
        const char* xb = X + kr * 128;
        const char* wb = W + (size_t)kr * (64 * 6144) + hp * 128;
        const unsigned st = lds0 + (kt & 3) * K1_STAGE;
#pragma unroll
        for (int pp = 0; pp < 4; ++pp)
            if (pp >= lo && pp < hi) glds16p<K1_XNT>(voffX[pp], xb, st + (4 * lw + pp) * 1024);
#pragma unroll
        for (int sec = 0; sec < 3; ++sec)
#pragma unroll
            for (int pp = 0; pp < 2; ++pp)
                if (4 + 2 * sec + pp >= lo && 4 + 2 * sec + pp < hi)
                    glds16p<K1_WNT>(voffW[pp], wb + sec * 2048, st + 16384 + sec * 8192 + (2 * lw + pp) * 1024);
    };
    DG(13);
    unsigned cpf;
    auto codepf = [&]() {
        unsigned long long pc; unsigned lo, hi;
        asm volatile("s_getpc_b64 %0\n.Lcp_k1a:\n\ts_mov_b32 %1, %3\n\ts_mov_b32 %2, %4" : "=s"(pc), "=s"(lo), "=s"(hi) : "n"(0), "n"(0));
        if (CPF & 1) lo = 0; else asm volatile("s_mov_b32 %0, .Lcp_k2a-.Lcp_k1a" : "=s"(lo));
        if (CPF & 2) asm volatile("s_mov_b32 %0, .Lcp_k2z-.Lcp_k1a" : "=s"(hi)); else asm volatile("s_mov_b32 %0, .Lcp_k1z-.Lcp_k1a" : "=s"(hi));
        unsigned off = lo + (unsigned)(idx + 32 * (lane & 7) + 256 * w) * 64u;
        off = off < hi ? off : hi;
        asm volatile("global_load_dword %0, %1, %2" : "=v"(cpf) : "v"(off), "s"(pc) : "memory");
    };
    if (w < 4) { issue(0, 0, 5); issue(1, 0, 5); codepf(); }
    else { issue(0, 5, 10); issue(1, 5, 10); }
    DG(14);
    int w2 = w;
    asm volatile("" : "+s"(w2));

    if (w2 >= 4) {
#define WAITN_BAR(N) asm volatile("s_waitcnt vmcnt(%0) lgkmcnt(0)\n\ts_barrier" :: "n"(N) : "memory")
        issue(2, 0, K1_LA);
        asm volatile("s_waitcnt vmcnt(%0)" :: "n"(5 + K1_LA) : "memory");
#pragma unroll 1
        for (int kt = 0; kt < 13; ++kt) {
            WAITN_BAR(10 + K1_LA);
            issue(kt + 2, K1_LA, 10);
            issue(kt + 3, 0, K1_LA);
        }
        WAITN_BAR(10 + K1_LA);
        issue(15, K1_LA, 10);
        WAIT_BAR(10);
        WAIT_BAR(0);
        if (lw == 0 && lane < 2) *(unsigned*)(ws + WS_CNT + b * 256 + lane * 128) = 0u;
        DG_END(5);
        return;
    }

    const int wm = w >> 1, wn = w & 1;
    const int r = lane & 31, hh = lane >> 5;
    const int g = lane >> 4, q = (lane & 15) >> 2, p = lane & 3;
    const int xoff0 = (64 * wm + r) * 128 + ((hh ^ ((r >> 1) & 7)) << 4);
    const int woff0 = 16384 + (8 * hh + q) * 128 + (((4 * wn + 2 * (g & 1) + (p >> 1)) ^ ((q >> 1) << 2)) << 4) + (p & 1) * 8;

    f32x16 acc[2][3];
#pragma unroll
    for (int m = 0; m < 2; ++m)
#pragma unroll
        for (int s = 0; s < 3; ++s) acc[m][s] = f32x16{};
    bf16x8 xf[2][2];
    s16x4 wl[2][3], wh[2][3];
#define K1_WF(f, sec) cat8(wl[f][sec], wh[f][sec])
#define K1_STEP(F, st, t, BAR_HERE) do { \
        const lds_ptr xp_ = (st) + (xoff0 ^ ((t) << 5)); const lds_ptr wp_ = (st) + woff0 + (t) * 2048; \
        acc[0][0] = MFMA32(K1_WF(F, 0), xf[F][0], acc[0][0]); SBAR(); \
        if (BAR_HERE) { K1_WB(BAR_HERE); SBAR(); } \
        xf[F ^ 1][0] = lds_rd128(xp_); wl[F ^ 1][0] = lds_tr(wp_); wh[F ^ 1][0] = lds_tr(wp_ + 512); SBAR(); \
        acc[0][1] = MFMA32(K1_WF(F, 1), xf[F][0], acc[0][1]); SBAR(); \
        wl[F ^ 1][1] = lds_tr(wp_ + 8192); wh[F ^ 1][1] = lds_tr(wp_ + 8192 + 512); SBAR(); \
        acc[0][2] = MFMA32(xf[F][0], K1_WF(F, 2), acc[0][2]); SBAR(); \
        wl[F ^ 1][2] = lds_tr(wp_ + 16384); wh[F ^ 1][2] = lds_tr(wp_ + 16384 + 512); xf[F ^ 1][1] = lds_rd128(xp_ + 4096); SBAR(); \
        acc[1][0] = MFMA32(K1_WF(F, 0), xf[F][1], acc[1][0]); SBAR(); \
        acc[1][1] = MFMA32(K1_WF(F, 1), xf[F][1], acc[1][1]); \
        acc[1][2] = MFMA32(xf[F][1], K1_WF(F, 2), acc[1][2]); SBAR(); \
    } while (0)

    WAIT_BAR(6);
    DG(3);
    {
        const lds_ptr xp_ = lds + xoff0; const lds_ptr wp_ = lds + woff0;
        xf[0][0] = lds_rd128(xp_); xf[0][1] = lds_rd128(xp_ + 4096);
#pragma unroll
        for (int sec = 0; sec < 3; ++sec) { wl[0][sec] = lds_tr(wp_ + sec * 8192); wh[0][sec] = lds_tr(wp_ + sec * 8192 + 512); }
    }
    SBAR();
#pragma unroll K1U
    for (int kt = 0; kt < 15; ++kt) {
        const lds_ptr st = lds + (kt & 3) * K1_STAGE;
        const lds_ptr sn = lds + ((kt + 1) & 3) * K1_STAGE;
        K1_STEP(0, st, 1, false);
        K1_STEP(1, st, 2, false);
        K1_STEP(0, st, 3, false);
        K1_STEP(1, sn, 0, kt + 1);
    }
    {
        const lds_ptr st = lds + 3 * K1_STAGE;
        K1_STEP(0, st, 1, false);
        K1_STEP(1, st, 2, false);
        K1_STEP(0, st, 3, false);
#pragma unroll
        for (int m = 0; m < 2; ++m) {
            acc[m][1] = MFMA32(K1_WF(1, 1), xf[1][m], acc[m][1]);
            acc[m][2] = MFMA32(xf[1][m], K1_WF(1, 2), acc[m][2]);
            acc[m][0] = MFMA32(K1_WF(1, 0), xf[1][m], acc[m][0]);
        }
    }
    DG(4);
    asm volatile("s_waitcnt vmcnt(0)\n.Lcp_k1z:" :: "v"(cpf));
    {
        const int h = 2 * hp + wn;
        const float qscale = 0.17677669529663687f * 1.4426950408889634f;
        const __amdgpu_buffer_rsrc_t rs = __builtin_amdgcn_make_buffer_rsrc(ws, 0, 32 << 20, 0x00020000);
#pragma unroll
        for (int m = 0; m < 2; ++m) {
            const int T = tb * 4 + wm * 2 + m;
            const unsigned dq = (unsigned)WS_Q + QT(h, T) * 2048 + lane * 16;
            const unsigned dk = (unsigned)WS_KV + (h * 64 + T) * 4096 + lane * 16;
            K1_ST(pack8n(acc[m][1], 0), rs, dk);
            K1_ST(pack8n(acc[m][1], 1), rs, dk + 1024);
            K1_ST(pack8n(acc[m][2], 0), rs, dk + 2048);
            K1_ST(pack8n(acc[m][2], 1), rs, dk + 3072);
            K1_ST(pack8(acc[m][0], 0, qscale), rs, dq);
            K1_ST(pack8(acc[m][0], 1, qscale), rs, dq + 1024);
        }
    }
    DG_END(5);
}

constexpr int K2_SLOT = 32768;
constexpr int K2_SF = 65536;
constexpr int K2_CV = 98304;
constexpr int CV_HA = 0, CV_HB = 4160, CV_KV = 8320, CV_BYTES = 8320 + 480 + 4096;
constexpr int K2_LX = K2_CV + 4 * CV_BYTES;
constexpr int K2_FL = K2_LX + 2048;
constexpr int K2_SCR = K2_FL + 256;
constexpr int K2_PAD = K2_SCR + 4096;
constexpr int K2_LDS = K2_PAD + 1024;
#define NB8 0
#define WW 0
constexpr int F_X = 0, F_R = 32, F_W2 = 64, C_OST = 68, C_S1 = 76, C_S2 = 80, C_S3 = 128, C_ZST = 88, F_ZGO = 92,
              C_KST = 96, C_U0 = 100, C_LD = 108, F_KGO = 116, C_END = 120, C_OL = 72, C_HF = 84;
#define NG_IN 0
#define GATE_WORD C_END
#define GATE_VAL 0
#define X_NZ 4
#define X_NW3 8
#define ZP_GATE 4u
#define ZL_GATE 4
#define ZL_AT 31
#define Z_SLEEP0 0
#define O_SLEEP 0
#define Z_SLEEP 8
#define OWIN 16
#define OW0 8
#define WD 2
#define W3_AT 10
#define X_NOLDW 0
#define X_NOLDO 0
#define X_NOCHK 0
#define KROT (nt * 2)
#define NG_LAST 0
#define PACE_IN "s_nop 7"
#define PACE_OUT "s_nop 1"
#define P3_PRIO 1
#define TOP_SLEEP 2
#define A_AUX 16
#define B_AUX 16
#define B_LDS 0
#define TL 8
#define XR_AT 62
#define W3_AUX 0
#define W3M 13
#define A16 1
#define SEL4 2
#define KV_DOT 1
#define ZE_PRIO 3
#define P3_T1 99
#define P3_T2 99
#define AUX_PRIO 2

__global__ __launch_bounds__(768, 3) void k2_attn(char* __restrict__ ws, const char* __restrict__ Aq,
                                                  const char* __restrict__ Ak, const char* __restrict__ Av,
                                                  const char* __restrict__ shiftk, const char* __restrict__ ssmk,
                                                  const unsigned* __restrict__ ssmD, const char* __restrict__ Wo,
                                                  char* __restrict__ out) {
    __shared__ __attribute__((aligned(16))) char lds_raw[K2_LDS];
    const lds_ptr lds = (lds_ptr)lds_raw;
    const lds_ptr fl = lds + K2_FL;
    const unsigned lds0 = (unsigned)(uintptr_t)lds;
    const int tid = threadIdx.x, lane = tid & 63;
    const int w = __builtin_amdgcn_readfirstlane(tid >> 6);
    const int b = blockIdx.x, xcd = b & 7, idx = b >> 3;
    const int h = xcd * 4 + (idx >> 3), qb = idx & 7;
    const int tt = xcd * 2 + (idx >> 4), nt = idx & 15;
    const char* kvg = ws + WS_KV + (size_t)h * 64 * 4096;
    const __amdgpu_buffer_rsrc_t rs = __builtin_amdgcn_make_buffer_rsrc(ws, 0, 32 << 20, 0x00020000);
    const __amdgpu_buffer_rsrc_t ro = __builtin_amdgcn_make_buffer_rsrc(out, 0, 8 << 20, 0x00020000);
    gu32* const cnt = (gu32*)(ws + WS_CNT);
    auto signal = [&]() {
        (void)__hip_atomic_fetch_add(cnt + ((lane & 31) * 8 + (lane < 32 ? xcd : qb)) * 64, 1u, RLX_AGENT);
    };
    const unsigned wvoff = (unsigned)(lane >> 3) * 2048u + nt * 128 + (((lane & 7) ^ (((lane >> 4) & 1) << 2)) << 4);
    const int krot = KROT;
    auto wsrc = [&](int hf, int lp) { return Wo + (size_t)(hf * 64 + ((lp + krot * 2) & 63)) * 16384; };
    DG_DECL(1)
    const unsigned voff = lane * 16;
    const int cw = w & 3;
    auto issue = [&](int st) {
        const char* src = kvg + (st * 4 + cw) * 4096;
        const unsigned dst = lds0 + (st & 1) * K2_SLOT + cw * 4096;
#pragma unroll
        for (int i = 0; i < 4; ++i) glds16(voff, src + i * 1024, dst + i * 1024);
#pragma unroll
        for (int i = 0; i < 4; ++i) glds16(voff, src + 131072 + i * 1024, dst + 16384 + i * 1024);
    };
    asm volatile(".Lcp_k2a:");
    if (w >= 8) {
        __builtin_amdgcn_s_sleep(TOP_SLEEP);
#pragma unroll
        for (int i = 2; i < 8; ++i)
            glds16(voff, kvg + (i & 1) * 131072 + (i >> 1) * 4096 + cw * 1024, lds0 + (i & 1) * 16384 + (i >> 1) * 4096 + cw * 1024);
        RAW_BAR();
        issue(1);
        WAIT_BAR(8);
    } else {
        const int tq = cw * 2 + (w >> 2);
        const char* qg = ws + WS_Q + (size_t)QT(h, qb * 8 + tq) * 2048;
        glds16(voff, kvg + (w >> 2) * 131072 + cw * 1024, lds0 + (w >> 2) * 16384 + cw * 1024);
        glds16(voff, qg, lds0 + K2_SF + tq * 2048);
        glds16(voff, qg + 1024, lds0 + K2_SF + tq * 2048 + 1024);
    }
    int w2 = w;
    asm volatile("" : "+s"(w2));

    if (w2 >= 8) {
        __builtin_amdgcn_s_setprio(3);
        const int ch = xcd * 128 + idx * 4 + cw;
        const lds_ptr cv = lds + K2_CV + cw * CV_BYTES;
        {
            const u32x4 z4 = {0u, 0u, 0u, 0u};
            if (lane < 2) *(LAS u32x4*)(cv + CV_HA + lane * 16) = z4;
            if (lane < 30) *(LAS u32x4*)(cv + CV_KV + lane * 16) = z4;
            if (cw == 0 && lane < 16) *(LAS u32x4*)(fl + lane * 16) = z4;
        }
        const int c0 = xcd * 128 + cw * 32, cq = lane & 7, rg = lane >> 3;
        const int row0 = idx * 64 + rg * 8;
        const unsigned roff = (unsigned)row0 * 2048u + (c0 + cq * 4) * 2;
        const unsigned toffs = (unsigned)(c0 + (lane >> 3)) * 4096u + idx * 128;
        u32x4 sk[2];
        sk[0] = gld128((unsigned)(c0 + cq * 4) * 8u, shiftk); sk[1] = gld128((unsigned)(c0 + cq * 4) * 8u, shiftk + 16);
        unsigned dd = gld32((unsigned)(ch >> 1) * 4u, ssmD);
        auto stage = [&](const char* src, int dst) {
            const unsigned sbase = (unsigned)(idx * 64 + cw * 16 + (lane >> 4)) * 2048u + xcd * 256;
#pragma unroll
            for (int i = 0; i < 4; ++i)
                glds16p<K2_SNT>(sbase + (((lane & 15) ^ (((2 * cw + (i >> 1)) & 3) << 2)) << 4), src + i * 8192, lds0 + dst + cw * 4096 + i * 1024);
        };
        auto blank = [&](unsigned off) {
            const u32x4 e4 = {EMPTY, EMPTY, EMPTY, EMPTY};
            CFENCE();
            st_sc1(e4, rs, off + voff); st_sc1(e4, rs, off + 1024 + voff);
            CFENCE();
        };
        WAIT_BAR(3);
        DG(12);
        issue(2);
        stage(Ak, K2_SF);
        glds16((unsigned)(idx * 64 - (idx ? 4 : 0) + (lane >> 4)) * 2048u + xcd * 256 + (((lane & 15) ^ 12) << 4), Ak, lds0 + K2_SCR + cw * 1024);
        blank((unsigned)WS_O + (unsigned)((qb * 8 + cw * 2) * 32 + h) * 2048u);
        auto warm = [&](int g) {
#pragma unroll
            for (int i = 0; i < 4; ++i) glds16(wvoff, wsrc(idx >> 4, 16 + g * 16 + cw * 4 + i), lds0 + K2_PAD);
        };
        WAIT_BAR(7);
        DG(9);
        issue(3);
        stage(Av, K2_SF + 16384);
        blank((unsigned)WS_O + (unsigned)((qb * 8 + cw * 2 + 1) * 32 + h) * 2048u);
        WAIT_BAR(6);
        DG(3);
        issue(4);
#pragma unroll
        for (int i = 0; i < 4; ++i) glds16p<K2_SNT>(voff, ssmk + (size_t)ch * 4096 + i * 1024, lds0 + K2_CV + cw * CV_BYTES + CV_HA + 32 + i * 1024);
        blank((unsigned)WS_ZT + ch * 4096); blank((unsigned)WS_ZT + ch * 4096 + 2048);
        WAIT_BAR(8);
        DG(5);
        issue(5);
        SBAR();
        asm volatile("" : "+v"(sk[0]), "+v"(sk[1]), "+v"(dd));
        dd = __builtin_amdgcn_readfirstlane(dd);
        {
            u32x2 kr[11], vr[8];
            lds_ptr kb = lds + K2_SF + rg * 2048 + ((cw ^ (rg & 3)) << 6) + cq * 8;
            asm volatile("" : "+v"(kb));
            const lds_ptr hb = lds + (rg ? K2_SF + rg * 2048 - 768 : K2_SCR + cw * 1024 + 256) + ((cw ^ ((rg + 3) & 3)) << 6) + cq * 8;
#pragma unroll
            for (int i = 0; i < 3; ++i) kr[i] = *(const LAS u32x2*)(hb + i * 256);
#pragma unroll
            for (int i = 0; i < 8; ++i) kr[3 + i] = *(const LAS u32x2*)(kb + i * 256);
#pragma unroll
            for (int i = 0; i < 8; ++i) vr[i] = *(const LAS u32x2*)(kb + 16384 + i * 256);
            if (row0 == 0) { kr[0] = u32x2{0u, 0u}; kr[1] = u32x2{0u, 0u}; kr[2] = u32x2{0u, 0u}; }
            const lds_ptr tb = cv + CV_KV + 480;
#pragma unroll
            for (int c = 0; c < 4; ++c) {
                const unsigned s01 = sk[c >> 1][(c & 1) * 2], s23 = sk[c >> 1][(c & 1) * 2 + 1];
                unsigned kp[10];
                float kv[8], vf[8];
#pragma unroll
                for (int j = 0; j < 10; ++j) kp[j] = __builtin_amdgcn_perm(kr[j][c >> 1], kr[j + 1][c >> 1], (c & 1) ? 0x07060302u : 0x05040100u);
                SBAR();
#pragma unroll
                for (int i = 0; i < 8; ++i) asm("v_dot2_f32_bf16 %0, %1, %2, 0" : "=v"(kv[i]) : "v"(kp[i]), "v"(s23));
                SBAR();
#pragma unroll
                for (int i = 0; i < 8; ++i) asm("v_dot2_f32_bf16 %0, %1, %2, %0" : "+v"(kv[i]) : "v"(kp[i + 2]), "v"(s01));
                SBAR();
#pragma unroll
                for (int i = 0; i < 8; ++i) vf[i] = (c & 1) ? bfhi(vr[i][c >> 1]) : bflo(vr[i][c >> 1]);
                SBAR();
#pragma unroll
                for (int i = 0; i < 8; ++i) kv[i] = mul_vv(kv[i], vf[i]);
                u32x4 o4;
#pragma unroll
                for (int i = 0; i < 4; ++i) o4[i] = cvtpk(kv[7 - 2 * i], kv[6 - 2 * i]);
                *(LAS u32x4*)(tb + (cq * 4 + c) * 128 + ((rg ^ cq) << 4)) = o4;
            }
#pragma unroll
            for (int i = 0; i < 4; ++i) {
                const u32x4 t = *(const LAS u32x4*)(tb + i * 1024 + lane * 16);
                st_sc1(t, rs, (unsigned)WS_Q + (xcd * 32 + idx) * 16384 + cw * 4096 + i * 1024 + (lane >> 3) * 128 + (((lane & 7) ^ (2 * i + (lane >> 5))) << 4));
            }
        }
        CFENCE();
        DG(7);
        WAIT_BAR(4);
        DG(6);
        issue(6);
        asm volatile("s_waitcnt vmcnt(8)" ::: "memory");
        if (lds_add_rtn(fl + C_KST, lane) == 3u) signal();
#pragma unroll
        for (int i = 0; i < 8; ++i)
            glds16p<K2_WNT>(wvoff, wsrc(i >> 2, cw * 4 + (i & 3)), lds0 + K2_SF + (i >> 2) * 16384 + (cw * 4 + (i & 3)) * 1024);
        {
            const u32x4 z4 = {0u, 0u, 0u, 0u};
            if (lane < 2) *(LAS u32x4*)(cv + CV_HB + lane * 16) = z4;
#pragma unroll
            for (int i = 0; i < 4; ++i) {
                const int off = 32 + i * 1024 + lane * 16;
                const u32x4 hv = *(const LAS u32x4*)(cv + CV_HA + off);
                const unsigned pv = *(const LAS unsigned*)(cv + CV_HA + off - 4);
                u32x4 sh;
                sh[0] = __builtin_amdgcn_alignbit(hv[0], pv, 16);
                sh[1] = __builtin_amdgcn_alignbit(hv[1], hv[0], 16);
                sh[2] = __builtin_amdgcn_alignbit(hv[2], hv[1], 16);
                sh[3] = __builtin_amdgcn_alignbit(hv[3], hv[2], 16);
                *(LAS u32x4*)(cv + CV_HB + off) = sh;
            }
        }
        WAIT_BAR(8);
        DG(8);
        issue(7);
#pragma unroll
        for (int i = 0; i < 8; ++i)
            glds16p<K2_WNT>(wvoff, wsrc(idx >> 4, 32 + cw * 8 + i), lds0 + K2_SCR + cw * 1024);
        WAIT_BAR(8);
        DG(11);
        if (cw == 0) { poll_ge(cnt + b * 64, 64u); lds_add(fl + F_KGO, lane); }
        else lds_wait_ge(fl + F_KGO, 1u);
        {
            u32x4 t[4];
#pragma unroll
            for (int i = 0; i < 4; ++i) t[i] = ld_sc1s(rs, (unsigned)WS_Q + (xcd * 32 + (lane >> 3)) * 16384 + (idx * 4 + cw) * 128 + (lane & 7) * 16, i * 131072);
#pragma unroll
            for (int i = 0; i < 4; ++i) *(LAS u32x4*)(cv + CV_KV + 480 + i * 1024 + lane * 16) = t[i];
        }
        const int r1 = lane & 15, q = lane >> 4;
        const int toff = (r1 & 1) ? CV_HA + 2 * (16 + r1 + 9 - 8 * q) : CV_HB + 2 * (16 + r1 + 10 - 8 * q);
        const int boff = CV_KV + 448 + 32 * r1 + 16 * q;
        f32x4 y[8];
        u32x4 tf[8];
        bf16x8 bf[8];
        const lds_ptr bp = cv + boff - 448;
        lds_ptr tp[4];
#pragma unroll
        for (int i = 0; i < 4; ++i) { tp[i] = cv + toff + 1024 * i; asm volatile("" : "+v"(tp[i])); }
#define LDT(bb, a, hf) do { const LAS unsigned* t_ = (const LAS unsigned*)(tp[(a) >> 1] + 64 * (bb) + 512 * ((a) & 1) + 8 * (hf)); \
            tf[a][2 * (hf)] = t_[0]; tf[a][2 * (hf) + 1] = t_[1]; SBAR(); } while (0)
#define LDB(bb, a) do { bf[a] = lds_rd128(bp + 448 - 64 * (bb) + 512 * (a)); SBAR(); } while (0)
#define CM(a, ww) do { asm volatile("v_mfma_f32_16x16x32_bf16 %0, %1, %2, %0" : "+v"(y[(a) + (ww)]) : "v"(tf[a]), "v"(bf[ww])); SBAR(); } while (0)
#define CM0(a, ww) do { asm volatile("v_mfma_f32_16x16x32_bf16 %0, %1, %2, 0" : "=&v"(y[(a) + (ww)]) : "v"(tf[a]), "v"(bf[ww])); SBAR(); } while (0)
#define PC() do { asm volatile(PACE_OUT); SBAR(); } while (0)
#pragma unroll
        for (int a = 0; a < 8; ++a) { if (a < 5) { LDT(0, a, 0); LDT(0, a, 1); } LDB(0, 7 - a); }
#pragma unroll
        for (int i = 0; i < 8; ++i)
            glds16p<K2_WNT>(wvoff, wsrc(i >> 2, 16 + cw * 4 + (i & 3)), lds0 + (i >> 2) * 16384 + (cw * 4 + (i & 3)) * 1024);
#pragma unroll
        for (int i = 0; i < 4; ++i)
            glds16p<K2_QNT>((unsigned)(idx * 64 + cw * 16 + (lane >> 4)) * 2048u + xcd * 256 + (lane & 15) * 16, Aq + i * 8192, lds0 + K2_SCR + cw * 1024);
        RAW_BAR();
        asm volatile("s_waitcnt vmcnt(4)" ::: "memory");
        lds_add(fl + C_S1, lane);
#define ZFL() PC()
        __builtin_amdgcn_s_setprio(AUX_PRIO);
        const float dch = (ch & 1) ? bfhi(dd) : bflo(dd);
        const lds_ptr kdp = cv + CV_KV + 480 + 32 * r1 + 16 * (q >> 1) + ((q & 1) ? 0 : 8);
        float kd[8][4];
        u32x2 kk;
        unsigned kc0, kc1, zo[8][2];
#define KD_RD(t) do { kk = *(const LAS u32x2*)(kdp + 512 * (t)); SBAR(); } while (0)
#define KD_UP0(t) do { kd[t][0] = bfhi(kk[1]); kd[t][1] = bflo(kk[1]); SBAR(); } while (0)
#define KD_UP1(t) do { kd[t][2] = bfhi(kk[0]); kd[t][3] = bflo(kk[0]); SBAR(); } while (0)
#define KD_MU0(t) do { kd[t][0] = mul_sv(dch, kd[t][0]); kd[t][1] = mul_sv(dch, kd[t][1]); SBAR(); } while (0)
#define KD_MU1(t) do { kd[t][2] = mul_sv(dch, kd[t][2]); kd[t][3] = mul_sv(dch, kd[t][3]); SBAR(); } while (0)
#define KD_CV(t) do { kc0 = cvtpk(kd[t][0], kd[t][1]); kc1 = cvtpk(kd[t][2], kd[t][3]); SBAR(); } while (0)
#define KD_X0(t) do { kd[t][0] = bflo(kc0); kd[t][1] = bfhi(kc0); SBAR(); } while (0)
#define KD_X1(t) do { kd[t][2] = bflo(kc1); kd[t][3] = bfhi(kc1); SBAR(); } while (0)
#define ZF0(t) do { kc0 = cvtpk(y[t][0], y[t][1]); kc1 = cvtpk(y[t][2], y[t][3]); SBAR(); } while (0)
#define ZF1(t) do { y[t][0] = bflo(kc0); y[t][1] = bfhi(kc0); SBAR(); } while (0)
#define ZF2(t) do { y[t][2] = bflo(kc1); y[t][3] = bfhi(kc1); SBAR(); } while (0)
#define ZF3(t) do { y[t][0] = add_vv(y[t][0], kd[t][0]); y[t][1] = add_vv(y[t][1], kd[t][1]); SBAR(); } while (0)
#define ZF4(t) do { y[t][2] = add_vv(y[t][2], kd[t][2]); y[t][3] = add_vv(y[t][3], kd[t][3]); SBAR(); } while (0)
#define ZF5(t) do { zo[t][0] = nz(cvtpk(y[t][0], y[t][1])); zo[t][1] = nz(cvtpk(y[t][2], y[t][3])); SBAR(); } while (0)
#define ZST(pr) do { auto s0 = __builtin_amdgcn_permlane16_swap(zo[2 * (pr)][0], zo[2 * (pr) + 1][0], false, false); \
            auto s1 = __builtin_amdgcn_permlane16_swap(zo[2 * (pr)][1], zo[2 * (pr) + 1][1], false, false); \
            st_sc1(u32x4{s0[0], s1[0], s0[1], s1[1]}, rs, (unsigned)WS_ZT + ch * 4096 + (pr) * 1024 + voff); SBAR(); } while (0)
#define W3(i) do { if ((i) == 0) PC(); } while (0)
#define HF() do { lds_add(fl + C_HF, lane); SBAR(); } while (0)
        CM0(0, 7); LDB(1, 7); CM0(0, 6); LDT(0, 5, 0); CM0(0, 5); LDT(0, 5, 1); CM0(0, 4); LDT(0, 6, 0); CM0(0, 3); LDT(0, 6, 1); CM0(0, 2); LDT(0, 7, 0); CM0(0, 1); LDT(0, 7, 1); CM0(0, 0); KD_RD(0);
        CM(1, 6); LDB(1, 6); CM(1, 5); LDT(1, 0, 0); CM(1, 4); LDT(1, 0, 1); CM(1, 3); PC(); CM(1, 2); PC(); CM(1, 1); KD_UP0(0); CM(1, 0); KD_UP1(0);
        CM(2, 5); LDB(1, 5); CM(2, 4); LDT(1, 1, 0); CM(2, 3); LDT(1, 1, 1); CM(2, 2); KD_MU0(0); CM(2, 1); KD_MU1(0); CM(2, 0); KD_CV(0);
        CM(3, 4); LDB(1, 4); CM(3, 3); LDT(1, 2, 0); CM(3, 2); LDT(1, 2, 1); CM(3, 1); KD_X0(0); CM(3, 0); KD_X1(0);
        CM(4, 3); LDB(1, 3); CM(4, 2); LDT(1, 3, 0); CM(4, 1); LDT(1, 3, 1); CM(4, 0); KD_RD(1);
        CM(5, 2); LDB(1, 2); CM(5, 1); LDT(1, 4, 0); CM(5, 0); LDT(1, 4, 1);
        CM(6, 1); LDB(1, 1); CM(6, 0); PC();
        CM(7, 0); LDB(1, 0);
        CM(0, 7); LDB(2, 7); CM(0, 6); LDT(1, 5, 0); CM(0, 5); LDT(1, 5, 1); CM(0, 4); LDT(1, 6, 0); CM(0, 3); LDT(1, 6, 1); CM(0, 2); LDT(1, 7, 0); CM(0, 1); LDT(1, 7, 1); CM(0, 0); PC();
        CM(1, 6); LDB(2, 6); CM(1, 5); LDT(2, 0, 0); CM(1, 4); LDT(2, 0, 1); CM(1, 3); KD_UP0(1); CM(1, 2); KD_UP1(1); CM(1, 1); KD_MU0(1); CM(1, 0); KD_MU1(1);
        CM(2, 5); LDB(2, 5); CM(2, 4); LDT(2, 1, 0); CM(2, 3); LDT(2, 1, 1); CM(2, 2); KD_CV(1); CM(2, 1); KD_X0(1); CM(2, 0); KD_X1(1);
        CM(3, 4); LDB(2, 4); CM(3, 3); LDT(2, 2, 0); CM(3, 2); LDT(2, 2, 1); CM(3, 1); KD_RD(2); CM(3, 0); PC();
        CM(4, 3); LDB(2, 3); CM(4, 2); LDT(2, 3, 0); CM(4, 1); LDT(2, 3, 1); CM(4, 0); PC();
        CM(5, 2); LDB(2, 2); CM(5, 1); LDT(2, 4, 0); CM(5, 0); LDT(2, 4, 1);
        CM(6, 1); LDB(2, 1); CM(6, 0); KD_UP0(2);
        CM(7, 0); LDB(2, 0);
        CM(0, 7); LDB(3, 7); CM(0, 6); LDT(2, 5, 0); CM(0, 5); LDT(2, 5, 1); CM(0, 4); LDT(2, 6, 0); CM(0, 3); LDT(2, 6, 1); CM(0, 2); LDT(2, 7, 0); CM(0, 1); LDT(2, 7, 1); CM(0, 0); KD_UP1(2);
        CM(1, 6); LDB(3, 6); CM(1, 5); LDT(3, 0, 0); CM(1, 4); LDT(3, 0, 1); CM(1, 3); KD_MU0(2); CM(1, 2); KD_MU1(2); CM(1, 1); KD_CV(2); CM(1, 0); KD_X0(2);
        CM(2, 5); LDB(3, 5); CM(2, 4); LDT(3, 1, 0); CM(2, 3); LDT(3, 1, 1); CM(2, 2); KD_X1(2); CM(2, 1); KD_RD(3); CM(2, 0); PC();
        CM(3, 4); LDB(3, 4); CM(3, 3); LDT(3, 2, 0); CM(3, 2); LDT(3, 2, 1); CM(3, 1); PC(); CM(3, 0); KD_UP0(3);
        CM(4, 3); LDB(3, 3); CM(4, 2); LDT(3, 3, 0); CM(4, 1); LDT(3, 3, 1); CM(4, 0); KD_UP1(3);
        CM(5, 2); LDB(3, 2); CM(5, 1); LDT(3, 4, 0); CM(5, 0); LDT(3, 4, 1);
        CM(6, 1); LDB(3, 1); CM(6, 0); KD_MU0(3);
        CM(7, 0); LDB(3, 0);
        CM(0, 7); LDB(4, 7); CM(0, 6); LDT(3, 5, 0); CM(0, 5); LDT(3, 5, 1); CM(0, 4); LDT(3, 6, 0); CM(0, 3); LDT(3, 6, 1); CM(0, 2); LDT(3, 7, 0); CM(0, 1); LDT(3, 7, 1); CM(0, 0); KD_MU1(3);
        CM(1, 6); LDB(4, 6); CM(1, 5); LDT(4, 0, 0); CM(1, 4); LDT(4, 0, 1); CM(1, 3); KD_CV(3); CM(1, 2); KD_X0(3); CM(1, 1); KD_X1(3); CM(1, 0); KD_RD(4);
        CM(2, 5); LDB(4, 5); CM(2, 4); LDT(4, 1, 0); CM(2, 3); LDT(4, 1, 1); CM(2, 2); PC(); CM(2, 1); PC(); CM(2, 0); KD_UP0(4);
        CM(3, 4); LDB(4, 4); CM(3, 3); LDT(4, 2, 0); CM(3, 2); LDT(4, 2, 1); CM(3, 1); KD_UP1(4); CM(3, 0); KD_MU0(4);
        CM(4, 3); LDB(4, 3); CM(4, 2); LDT(4, 3, 0); CM(4, 1); LDT(4, 3, 1); CM(4, 0); KD_MU1(4);
        CM(5, 2); LDB(4, 2); CM(5, 1); LDT(4, 4, 0); CM(5, 0); LDT(4, 4, 1);
        CM(6, 1); LDB(4, 1); CM(6, 0); KD_CV(4);
        CM(7, 0); LDB(4, 0);
        CM(0, 7); LDB(5, 7); CM(0, 6); LDT(4, 5, 0); CM(0, 5); LDT(4, 5, 1); CM(0, 4); LDT(4, 6, 0); CM(0, 3); LDT(4, 6, 1); CM(0, 2); LDT(4, 7, 0); CM(0, 1); LDT(4, 7, 1); CM(0, 0); KD_X0(4);
        CM(1, 6); LDB(5, 6); CM(1, 5); LDT(5, 0, 0); CM(1, 4); LDT(5, 0, 1); CM(1, 3); KD_X1(4); CM(1, 2); KD_RD(5); CM(1, 1); PC(); CM(1, 0); PC();
        CM(2, 5); LDB(5, 5); CM(2, 4); LDT(5, 1, 0); CM(2, 3); LDT(5, 1, 1); CM(2, 2); KD_UP0(5); CM(2, 1); KD_UP1(5); CM(2, 0); KD_MU0(5);
        CM(3, 4); LDB(5, 4); CM(3, 3); LDT(5, 2, 0); CM(3, 2); LDT(5, 2, 1); CM(3, 1); KD_MU1(5); CM(3, 0); KD_CV(5);
        CM(4, 3); LDB(5, 3); CM(4, 2); LDT(5, 3, 0); CM(4, 1); LDT(5, 3, 1); CM(4, 0); KD_X0(5);
        CM(5, 2); LDB(5, 2); CM(5, 1); LDT(5, 4, 0); CM(5, 0); LDT(5, 4, 1);
        CM(6, 1); LDB(5, 1); CM(6, 0); KD_X1(5);
        CM(7, 0); LDB(5, 0);
        CM(0, 7); LDB(6, 7); CM(0, 6); LDT(5, 5, 0); CM(0, 5); LDT(5, 5, 1); CM(0, 4); LDT(5, 6, 0); CM(0, 3); LDT(5, 6, 1); CM(0, 2); LDT(5, 7, 0); CM(0, 1); LDT(5, 7, 1); CM(0, 0); KD_RD(6);
        CM(1, 6); LDB(6, 6); CM(1, 5); LDT(6, 0, 0); CM(1, 4); LDT(6, 0, 1); CM(1, 3); PC(); CM(1, 2); PC(); CM(1, 1); KD_UP0(6); CM(1, 0); KD_UP1(6);
        CM(2, 5); LDB(6, 5); CM(2, 4); LDT(6, 1, 0); CM(2, 3); LDT(6, 1, 1); CM(2, 2); KD_MU0(6); CM(2, 1); KD_MU1(6); CM(2, 0); KD_CV(6);
        CM(3, 4); LDB(6, 4); CM(3, 3); LDT(6, 2, 0); CM(3, 2); LDT(6, 2, 1); CM(3, 1); KD_X0(6); CM(3, 0); KD_X1(6);
        CM(4, 3); LDB(6, 3); CM(4, 2); LDT(6, 3, 0); CM(4, 1); LDT(6, 3, 1); CM(4, 0); KD_RD(7);
        CM(5, 2); LDB(6, 2); CM(5, 1); LDT(6, 4, 0); CM(5, 0); LDT(6, 4, 1);
        CM(6, 1); LDB(6, 1); CM(6, 0); PC();
        CM(7, 0); LDB(6, 0);
        CM(0, 7); LDB(7, 7); CM(0, 6); LDT(6, 5, 0); CM(0, 5); LDT(6, 5, 1); CM(0, 4); LDT(6, 6, 0); CM(0, 3); LDT(6, 6, 1); CM(0, 2); LDT(6, 7, 0); CM(0, 1); LDT(6, 7, 1); CM(0, 0); PC();
        CM(1, 6); LDB(7, 6); CM(1, 5); LDT(7, 0, 0); CM(1, 4); LDT(7, 0, 1); CM(1, 3); KD_UP0(7); CM(1, 2); KD_UP1(7); CM(1, 1); KD_MU0(7); CM(1, 0); KD_MU1(7);
        CM(2, 5); LDB(7, 5); CM(2, 4); LDT(7, 1, 0); CM(2, 3); LDT(7, 1, 1); CM(2, 2); KD_CV(7); CM(2, 1); KD_X0(7); CM(2, 0); KD_X1(7);
        CM(3, 4); LDB(7, 4); CM(3, 3); LDT(7, 2, 0); CM(3, 2); LDT(7, 2, 1); CM(3, 1); PC(); CM(3, 0); PC();
        CM(4, 3); LDB(7, 3); CM(4, 2); LDT(7, 3, 0); CM(4, 1); LDT(7, 3, 1); CM(4, 0); PC();
        CM(5, 2); LDB(7, 2); CM(5, 1); LDT(7, 4, 0); CM(5, 0); LDT(7, 4, 1);
        CM(6, 1); LDB(7, 1); CM(6, 0); PC();
        CM(7, 0); LDB(7, 0);
        CM(0, 3); LDT(7, 5, 0); CM(0, 2); LDT(7, 5, 1); CM(1, 2); LDT(7, 6, 0); CM(1, 1); LDT(7, 6, 1); CM(2, 1); LDT(7, 7, 0); CM(2, 0); LDT(7, 7, 1);
        CM(3, 0); HF(); CM(0, 1); ZFL(); CM(0, 0); W3(0); CM(1, 0); ZF0(2); W3(1); CM(0, 5); ZF1(2); W3(2); CM(0, 4); ZF2(2); W3(3);
        CM(1, 4); ZF3(2); W3(4); CM(1, 3); ZF4(2); W3(5); CM(2, 3); ZF5(2); W3(6); CM(2, 2); ZF0(3); W3(7); CM(3, 2); ZF1(3);  CM(3, 1); ZF2(3);
        CM(4, 1); ZF3(3);  CM(4, 0); ZF4(3);  CM(5, 0); ZF5(3);  CM(0, 7); ZST(1);  CM(0, 6); ZF0(0);  CM(1, 6); ZF1(0);
        CM(1, 5); ZF2(0);  CM(2, 5); ZF3(0);  CM(2, 4); ZF4(0);  CM(3, 4); ZF5(0);  CM(3, 3); ZF0(1);  CM(4, 3); ZF1(1);
        CM(4, 2); ZF2(1);  CM(5, 2); ZF3(1);  CM(5, 1); ZF4(1);  CM(6, 1); ZF5(1);  CM(6, 0); ZST(0);  CM(7, 0); ZF0(4);
        DG(4);
        ZF1(4); ZF2(4); ZF3(4); ZF4(4); ZF5(4); ZF0(5); ZF1(5); ZF2(5); ZF3(5); ZF4(5); ZF5(5); ZST(2); ZF0(6); ZF1(6); ZF2(6); ZF3(6); ZF4(6); ZF5(6); ZF0(7); ZF1(7); ZF2(7); ZF3(7); ZF4(7); ZF5(7); ZST(3);
        DG(10);
        __builtin_amdgcn_s_setprio(3);
        while ((unsigned)__builtin_amdgcn_readfirstlane(*(volatile LAS unsigned*)(fl + C_OL)) < ZL_GATE) __builtin_amdgcn_s_sleep(4);
        CFENCE();
        const int arow = idx * 64 + (cw >> 1) * 32 + rg * 4, acol = xcd * 256 + (cw & 1) * 128 + cq * 16;
        u32x4 qr[4];
#pragma unroll
        for (int i = 0; i < 4; ++i) qr[i] = gld128((unsigned)(arow * 2048 + acol), Aq + i * 2048);
        {
            const lds_ptr tr = cv + CV_KV + 480;
            u32x4 t[4];
            const int ck = (cw >> 1) * 4 + ((lane ^ ((lane >> 5) * 2)) & 3);
            const unsigned zb = (unsigned)WS_ZT + (unsigned)(xcd * 128 + (cw & 1) * 64 + (lane >> 4) * 4) * 4096u + (idx >> 3) * 1024 +
                                ((idx & 3) * 4 + ((idx >> 2) & 1) * 16 + (ck >> 1) + (ck & 1) * 32) * 16;
#pragma unroll 1
            for (unsigned spins = 0; spins < (1u << 20); ++spins) {
#pragma unroll
                for (int i = 0; i < 4; ++i) t[i] = ld_sc1s(rs, zb + ((((lane >> 2) & 3) ^ i) << 12), i * 65536);
                if (!missing(mx4(mx4(mx8(t[0], t[1]), t[2]), t[3]))) break;
                __builtin_amdgcn_s_sleep(Z_SLEEP);
            }
            DG(13);
            __builtin_amdgcn_s_setprio(ZE_PRIO);
#pragma unroll
            for (int i = 0; i < 4; ++i) *(LAS u32x4*)(tr + i * 1024 + lane * 16) = t[i];
#pragma unroll
            for (int i = 0; i < 4; ++i) asm volatile("" : "+v"(qr[i]));
            u32x2 zc[8];
#pragma unroll
            for (int c = 0; c < 8; ++c)
                zc[c] = *(const LAS u32x2*)(tr + cq * 512 + (c >> 2) * 256 + ((((c & 3) * 4 + (rg >> 1)) ^ (cq * 2)) << 4) + (rg & 1) * 8);
#pragma unroll
            for (int i = 0; i < 4; ++i) {
                u32x4 o;
#pragma unroll
                for (int c = 0; c < 4; ++c) {
                    const unsigned z0 = zc[2 * c][i >> 1], z1 = zc[2 * c + 1][i >> 1];
                    o[c] = cvtpk(mul_vv(bflo(qr[i][c]), (i & 1) ? bfhi(z0) : bflo(z0)), mul_vv(bfhi(qr[i][c]), (i & 1) ? bfhi(z1) : bflo(z1)));
                }
                __builtin_amdgcn_raw_buffer_store_b128(o, ro, arow * 4096 + acol, i * 4096, OUT_AUX);
            }
        }
        asm volatile(".Lcp_k2z:");
        DG_END(14);
        return;
    }

    const int half = w >> 2;
    const int TA = qb * 8 + (w & 3) * 2 + half, TB = TA ^ 1;
    bf16x8 sel;
    {
        const short one = (((lane >> 2) & 1) == ((lane >> 4) & 1) && (lane & 11) == 0) ? (short)0x3f80 : (short)0;
#pragma unroll
        for (int i = 0; i < 8; ++i) sel[i] = one;
    }
    f32x16 oA = f32x16{}, oB = f32x16{};
    f32x4 lA = f32x4{}, lB = f32x4{};
    f32x16 negm, sA, sB;
    lds_ptr lp = lds + half * 16384 + lane * 16;
    bf16x8 k0[2], k1[2], v0[2], v1[2];
    u32x4 p0, p1 = {0u, 0u, 0u, 0u};
#define EX(X, i) X[i] = __builtin_amdgcn_exp2f(X[i])
    lds_ptr qpa = lds + K2_SF + (TA & 7) * 2048 + lane * 16, qpb = lds + K2_SF + (TB & 7) * 2048 + lane * 16;
    asm volatile("" : "+v"(qpa), "+v"(qpb), "+v"(lp), "+v"(sel), "+v"(oA), "+v"(oB), "+v"(lA), "+v"(lB), "+v"(p1));
    WAIT_BAR(0);
    DG(3);
    const bf16x8 qa0 = lds_rd128(qpa), qb0 = lds_rd128(qpb);
    const bf16x8 x0 = lds_rd128(lds + lane * 16), x1 = lds_rd128(lds + lane * 16 + 1024);
    const bf16x8 qa1 = lds_rd128(qpa + 1024), qb1 = lds_rd128(qpb + 1024);
    {
        k0[0] = lds_rd128(lp); k1[0] = lds_rd128(lp + 1024);
        v0[0] = lds_rd128(lp + 2048); v1[0] = lds_rd128(lp + 3072);
        sA = MFMA32(x0, qa0, f32x16{});
        sB = MFMA32(x0, qb0, f32x16{});
        sA = MFMA32(x1, qa1, sA);
        sB = MFMA32(x1, qb1, sB);
        float m = fmx(sA[0], sA[1]), m2 = fmx(sB[0], sB[1]);
#pragma unroll
        for (int i = 2; i < 16; i += 2) m = fmx(fmx(m, sA[i]), sA[i + 1]);
#pragma unroll
        for (int i = 2; i < 16; i += 2) m2 = fmx(fmx(m2, sB[i]), sB[i + 1]);
        m = fmx(m, m2);
        auto rr = __builtin_amdgcn_permlane32_swap(__float_as_uint(m), __float_as_uint(m), false, false);
        m = fmx(__uint_as_float(rr[0]), __uint_as_float(rr[1]));
#pragma unroll
        for (int i = 0; i < 16; ++i) negm[i] = -m;
        sA = MFMA32(k0[0], qa0, negm);
        sA = MFMA32(k1[0], qa1, sA);
        v1[1] = v1[0];
        SBAR();
        RAW_BAR();
        SBAR();
        EX(sA, 0); EX(sA, 1);
    }
    DG(10);
#define SUM16(l, p) asm volatile("v_mfma_f32_16x16x32_bf16 %0, %1, %2, %0" : "+v"(l) : "v"(sel), "v"(p))
#define HALF(C, oC, lC, N, qx, qy, ka, kb, oP, lP, vprev, vcur0, PRE) do { \
        oP = MFMA32(vprev, PB(p1), oP); SBAR(); \
        PRE; EX(C, 2); EX(C, 3); EX(C, 4); SBAR(); \
        N = MFMA32(ka, qx, negm); SBAR(); \
        EX(C, 5); EX(C, 6); EX(C, 7); SBAR(); \
        SUM16(lP, p1); SBAR(); \
        p0[0] = cvtpk(C[0], C[1]); p0[1] = cvtpk(C[2], C[3]); SBAR(); \
        N = MFMA32(kb, qy, N); SBAR(); \
        p0[2] = cvtpk(C[4], C[5]); p0[3] = cvtpk(C[6], C[7]); SBAR(); EX(C, 8); EX(C, 9); SBAR(); \
        oC = MFMA32(vcur0, PB(p0), oC); SBAR(); \
        EX(C, 10); EX(C, 11); EX(C, 12); SBAR(); \
        SUM16(lC, p0); SBAR(); \
        EX(C, 13); EX(C, 14); EX(C, 15); SBAR(); \
        p1[0] = cvtpk(C[8], C[9]); p1[1] = cvtpk(C[10], C[11]); p1[2] = cvtpk(C[12], C[13]); p1[3] = cvtpk(C[14], C[15]); SBAR(); \
        EX(N, 0); EX(N, 1); SBAR(); \
    } while (0)
#define RD4(Y, nx) do { v1[Y] = lds_rd128((nx) + 3072); v0[Y] = lds_rd128((nx) + 2048); \
        k1[Y] = lds_rd128((nx) + 1024); k0[Y] = lds_rd128(nx); } while (0)
#define KT(X, nx, PRE0) do { \
        HALF(sA, oA, lA, sB, qb0, qb1, k0[X], k1[X], oB, lB, v1[X ^ 1], v0[X], PRE0; RD4(X ^ 1, nx)); \
        HALF(sB, oB, lB, sA, qa0, qa1, k0[X ^ 1], k1[X ^ 1], oA, lA, v1[X], v0[X], (void)0); \
    } while (0)
#define RING_BAR() do { unsigned long long sv_; \
        asm volatile("s_cmp_eq_u32 %1, 7\n\ts_cbranch_scc1 1f\n\ts_barrier\n1:\n\ts_mov_b64 %0, exec\n\ts_mov_b64 exec, 1\n\tds_add_u32 %2, %3\n\ts_mov_b64 exec, %0" \
                     : "=&s"(sv_) : "s"(st), "v"(fl + C_LD), "v"(1u) : "memory", "scc"); } while (0)
    SBAR();
#pragma unroll 1
    for (int st = 0; st < 8; ++st) {
        const lds_ptr cur = lp + (st & 1) * K2_SLOT;
        const lds_ptr nxt = lp + ((st + 1) & 1) * K2_SLOT;
        PRIO(2);
        KT(0, cur + 4096, (void)0);
        PRIO(1);
        KT(1, cur + 8192, (void)0);
        PRIO(0);
        KT(0, cur + 12288, (void)0);
        PRIO(3);
        KT(1, nxt, RAW_BAR(); SBAR());
    }
    oB = MFMA32(v1[1], PB(p1), oB);
    SBAR();
    SUM16(lB, p1);
    asm volatile("s_nop 7" : "+v"(oB) :: "memory");
    SBAR();
    DG(4);
    {
        const lds_ptr xo = lds + K2_SLOT + w * 4096 + lane * 16;
        *(LAS float*)(lds + K2_LX + w * 256 + lane * 4) = lB[0];
#pragma unroll
        for (int i = 0; i < 4; ++i) *(LAS f32x4*)(xo + i * 1024) = f32x4{oB[4 * i], oB[4 * i + 1], oB[4 * i + 2], oB[4 * i + 3]};
        lds_add(fl + F_X + w * 4, lane);
        lds_add(fl + C_END + half * 4, lane);
        lds_wait_ge(fl + F_X + (w ^ 4) * 4, 1u);
        const lds_ptr xi = lds + K2_SLOT + (w ^ 4) * 4096 + lane * 16;
        f32x4 t[4];
        const float lx = *(const LAS float*)(lds + K2_LX + (w ^ 4) * 256 + (lane & 31) * 4);
        { auto rr = __builtin_amdgcn_permlane32_swap(__float_as_uint(lA[0]), __float_as_uint(lA[0]), false, false); lA[0] = __uint_as_float(rr[0]); }
#pragma unroll
        for (int i = 0; i < 4; ++i) t[i] = *(const LAS f32x4*)(xi + i * 1024);
        SBAR();
        lA[0] += lx;
#pragma unroll
        for (int i = 0; i < 16; ++i) { float o = oA[i]; acc_vv(o, t[i >> 2][i & 3]); oA[i] = o; }
    }
    DG(9);
    const float l = lA[0];
    u32x4 on0, on1;
    auto finish = [&](float ls) {
        float rl = __builtin_amdgcn_rcpf(ls);
        asm volatile("s_nop 1" : "+v"(rl));
        f32x16 o;
#pragma unroll
        for (int i = 0; i < 16; ++i) o[i] = mul_vv(oA[i], rl);
        on0 = nz4(pack8n(o, 0)); on1 = nz4(pack8n(o, 1));
    };
    finish(l);
    {
        float t0, t1;
        asm("v_mul_f32_e32 %0, 0, %2\n\tv_mul_f32_e32 %1, 0, %3\n\tv_fmac_f32_e32 %0, 0, %4\n\tv_fmac_f32_e32 %1, 0, %5\n\tv_fmac_f32_e32 %0, 0, %6\n\tv_fmac_f32_e32 %1, 0, %7\n\tv_fmac_f32_e32 %0, 0, %8\n\tv_fmac_f32_e32 %1, 0, %9\n\t"
            "v_fmac_f32_e32 %0, 0, %10\n\tv_fmac_f32_e32 %1, 0, %11\n\tv_fmac_f32_e32 %0, 0, %12\n\tv_fmac_f32_e32 %1, 0, %13\n\tv_fmac_f32_e32 %0, 0, %14\n\tv_fmac_f32_e32 %1, 0, %15\n\tv_fmac_f32_e32 %0, 0, %16\n\tv_fmac_f32_e32 %1, 0, %17"
            : "=&v"(t0), "=&v"(t1) : "v"(oA[0]), "v"(oA[1]), "v"(oA[2]), "v"(oA[3]), "v"(oA[4]), "v"(oA[5]), "v"(oA[6]), "v"(oA[7]), "v"(oA[8]), "v"(oA[9]), "v"(oA[10]), "v"(oA[11]), "v"(oA[12]), "v"(oA[13]), "v"(oA[14]), "v"(oA[15]));
        const bool ok = (l <= 1.0e30f) && (l >= 1.0e-18f) && (t0 + t1 == 0.f);
        if (__builtin_expect(__any(!ok), 0)) {
            float m = -INFINITY, ls = 0.f;
            oA = f32x16{};
#pragma unroll 1
            for (int t = 0; t < 64; ++t) {
                const char* g = kvg + t * 4096 + lane * 16;
                const bf16x8 a0 = *(const bf16x8*)g, a1 = *(const bf16x8*)(g + 1024);
                const bf16x8 b0 = *(const bf16x8*)(g + 2048), b1 = *(const bf16x8*)(g + 3072);
                f32x16 s = MFMA32(a0, qa0, f32x16{});
                s = MFMA32(a1, qa1, s);
                float tm = s[0];
#pragma unroll
                for (int i = 1; i < 16; ++i) tm = __builtin_fmaxf(tm, s[i]);
                auto rr = __builtin_amdgcn_permlane32_swap(__float_as_uint(tm), __float_as_uint(tm), false, false);
                tm = __builtin_fmaxf(__uint_as_float(rr[0]), __uint_as_float(rr[1]));
                const float mn = __builtin_fmaxf(m, tm);
                const float alpha = __builtin_amdgcn_exp2f(m - mn);
                m = mn;
                float ps = 0.f;
#pragma unroll
                for (int i = 0; i < 16; ++i) { s[i] = __builtin_amdgcn_exp2f(s[i] - mn); ps += s[i]; oA[i] *= alpha; }
                ls = ls * alpha + ps;
                oA = MFMA32(b0, PB(pack8n(s, 0)), oA);
                oA = MFMA32(b1, PB(pack8n(s, 1)), oA);
            }
            auto rr = __builtin_amdgcn_permlane32_swap(__float_as_uint(ls), __float_as_uint(ls), false, false);
            asm volatile("s_nop 7\n\ts_nop 7" : "+v"(oA));
            finish(__uint_as_float(rr[0]) + __uint_as_float(rr[1]));
        }
    }
    u32x4 w3[4];
    {
        const unsigned og = (unsigned)WS_O + (unsigned)(TA * 32 + h) * 2048u + lane * 16;
        st_sc1(on0, rs, og);
        st_sc1(on1, rs, og + 1024);
        lds_add(fl + C_S1, lane);
        const char* s2 = wsrc(half ^ 1, 32 + (w & 3) * 4);
        const char* s3 = wsrc(half, 48 + (w & 3) * 4);
        lds_wait_ge(fl + C_S1, 12u);
        DG(5);
#pragma unroll
        for (int i = 0; i < 4; ++i)
            glds16p<K2_WNT>(wvoff, s2 + i * 16384, lds0 + K2_SLOT + ((w ^ 4) * 4 + i) * 1024);
#pragma unroll
        for (int i = 0; i < 4; ++i) w3[i] = gld128(wvoff, s3 + i * 16384);
    }
    {
        const int half = w & 1;
        const int T3 = tt * 4 + (w >> 1);
        const unsigned og = (unsigned)WS_O + (unsigned)T3 * 65536u + half * 32768 + lane * 16;
        const int r = lane & 31, hh = lane >> 5;
        const int g = lane >> 4, q = (lane & 15) >> 2, p = lane & 3;
        int woff[2];
#pragma unroll
        for (int n = 0; n < 2; ++n) {
            const int c = 4 * (n ^ half) + 2 * (p & 1) + (g & 1);
            woff[n] = (4 * hh + q) * 128 + ((c ^ ((q >> 1) << 2)) << 4) + (p >> 1) * 8;
        }
        f32x16 acc[2] = {f32x16{}, f32x16{}};
        lds_ptr wl[3][2] = {{lds + half * 16384 + woff[0], lds + half * 16384 + woff[1]},
                            {lds + K2_SF + half * 16384 + woff[0], lds + K2_SF + half * 16384 + woff[1]},
                            {lds + K2_CV + half * 2 * CV_BYTES + woff[0], lds + K2_CV + half * 2 * CV_BYTES + woff[1]}};
        asm volatile("" : "+v"(wl[0][0]), "+v"(wl[0][1]), "+v"(wl[1][0]), "+v"(wl[1][1]), "+v"(wl[2][0]), "+v"(wl[2][1]));
#define MS(m) ((m) < 64 - 2 * TL ? (m) >> 1 : 32 - TL + ((m) - (64 - 2 * TL)) % TL)
#define MN(m) ((m) < 64 - 2 * TL ? (m) & 1 : (m) < 64 - TL)
        constexpr int WR = 2 * WD + 2;
        s16x4 wa[WR], wb[WR];
#define LDW(m) do { const int s_ = MS(m), f_ = s_ < 8 ? 1 : s_ < 24 ? 0 : 2; \
            const int o_ = f_ == 2 ? ((s_ >> 2) & 1) * CV_BYTES + (s_ & 3) * 2048 : (f_ ? 0 : ((s_ >> 3) - 1) * K2_SLOT) + (s_ & 7) * 2048; \
            wa[(m) % WR] = lds_tr(wl[f_][MN(m)] + o_); wb[(m) % WR] = lds_tr(wl[f_][MN(m)] + o_ + 1024); SBAR(); } while (0)
#pragma unroll
        for (int m = 0; m < 2 * WD; ++m) LDW(m);
        u32x4 of[OWIN];
#define LDO(i) do { of[(i) % OWIN] = ld_sc1s(rs, og, (((i) + krot) & 31) * 1024); SBAR(); } while (0)
        for (unsigned spins = 0; spins < (1u << 20); ++spins) {
#pragma unroll
            for (int i = 0; i < OW0; ++i) LDO(i);
            unsigned m = mx8(of[0], of[1]);
#pragma unroll
            for (int i = 2; i < OW0; ++i) m = mx4(m, of[i]);
            if (!missing(m)) break;
            DGC(12);
        }
        lds_add(fl + C_S2, lane);
#pragma unroll
        for (int i = OW0; i < OWIN; ++i) LDO(i);
        DG(6);
        __builtin_amdgcn_s_setprio(P3_PRIO);
        SBAR();
        unsigned fv = 0;
#define FLAG_RD(a) do { fv = *(volatile LAS unsigned*)(a); SBAR(); } while (0)
#define FLAG_CHK(a, n) do { if (__builtin_expect((unsigned)__builtin_amdgcn_readfirstlane(fv) < (n), 0)) lds_wait_ge(a, n); CFENCE(); SBAR(); } while (0)
        lds_ptr xo = lds + K2_SF + w * 4096 + lane * 16, xi = lds + K2_SF + (w ^ 1) * 4096 + lane * 16;
        int bo = (T3 * 32 + (lane & 15)) * 4096 + 2048 + nt * 128 + half * 64 + g * 16;
        lds_ptr w3d = lds + K2_CV + (w >> 1) * CV_BYTES + (w & 1) * 4096 + lane * 16;
        asm volatile("" : "+v"(xo), "+v"(xi), "+v"(bo), "+v"(w3d));
        f32x4 xt[4];
        constexpr int M3 = TL > 8 ? 64 - 2 * TL + TL - 8 - 2 * WD : 48 - 2 * WD;
        sfor(std::make_integer_sequence<int, 64>{}, [&](auto mc) __attribute__((always_inline)) {
            constexpr int m = decltype(mc)::value, s = MS(m), n = MN(m);
            constexpr bool first = m < 64 - 2 * TL ? n == 0 : n == 1;
            constexpr bool last = m < 64 - 2 * TL ? n == 1 : n == 0;
            acc[n] = MFMA32(cat8(wa[m % WR], wb[m % WR]), PB(of[s % OWIN]), acc[n]);
            SBAR();
            if constexpr (m == 32 - 2 * WD) FLAG_CHK(fl + C_S2, 8u);
            if constexpr (m == M3) FLAG_CHK(fl + C_S3, 8u);
            if constexpr (m + 2 * WD < 64) LDW(m + 2 * WD);
            if constexpr (first && (s & 1) && s + 1 >= OW0 && s + 1 < 32) {
                if (__builtin_expect(missing(mx8(of[(s + 1) % OWIN], of[(s + 2) % OWIN])), 0)) {
#pragma unroll 1
                    for (unsigned spins = 0; spins < (1u << 20); ++spins) {
                        __builtin_amdgcn_s_sleep(2);
                        LDO(s + 1); LDO(s + 2);
                        DGC(13);
                        if (!missing(mx8(of[(s + 1) % OWIN], of[(s + 2) % OWIN]))) break;
                    }
                }
            }
            if constexpr (last && s + OWIN < 32) LDO(s + OWIN);
            if constexpr (m == 15 - 2 * WD) lds_add(fl + C_U0, lane);
            if constexpr (m == 29 - 2 * WD) FLAG_RD(fl + C_S2);
            if constexpr (m == W3M - 3) FLAG_RD(fl + C_HF);
            if constexpr (m == W3M) FLAG_CHK(fl + C_HF, 4u);
            if constexpr (m >= W3M && m < W3M + 8 && ((m - W3M) & 1) == 0) {
                asm volatile("" : "+v"(w3[((m - W3M) >> 1) & 3]));
                *(LAS u32x4*)(w3d + (((m - W3M) >> 1) & 3) * 1024) = w3[((m - W3M) >> 1) & 3];
            }
            if constexpr (m == W3M + 6) lds_add(fl + C_S3, lane);
            if constexpr (m == M3 - 3) FLAG_RD(fl + C_S3);
            if constexpr (m == 64 - TL - 3) FLAG_RD(fl + C_U0);
            if constexpr (m == 64 - TL) FLAG_CHK(fl + C_U0, 8u);
            if constexpr (m == 64 - TL + 1 || m == 64 - TL + 2) {
                constexpr int i = 2 * (m - (64 - TL + 1));
                *(LAS f32x4*)(xo + i * 1024) = f32x4{acc[1][4 * i], acc[1][4 * i + 1], acc[1][4 * i + 2], acc[1][4 * i + 3]};
                *(LAS f32x4*)(xo + i * 1024 + 1024) = f32x4{acc[1][4 * i + 4], acc[1][4 * i + 5], acc[1][4 * i + 6], acc[1][4 * i + 7]};
            }
            if constexpr (m == 64 - TL + 2) lds_add(fl + F_R + w * 4, lane);
            if constexpr (m == XR_AT - 2) FLAG_RD(fl + F_R + (w ^ 1) * 4);
            if constexpr (m == XR_AT) {
                FLAG_CHK(fl + F_R + (w ^ 1) * 4, 1u);
#pragma unroll
                for (int i = 0; i < 4; ++i) xt[i] = *(const LAS f32x4*)(xi + i * 1024);
            }
            if constexpr (ZL_GATE && last && s + OWIN == ZL_AT) lds_add(fl + C_OL, lane);
            if constexpr (m == 2 * P3_T1 + 1) __builtin_amdgcn_s_setprio(P3_PRIO - 1);
            SBAR();
            if constexpr (m == 31) DG(11);
            if constexpr (m == 47) DG(14);
        });
        asm volatile("s_nop 11" : "+v"(acc[0]));
        DG(7);
#pragma unroll
        for (int i = 0; i < 16; ++i) { float o = xt[i >> 2][i & 3]; acc_vv(o, acc[0][i]); acc[1][i] = o; }
        u32x4 pa = pack8n(acc[1], 0), pb = pack8n(acc[1], 1);
#pragma unroll
        for (int i = 0; i < 4; ++i) {
            auto sw = __builtin_amdgcn_permlane16_swap(pa[i], pb[i], false, false);
            pa[i] = sw[0]; pb[i] = sw[1];
        }
        asm volatile("" :: "v"(acc[0]));
        __builtin_amdgcn_raw_buffer_store_b128(pa, ro, bo, 0, OUT_AUX);
        __builtin_amdgcn_raw_buffer_store_b128(pb, ro, bo, 65536, OUT_AUX);
    }
    DG_END(8);
}

extern "C" void kernel_launch(void* const* d_in, const int* in_sizes, int n_in,
                              void* d_out, int out_size, void* d_ws, size_t ws_size,
                              hipStream_t stream) {
    const void* A_q = d_in[0];
    const void* A_k = d_in[1];
    const void* A_v = d_in[2];
    const void* A_shift = d_in[3];
    const void* A_ssmk = d_in[4];
    const void* A_D = d_in[5];
    const void* B_x = d_in[6];
    const void* B_wqkv = d_in[7];
    const void* B_wo = d_in[8];
    char* ws = (char*)d_ws;
    k1_qkv<<<256, 512, 0, stream>>>((const char*)B_x, (const char*)B_wqkv, ws);
    k2_attn<<<256, 768, 0, stream>>>(ws, (const char*)A_q, (const char*)A_k, (const char*)A_v, (const char*)A_shift,
                                     (const char*)A_ssmk, (const unsigned*)A_D, (const char*)B_wo, (char*)d_out);
}
```

```cpp
#include <hip/hip_runtime.h>
#include <hip/hip_bf16.h>
#include <stdint.h>
#include <utility>

typedef short bf16x8 __attribute__((ext_vector_type(8)));
typedef short s16x4 __attribute__((ext_vector_type(4)));
typedef float f32x16 __attribute__((ext_vector_type(16)));
typedef float f32x4 __attribute__((ext_vector_type(4)));
typedef float f32x2 __attribute__((ext_vector_type(2)));
typedef unsigned u32x4 __attribute__((ext_vector_type(4)));
typedef unsigned u32x2 __attribute__((ext_vector_type(2)));
typedef __bf16 bf16x2_t __attribute__((ext_vector_type(2)));

#define LAS __attribute__((address_space(3)))
typedef LAS char* lds_ptr;
#define DI __device__ __forceinline__
#define SBAR() __builtin_amdgcn_sched_barrier(0)
#define MFMA32(a, b, c) __builtin_amdgcn_mfma_f32_32x32x16_bf16((a), (b), (c), 0, 0, 0)
#define MFMA16(a, b, c) __builtin_amdgcn_mfma_f32_16x16x32_bf16((a), (b), (c), 0, 0, 0)
typedef __bf16 bfx8_t __attribute__((ext_vector_type(8)));
typedef __bf16 bfx16_t __attribute__((ext_vector_type(16)));
typedef float f32x8 __attribute__((ext_vector_type(8)));
#define SPM 1
#define WAIT_BAR(N) asm volatile("s_waitcnt vmcnt(" #N ") lgkmcnt(0)\n\ts_barrier" ::: "memory")
#define LGKM_BAR() asm volatile("s_waitcnt lgkmcnt(0)\n\ts_barrier" ::: "memory")

#define DG_DECL(K)
#define DG(i)
#define DG_END(i)
#define DGC(i)

constexpr int S_LEN = 2048, DM = 1024, NH = 32;

#define KQ 1
#define OQ 0
#define QT(h, T) (KQ ? (h) * 64 + (T) : (T) * 32 + (h))
constexpr size_t WS_Q = 0;
constexpr size_t WS_KV = 4u << 20;
constexpr size_t WS_O = OQ ? WS_Q : 12u << 20;
constexpr size_t WS_KVR = 16u << 20;
#define ZK 0
constexpr size_t WS_ZT = ZK ? WS_KVR : 20u << 20;
constexpr size_t WS_CNT = 24u << 20;

template <int... I, class F> DI void sfor(std::integer_sequence<int, I...>, F&& f) { (f(std::integral_constant<int, I>{}), ...); }
DI unsigned cvtpk(float lo, float hi) {
    f32x2 v = {lo, hi};
    return __builtin_bit_cast(unsigned, __builtin_convertvector(v, bf16x2_t));
}
DI float bflo(unsigned u) { return __uint_as_float(u << 16); }
DI float bfhi(unsigned u) { return __uint_as_float(u & 0xffff0000u); }

DI void glds16(unsigned voff, const void* sbase, unsigned lds_dst) {
    unsigned sv;
    asm volatile("s_mov_b32 %0, m0\n\ts_mov_b32 m0, %3\n\ts_nop 0\n\tglobal_load_lds_dwordx4 %1, %2\n\ts_mov_b32 m0, %0"
                 : "=&s"(sv) : "v"(voff), "s"(sbase), "s"(lds_dst) : "memory");
}
template <int NT> DI void glds16p(unsigned voff, const void* sbase, unsigned lds_dst) {
    if constexpr (NT) {
        unsigned sv;
        asm volatile("s_mov_b32 %0, m0\n\ts_mov_b32 m0, %3\n\ts_nop 0\n\tglobal_load_lds_dwordx4 %1, %2 nt\n\ts_mov_b32 m0, %0"
                     : "=&s"(sv) : "v"(voff), "s"(sbase), "s"(lds_dst) : "memory");
    } else glds16(voff, sbase, lds_dst);
}
#define K1_XNT 0
#define K1_WNT 0
#define K2_SNT 1
#define K2_QNT 0
#define K2_WNT 0
#define OUT_AUX 16
#define K1_SAUX 16
DI bf16x8 lds_rd128(lds_ptr p) { return *(const LAS bf16x8*)p; }
DI s16x4 lds_tr(lds_ptr p) {
    return __builtin_bit_cast(s16x4, __builtin_amdgcn_ds_read_tr16_b64_v4i16((LAS s16x4*)p));
}
DI bf16x8 cat8(s16x4 a, s16x4 b) { return __builtin_shufflevector(a, b, 0, 1, 2, 3, 4, 5, 6, 7); }
DI u32x4 pack8(const f32x16& x, int s, float sc) {
    u32x4 p;
    p[0] = cvtpk(x[8 * s + 0] * sc, x[8 * s + 1] * sc);
    p[1] = cvtpk(x[8 * s + 2] * sc, x[8 * s + 3] * sc);
    p[2] = cvtpk(x[8 * s + 4] * sc, x[8 * s + 5] * sc);
    p[3] = cvtpk(x[8 * s + 6] * sc, x[8 * s + 7] * sc);
    return p;
}
DI u32x4 pack8n(const f32x16& x, int s) {
    u32x4 p;
    p[0] = cvtpk(x[8 * s + 0], x[8 * s + 1]);
    p[1] = cvtpk(x[8 * s + 2], x[8 * s + 3]);
    p[2] = cvtpk(x[8 * s + 4], x[8 * s + 5]);
    p[3] = cvtpk(x[8 * s + 6], x[8 * s + 7]);
    return p;
}
DI void rnd2(float& a, float& b) { unsigned u = cvtpk(a, b); a = bflo(u); b = bfhi(u); }

#define CONV_PACE()
#define RAW_BAR() asm volatile("s_barrier" ::: "memory")
DI unsigned gld32(unsigned voff, const void* sbase) {
    unsigned r;
    asm volatile("global_load_dword %0, %1, %2" : "=v"(r) : "v"(voff), "s"(sbase) : "memory");
    return r;
}
DI u32x2 gld64(unsigned voff, const void* sbase) {
    u32x2 r;
    asm volatile("global_load_dwordx2 %0, %1, %2" : "=v"(r) : "v"(voff), "s"(sbase) : "memory");
    return r;
}
DI float mul_sv(float s, float v) { float r; asm("v_mul_f32_e32 %0, %1, %2" : "=v"(r) : "s"(s), "v"(v)); return r; }
DI float mul_vv(float a, float b) { float r; asm("v_mul_f32_e32 %0, %1, %2" : "=v"(r) : "v"(a), "v"(b)); return r; }
DI float add_vv(float a, float b) { float r; asm("v_add_f32_e32 %0, %1, %2" : "=v"(r) : "v"(a), "v"(b)); return r; }
DI void acc_vv(float& a, float b) { asm("v_add_f32_e32 %0, %0, %1" : "+v"(a) : "v"(b)); }
DI void fmac_vv(float& acc, float a, float b) { asm("v_fmac_f32_e32 %0, %1, %2" : "+v"(acc) : "v"(a), "v"(b)); }
DI void fmac_sv(float& acc, float s, float v) { asm("v_fmac_f32_e32 %0, %1, %2" : "+v"(acc) : "s"(s), "v"(v)); }
typedef __attribute__((address_space(1))) unsigned gu32;
#define RLX_AGENT __ATOMIC_RELAXED, __HIP_MEMORY_SCOPE_AGENT
#define CFENCE() asm volatile("" ::: "memory")
#define DRAIN() asm volatile("s_waitcnt vmcnt(0)" ::: "memory")
constexpr int AUX_SC1 = 16;
DI u32x4 ld_sc1(__amdgpu_buffer_rsrc_t rs, unsigned off) { return __builtin_amdgcn_raw_buffer_load_b128(rs, (int)off, 0, AUX_SC1); }
DI u32x4 ld_sc1s(__amdgpu_buffer_rsrc_t rs, unsigned off, int soff) { return __builtin_amdgcn_raw_buffer_load_b128(rs, (int)off, soff, AUX_SC1); }
DI void st_sc1(u32x4 v, __amdgpu_buffer_rsrc_t rs, unsigned off) { __builtin_amdgcn_raw_buffer_store_b128(v, rs, (int)off, 0, AUX_SC1); }
constexpr unsigned EMPTY = 0xffffffffu;
DI unsigned nz(unsigned x) { return x < EMPTY - 1u ? x : EMPTY - 1u; }
DI u32x4 nz4(u32x4 v) { return u32x4{nz(v[0]), nz(v[1]), nz(v[2]), nz(v[3])}; }
DI float fmx(float a, float b) { return __builtin_elementwise_maximum(a, b); }
DI unsigned umax(unsigned a, unsigned b) { return a > b ? a : b; }
DI unsigned umax3(unsigned a, unsigned b, unsigned c) { return umax(umax(a, b), c); }
DI unsigned mx4(unsigned m, u32x4 v) { return umax3(umax3(m, v[0], v[1]), v[2], v[3]); }
DI unsigned mx8(u32x4 a, u32x4 b) { return umax(umax3(umax3(umax3(a[0], a[1], a[2]), a[3], b[0]), b[1], b[2]), b[3]); }
DI bool missing(unsigned m) { return __builtin_amdgcn_ballot_w64(m == EMPTY) != 0; }
DI unsigned lds_add_rtn(lds_ptr p, int lane) {
    unsigned old = 0;
    CFENCE();
    if (lane == 0) old = __hip_atomic_fetch_add((LAS unsigned*)p, 1u, __ATOMIC_RELAXED, __HIP_MEMORY_SCOPE_WORKGROUP);
    old = __builtin_amdgcn_readfirstlane(old);
    CFENCE();
    return old;
}
DI void lds_add(lds_ptr p, int lane) {
    unsigned long long sv;
    asm volatile("s_mov_b64 %0, exec\n\ts_mov_b64 exec, 1\n\tds_add_u32 %1, %2\n\ts_mov_b64 exec, %0"
                 : "=&s"(sv) : "v"(p), "v"(1u) : "memory");
}
DI void lds_wait_ge(lds_ptr p, unsigned v) {
    CFENCE();
    while ((unsigned)__builtin_amdgcn_readfirstlane(*(volatile LAS unsigned*)p) < v) __builtin_amdgcn_s_sleep(1);
    CFENCE();
}
DI void poll_ge(gu32* word, unsigned want) {
    CFENCE();
    for (unsigned spins = 0; spins < (1u << 22); ++spins) {
        if ((unsigned)__builtin_amdgcn_readfirstlane(__hip_atomic_load(word, RLX_AGENT)) >= want) break;
        __builtin_amdgcn_s_sleep(1);
    }
    __builtin_amdgcn_fence(__ATOMIC_ACQUIRE, "wavefront");
    CFENCE();
}
#define PB(x) __builtin_bit_cast(bf16x8, x)
#define PRIO(x) do { __builtin_amdgcn_s_setprio(x); SBAR(); } while (0)
DI u32x4 gld128(unsigned voff, const void* sbase) {
    u32x4 r;
    asm volatile("global_load_dwordx4 %0, %1, %2" : "=v"(r) : "v"(voff), "s"(sbase) : "memory");
    return r;
}

#define K1_UNR 15
#define K1_LA 5
#define K1_ROT ((tb & 3) * 4)
#define K1_PF 0
#define K1_PG 0
#define K1_TW 0
#define CPF 3
#define CPF_POS 1
#define PG1_AT 0
#define PG2_AT 2
#define PG3_AT 6
#define K1_WB(n) WAIT_BAR(1)
#define PF1_AT -1
#define PF1_LO 4
#define PF1_N 4
#define PF2_AT 1
#define PF2_LO 8
#define PF2_N 4
#define PF3_AT 5
#define PF3_LO 12
#define PF3_N 4
#define K1_KR ((kt + rot) & 15)
#define K1_ST(v, rs, off) __builtin_amdgcn_raw_buffer_store_b128(v, rs, (int)(off), 0, K1_SAUX)
constexpr int K1_STAGE = 40960, K1U = K1_UNR;
__global__ __launch_bounds__(512, 2) void k1_qkv(const char* __restrict__ X, const char* __restrict__ W,
                                                 char* __restrict__ ws) {
    __shared__ __attribute__((aligned(16))) char lds_raw[4 * K1_STAGE];
    const lds_ptr lds = (lds_ptr)lds_raw;
    const unsigned lds0 = (unsigned)(uintptr_t)lds;
    const int tid = threadIdx.x, lane = tid & 63;
    const int w = __builtin_amdgcn_readfirstlane(tid >> 6);
    const int b = blockIdx.x, xcd = b & 7, idx = b >> 3;
    const int hp = (xcd >> 1) * 4 + (idx & 3), tb = (xcd & 1) * 8 + (idx >> 2);
    DG_DECL(0)

    const int lw = w & 3;
    const int rot = K1_ROT;
    unsigned voffX[4], voffW[2];
#pragma unroll
    for (int pp = 0; pp < 4; ++pp) {
        const int row = 32 * lw + 8 * pp + (lane >> 3);
        const int ch = (lane & 7) ^ ((row >> 1) & 7);
        voffX[pp] = (unsigned)(tb * 128 + row) * 2048u + ch * 16;
    }
#pragma unroll
    for (int pp = 0; pp < 2; ++pp) {
        const int krow = 16 * lw + 8 * pp + (lane >> 3);
        const int ch = (lane & 7) ^ (((lane >> 4) & 1) << 2);
        voffW[pp] = (unsigned)krow * 6144u + ch * 16;
    }
    auto issue = [&](int kt, int lo, int hi) {
        const int kr = K1_KR;
        const char* xb = X + kr * 128;
        const char* wb = W + (size_t)kr * (64 * 6144) + hp * 128;
        const unsigned st = lds0 + (kt & 3) * K1_STAGE;
#pragma unroll
        for (int pp = 0; pp < 4; ++pp)
            if (pp >= lo && pp < hi) glds16p<K1_XNT>(voffX[pp], xb, st + (4 * lw + pp) * 1024);
#pragma unroll
        for (int sec = 0; sec < 3; ++sec)
#pragma unroll
            for (int pp = 0; pp < 2; ++pp)
                if (4 + 2 * sec + pp >= lo && 4 + 2 * sec + pp < hi)
                    glds16p<K1_WNT>(voffW[pp], wb + sec * 2048, st + 16384 + sec * 8192 + (2 * lw + pp) * 1024);
    };
    DG(13);
    unsigned cpf;
    auto codepf = [&]() {
        unsigned long long pc; unsigned lo, hi;
        asm volatile("s_getpc_b64 %0\n.Lcp_k1a:\n\ts_mov_b32 %1, %3\n\ts_mov_b32 %2, %4" : "=s"(pc), "=s"(lo), "=s"(hi) : "n"(0), "n"(0));
        if (CPF & 1) lo = 0; else asm volatile("s_mov_b32 %0, .Lcp_k2a-.Lcp_k1a" : "=s"(lo));
        if (CPF & 2) asm volatile("s_mov_b32 %0, .Lcp_k2z-.Lcp_k1a" : "=s"(hi)); else asm volatile("s_mov_b32 %0, .Lcp_k1z-.Lcp_k1a" : "=s"(hi));
        unsigned off = lo + (unsigned)(idx + 32 * (lane & 7) + 256 * w) * 64u;
        off = off < hi ? off : hi;
        asm volatile("global_load_dword %0, %1, %2" : "=v"(cpf) : "v"(off), "s"(pc) : "memory");
    };
    if (w < 4) { issue(0, 0, 5); issue(1, 0, 5); codepf(); }
    else { issue(0, 5, 10); issue(1, 5, 10); }
    DG(14);
    int w2 = w;
    asm volatile("" : "+s"(w2));

    if (w2 >= 4) {
#define WAITN_BAR(N) asm volatile("s_waitcnt vmcnt(%0) lgkmcnt(0)\n\ts_barrier" :: "n"(N) : "memory")
        issue(2, 0, K1_LA);
        asm volatile("s_waitcnt vmcnt(%0)" :: "n"(5 + K1_LA) : "memory");
#pragma unroll 1
        for (int kt = 0; kt < 13; ++kt) {
            WAITN_BAR(10 + K1_LA);
            issue(kt + 2, K1_LA, 10);
            issue(kt + 3, 0, K1_LA);
        }
        WAITN_BAR(10 + K1_LA);
        issue(15, K1_LA, 10);
        WAIT_BAR(10);
        WAIT_BAR(0);
        if (lw == 0 && lane < 2) *(unsigned*)(ws + WS_CNT + b * 256 + lane * 128) = 0u;
        DG_END(5);
        return;
    }

    const int wm = w >> 1, wn = w & 1;
    const int r = lane & 31, hh = lane >> 5;
    const int g = lane >> 4, q = (lane & 15) >> 2, p = lane & 3;
    const int xoff0 = (64 * wm + r) * 128 + ((hh ^ ((r >> 1) & 7)) << 4);
    const int woff0 = 16384 + (8 * hh + q) * 128 + (((4 * wn + 2 * (g & 1) + (p >> 1)) ^ ((q >> 1) << 2)) << 4) + (p & 1) * 8;

    f32x16 acc[2][3];
#pragma unroll
    for (int m = 0; m < 2; ++m)
#pragma unroll
        for (int s = 0; s < 3; ++s) acc[m][s] = f32x16{};
    bf16x8 xf[2][2];
    s16x4 wl[2][3], wh[2][3];
#define K1_WF(f, sec) cat8(wl[f][sec], wh[f][sec])
#define K1_STEP(F, st, t, BAR_HERE) do { \
        const lds_ptr xp_ = (st) + (xoff0 ^ ((t) << 5)); const lds_ptr wp_ = (st) + woff0 + (t) * 2048; \
        acc[0][0] = MFMA32(K1_WF(F, 0), xf[F][0], acc[0][0]); SBAR(); \
        if (BAR_HERE) { K1_WB(BAR_HERE); SBAR(); } \
        xf[F ^ 1][0] = lds_rd128(xp_); wl[F ^ 1][0] = lds_tr(wp_); wh[F ^ 1][0] = lds_tr(wp_ + 512); SBAR(); \
        acc[0][1] = MFMA32(K1_WF(F, 1), xf[F][0], acc[0][1]); SBAR(); \
        wl[F ^ 1][1] = lds_tr(wp_ + 8192); wh[F ^ 1][1] = lds_tr(wp_ + 8192 + 512); SBAR(); \
        acc[0][2] = MFMA32(xf[F][0], K1_WF(F, 2), acc[0][2]); SBAR(); \
        wl[F ^ 1][2] = lds_tr(wp_ + 16384); wh[F ^ 1][2] = lds_tr(wp_ + 16384 + 512); xf[F ^ 1][1] = lds_rd128(xp_ + 4096); SBAR(); \
        acc[1][0] = MFMA32(K1_WF(F, 0), xf[F][1], acc[1][0]); SBAR(); \
        acc[1][1] = MFMA32(K1_WF(F, 1), xf[F][1], acc[1][1]); \
        acc[1][2] = MFMA32(xf[F][1], K1_WF(F, 2), acc[1][2]); SBAR(); \
    } while (0)

    WAIT_BAR(6);
    DG(3);
    {
        const lds_ptr xp_ = lds + xoff0; const lds_ptr wp_ = lds + woff0;
        xf[0][0] = lds_rd128(xp_); xf[0][1] = lds_rd128(xp_ + 4096);
#pragma unroll
        for (int sec = 0; sec < 3; ++sec) { wl[0][sec] = lds_tr(wp_ + sec * 8192); wh[0][sec] = lds_tr(wp_ + sec * 8192 + 512); }
    }
    SBAR();
#pragma unroll K1U
    for (int kt = 0; kt < 15; ++kt) {
        const lds_ptr st = lds + (kt & 3) * K1_STAGE;
        const lds_ptr sn = lds + ((kt + 1) & 3) * K1_STAGE;
        K1_STEP(0, st, 1, false);
        K1_STEP(1, st, 2, false);
        K1_STEP(0, st, 3, false);
        K1_STEP(1, sn, 0, kt + 1);
    }
    {
        const lds_ptr st = lds + 3 * K1_STAGE;
        K1_STEP(0, st, 1, false);
        K1_STEP(1, st, 2, false);
        K1_STEP(0, st, 3, false);
#pragma unroll
        for (int m = 0; m < 2; ++m) {
            acc[m][1] = MFMA32(K1_WF(1, 1), xf[1][m], acc[m][1]);
            acc[m][2] = MFMA32(xf[1][m], K1_WF(1, 2), acc[m][2]);
            acc[m][0] = MFMA32(K1_WF(1, 0), xf[1][m], acc[m][0]);
        }
    }
    DG(4);
    asm volatile("s_waitcnt vmcnt(0)\n.Lcp_k1z:" :: "v"(cpf));
    {
        const int h = 2 * hp + wn;
        const float qscale = 0.17677669529663687f * 1.4426950408889634f;
        const __amdgpu_buffer_rsrc_t rs = __builtin_amdgcn_make_buffer_rsrc(ws, 0, 32 << 20, 0x00020000);
#pragma unroll
        for (int m = 0; m < 2; ++m) {
            const int T = tb * 4 + wm * 2 + m;
            const unsigned dq = (unsigned)WS_Q + QT(h, T) * 2048 + lane * 16;
            const unsigned dk = (unsigned)WS_KV + (h * 64 + T) * 4096 + lane * 16;
            const unsigned dkk = SPM ? (unsigned)WS_KV + (h * 64 + T) * 4096 + ((lane & 51) | ((lane & 4) << 1) | ((lane & 8) >> 1)) * 16 : dk;
            K1_ST(pack8n(acc[m][1], 0), rs, dkk);
            K1_ST(pack8n(acc[m][1], 1), rs, dkk + 1024);
            K1_ST(pack8n(acc[m][2], 0), rs, dk + 2048);
            K1_ST(pack8n(acc[m][2], 1), rs, dk + 3072);
            K1_ST(pack8(acc[m][0], 0, qscale), rs, dq);
            K1_ST(pack8(acc[m][0], 1, qscale), rs, dq + 1024);
        }
    }
    DG_END(5);
}

constexpr int K2_SLOT = 32768;
constexpr int K2_SF = 65536;
constexpr int K2_CV = 98304;
constexpr int CV_HA = 0, CV_HB = 4160, CV_KV = 8320, CV_BYTES = 8320 + 480 + 4096;
constexpr int K2_LX = K2_CV + 4 * CV_BYTES;
constexpr int K2_FL = K2_LX + 2048;
constexpr int K2_SCR = K2_FL + 256;
constexpr int K2_PAD = K2_SCR + 4096;
constexpr int K2_LDS = K2_PAD + 1024;
#define NB8 0
#define WW 0
constexpr int F_X = 0, F_R = 32, F_W2 = 64, C_OST = 68, C_S1 = 76, C_S2 = 80, C_S3 = 128, C_ZST = 88, F_ZGO = 92,
              C_KST = 96, C_U0 = 100, C_LD = 108, F_KGO = 116, C_END = 120, C_OL = 72, C_HF = 84;
#define NG_IN 0
#define GATE_WORD C_END
#define GATE_VAL 0
#define X_NZ 4
#define X_NW3 8
#define ZP_GATE 4u
#define ZL_GATE 4
#define ZL_AT 31
#define Z_SLEEP0 0
#define O_SLEEP 0
#define Z_SLEEP 8
#define OWIN 16
#define OW0 8
#define WD 2
#define W3_AT 10
#define X_NOLDW 0
#define X_NOLDO 0
#define X_NOCHK 0
#define KROT (nt * 2)
#define NG_LAST 0
#define PACE_IN "s_nop 7"
#define PACE_OUT "s_nop 1"
#define P3_PRIO 1
#define TOP_SLEEP 2
#define A_AUX 16
#define B_AUX 16
#define B_LDS 0
#define TL 8
#define XR_AT 62
#define W3_AUX 0
#define W3M 13
#define A16 1
#define SEL4 2
#define KV_DOT 1
#define ZE_PRIO 3
#define P3_T1 99
#define P3_T2 99
#define AUX_PRIO 2

__global__ __launch_bounds__(768, 3) void k2_attn(char* __restrict__ ws, const char* __restrict__ Aq,
                                                  const char* __restrict__ Ak, const char* __restrict__ Av,
                                                  const char* __restrict__ shiftk, const char* __restrict__ ssmk,
                                                  const unsigned* __restrict__ ssmD, const char* __restrict__ Wo,
                                                  char* __restrict__ out) {
    __shared__ __attribute__((aligned(16))) char lds_raw[K2_LDS];
    const lds_ptr lds = (lds_ptr)lds_raw;
    const lds_ptr fl = lds + K2_FL;
    const unsigned lds0 = (unsigned)(uintptr_t)lds;
    const int tid = threadIdx.x, lane = tid & 63;
    const int w = __builtin_amdgcn_readfirstlane(tid >> 6);
    const int b = blockIdx.x, xcd = b & 7, idx = b >> 3;
    const int h = xcd * 4 + (idx >> 3), qb = idx & 7;
    const int tt = xcd * 2 + (idx >> 4), nt = idx & 15;
    const char* kvg = ws + WS_KV + (size_t)h * 64 * 4096;
    const __amdgpu_buffer_rsrc_t rs = __builtin_amdgcn_make_buffer_rsrc(ws, 0, 32 << 20, 0x00020000);
    const __amdgpu_buffer_rsrc_t ro = __builtin_amdgcn_make_buffer_rsrc(out, 0, 8 << 20, 0x00020000);
    gu32* const cnt = (gu32*)(ws + WS_CNT);
    auto signal = [&]() {
        (void)__hip_atomic_fetch_add(cnt + ((lane & 31) * 8 + (lane < 32 ? xcd : qb)) * 64, 1u, RLX_AGENT);
    };
    const unsigned wvoff = (unsigned)(lane >> 3) * 2048u + nt * 128 + (((lane & 7) ^ (((lane >> 4) & 1) << 2)) << 4);
    const int krot = KROT;
    auto wsrc = [&](int hf, int lp) { return Wo + (size_t)(hf * 64 + ((lp + krot * 2) & 63)) * 16384; };
    DG_DECL(1)
    const unsigned voff = lane * 16;
    const int cw = w & 3;
    auto issue = [&](int st) {
        const char* src = kvg + (st * 4 + cw) * 4096;
        const unsigned dst = lds0 + (st & 1) * K2_SLOT + cw * 4096;
#pragma unroll
        for (int i = 0; i < 4; ++i) glds16(voff, src + i * 1024, dst + i * 1024);
#pragma unroll
        for (int i = 0; i < 4; ++i) glds16(voff, src + 131072 + i * 1024, dst + 16384 + i * 1024);
    };
    asm volatile(".Lcp_k2a:");
    if (w >= 8) {
        __builtin_amdgcn_s_sleep(TOP_SLEEP);
#pragma unroll
        for (int i = 2; i < 8; ++i)
            glds16(voff, kvg + (i & 1) * 131072 + (i >> 1) * 4096 + cw * 1024, lds0 + (i & 1) * 16384 + (i >> 1) * 4096 + cw * 1024);
        RAW_BAR();
        issue(1);
        WAIT_BAR(8);
    } else {
        const int tq = cw * 2 + (w >> 2);
        const char* qg = ws + WS_Q + (size_t)QT(h, qb * 8 + tq) * 2048;
        glds16(voff, kvg + (w >> 2) * 131072 + cw * 1024, lds0 + (w >> 2) * 16384 + cw * 1024);
        glds16(voff, qg, lds0 + K2_SF + tq * 2048);
        glds16(voff, qg + 1024, lds0 + K2_SF + tq * 2048 + 1024);
    }
    int w2 = w;
    asm volatile("" : "+s"(w2));

    if (w2 >= 8) {
        __builtin_amdgcn_s_setprio(3);
        const int ch = xcd * 128 + idx * 4 + cw;
        const lds_ptr cv = lds + K2_CV + cw * CV_BYTES;
        {
            const u32x4 z4 = {0u, 0u, 0u, 0u};
            if (lane < 2) *(LAS u32x4*)(cv + CV_HA + lane * 16) = z4;
            if (lane < 30) *(LAS u32x4*)(cv + CV_KV + lane * 16) = z4;
            if (cw == 0 && lane < 16) *(LAS u32x4*)(fl + lane * 16) = z4;
        }
        const int c0 = xcd * 128 + cw * 32, cq = lane & 7, rg = lane >> 3;
        const int row0 = idx * 64 + rg * 8;
        const unsigned roff = (unsigned)row0 * 2048u + (c0 + cq * 4) * 2;
        const unsigned toffs = (unsigned)(c0 + (lane >> 3)) * 4096u + idx * 128;
        u32x4 sk[2];
        sk[0] = gld128((unsigned)(c0 + cq * 4) * 8u, shiftk); sk[1] = gld128((unsigned)(c0 + cq * 4) * 8u, shiftk + 16);
        unsigned dd = gld32((unsigned)(ch >> 1) * 4u, ssmD);
        auto stage = [&](const char* src, int dst) {
            const unsigned sbase = (unsigned)(idx * 64 + cw * 16 + (lane >> 4)) * 2048u + xcd * 256;
#pragma unroll
            for (int i = 0; i < 4; ++i)
                glds16p<K2_SNT>(sbase + (((lane & 15) ^ (((2 * cw + (i >> 1)) & 3) << 2)) << 4), src + i * 8192, lds0 + dst + cw * 4096 + i * 1024);
        };
        auto blank = [&](unsigned off) {
            const u32x4 e4 = {EMPTY, EMPTY, EMPTY, EMPTY};
            CFENCE();
            st_sc1(e4, rs, off + voff); st_sc1(e4, rs, off + 1024 + voff);
            CFENCE();
        };
        WAIT_BAR(3);
        DG(12);
        issue(2);
        stage(Ak, K2_SF);
        glds16((unsigned)(idx * 64 - (idx ? 4 : 0) + (lane >> 4)) * 2048u + xcd * 256 + (((lane & 15) ^ 12) << 4), Ak, lds0 + K2_SCR + cw * 1024);
        blank((unsigned)WS_O + (unsigned)((qb * 8 + cw * 2) * 32 + h) * 2048u);
        auto warm = [&](int g) {
#pragma unroll
            for (int i = 0; i < 4; ++i) glds16(wvoff, wsrc(idx >> 4, 16 + g * 16 + cw * 4 + i), lds0 + K2_PAD);
        };
        WAIT_BAR(7);
        DG(9);
        issue(3);
        stage(Av, K2_SF + 16384);
        blank((unsigned)WS_O + (unsigned)((qb * 8 + cw * 2 + 1) * 32 + h) * 2048u);
        WAIT_BAR(6);
        DG(3);
        issue(4);
#pragma unroll
        for (int i = 0; i < 4; ++i) glds16p<K2_SNT>(voff, ssmk + (size_t)ch * 4096 + i * 1024, lds0 + K2_CV + cw * CV_BYTES + CV_HA + 32 + i * 1024);
        blank((unsigned)WS_ZT + ch * 4096); blank((unsigned)WS_ZT + ch * 4096 + 2048);
        WAIT_BAR(8);
        DG(5);
        issue(5);
        SBAR();
        asm volatile("" : "+v"(sk[0]), "+v"(sk[1]), "+v"(dd));
        dd = __builtin_amdgcn_readfirstlane(dd);
        {
            u32x2 kr[11], vr[8];
            lds_ptr kb = lds + K2_SF + rg * 2048 + ((cw ^ (rg & 3)) << 6) + cq * 8;
            asm volatile("" : "+v"(kb));
            const lds_ptr hb = lds + (rg ? K2_SF + rg * 2048 - 768 : K2_SCR + cw * 1024 + 256) + ((cw ^ ((rg + 3) & 3)) << 6) + cq * 8;
#pragma unroll
            for (int i = 0; i < 3; ++i) kr[i] = *(const LAS u32x2*)(hb + i * 256);
#pragma unroll
            for (int i = 0; i < 8; ++i) kr[3 + i] = *(const LAS u32x2*)(kb + i * 256);
#pragma unroll
            for (int i = 0; i < 8; ++i) vr[i] = *(const LAS u32x2*)(kb + 16384 + i * 256);
            if (row0 == 0) { kr[0] = u32x2{0u, 0u}; kr[1] = u32x2{0u, 0u}; kr[2] = u32x2{0u, 0u}; }
            const lds_ptr tb = cv + CV_KV + 480;
#pragma unroll
            for (int c = 0; c < 4; ++c) {
                const unsigned s01 = sk[c >> 1][(c & 1) * 2], s23 = sk[c >> 1][(c & 1) * 2 + 1];
                unsigned kp[10];
                float kv[8], vf[8];
#pragma unroll
                for (int j = 0; j < 10; ++j) kp[j] = __builtin_amdgcn_perm(kr[j][c >> 1], kr[j + 1][c >> 1], (c & 1) ? 0x07060302u : 0x05040100u);
                SBAR();
#pragma unroll
                for (int i = 0; i < 8; ++i) asm("v_dot2_f32_bf16 %0, %1, %2, 0" : "=v"(kv[i]) : "v"(kp[i]), "v"(s23));
                SBAR();
#pragma unroll
                for (int i = 0; i < 8; ++i) asm("v_dot2_f32_bf16 %0, %1, %2, %0" : "+v"(kv[i]) : "v"(kp[i + 2]), "v"(s01));
                SBAR();
#pragma unroll
                for (int i = 0; i < 8; ++i) vf[i] = (c & 1) ? bfhi(vr[i][c >> 1]) : bflo(vr[i][c >> 1]);
                SBAR();
#pragma unroll
                for (int i = 0; i < 8; ++i) kv[i] = mul_vv(kv[i], vf[i]);
                u32x4 o4;
#pragma unroll
                for (int i = 0; i < 4; ++i) o4[i] = cvtpk(kv[7 - 2 * i], kv[6 - 2 * i]);
                *(LAS u32x4*)(tb + (cq * 4 + c) * 128 + ((rg ^ cq) << 4)) = o4;
            }
#pragma unroll
            for (int i = 0; i < 4; ++i) {
                const u32x4 t = *(const LAS u32x4*)(tb + i * 1024 + lane * 16);
                st_sc1(t, rs, (unsigned)WS_Q + (xcd * 32 + idx) * 16384 + cw * 4096 + i * 1024 + (lane >> 3) * 128 + (((lane & 7) ^ (2 * i + (lane >> 5))) << 4));
            }
        }
        CFENCE();
        DG(7);
        WAIT_BAR(4);
        DG(6);
        issue(6);
        asm volatile("s_waitcnt vmcnt(8)" ::: "memory");
        if (lds_add_rtn(fl + C_KST, lane) == 3u) signal();
#pragma unroll
        for (int i = 0; i < 8; ++i)
            glds16p<K2_WNT>(wvoff, wsrc(i >> 2, cw * 4 + (i & 3)), lds0 + K2_SF + (i >> 2) * 16384 + (cw * 4 + (i & 3)) * 1024);
        {
            const u32x4 z4 = {0u, 0u, 0u, 0u};
            if (lane < 2) *(LAS u32x4*)(cv + CV_HB + lane * 16) = z4;
#pragma unroll
            for (int i = 0; i < 4; ++i) {
                const int off = 32 + i * 1024 + lane * 16;
                const u32x4 hv = *(const LAS u32x4*)(cv + CV_HA + off);
                const unsigned pv = *(const LAS unsigned*)(cv + CV_HA + off - 4);
                u32x4 sh;
                sh[0] = __builtin_amdgcn_alignbit(hv[0], pv, 16);
                sh[1] = __builtin_amdgcn_alignbit(hv[1], hv[0], 16);
                sh[2] = __builtin_amdgcn_alignbit(hv[2], hv[1], 16);
                sh[3] = __builtin_amdgcn_alignbit(hv[3], hv[2], 16);
                *(LAS u32x4*)(cv + CV_HB + off) = sh;
            }
        }
        WAIT_BAR(8);
        DG(8);
        issue(7);
#pragma unroll
        for (int i = 0; i < 8; ++i)
            glds16p<K2_WNT>(wvoff, wsrc(idx >> 4, 32 + cw * 8 + i), lds0 + K2_SCR + cw * 1024);
        WAIT_BAR(8);
        DG(11);
        if (cw == 0) { poll_ge(cnt + b * 64, 64u); lds_add(fl + F_KGO, lane); }
        else lds_wait_ge(fl + F_KGO, 1u);
        {
            u32x4 t[4];
#pragma unroll
            for (int i = 0; i < 4; ++i) t[i] = ld_sc1s(rs, (unsigned)WS_Q + (xcd * 32 + (lane >> 3)) * 16384 + (idx * 4 + cw) * 128 + (lane & 7) * 16, i * 131072);
#pragma unroll
            for (int i = 0; i < 4; ++i) *(LAS u32x4*)(cv + CV_KV + 480 + i * 1024 + lane * 16) = t[i];
        }
        const int r1 = lane & 15, q = lane >> 4;
        const int toff = (r1 & 1) ? CV_HA + 2 * (16 + r1 + 9 - 8 * q) : CV_HB + 2 * (16 + r1 + 10 - 8 * q);
        const int boff = CV_KV + 448 + 32 * r1 + 16 * q;
        f32x4 y[8];
        u32x4 tf[8];
        bf16x8 bf[8];
        const lds_ptr bp = cv + boff - 448;
        lds_ptr tp[4];
#pragma unroll
        for (int i = 0; i < 4; ++i) { tp[i] = cv + toff + 1024 * i; asm volatile("" : "+v"(tp[i])); }
#define LDT(bb, a, hf) do { const LAS unsigned* t_ = (const LAS unsigned*)(tp[(a) >> 1] + 64 * (bb) + 512 * ((a) & 1) + 8 * (hf)); \
            tf[a][2 * (hf)] = t_[0]; tf[a][2 * (hf) + 1] = t_[1]; SBAR(); } while (0)
#define LDB(bb, a) do { bf[a] = lds_rd128(bp + 448 - 64 * (bb) + 512 * (a)); SBAR(); } while (0)
#define CM(a, ww) do { asm volatile("v_mfma_f32_16x16x32_bf16 %0, %1, %2, %0" : "+v"(y[(a) + (ww)]) : "v"(tf[a]), "v"(bf[ww])); SBAR(); } while (0)
#define CM0(a, ww) do { asm volatile("v_mfma_f32_16x16x32_bf16 %0, %1, %2, 0" : "=&v"(y[(a) + (ww)]) : "v"(tf[a]), "v"(bf[ww])); SBAR(); } while (0)
#define PC() do { asm volatile(PACE_OUT); SBAR(); } while (0)
#pragma unroll
        for (int a = 0; a < 8; ++a) { if (a < 5) { LDT(0, a, 0); LDT(0, a, 1); } LDB(0, 7 - a); }
#pragma unroll
        for (int i = 0; i < 8; ++i)
            glds16p<K2_WNT>(wvoff, wsrc(i >> 2, 16 + cw * 4 + (i & 3)), lds0 + (i >> 2) * 16384 + (cw * 4 + (i & 3)) * 1024);
#pragma unroll
        for (int i = 0; i < 4; ++i)
            glds16p<K2_QNT>((unsigned)(idx * 64 + cw * 16 + (lane >> 4)) * 2048u + xcd * 256 + (lane & 15) * 16, Aq + i * 8192, lds0 + K2_SCR + cw * 1024);
        RAW_BAR();
        asm volatile("s_waitcnt vmcnt(4)" ::: "memory");
        lds_add(fl + C_S1, lane);
#define ZFL() PC()
        __builtin_amdgcn_s_setprio(AUX_PRIO);
        const float dch = (ch & 1) ? bfhi(dd) : bflo(dd);
        const lds_ptr kdp = cv + CV_KV + 480 + 32 * r1 + 16 * (q >> 1) + ((q & 1) ? 0 : 8);
        float kd[8][4];
        u32x2 kk;
        unsigned kc0, kc1, zo[8][2];
#define KD_RD(t) do { kk = *(const LAS u32x2*)(kdp + 512 * (t)); SBAR(); } while (0)
#define KD_UP0(t) do { kd[t][0] = bfhi(kk[1]); kd[t][1] = bflo(kk[1]); SBAR(); } while (0)
#define KD_UP1(t) do { kd[t][2] = bfhi(kk[0]); kd[t][3] = bflo(kk[0]); SBAR(); } while (0)
#define KD_MU0(t) do { kd[t][0] = mul_sv(dch, kd[t][0]); kd[t][1] = mul_sv(dch, kd[t][1]); SBAR(); } while (0)
#define KD_MU1(t) do { kd[t][2] = mul_sv(dch, kd[t][2]); kd[t][3] = mul_sv(dch, kd[t][3]); SBAR(); } while (0)
#define KD_CV(t) do { kc0 = cvtpk(kd[t][0], kd[t][1]); kc1 = cvtpk(kd[t][2], kd[t][3]); SBAR(); } while (0)
#define KD_X0(t) do { kd[t][0] = bflo(kc0); kd[t][1] = bfhi(kc0); SBAR(); } while (0)
#define KD_X1(t) do { kd[t][2] = bflo(kc1); kd[t][3] = bfhi(kc1); SBAR(); } while (0)
#define ZF0(t) do { kc0 = cvtpk(y[t][0], y[t][1]); kc1 = cvtpk(y[t][2], y[t][3]); SBAR(); } while (0)
#define ZF1(t) do { y[t][0] = bflo(kc0); y[t][1] = bfhi(kc0); SBAR(); } while (0)
#define ZF2(t) do { y[t][2] = bflo(kc1); y[t][3] = bfhi(kc1); SBAR(); } while (0)
#define ZF3(t) do { y[t][0] = add_vv(y[t][0], kd[t][0]); y[t][1] = add_vv(y[t][1], kd[t][1]); SBAR(); } while (0)
#define ZF4(t) do { y[t][2] = add_vv(y[t][2], kd[t][2]); y[t][3] = add_vv(y[t][3], kd[t][3]); SBAR(); } while (0)
#define ZF5(t) do { zo[t][0] = nz(cvtpk(y[t][0], y[t][1])); zo[t][1] = nz(cvtpk(y[t][2], y[t][3])); SBAR(); } while (0)
#define ZST(pr) do { auto s0 = __builtin_amdgcn_permlane16_swap(zo[2 * (pr)][0], zo[2 * (pr) + 1][0], false, false); \
            auto s1 = __builtin_amdgcn_permlane16_swap(zo[2 * (pr)][1], zo[2 * (pr) + 1][1], false, false); \
            st_sc1(u32x4{s0[0], s1[0], s0[1], s1[1]}, rs, (unsigned)WS_ZT + ch * 4096 + (pr) * 1024 + voff); SBAR(); } while (0)
#define W3(i) do { if ((i) == 0) PC(); } while (0)
#define HF() do { lds_add(fl + C_HF, lane); SBAR(); } while (0)
        CM0(0, 7); LDB(1, 7); CM0(0, 6); LDT(0, 5, 0); CM0(0, 5); LDT(0, 5, 1); CM0(0, 4); LDT(0, 6, 0); CM0(0, 3); LDT(0, 6, 1); CM0(0, 2); LDT(0, 7, 0); CM0(0, 1); LDT(0, 7, 1); CM0(0, 0); KD_RD(0);
        CM(1, 6); LDB(1, 6); CM(1, 5); LDT(1, 0, 0); CM(1, 4); LDT(1, 0, 1); CM(1, 3); PC(); CM(1, 2); PC(); CM(1, 1); KD_UP0(0); CM(1, 0); KD_UP1(0);
        CM(2, 5); LDB(1, 5); CM(2, 4); LDT(1, 1, 0); CM(2, 3); LDT(1, 1, 1); CM(2, 2); KD_MU0(0); CM(2, 1); KD_MU1(0); CM(2, 0); KD_CV(0);
        CM(3, 4); LDB(1, 4); CM(3, 3); LDT(1, 2, 0); CM(3, 2); LDT(1, 2, 1); CM(3, 1); KD_X0(0); CM(3, 0); KD_X1(0);
        CM(4, 3); LDB(1, 3); CM(4, 2); LDT(1, 3, 0); CM(4, 1); LDT(1, 3, 1); CM(4, 0); KD_RD(1);
        CM(5, 2); LDB(1, 2); CM(5, 1); LDT(1, 4, 0); CM(5, 0); LDT(1, 4, 1);
        CM(6, 1); LDB(1, 1); CM(6, 0); PC();
        CM(7, 0); LDB(1, 0);
        CM(0, 7); LDB(2, 7); CM(0, 6); LDT(1, 5, 0); CM(0, 5); LDT(1, 5, 1); CM(0, 4); LDT(1, 6, 0); CM(0, 3); LDT(1, 6, 1); CM(0, 2); LDT(1, 7, 0); CM(0, 1); LDT(1, 7, 1); CM(0, 0); PC();
        CM(1, 6); LDB(2, 6); CM(1, 5); LDT(2, 0, 0); CM(1, 4); LDT(2, 0, 1); CM(1, 3); KD_UP0(1); CM(1, 2); KD_UP1(1); CM(1, 1); KD_MU0(1); CM(1, 0); KD_MU1(1);
        CM(2, 5); LDB(2, 5); CM(2, 4); LDT(2, 1, 0); CM(2, 3); LDT(2, 1, 1); CM(2, 2); KD_CV(1); CM(2, 1); KD_X0(1); CM(2, 0); KD_X1(1);
        CM(3, 4); LDB(2, 4); CM(3, 3); LDT(2, 2, 0); CM(3, 2); LDT(2, 2, 1); CM(3, 1); KD_RD(2); CM(3, 0); PC();
        CM(4, 3); LDB(2, 3); CM(4, 2); LDT(2, 3, 0); CM(4, 1); LDT(2, 3, 1); CM(4, 0); PC();
        CM(5, 2); LDB(2, 2); CM(5, 1); LDT(2, 4, 0); CM(5, 0); LDT(2, 4, 1);
        CM(6, 1); LDB(2, 1); CM(6, 0); KD_UP0(2);
        CM(7, 0); LDB(2, 0);
        CM(0, 7); LDB(3, 7); CM(0, 6); LDT(2, 5, 0); CM(0, 5); LDT(2, 5, 1); CM(0, 4); LDT(2, 6, 0); CM(0, 3); LDT(2, 6, 1); CM(0, 2); LDT(2, 7, 0); CM(0, 1); LDT(2, 7, 1); CM(0, 0); KD_UP1(2);
        CM(1, 6); LDB(3, 6); CM(1, 5); LDT(3, 0, 0); CM(1, 4); LDT(3, 0, 1); CM(1, 3); KD_MU0(2); CM(1, 2); KD_MU1(2); CM(1, 1); KD_CV(2); CM(1, 0); KD_X0(2);
        CM(2, 5); LDB(3, 5); CM(2, 4); LDT(3, 1, 0); CM(2, 3); LDT(3, 1, 1); CM(2, 2); KD_X1(2); CM(2, 1); KD_RD(3); CM(2, 0); PC();
        CM(3, 4); LDB(3, 4); CM(3, 3); LDT(3, 2, 0); CM(3, 2); LDT(3, 2, 1); CM(3, 1); PC(); CM(3, 0); KD_UP0(3);
        CM(4, 3); LDB(3, 3); CM(4, 2); LDT(3, 3, 0); CM(4, 1); LDT(3, 3, 1); CM(4, 0); KD_UP1(3);
        CM(5, 2); LDB(3, 2); CM(5, 1); LDT(3, 4, 0); CM(5, 0); LDT(3, 4, 1);
        CM(6, 1); LDB(3, 1); CM(6, 0); KD_MU0(3);
        CM(7, 0); LDB(3, 0);
        CM(0, 7); LDB(4, 7); CM(0, 6); LDT(3, 5, 0); CM(0, 5); LDT(3, 5, 1); CM(0, 4); LDT(3, 6, 0); CM(0, 3); LDT(3, 6, 1); CM(0, 2); LDT(3, 7, 0); CM(0, 1); LDT(3, 7, 1); CM(0, 0); KD_MU1(3);
        CM(1, 6); LDB(4, 6); CM(1, 5); LDT(4, 0, 0); CM(1, 4); LDT(4, 0, 1); CM(1, 3); KD_CV(3); CM(1, 2); KD_X0(3); CM(1, 1); KD_X1(3); CM(1, 0); KD_RD(4);
        CM(2, 5); LDB(4, 5); CM(2, 4); LDT(4, 1, 0); CM(2, 3); LDT(4, 1, 1); CM(2, 2); PC(); CM(2, 1); PC(); CM(2, 0); KD_UP0(4);
        CM(3, 4); LDB(4, 4); CM(3, 3); LDT(4, 2, 0); CM(3, 2); LDT(4, 2, 1); CM(3, 1); KD_UP1(4); CM(3, 0); KD_MU0(4);
        CM(4, 3); LDB(4, 3); CM(4, 2); LDT(4, 3, 0); CM(4, 1); LDT(4, 3, 1); CM(4, 0); KD_MU1(4);
        CM(5, 2); LDB(4, 2); CM(5, 1); LDT(4, 4, 0); CM(5, 0); LDT(4, 4, 1);
        CM(6, 1); LDB(4, 1); CM(6, 0); KD_CV(4);
        CM(7, 0); LDB(4, 0);
        CM(0, 7); LDB(5, 7); CM(0, 6); LDT(4, 5, 0); CM(0, 5); LDT(4, 5, 1); CM(0, 4); LDT(4, 6, 0); CM(0, 3); LDT(4, 6, 1); CM(0, 2); LDT(4, 7, 0); CM(0, 1); LDT(4, 7, 1); CM(0, 0); KD_X0(4);
        CM(1, 6); LDB(5, 6); CM(1, 5); LDT(5, 0, 0); CM(1, 4); LDT(5, 0, 1); CM(1, 3); KD_X1(4); CM(1, 2); KD_RD(5); CM(1, 1); PC(); CM(1, 0); PC();
        CM(2, 5); LDB(5, 5); CM(2, 4); LDT(5, 1, 0); CM(2, 3); LDT(5, 1, 1); CM(2, 2); KD_UP0(5); CM(2, 1); KD_UP1(5); CM(2, 0); KD_MU0(5);
        CM(3, 4); LDB(5, 4); CM(3, 3); LDT(5, 2, 0); CM(3, 2); LDT(5, 2, 1); CM(3, 1); KD_MU1(5); CM(3, 0); KD_CV(5);
        CM(4, 3); LDB(5, 3); CM(4, 2); LDT(5, 3, 0); CM(4, 1); LDT(5, 3, 1); CM(4, 0); KD_X0(5);
        CM(5, 2); LDB(5, 2); CM(5, 1); LDT(5, 4, 0); CM(5, 0); LDT(5, 4, 1);
        CM(6, 1); LDB(5, 1); CM(6, 0); KD_X1(5);
        CM(7, 0); LDB(5, 0);
        CM(0, 7); LDB(6, 7); CM(0, 6); LDT(5, 5, 0); CM(0, 5); LDT(5, 5, 1); CM(0, 4); LDT(5, 6, 0); CM(0, 3); LDT(5, 6, 1); CM(0, 2); LDT(5, 7, 0); CM(0, 1); LDT(5, 7, 1); CM(0, 0); KD_RD(6);
        CM(1, 6); LDB(6, 6); CM(1, 5); LDT(6, 0, 0); CM(1, 4); LDT(6, 0, 1); CM(1, 3); PC(); CM(1, 2); PC(); CM(1, 1); KD_UP0(6); CM(1, 0); KD_UP1(6);
        CM(2, 5); LDB(6, 5); CM(2, 4); LDT(6, 1, 0); CM(2, 3); LDT(6, 1, 1); CM(2, 2); KD_MU0(6); CM(2, 1); KD_MU1(6); CM(2, 0); KD_CV(6);
        CM(3, 4); LDB(6, 4); CM(3, 3); LDT(6, 2, 0); CM(3, 2); LDT(6, 2, 1); CM(3, 1); KD_X0(6); CM(3, 0); KD_X1(6);
        CM(4, 3); LDB(6, 3); CM(4, 2); LDT(6, 3, 0); CM(4, 1); LDT(6, 3, 1); CM(4, 0); KD_RD(7);
        CM(5, 2); LDB(6, 2); CM(5, 1); LDT(6, 4, 0); CM(5, 0); LDT(6, 4, 1);
        CM(6, 1); LDB(6, 1); CM(6, 0); PC();
        CM(7, 0); LDB(6, 0);
        CM(0, 7); LDB(7, 7); CM(0, 6); LDT(6, 5, 0); CM(0, 5); LDT(6, 5, 1); CM(0, 4); LDT(6, 6, 0); CM(0, 3); LDT(6, 6, 1); CM(0, 2); LDT(6, 7, 0); CM(0, 1); LDT(6, 7, 1); CM(0, 0); PC();
        CM(1, 6); LDB(7, 6); CM(1, 5); LDT(7, 0, 0); CM(1, 4); LDT(7, 0, 1); CM(1, 3); KD_UP0(7); CM(1, 2); KD_UP1(7); CM(1, 1); KD_MU0(7); CM(1, 0); KD_MU1(7);
        CM(2, 5); LDB(7, 5); CM(2, 4); LDT(7, 1, 0); CM(2, 3); LDT(7, 1, 1); CM(2, 2); KD_CV(7); CM(2, 1); KD_X0(7); CM(2, 0); KD_X1(7);
        CM(3, 4); LDB(7, 4); CM(3, 3); LDT(7, 2, 0); CM(3, 2); LDT(7, 2, 1); CM(3, 1); PC(); CM(3, 0); PC();
        CM(4, 3); LDB(7, 3); CM(4, 2); LDT(7, 3, 0); CM(4, 1); LDT(7, 3, 1); CM(4, 0); PC();
        CM(5, 2); LDB(7, 2); CM(5, 1); LDT(7, 4, 0); CM(5, 0); LDT(7, 4, 1);
        CM(6, 1); LDB(7, 1); CM(6, 0); PC();
        CM(7, 0); LDB(7, 0);
        CM(0, 3); LDT(7, 5, 0); CM(0, 2); LDT(7, 5, 1); CM(1, 2); LDT(7, 6, 0); CM(1, 1); LDT(7, 6, 1); CM(2, 1); LDT(7, 7, 0); CM(2, 0); LDT(7, 7, 1);
        CM(3, 0); HF(); CM(0, 1); ZFL(); CM(0, 0); W3(0); CM(1, 0); ZF0(2); W3(1); CM(0, 5); ZF1(2); W3(2); CM(0, 4); ZF2(2); W3(3);
        CM(1, 4); ZF3(2); W3(4); CM(1, 3); ZF4(2); W3(5); CM(2, 3); ZF5(2); W3(6); CM(2, 2); ZF0(3); W3(7); CM(3, 2); ZF1(3);  CM(3, 1); ZF2(3);
        CM(4, 1); ZF3(3);  CM(4, 0); ZF4(3);  CM(5, 0); ZF5(3);  CM(0, 7); ZST(1);  CM(0, 6); ZF0(0);  CM(1, 6); ZF1(0);
        CM(1, 5); ZF2(0);  CM(2, 5); ZF3(0);  CM(2, 4); ZF4(0);  CM(3, 4); ZF5(0);  CM(3, 3); ZF0(1);  CM(4, 3); ZF1(1);
        CM(4, 2); ZF2(1);  CM(5, 2); ZF3(1);  CM(5, 1); ZF4(1);  CM(6, 1); ZF5(1);  CM(6, 0); ZST(0);  CM(7, 0); ZF0(4);
        DG(4);
        ZF1(4); ZF2(4); ZF3(4); ZF4(4); ZF5(4); ZF0(5); ZF1(5); ZF2(5); ZF3(5); ZF4(5); ZF5(5); ZST(2); ZF0(6); ZF1(6); ZF2(6); ZF3(6); ZF4(6); ZF5(6); ZF0(7); ZF1(7); ZF2(7); ZF3(7); ZF4(7); ZF5(7); ZST(3);
        DG(10);
        __builtin_amdgcn_s_setprio(3);
        while ((unsigned)__builtin_amdgcn_readfirstlane(*(volatile LAS unsigned*)(fl + C_OL)) < ZL_GATE) __builtin_amdgcn_s_sleep(4);
        CFENCE();
        const int arow = idx * 64 + (cw >> 1) * 32 + rg * 4, acol = xcd * 256 + (cw & 1) * 128 + cq * 16;
        u32x4 qr[4];
#pragma unroll
        for (int i = 0; i < 4; ++i) qr[i] = gld128((unsigned)(arow * 2048 + acol), Aq + i * 2048);
        {
            const lds_ptr tr = cv + CV_KV + 480;
            u32x4 t[4];
            const int ck = (cw >> 1) * 4 + ((lane ^ ((lane >> 5) * 2)) & 3);
            const unsigned zb = (unsigned)WS_ZT + (unsigned)(xcd * 128 + (cw & 1) * 64 + (lane >> 4) * 4) * 4096u + (idx >> 3) * 1024 +
                                ((idx & 3) * 4 + ((idx >> 2) & 1) * 16 + (ck >> 1) + (ck & 1) * 32) * 16;
#pragma unroll 1
            for (unsigned spins = 0; spins < (1u << 20); ++spins) {
#pragma unroll
                for (int i = 0; i < 4; ++i) t[i] = ld_sc1s(rs, zb + ((((lane >> 2) & 3) ^ i) << 12), i * 65536);
                if (!missing(mx4(mx4(mx8(t[0], t[1]), t[2]), t[3]))) break;
                __builtin_amdgcn_s_sleep(Z_SLEEP);
            }
            DG(13);
            __builtin_amdgcn_s_setprio(ZE_PRIO);
#pragma unroll
            for (int i = 0; i < 4; ++i) *(LAS u32x4*)(tr + i * 1024 + lane * 16) = t[i];
#pragma unroll
            for (int i = 0; i < 4; ++i) asm volatile("" : "+v"(qr[i]));
            u32x2 zc[8];
#pragma unroll
            for (int c = 0; c < 8; ++c)
                zc[c] = *(const LAS u32x2*)(tr + cq * 512 + (c >> 2) * 256 + ((((c & 3) * 4 + (rg >> 1)) ^ (cq * 2)) << 4) + (rg & 1) * 8);
#pragma unroll
            for (int i = 0; i < 4; ++i) {
                u32x4 o;
#pragma unroll
                for (int c = 0; c < 4; ++c) {
                    const unsigned z0 = zc[2 * c][i >> 1], z1 = zc[2 * c + 1][i >> 1];
                    o[c] = cvtpk(mul_vv(bflo(qr[i][c]), (i & 1) ? bfhi(z0) : bflo(z0)), mul_vv(bfhi(qr[i][c]), (i & 1) ? bfhi(z1) : bflo(z1)));
                }
                __builtin_amdgcn_raw_buffer_store_b128(o, ro, arow * 4096 + acol, i * 4096, OUT_AUX);
            }
        }
        asm volatile(".Lcp_k2z:");
        DG_END(14);
        return;
    }

    const int half = w >> 2;
    const int TA = qb * 8 + (w & 3) * 2 + half, TB = TA ^ 1;
    bf16x8 sel;
    {
        const short one = (((lane >> 2) & 1) == ((lane >> 4) & 1) && (lane & 11) == 0) ? (short)0x3f80 : (short)0;
#pragma unroll
        for (int i = 0; i < 8; ++i) sel[i] = SPM ? ((lane & 15) == (i & 4) ? (short)0x3f80 : (short)0) : one;
    }
    int sidx = (int)0xdddddddd;
#define LO8(x) __builtin_bit_cast(bfx16_t, __builtin_shufflevector(x, x, 0, 1, 2, 3, 4, 5, 6, 7))
#define HI8(x) __builtin_bit_cast(bfx16_t, __builtin_shufflevector(x, x, 8, 9, 10, 11, 12, 13, 14, 15))
#define SPV(v, p, o) __builtin_amdgcn_smfmac_f32_32x32x32_bf16(__builtin_bit_cast(bfx8_t, v), p, o, sidx, 0, 0)
#define SSUM(p, l) __builtin_amdgcn_smfmac_f32_16x16x64_bf16(__builtin_bit_cast(bfx8_t, sel), p, l, sidx, 0, 0)
    f32x16 oA = f32x16{}, oB = f32x16{};
    f32x4 lA = f32x4{}, lB = f32x4{};
    f32x16 negm, sA, sB;
    lds_ptr lp = lds + half * 16384 + lane * 16;
    bf16x8 k0[2], k1[2], v0[2], v1[2];
    u32x4 p0, p1 = {0u, 0u, 0u, 0u};
#define EX(X, i) X[i] = __builtin_amdgcn_exp2f(X[i])
    lds_ptr qpa = lds + K2_SF + (TA & 7) * 2048 + lane * 16, qpb = lds + K2_SF + (TB & 7) * 2048 + lane * 16;
    asm volatile("" : "+v"(qpa), "+v"(qpb), "+v"(lp), "+v"(sel), "+v"(oA), "+v"(oB), "+v"(lA), "+v"(lB), "+v"(sidx));
    WAIT_BAR(0);
    DG(3);
    const bf16x8 qa0 = lds_rd128(qpa), qb0 = lds_rd128(qpb);
    const bf16x8 x0 = lds_rd128(lds + lane * 16), x1 = lds_rd128(lds + lane * 16 + 1024);
    const bf16x8 qa1 = lds_rd128(qpa + 1024), qb1 = lds_rd128(qpb + 1024);
    {
        k0[0] = lds_rd128(lp); k1[0] = lds_rd128(lp + 1024);
        v0[0] = lds_rd128(lp + 2048); v1[0] = lds_rd128(lp + 3072);
        sA = MFMA32(x0, qa0, f32x16{});
        sB = MFMA32(x0, qb0, f32x16{});
        sA = MFMA32(x1, qa1, sA);
        sB = MFMA32(x1, qb1, sB);
        float m = fmx(sA[0], sA[1]), m2 = fmx(sB[0], sB[1]);
#pragma unroll
        for (int i = 2; i < 16; i += 2) m = fmx(fmx(m, sA[i]), sA[i + 1]);
#pragma unroll
        for (int i = 2; i < 16; i += 2) m2 = fmx(fmx(m2, sB[i]), sB[i + 1]);
        m = fmx(m, m2);
        auto rr = __builtin_amdgcn_permlane32_swap(__float_as_uint(m), __float_as_uint(m), false, false);
        m = fmx(__uint_as_float(rr[0]), __uint_as_float(rr[1]));
#pragma unroll
        for (int i = 0; i < 16; ++i) negm[i] = -m;
        sA = MFMA32(k0[0], qa0, negm);
        sA = MFMA32(k1[0], qa1, sA);
        v1[1] = v1[0];
#pragma unroll
        for (int i = 8; i < 16; ++i) sB[i] = 0.f;
        asm volatile("" : "+v"(sB));
        SBAR();
        RAW_BAR();
        SBAR();
        EX(sA, 0); EX(sA, 1);
    }
    DG(10);
#define SUM16(l, p) asm volatile("v_mfma_f32_16x16x32_bf16 %0, %1, %2, %0" : "+v"(l) : "v"(sel), "v"(p))
#define HALF(C, oC, lC, N, qx, qy, ka, kb, oP, lP, vprev, vcur0, PRE) do { \
        oP = SPV(vprev, HI8(N), oP); SBAR(); \
        PRE; EX(C, 2); EX(C, 3); EX(C, 4); SBAR(); \
        lP = SSUM(HI8(N), lP); SBAR(); \
        EX(C, 5); SBAR(); \
        N = MFMA32(ka, qx, negm); SBAR(); \
        EX(C, 6); EX(C, 7); EX(C, 8); SBAR(); \
        N = MFMA32(kb, qy, N); SBAR(); \
        EX(C, 9); EX(C, 10); EX(C, 11); SBAR(); \
        oC = SPV(vcur0, LO8(C), oC); SBAR(); \
        EX(C, 12); EX(C, 13); EX(C, 14); SBAR(); \
        lC = SSUM(LO8(C), lC); SBAR(); \
        EX(C, 15); EX(N, 0); EX(N, 1); SBAR(); \
    } while (0)
#define RD4(Y, nx) do { v1[Y] = lds_rd128((nx) + 3072); v0[Y] = lds_rd128((nx) + 2048); \
        k1[Y] = lds_rd128((nx) + 1024); k0[Y] = lds_rd128(nx); } while (0)
#define KT(X, nx, PRE0) do { \
        HALF(sA, oA, lA, sB, qb0, qb1, k0[X], k1[X], oB, lB, v1[X ^ 1], v0[X], PRE0; RD4(X ^ 1, nx)); \
        HALF(sB, oB, lB, sA, qa0, qa1, k0[X ^ 1], k1[X ^ 1], oA, lA, v1[X], v0[X], (void)0); \
    } while (0)
#define RING_BAR() do { unsigned long long sv_; \
        asm volatile("s_cmp_eq_u32 %1, 7\n\ts_cbranch_scc1 1f\n\ts_barrier\n1:\n\ts_mov_b64 %0, exec\n\ts_mov_b64 exec, 1\n\tds_add_u32 %2, %3\n\ts_mov_b64 exec, %0" \
                     : "=&s"(sv_) : "s"(st), "v"(fl + C_LD), "v"(1u) : "memory", "scc"); } while (0)
    SBAR();
#pragma unroll 1
    for (int st = 0; st < 8; ++st) {
        const lds_ptr cur = lp + (st & 1) * K2_SLOT;
        const lds_ptr nxt = lp + ((st + 1) & 1) * K2_SLOT;
        PRIO(2);
        KT(0, cur + 4096, (void)0);
        PRIO(1);
        KT(1, cur + 8192, (void)0);
        PRIO(0);
        KT(0, cur + 12288, (void)0);
        PRIO(3);
        KT(1, nxt, RAW_BAR(); SBAR());
    }
    oB = SPV(v1[1], HI8(sB), oB);
    SBAR();
    lB = SSUM(HI8(sB), lB);
    SBAR();
    DG(4);
    {
        const lds_ptr xo = lds + K2_SLOT + w * 4096 + lane * 16;
        *(LAS float*)(lds + K2_LX + w * 256 + lane * 4) = lB[0];
#pragma unroll
        for (int i = 0; i < 4; ++i) *(LAS f32x4*)(xo + i * 1024) = f32x4{oB[4 * i], oB[4 * i + 1], oB[4 * i + 2], oB[4 * i + 3]};
        lds_add(fl + F_X + w * 4, lane);
        lds_add(fl + C_END + half * 4, lane);
        lds_wait_ge(fl + F_X + (w ^ 4) * 4, 1u);
        const lds_ptr xi = lds + K2_SLOT + (w ^ 4) * 4096 + lane * 16;
        f32x4 t[4];
        const float lx = *(const LAS float*)(lds + K2_LX + (w ^ 4) * 256 + (lane & 31) * 4);
        { auto rr = __builtin_amdgcn_permlane32_swap(__float_as_uint(lA[0]), __float_as_uint(lA[0]), false, false); lA[0] = __uint_as_float(rr[0]); }
#pragma unroll
        for (int i = 0; i < 4; ++i) t[i] = *(const LAS f32x4*)(xi + i * 1024);
        SBAR();
        lA[0] += lx;
#pragma unroll
        for (int i = 0; i < 16; ++i) { float o = oA[i]; acc_vv(o, t[i >> 2][i & 3]); oA[i] = o; }
    }
    DG(9);
    const float l = lA[0];
    u32x4 on0, on1;
    auto finish = [&](float ls) {
        float rl = __builtin_amdgcn_rcpf(ls);
        asm volatile("s_nop 1" : "+v"(rl));
        f32x16 o;
#pragma unroll
        for (int i = 0; i < 16; ++i) o[i] = mul_vv(oA[i], rl);
        on0 = nz4(pack8n(o, 0)); on1 = nz4(pack8n(o, 1));
    };
    finish(l);
    {
        float t0, t1;
        asm("v_mul_f32_e32 %0, 0, %2\n\tv_mul_f32_e32 %1, 0, %3\n\tv_fmac_f32_e32 %0, 0, %4\n\tv_fmac_f32_e32 %1, 0, %5\n\tv_fmac_f32_e32 %0, 0, %6\n\tv_fmac_f32_e32 %1, 0, %7\n\tv_fmac_f32_e32 %0, 0, %8\n\tv_fmac_f32_e32 %1, 0, %9\n\t"
            "v_fmac_f32_e32 %0, 0, %10\n\tv_fmac_f32_e32 %1, 0, %11\n\tv_fmac_f32_e32 %0, 0, %12\n\tv_fmac_f32_e32 %1, 0, %13\n\tv_fmac_f32_e32 %0, 0, %14\n\tv_fmac_f32_e32 %1, 0, %15\n\tv_fmac_f32_e32 %0, 0, %16\n\tv_fmac_f32_e32 %1, 0, %17"
            : "=&v"(t0), "=&v"(t1) : "v"(oA[0]), "v"(oA[1]), "v"(oA[2]), "v"(oA[3]), "v"(oA[4]), "v"(oA[5]), "v"(oA[6]), "v"(oA[7]), "v"(oA[8]), "v"(oA[9]), "v"(oA[10]), "v"(oA[11]), "v"(oA[12]), "v"(oA[13]), "v"(oA[14]), "v"(oA[15]));
        const bool ok = (l <= 1.0e30f) && (l >= 1.0e-18f) && (t0 + t1 == 0.f);
        if (__builtin_expect(__any(!ok), 0)) {
            float m = -INFINITY, ls = 0.f;
            oA = f32x16{};
#pragma unroll 1
            for (int t = 0; t < 64; ++t) {
                const char* g = kvg + t * 4096 + lane * 16;
                const bf16x8 a0 = *(const bf16x8*)g, a1 = *(const bf16x8*)(g + 1024);
                const bf16x8 b0 = *(const bf16x8*)(g + 2048), b1 = *(const bf16x8*)(g + 3072);
                f32x16 s = MFMA32(a0, qa0, f32x16{});
                s = MFMA32(a1, qa1, s);
                float tm = s[0];
#pragma unroll
                for (int i = 1; i < 16; ++i) tm = __builtin_fmaxf(tm, s[i]);
                auto rr = __builtin_amdgcn_permlane32_swap(__float_as_uint(tm), __float_as_uint(tm), false, false);
                tm = __builtin_fmaxf(__uint_as_float(rr[0]), __uint_as_float(rr[1]));
                const float mn = __builtin_fmaxf(m, tm);
                const float alpha = __builtin_amdgcn_exp2f(m - mn);
                m = mn;
                float ps = 0.f;
#pragma unroll
                for (int i = 0; i < 16; ++i) { s[i] = bfhi(__float_as_uint(__builtin_amdgcn_exp2f(s[i] - mn))); ps += s[i]; oA[i] *= alpha; }
                ls = ls * alpha + ps;
                oA = SPV(b0, LO8(s), oA);
                oA = SPV(b1, HI8(s), oA);
            }
            auto rr = __builtin_amdgcn_permlane32_swap(__float_as_uint(ls), __float_as_uint(ls), false, false);
            asm volatile("s_nop 7\n\ts_nop 7" : "+v"(oA));
            finish(__uint_as_float(rr[0]) + __uint_as_float(rr[1]));
        }
    }
    u32x4 w3[4];
    {
        const unsigned og = (unsigned)WS_O + (unsigned)(TA * 32 + h) * 2048u + lane * 16;
        st_sc1(on0, rs, og);
        st_sc1(on1, rs, og + 1024);
        lds_add(fl + C_S1, lane);
        const char* s2 = wsrc(half ^ 1, 32 + (w & 3) * 4);
        const char* s3 = wsrc(half, 48 + (w & 3) * 4);
        lds_wait_ge(fl + C_S1, 12u);
        DG(5);
#pragma unroll
        for (int i = 0; i < 4; ++i)
            glds16p<K2_WNT>(wvoff, s2 + i * 16384, lds0 + K2_SLOT + ((w ^ 4) * 4 + i) * 1024);
#pragma unroll
        for (int i = 0; i < 4; ++i) w3[i] = gld128(wvoff, s3 + i * 16384);
    }
    {
        const int half = w & 1;
        const int T3 = tt * 4 + (w >> 1);
        const unsigned og = (unsigned)WS_O + (unsigned)T3 * 65536u + half * 32768 + lane * 16;
        const int r = lane & 31, hh = lane >> 5;
        const int g = lane >> 4, q = (lane & 15) >> 2, p = lane & 3;
        int woff[2];
#pragma unroll
        for (int n = 0; n < 2; ++n) {
            const int c = 4 * (n ^ half) + 2 * (p & 1) + (g & 1);
            woff[n] = (4 * hh + q) * 128 + ((c ^ ((q >> 1) << 2)) << 4) + (p >> 1) * 8;
        }
        f32x16 acc[2] = {f32x16{}, f32x16{}};
        lds_ptr wl[3][2] = {{lds + half * 16384 + woff[0], lds + half * 16384 + woff[1]},
                            {lds + K2_SF + half * 16384 + woff[0], lds + K2_SF + half * 16384 + woff[1]},
                            {lds + K2_CV + half * 2 * CV_BYTES + woff[0], lds + K2_CV + half * 2 * CV_BYTES + woff[1]}};
        asm volatile("" : "+v"(wl[0][0]), "+v"(wl[0][1]), "+v"(wl[1][0]), "+v"(wl[1][1]), "+v"(wl[2][0]), "+v"(wl[2][1]));
#define MS(m) ((m) < 64 - 2 * TL ? (m) >> 1 : 32 - TL + ((m) - (64 - 2 * TL)) % TL)
#define MN(m) ((m) < 64 - 2 * TL ? (m) & 1 : (m) < 64 - TL)
        constexpr int WR = 2 * WD + 2;
        s16x4 wa[WR], wb[WR];
#define LDW(m) do { const int s_ = MS(m), f_ = s_ < 8 ? 1 : s_ < 24 ? 0 : 2; \
            const int o_ = f_ == 2 ? ((s_ >> 2) & 1) * CV_BYTES + (s_ & 3) * 2048 : (f_ ? 0 : ((s_ >> 3) - 1) * K2_SLOT) + (s_ & 7) * 2048; \
            wa[(m) % WR] = lds_tr(wl[f_][MN(m)] + o_); wb[(m) % WR] = lds_tr(wl[f_][MN(m)] + o_ + 1024); SBAR(); } while (0)
#pragma unroll
        for (int m = 0; m < 2 * WD; ++m) LDW(m);
        u32x4 of[OWIN];
#define LDO(i) do { of[(i) % OWIN] = ld_sc1s(rs, og, (((i) + krot) & 31) * 1024); SBAR(); } while (0)
        for (unsigned spins = 0; spins < (1u << 20); ++spins) {
#pragma unroll
            for (int i = 0; i < OW0; ++i) LDO(i);
            unsigned m = mx8(of[0], of[1]);
#pragma unroll
            for (int i = 2; i < OW0; ++i) m = mx4(m, of[i]);
            if (!missing(m)) break;
            DGC(12);
        }
        lds_add(fl + C_S2, lane);
#pragma unroll
        for (int i = OW0; i < OWIN; ++i) LDO(i);
        DG(6);
        __builtin_amdgcn_s_setprio(P3_PRIO);
        SBAR();
        unsigned fv = 0;
#define FLAG_RD(a) do { fv = *(volatile LAS unsigned*)(a); SBAR(); } while (0)
#define FLAG_CHK(a, n) do { if (__builtin_expect((unsigned)__builtin_amdgcn_readfirstlane(fv) < (n), 0)) lds_wait_ge(a, n); CFENCE(); SBAR(); } while (0)
        lds_ptr xo = lds + K2_SF + w * 4096 + lane * 16, xi = lds + K2_SF + (w ^ 1) * 4096 + lane * 16;
        int bo = (T3 * 32 + (lane & 15)) * 4096 + 2048 + nt * 128 + half * 64 + g * 16;
        lds_ptr w3d = lds + K2_CV + (w >> 1) * CV_BYTES + (w & 1) * 4096 + lane * 16;
        asm volatile("" : "+v"(xo), "+v"(xi), "+v"(bo), "+v"(w3d));
        f32x4 xt[4];
        constexpr int M3 = TL > 8 ? 64 - 2 * TL + TL - 8 - 2 * WD : 48 - 2 * WD;
        sfor(std::make_integer_sequence<int, 64>{}, [&](auto mc) __attribute__((always_inline)) {
            constexpr int m = decltype(mc)::value, s = MS(m), n = MN(m);
            constexpr bool first = m < 64 - 2 * TL ? n == 0 : n == 1;
            constexpr bool last = m < 64 - 2 * TL ? n == 1 : n == 0;
            acc[n] = MFMA32(cat8(wa[m % WR], wb[m % WR]), PB(of[s % OWIN]), acc[n]);
            SBAR();
            if constexpr (m == 32 - 2 * WD) FLAG_CHK(fl + C_S2, 8u);
            if constexpr (m == M3) FLAG_CHK(fl + C_S3, 8u);
            if constexpr (m + 2 * WD < 64) LDW(m + 2 * WD);
            if constexpr (first && (s & 1) && s + 1 >= OW0 && s + 1 < 32) {
                if (__builtin_expect(missing(mx8(of[(s + 1) % OWIN], of[(s + 2) % OWIN])), 0)) {
#pragma unroll 1
                    for (unsigned spins = 0; spins < (1u << 20); ++spins) {
                        __builtin_amdgcn_s_sleep(2);
                        LDO(s + 1); LDO(s + 2);
                        DGC(13);
                        if (!missing(mx8(of[(s + 1) % OWIN], of[(s + 2) % OWIN]))) break;
                    }
                }
            }
            if constexpr (last && s + OWIN < 32) LDO(s + OWIN);
            if constexpr (m == 15 - 2 * WD) lds_add(fl + C_U0, lane);
            if constexpr (m == 29 - 2 * WD) FLAG_RD(fl + C_S2);
            if constexpr (m == W3M - 3) FLAG_RD(fl + C_HF);
            if constexpr (m == W3M) FLAG_CHK(fl + C_HF, 4u);
            if constexpr (m >= W3M && m < W3M + 8 && ((m - W3M) & 1) == 0) {
                asm volatile("" : "+v"(w3[((m - W3M) >> 1) & 3]));
                *(LAS u32x4*)(w3d + (((m - W3M) >> 1) & 3) * 1024) = w3[((m - W3M) >> 1) & 3];
            }
            if constexpr (m == W3M + 6) lds_add(fl + C_S3, lane);
            if constexpr (m == M3 - 3) FLAG_RD(fl + C_S3);
            if constexpr (m == 64 - TL - 3) FLAG_RD(fl + C_U0);
            if constexpr (m == 64 - TL) FLAG_CHK(fl + C_U0, 8u);
            if constexpr (m == 64 - TL + 1 || m == 64 - TL + 2) {
                constexpr int i = 2 * (m - (64 - TL + 1));
                *(LAS f32x4*)(xo + i * 1024) = f32x4{acc[1][4 * i], acc[1][4 * i + 1], acc[1][4 * i + 2], acc[1][4 * i + 3]};
                *(LAS f32x4*)(xo + i * 1024 + 1024) = f32x4{acc[1][4 * i + 4], acc[1][4 * i + 5], acc[1][4 * i + 6], acc[1][4 * i + 7]};
            }
            if constexpr (m == 64 - TL + 2) lds_add(fl + F_R + w * 4, lane);
            if constexpr (m == XR_AT - 2) FLAG_RD(fl + F_R + (w ^ 1) * 4);
            if constexpr (m == XR_AT) {
                FLAG_CHK(fl + F_R + (w ^ 1) * 4, 1u);
#pragma unroll
                for (int i = 0; i < 4; ++i) xt[i] = *(const LAS f32x4*)(xi + i * 1024);
            }
            if constexpr (ZL_GATE && last && s + OWIN == ZL_AT) lds_add(fl + C_OL, lane);
            if constexpr (m == 2 * P3_T1 + 1) __builtin_amdgcn_s_setprio(P3_PRIO - 1);
            SBAR();
            if constexpr (m == 31) DG(11);
            if constexpr (m == 47) DG(14);
        });
        asm volatile("s_nop 11" : "+v"(acc[0]));
        DG(7);
#pragma unroll
        for (int i = 0; i < 16; ++i) { float o = xt[i >> 2][i & 3]; acc_vv(o, acc[0][i]); acc[1][i] = o; }
        u32x4 pa = pack8n(acc[1], 0), pb = pack8n(acc[1], 1);
#pragma unroll
        for (int i = 0; i < 4; ++i) {
            auto sw = __builtin_amdgcn_permlane16_swap(pa[i], pb[i], false, false);
            pa[i] = sw[0]; pb[i] = sw[1];
        }
        asm volatile("" :: "v"(acc[0]));
        __builtin_amdgcn_raw_buffer_store_b128(pa, ro, bo, 0, OUT_AUX);
        __builtin_amdgcn_raw_buffer_store_b128(pb, ro, bo, 65536, OUT_AUX);
    }
    DG_END(8);
}

extern "C" void kernel_launch(void* const* d_in, const int* in_sizes, int n_in,
                              void* d_out, int out_size, void* d_ws, size_t ws_size,
                              hipStream_t stream) {
    const void* A_q = d_in[0];
    const void* A_k = d_in[1];
    const void* A_v = d_in[2];
    const void* A_shift = d_in[3];
    const void* A_ssmk = d_in[4];
    const void* A_D = d_in[5];
    const void* B_x = d_in[6];
    const void* B_wqkv = d_in[7];
    const void* B_wo = d_in[8];
    char* ws = (char*)d_ws;
    k1_qkv<<<256, 512, 0, stream>>>((const char*)B_x, (const char*)B_wqkv, ws);
    k2_attn<<<256, 768, 0, stream>>>(ws, (const char*)A_q, (const char*)A_k, (const char*)A_v, (const char*)A_shift,
                                     (const char*)A_ssmk, (const unsigned*)A_D, (const char*)B_wo, (char*)d_out);
}
```

```cpp
#include <hip/hip_runtime.h>
#include <hip/hip_bf16.h>
#include <stdint.h>
#include <utility>

typedef short bf16x8 __attribute__((ext_vector_type(8)));
typedef short s16x4 __attribute__((ext_vector_type(4)));
typedef float f32x16 __attribute__((ext_vector_type(16)));
typedef float f32x4 __attribute__((ext_vector_type(4)));
typedef float f32x2 __attribute__((ext_vector_type(2)));
typedef unsigned u32x4 __attribute__((ext_vector_type(4)));
typedef unsigned u32x2 __attribute__((ext_vector_type(2)));
typedef __bf16 bf16x2_t __attribute__((ext_vector_type(2)));

#define LAS __attribute__((address_space(3)))
typedef LAS char* lds_ptr;
#define DI __device__ __forceinline__
#define SBAR() __builtin_amdgcn_sched_barrier(0)
#define MFMA32(a, b, c) __builtin_amdgcn_mfma_f32_32x32x16_bf16((a), (b), (c), 0, 0, 0)
#define MFMA16(a, b, c) __builtin_amdgcn_mfma_f32_16x16x32_bf16((a), (b), (c), 0, 0, 0)
typedef __bf16 bfx8_t __attribute__((ext_vector_type(8)));
typedef __bf16 bfx16_t __attribute__((ext_vector_type(16)));
typedef float f32x8 __attribute__((ext_vector_type(8)));
#define SPM 1
#define WAIT_BAR(N) asm volatile("s_waitcnt vmcnt(" #N ") lgkmcnt(0)\n\ts_barrier" ::: "memory")
#define LGKM_BAR() asm volatile("s_waitcnt lgkmcnt(0)\n\ts_barrier" ::: "memory")

#define DG_DECL(K)
#define DG(i)
#define DG_END(i)
#define DGC(i)

#define DGV(i)
#define HS(i) SBAR(); const unsigned hs##i = (unsigned)__builtin_amdgcn_s_memrealtime(); SBAR()
#define HC(i) SBAR(); const unsigned hc##i = (unsigned)__builtin_amdgcn_s_memtime(); SBAR()
#define HB 0
#define HQ (400)
constexpr int S_LEN = 2048, DM = 1024, NH = 32;

#define KQ 1
#define OQ 0
#define QT(h, T) (KQ ? (h) * 64 + (T) : (T) * 32 + (h))
constexpr size_t WS_Q = 0;
constexpr size_t WS_KV = 4u << 20;
constexpr size_t WS_O = OQ ? WS_Q : 12u << 20;
constexpr size_t WS_KVR = 16u << 20;
#define ZK 0
constexpr size_t WS_ZT = ZK ? WS_KVR : 20u << 20;
constexpr size_t WS_CNT = 24u << 20;

template <int... I, class F> DI void sfor(std::integer_sequence<int, I...>, F&& f) { (f(std::integral_constant<int, I>{}), ...); }
DI unsigned cvtpk(float lo, float hi) {
    f32x2 v = {lo, hi};
    return __builtin_bit_cast(unsigned, __builtin_convertvector(v, bf16x2_t));
}
DI float bflo(unsigned u) { return __uint_as_float(u << 16); }
DI float bfhi(unsigned u) { return __uint_as_float(u & 0xffff0000u); }

DI void glds16(unsigned voff, const void* sbase, unsigned lds_dst) {
    unsigned sv;
    asm volatile("s_mov_b32 %0, m0\n\ts_mov_b32 m0, %3\n\ts_nop 0\n\tglobal_load_lds_dwordx4 %1, %2\n\ts_mov_b32 m0, %0"
                 : "=&s"(sv) : "v"(voff), "s"(sbase), "s"(lds_dst) : "memory");
}
template <int NT> DI void glds16p(unsigned voff, const void* sbase, unsigned lds_dst) {
    if constexpr (NT) {
        unsigned sv;
        asm volatile("s_mov_b32 %0, m0\n\ts_mov_b32 m0, %3\n\ts_nop 0\n\tglobal_load_lds_dwordx4 %1, %2 nt\n\ts_mov_b32 m0, %0"
                     : "=&s"(sv) : "v"(voff), "s"(sbase), "s"(lds_dst) : "memory");
    } else glds16(voff, sbase, lds_dst);
}
#define K1_XNT 0
#define K1_WNT 0
#define K2_SNT 1
#define K2_QNT 0
#define K2_WNT 0
#define OUT_AUX 16
#define K1_SAUX 16
DI bf16x8 lds_rd128(lds_ptr p) { return *(const LAS bf16x8*)p; }
DI s16x4 lds_tr(lds_ptr p) {
    return __builtin_bit_cast(s16x4, __builtin_amdgcn_ds_read_tr16_b64_v4i16((LAS s16x4*)p));
}
DI bf16x8 cat8(s16x4 a, s16x4 b) { return __builtin_shufflevector(a, b, 0, 1, 2, 3, 4, 5, 6, 7); }
DI u32x4 pack8(const f32x16& x, int s, float sc) {
    u32x4 p;
    p[0] = cvtpk(x[8 * s + 0] * sc, x[8 * s + 1] * sc);
    p[1] = cvtpk(x[8 * s + 2] * sc, x[8 * s + 3] * sc);
    p[2] = cvtpk(x[8 * s + 4] * sc, x[8 * s + 5] * sc);
    p[3] = cvtpk(x[8 * s + 6] * sc, x[8 * s + 7] * sc);
    return p;
}
DI u32x4 pack8n(const f32x16& x, int s) {
    u32x4 p;
    p[0] = cvtpk(x[8 * s + 0], x[8 * s + 1]);
    p[1] = cvtpk(x[8 * s + 2], x[8 * s + 3]);
    p[2] = cvtpk(x[8 * s + 4], x[8 * s + 5]);
    p[3] = cvtpk(x[8 * s + 6], x[8 * s + 7]);
    return p;
}
DI void rnd2(float& a, float& b) { unsigned u = cvtpk(a, b); a = bflo(u); b = bfhi(u); }

#define CONV_PACE()
#define RAW_BAR() asm volatile("s_barrier" ::: "memory")
DI unsigned gld32(unsigned voff, const void* sbase) {
    unsigned r;
    asm volatile("global_load_dword %0, %1, %2" : "=v"(r) : "v"(voff), "s"(sbase) : "memory");
    return r;
}
DI u32x2 gld64(unsigned voff, const void* sbase) {
    u32x2 r;
    asm volatile("global_load_dwordx2 %0, %1, %2" : "=v"(r) : "v"(voff), "s"(sbase) : "memory");
    return r;
}
DI float mul_sv(float s, float v) { float r; asm("v_mul_f32_e32 %0, %1, %2" : "=v"(r) : "s"(s), "v"(v)); return r; }
DI float mul_vv(float a, float b) { float r; asm("v_mul_f32_e32 %0, %1, %2" : "=v"(r) : "v"(a), "v"(b)); return r; }
DI float add_vv(float a, float b) { float r; asm("v_add_f32_e32 %0, %1, %2" : "=v"(r) : "v"(a), "v"(b)); return r; }
DI void acc_vv(float& a, float b) { asm("v_add_f32_e32 %0, %0, %1" : "+v"(a) : "v"(b)); }
DI void fmac_vv(float& acc, float a, float b) { asm("v_fmac_f32_e32 %0, %1, %2" : "+v"(acc) : "v"(a), "v"(b)); }
DI void fmac_sv(float& acc, float s, float v) { asm("v_fmac_f32_e32 %0, %1, %2" : "+v"(acc) : "s"(s), "v"(v)); }
typedef __attribute__((address_space(1))) unsigned gu32;
#define RLX_AGENT __ATOMIC_RELAXED, __HIP_MEMORY_SCOPE_AGENT
#define CFENCE() asm volatile("" ::: "memory")
#define DRAIN() asm volatile("s_waitcnt vmcnt(0)" ::: "memory")
constexpr int AUX_SC1 = 16;
DI u32x4 ld_sc1(__amdgpu_buffer_rsrc_t rs, unsigned off) { return __builtin_amdgcn_raw_buffer_load_b128(rs, (int)off, 0, AUX_SC1); }
DI u32x4 ld_sc1s(__amdgpu_buffer_rsrc_t rs, unsigned off, int soff) { return __builtin_amdgcn_raw_buffer_load_b128(rs, (int)off, soff, AUX_SC1); }
DI void st_sc1(u32x4 v, __amdgpu_buffer_rsrc_t rs, unsigned off) { __builtin_amdgcn_raw_buffer_store_b128(v, rs, (int)off, 0, AUX_SC1); }
constexpr unsigned EMPTY = 0xffffffffu;
DI unsigned nz(unsigned x) { return x < EMPTY - 1u ? x : EMPTY - 1u; }
DI u32x4 nz4(u32x4 v) { return u32x4{nz(v[0]), nz(v[1]), nz(v[2]), nz(v[3])}; }
DI float fmx(float a, float b) { return __builtin_elementwise_maximum(a, b); }
DI unsigned umax(unsigned a, unsigned b) { return a > b ? a : b; }
DI unsigned umax3(unsigned a, unsigned b, unsigned c) { return umax(umax(a, b), c); }
DI unsigned mx4(unsigned m, u32x4 v) { return umax3(umax3(m, v[0], v[1]), v[2], v[3]); }
DI unsigned mx8(u32x4 a, u32x4 b) { return umax(umax3(umax3(umax3(a[0], a[1], a[2]), a[3], b[0]), b[1], b[2]), b[3]); }
DI bool missing(unsigned m) { return __builtin_amdgcn_ballot_w64(m == EMPTY) != 0; }
DI unsigned lds_add_rtn(lds_ptr p, int lane) {
    unsigned old = 0;
    CFENCE();
    if (lane == 0) old = __hip_atomic_fetch_add((LAS unsigned*)p, 1u, __ATOMIC_RELAXED, __HIP_MEMORY_SCOPE_WORKGROUP);
    old = __builtin_amdgcn_readfirstlane(old);
    CFENCE();
    return old;
}
DI void lds_add(lds_ptr p, int lane) {
    unsigned long long sv;
    asm volatile("s_mov_b64 %0, exec\n\ts_mov_b64 exec, 1\n\tds_add_u32 %1, %2\n\ts_mov_b64 exec, %0"
                 : "=&s"(sv) : "v"(p), "v"(1u) : "memory");
}
DI void lds_wait_ge(lds_ptr p, unsigned v) {
    CFENCE();
    while ((unsigned)__builtin_amdgcn_readfirstlane(*(volatile LAS unsigned*)p) < v) __builtin_amdgcn_s_sleep(1);
    CFENCE();
}
DI void poll_ge(gu32* word, unsigned want) {
    CFENCE();
    for (unsigned spins = 0; spins < (1u << 22); ++spins) {
        if ((unsigned)__builtin_amdgcn_readfirstlane(__hip_atomic_load(word, RLX_AGENT)) >= want) break;
        __builtin_amdgcn_s_sleep(1);
    }
    __builtin_amdgcn_fence(__ATOMIC_ACQUIRE, "wavefront");
    CFENCE();
}
#define PB(x) __builtin_bit_cast(bf16x8, x)
#define PRIO(x) do { __builtin_amdgcn_s_setprio(x); SBAR(); } while (0)
DI u32x4 gld128(unsigned voff, const void* sbase) {
    u32x4 r;
    asm volatile("global_load_dwordx4 %0, %1, %2" : "=v"(r) : "v"(voff), "s"(sbase) : "memory");
    return r;
}

#define K1_UNR 15
#define K1_LA 5
#define K1_ROT ((tb & 3) * 4)
#define K1_PF 0
#define K1_PG 0
#define K1_TW 0
#define CPF 3
#define PK2 0
#define BK1 0
#define PKC 0
#define PKC_AT 6
#define PK2_AT 13
#define CPF_POS 1
#define PG1_AT 0
#define PG2_AT 2
#define PG3_AT 6
#define K1_WB(n) WAIT_BAR(1)
#define PF1_AT -1
#define PF1_LO 4
#define PF1_N 4
#define PF2_AT 1
#define PF2_LO 8
#define PF2_N 4
#define PF3_AT 5
#define PF3_LO 12
#define PF3_N 4
#define K1_KR ((kt + rot) & 15)
#define K1_ST(v, rs, off) __builtin_amdgcn_raw_buffer_store_b128(v, rs, (int)(off), 0, K1_SAUX)
constexpr int K1_STAGE = 40960, K1U = K1_UNR;
__global__ __launch_bounds__(512, 2) void k1_qkv(const char* __restrict__ X, const char* __restrict__ W,
                                                 char* __restrict__ ws, const char* __restrict__ Wo,
                                                 const char* __restrict__ Aq, const char* __restrict__ Ak,
                                                 const char* __restrict__ Av, const char* __restrict__ ssmk) {
    __shared__ __attribute__((aligned(16))) char lds_raw[4 * K1_STAGE];
    const lds_ptr lds = (lds_ptr)lds_raw;
    const unsigned lds0 = (unsigned)(uintptr_t)lds;
    const int tid = threadIdx.x, lane = tid & 63;
    const int w = __builtin_amdgcn_readfirstlane(tid >> 6);
    const int b = blockIdx.x, xcd = b & 7, idx = b >> 3;
    const int hp = (xcd >> 1) * 4 + (idx & 3), tb = (xcd & 1) * 8 + (idx >> 2);
    DG_DECL(0)
    HS(0);

    const int lw = w & 3;
    const int rot = K1_ROT;
    unsigned voffX[4], voffW[2];
#pragma unroll
    for (int pp = 0; pp < 4; ++pp) {
        const int row = 32 * lw + 8 * pp + (lane >> 3);
        const int ch = (lane & 7) ^ ((row >> 1) & 7);
        voffX[pp] = (unsigned)(tb * 128 + row) * 2048u + ch * 16;
    }
#pragma unroll
    for (int pp = 0; pp < 2; ++pp) {
        const int krow = 16 * lw + 8 * pp + (lane >> 3);
        const int ch = (lane & 7) ^ (((lane >> 4) & 1) << 2);
        voffW[pp] = (unsigned)krow * 6144u + ch * 16;
    }
    auto issue = [&](int kt, int lo, int hi) {
        const int kr = K1_KR;
        const char* xb = X + kr * 128;
        const char* wb = W + (size_t)kr * (64 * 6144) + hp * 128;
        const unsigned st = lds0 + (kt & 3) * K1_STAGE;
#pragma unroll
        for (int pp = 0; pp < 4; ++pp)
            if (pp >= lo && pp < hi) glds16p<K1_XNT>(voffX[pp], xb, st + (4 * lw + pp) * 1024);
#pragma unroll
        for (int sec = 0; sec < 3; ++sec)
#pragma unroll
            for (int pp = 0; pp < 2; ++pp)
                if (4 + 2 * sec + pp >= lo && 4 + 2 * sec + pp < hi)
                    glds16p<K1_WNT>(voffW[pp], wb + sec * 2048, st + 16384 + sec * 8192 + (2 * lw + pp) * 1024);
    };
    DG(13);
    unsigned cpf;
    auto codepf = [&]() {
        unsigned long long pc; unsigned lo, hi;
        asm volatile("s_getpc_b64 %0\n.Lcp_k1a:\n\ts_mov_b32 %1, %3\n\ts_mov_b32 %2, %4" : "=s"(pc), "=s"(lo), "=s"(hi) : "n"(0), "n"(0));
        if (CPF & 1) lo = 0; else asm volatile("s_mov_b32 %0, .Lcp_k2a-.Lcp_k1a" : "=s"(lo));
        if (CPF & 2) asm volatile("s_mov_b32 %0, .Lcp_k2z-.Lcp_k1a" : "=s"(hi)); else asm volatile("s_mov_b32 %0, .Lcp_k1z-.Lcp_k1a" : "=s"(hi));
        unsigned off = lo + (unsigned)(idx + 32 * (lane & 7) + 256 * w) * 64u;
        off = off < hi ? off : hi;
        asm volatile("global_load_dword %0, %1, %2" : "=v"(cpf) : "v"(off), "s"(pc) : "memory");
    };
    if (w < 4) { issue(0, 0, 5); issue(1, 0, 5); codepf(); }
    else { issue(0, 5, 10); issue(1, 5, 10); }
    DG(14);
    int w2 = w;
    asm volatile("" : "+s"(w2));

    if (w2 >= 4) {
#define WAITN_BAR(N) asm volatile("s_waitcnt vmcnt(%0) lgkmcnt(0)\n\ts_barrier" :: "n"(N) : "memory")
        issue(2, 0, K1_LA);
        asm volatile("s_waitcnt vmcnt(%0)" :: "n"(5 + K1_LA) : "memory");
#pragma unroll 1
        for (int kt = 0; kt < 13; ++kt) {
            WAITN_BAR(10 + K1_LA);
            issue(kt + 2, K1_LA, 10);
            issue(kt + 3, 0, K1_LA);
        }
        WAITN_BAR(10 + K1_LA);
        issue(15, K1_LA, 10);
        WAIT_BAR(10);
        WAIT_BAR(0);
        if (lw == 0 && lane < 2) *(unsigned*)(ws + WS_CNT + b * 256 + lane * 128) = 0u;
        DG_END(5);
        return;
    }

    const int wm = w >> 1, wn = w & 1;
    const int r = lane & 31, hh = lane >> 5;
    const int g = lane >> 4, q = (lane & 15) >> 2, p = lane & 3;
    const int xoff0 = (64 * wm + r) * 128 + ((hh ^ ((r >> 1) & 7)) << 4);
    const int woff0 = 16384 + (8 * hh + q) * 128 + (((4 * wn + 2 * (g & 1) + (p >> 1)) ^ ((q >> 1) << 2)) << 4) + (p & 1) * 8;

    f32x16 acc[2][3];
#pragma unroll
    for (int m = 0; m < 2; ++m)
#pragma unroll
        for (int s = 0; s < 3; ++s) acc[m][s] = f32x16{};
    bf16x8 xf[2][2];
    s16x4 wl[2][3], wh[2][3];
#define K1_WF(f, sec) cat8(wl[f][sec], wh[f][sec])
#define K1_STEP(F, st, t, BAR_HERE) do { \
        const lds_ptr xp_ = (st) + (xoff0 ^ ((t) << 5)); const lds_ptr wp_ = (st) + woff0 + (t) * 2048; \
        acc[0][0] = MFMA32(K1_WF(F, 0), xf[F][0], acc[0][0]); SBAR(); \
        if (BAR_HERE) { K1_WB(BAR_HERE); SBAR(); } \
        xf[F ^ 1][0] = lds_rd128(xp_); wl[F ^ 1][0] = lds_tr(wp_); wh[F ^ 1][0] = lds_tr(wp_ + 512); SBAR(); \
        acc[0][1] = MFMA32(K1_WF(F, 1), xf[F][0], acc[0][1]); SBAR(); \
        wl[F ^ 1][1] = lds_tr(wp_ + 8192); wh[F ^ 1][1] = lds_tr(wp_ + 8192 + 512); SBAR(); \
        acc[0][2] = MFMA32(xf[F][0], K1_WF(F, 2), acc[0][2]); SBAR(); \
        wl[F ^ 1][2] = lds_tr(wp_ + 16384); wh[F ^ 1][2] = lds_tr(wp_ + 16384 + 512); xf[F ^ 1][1] = lds_rd128(xp_ + 4096); SBAR(); \
        acc[1][0] = MFMA32(K1_WF(F, 0), xf[F][1], acc[1][0]); SBAR(); \
        acc[1][1] = MFMA32(K1_WF(F, 1), xf[F][1], acc[1][1]); \
        acc[1][2] = MFMA32(xf[F][1], K1_WF(F, 2), acc[1][2]); SBAR(); \
    } while (0)

    WAIT_BAR(6);
    DG(3);
    HS(1);
    HC(1);
    {
        const lds_ptr xp_ = lds + xoff0; const lds_ptr wp_ = lds + woff0;
        xf[0][0] = lds_rd128(xp_); xf[0][1] = lds_rd128(xp_ + 4096);
#pragma unroll
        for (int sec = 0; sec < 3; ++sec) { wl[0][sec] = lds_tr(wp_ + sec * 8192); wh[0][sec] = lds_tr(wp_ + sec * 8192 + 512); }
    }
    SBAR();
#pragma unroll K1U
    for (int kt = 0; kt < 15; ++kt) {
        const lds_ptr st = lds + (kt & 3) * K1_STAGE;
        const lds_ptr sn = lds + ((kt + 1) & 3) * K1_STAGE;
        K1_STEP(0, st, 1, false);
        K1_STEP(1, st, 2, false);
        K1_STEP(0, st, 3, false);
        K1_STEP(1, sn, 0, kt + 1);
    }
    {
        const lds_ptr st = lds + 3 * K1_STAGE;
        K1_STEP(0, st, 1, false);
        K1_STEP(1, st, 2, false);
        K1_STEP(0, st, 3, false);
#pragma unroll
        for (int m = 0; m < 2; ++m) {
            acc[m][1] = MFMA32(K1_WF(1, 1), xf[1][m], acc[m][1]);
            acc[m][2] = MFMA32(xf[1][m], K1_WF(1, 2), acc[m][2]);
            acc[m][0] = MFMA32(K1_WF(1, 0), xf[1][m], acc[m][0]);
        }
    }
    DG(4);
    HS(2); HC(8);
    if (b == HB && tid == 0) { unsigned* hp_ = (unsigned*)(ws + (30u << 20)); hp_[0] = hs0; hp_[1] = hs1; hp_[2] = hs2; hp_[3] = hc8 - hc1; }
    asm volatile(".Lcp_k1z:");
    {
        const int h = 2 * hp + wn;
        const float qscale = 0.17677669529663687f * 1.4426950408889634f;
        const __amdgpu_buffer_rsrc_t rs = __builtin_amdgcn_make_buffer_rsrc(ws, 0, 32 << 20, 0x00020000);
#pragma unroll
        for (int m = 0; m < 2; ++m) {
            const int T = tb * 4 + wm * 2 + m;
            const unsigned dq = (unsigned)WS_Q + QT(h, T) * 2048 + lane * 16;
            const unsigned dk = (unsigned)WS_KV + (h * 64 + T) * 4096 + lane * 16;
            const unsigned dkk = SPM ? (unsigned)WS_KV + (h * 64 + T) * 4096 + ((lane & 51) | ((lane & 4) << 1) | ((lane & 8) >> 1)) * 16 : dk;
            K1_ST(pack8n(acc[m][1], 0), rs, dkk);
            K1_ST(pack8n(acc[m][1], 1), rs, dkk + 1024);
            K1_ST(pack8n(acc[m][2], 0), rs, dk + 2048);
            K1_ST(pack8n(acc[m][2], 1), rs, dk + 3072);
            K1_ST(pack8(acc[m][0], 0, qscale), rs, dq);
            K1_ST(pack8(acc[m][0], 1, qscale), rs, dq + 1024);
        }
    }
    DG(5);
    asm volatile("s_waitcnt vmcnt(0)" :: "v"(cpf));
    DG_END(5);
}

constexpr int K2_SLOT = 32768;
constexpr int K2_SF = 65536;
constexpr int K2_CV = 98304;
constexpr int CV_HA = 0, CV_HB = 4160, CV_KV = 8320, CV_BYTES = 8320 + 480 + 4096;
constexpr int K2_LX = K2_CV + 4 * CV_BYTES;
constexpr int K2_FL = K2_LX + 2048;
constexpr int K2_SCR = K2_FL + 256;
constexpr int K2_PAD = K2_SCR + 4096;
constexpr int K2_LDS = K2_PAD + 1024;
#define NB8 0
#define WW 0
constexpr int F_X = 0, F_R = 32, F_W2 = 64, C_OST = 68, C_S1 = 76, C_S2 = 80, C_S3 = 128, C_ZST = 88, F_ZGO = 92,
              C_KST = 96, C_U0 = 100, C_LD = 108, F_KGO = 116, C_END = 120, C_OL = 72, C_HF = 84;
#define NG_IN 0
#define GATE_WORD C_END
#define GATE_VAL 0
#define X_NZ 4
#define X_NW3 8
#define ZP_GATE 4u
#define ZL_GATE 4
#define ZL_AT 31
#define Z_SLEEP0 0
#define O_SLEEP 0
#define Z_SLEEP 8
#define OWIN 16
#define OW0 8
#define OWR 8
#define WD 2
#define W3_AT 10
#define X_NOLDW 0
#define X_NOLDO 0
#define X_NOCHK 0
#define KROT (nt * 2)
#define NG_LAST 0
#define PACE_IN "s_nop 7"
#define PACE_OUT "s_nop 1"
#define P3_PRIO 1
#define TOP_SLEEP 2
#define A_AUX 16
#define B_AUX 16
#define B_LDS 0
#define TL 8
#define XR_AT 62
#define W3_AUX 0
#define LORD 0
#define AORD 0
#define S2M 17
#define W3M (LORD ? 23 : 13)
#define A16 1
#define SEL4 2
#define KV_DOT 1
#define ZE_PRIO 3
#define P3_T1 99
#define P3_T2 99
#define AUX_PRIO 2

__global__ __launch_bounds__(768, 3) void k2_attn(char* __restrict__ ws, const char* __restrict__ Aq,
                                                  const char* __restrict__ Ak, const char* __restrict__ Av,
                                                  const char* __restrict__ shiftk, const char* __restrict__ ssmk,
                                                  const unsigned* __restrict__ ssmD, const char* __restrict__ Wo,
                                                  char* __restrict__ out) {
    __shared__ __attribute__((aligned(16))) char lds_raw[K2_LDS];
    const lds_ptr lds = (lds_ptr)lds_raw;
    const lds_ptr fl = lds + K2_FL;
    const unsigned lds0 = (unsigned)(uintptr_t)lds;
    const int tid = threadIdx.x, lane = tid & 63;
    const int w = __builtin_amdgcn_readfirstlane(tid >> 6);
    const int b = blockIdx.x, xcd = b & 7, idx = b >> 3;
    const int h = xcd * 4 + (idx >> 3), qb = idx & 7;
    const int tt = xcd * 2 + (idx >> 4), nt = idx & 15;
    const char* kvg = ws + WS_KV + (size_t)h * 64 * 4096;
    const __amdgpu_buffer_rsrc_t rs = __builtin_amdgcn_make_buffer_rsrc(ws, 0, 32 << 20, 0x00020000);
    const __amdgpu_buffer_rsrc_t ro = __builtin_amdgcn_make_buffer_rsrc(out, 0, 8 << 20, 0x00020000);
    gu32* const cnt = (gu32*)(ws + WS_CNT);
    auto signal = [&]() {
        if (!BK1 || lane < 32) (void)__hip_atomic_fetch_add(cnt + ((lane & 31) * 8 + (lane < 32 ? xcd : qb)) * 64, 1u, RLX_AGENT);
    };
    const unsigned wvoff = (unsigned)(lane >> 3) * 2048u + nt * 128 + (((lane & 7) ^ (((lane >> 4) & 1) << 2)) << 4);
    const int krot = KROT;
    auto wsrc = [&](int hf, int lp) { return Wo + (size_t)(hf * 64 + ((lp + krot * 2) & 63)) * 16384; };
    DG_DECL(1)
    HS(4);
    const unsigned voff = lane * 16;
    const int cw = w & 3;
    auto issue = [&](int st) {
        const char* src = kvg + (st * 4 + cw) * 4096;
        const unsigned dst = lds0 + (st & 1) * K2_SLOT + cw * 4096;
#pragma unroll
        for (int i = 0; i < 4; ++i) glds16(voff, src + i * 1024, dst + i * 1024);
#pragma unroll
        for (int i = 0; i < 4; ++i) glds16(voff, src + 131072 + i * 1024, dst + 16384 + i * 1024);
    };
    asm volatile(".Lcp_k2a:");
    if (w >= 8) {
        __builtin_amdgcn_s_sleep(TOP_SLEEP);
#pragma unroll
        for (int i = 2; i < 8; ++i)
            glds16(voff, kvg + (i & 1) * 131072 + (i >> 1) * 4096 + cw * 1024, lds0 + (i & 1) * 16384 + (i >> 1) * 4096 + cw * 1024);
        RAW_BAR();
        issue(1);
        WAIT_BAR(8);
    } else {
        const int tq = cw * 2 + (w >> 2);
        const char* qg = ws + WS_Q + (size_t)QT(h, qb * 8 + tq) * 2048;
        glds16(voff, kvg + (w >> 2) * 131072 + cw * 1024, lds0 + (w >> 2) * 16384 + cw * 1024);
        glds16(voff, qg, lds0 + K2_SF + tq * 2048);
        glds16(voff, qg + 1024, lds0 + K2_SF + tq * 2048 + 1024);
    }
    int w2 = w;
    asm volatile("" : "+s"(w2));

    if (w2 >= 8) {
        __builtin_amdgcn_s_setprio(3);
        const int ch = xcd * 128 + idx * 4 + cw;
        const lds_ptr cv = lds + K2_CV + cw * CV_BYTES;
        {
            const u32x4 z4 = {0u, 0u, 0u, 0u};
            if (lane < 2) *(LAS u32x4*)(cv + CV_HA + lane * 16) = z4;
            if (lane < 30) *(LAS u32x4*)(cv + CV_KV + lane * 16) = z4;
            if (cw == 0 && lane < 16) *(LAS u32x4*)(fl + lane * 16) = z4;
        }
        const int c0 = xcd * 128 + cw * 32, cq = lane & 7, rg = lane >> 3;
        const int row0 = idx * 64 + rg * 8;
        const unsigned roff = (unsigned)row0 * 2048u + (c0 + cq * 4) * 2;
        const unsigned toffs = (unsigned)(c0 + (lane >> 3)) * 4096u + idx * 128;
        u32x4 sk[2];
        sk[0] = gld128((unsigned)(c0 + cq * 4) * 8u, shiftk); sk[1] = gld128((unsigned)(c0 + cq * 4) * 8u, shiftk + 16);
        unsigned dd = gld32((unsigned)(ch >> 1) * 4u, ssmD);
        auto stage = [&](const char* src, int dst) {
            const unsigned sbase = (unsigned)(idx * 64 + cw * 16 + (lane >> 4)) * 2048u + xcd * 256;
#pragma unroll
            for (int i = 0; i < 4; ++i)
                glds16p<K2_SNT>(sbase + (((lane & 15) ^ (((2 * cw + (i >> 1)) & 3) << 2)) << 4), src + i * 8192, lds0 + dst + cw * 4096 + i * 1024);
        };
        auto blank = [&](unsigned off) {
            const u32x4 e4 = {EMPTY, EMPTY, EMPTY, EMPTY};
            CFENCE();
            if (!BK1) { st_sc1(e4, rs, off + voff); st_sc1(e4, rs, off + 1024 + voff); }
            CFENCE();
        };
        WAIT_BAR(3);
        DG(12);
        issue(2);
        stage(Ak, K2_SF);
        glds16((unsigned)(idx * 64 - (idx ? 4 : 0) + (lane >> 4)) * 2048u + xcd * 256 + (((lane & 15) ^ 12) << 4), Ak, lds0 + K2_SCR + cw * 1024);
        blank((unsigned)WS_O + (unsigned)((qb * 8 + cw * 2) * 32 + h) * 2048u);
        auto warm = [&](int g) {
#pragma unroll
            for (int i = 0; i < 4; ++i) glds16(wvoff, wsrc(idx >> 4, 16 + g * 16 + cw * 4 + i), lds0 + K2_PAD);
        };
        WAITN_BAR(BK1 ? 5 : 7);
        DG(9);
        issue(3);
        stage(Av, K2_SF + 16384);
        blank((unsigned)WS_O + (unsigned)((qb * 8 + cw * 2 + 1) * 32 + h) * 2048u);
        WAITN_BAR(BK1 ? 4 : 6);
        DG(3);
        issue(4);
#pragma unroll
        for (int i = 0; i < 4; ++i) glds16p<K2_SNT>(voff, ssmk + (size_t)ch * 4096 + i * 1024, lds0 + K2_CV + cw * CV_BYTES + CV_HA + 32 + i * 1024);
        blank((unsigned)WS_ZT + ch * 4096); blank((unsigned)WS_ZT + ch * 4096 + 2048);
        WAITN_BAR(BK1 ? 4 : 8);
        DG(5);
        issue(5);
        SBAR();
        asm volatile("" : "+v"(sk[0]), "+v"(sk[1]), "+v"(dd));
        dd = __builtin_amdgcn_readfirstlane(dd);
        {
            u32x2 kr[11], vr[8];
            lds_ptr kb = lds + K2_SF + rg * 2048 + ((cw ^ (rg & 3)) << 6) + cq * 8;
            asm volatile("" : "+v"(kb));
            const lds_ptr hb = lds + (rg ? K2_SF + rg * 2048 - 768 : K2_SCR + cw * 1024 + 256) + ((cw ^ ((rg + 3) & 3)) << 6) + cq * 8;
#pragma unroll
            for (int i = 0; i < 3; ++i) kr[i] = *(const LAS u32x2*)(hb + i * 256);
#pragma unroll
            for (int i = 0; i < 8; ++i) kr[3 + i] = *(const LAS u32x2*)(kb + i * 256);
#pragma unroll
            for (int i = 0; i < 8; ++i) vr[i] = *(const LAS u32x2*)(kb + 16384 + i * 256);
            if (row0 == 0) { kr[0] = u32x2{0u, 0u}; kr[1] = u32x2{0u, 0u}; kr[2] = u32x2{0u, 0u}; }
            const lds_ptr tb = cv + CV_KV + 480;
#pragma unroll
            for (int c = 0; c < 4; ++c) {
                const unsigned s01 = sk[c >> 1][(c & 1) * 2], s23 = sk[c >> 1][(c & 1) * 2 + 1];
                unsigned kp[10];
                float kv[8], vf[8];
#pragma unroll
                for (int j = 0; j < 10; ++j) kp[j] = __builtin_amdgcn_perm(kr[j][c >> 1], kr[j + 1][c >> 1], (c & 1) ? 0x07060302u : 0x05040100u);
                SBAR();
#pragma unroll
                for (int i = 0; i < 8; ++i) asm("v_dot2_f32_bf16 %0, %1, %2, 0" : "=v"(kv[i]) : "v"(kp[i]), "v"(s23));
                SBAR();
#pragma unroll
                for (int i = 0; i < 8; ++i) asm("v_dot2_f32_bf16 %0, %1, %2, %0" : "+v"(kv[i]) : "v"(kp[i + 2]), "v"(s01));
                SBAR();
#pragma unroll
                for (int i = 0; i < 8; ++i) vf[i] = (c & 1) ? bfhi(vr[i][c >> 1]) : bflo(vr[i][c >> 1]);
                SBAR();
#pragma unroll
                for (int i = 0; i < 8; ++i) kv[i] = mul_vv(kv[i], vf[i]);
                u32x4 o4;
#pragma unroll
                for (int i = 0; i < 4; ++i) o4[i] = cvtpk(kv[7 - 2 * i], kv[6 - 2 * i]);
                *(LAS u32x4*)(tb + (cq * 4 + c) * 128 + ((rg ^ cq) << 4)) = o4;
            }
#pragma unroll
            for (int i = 0; i < 4; ++i) {
                const u32x4 t = *(const LAS u32x4*)(tb + i * 1024 + lane * 16);
                st_sc1(t, rs, (unsigned)WS_Q + (xcd * 32 + idx) * 16384 + cw * 4096 + i * 1024 + (lane >> 3) * 128 + (((lane & 7) ^ (2 * i + (lane >> 5))) << 4));
            }
        }
        CFENCE();
        DG(7);
        WAIT_BAR(4);
        DG(6);
        issue(6);
        asm volatile("s_waitcnt vmcnt(8)" ::: "memory");
        if (lds_add_rtn(fl + C_KST, lane) == 3u) signal();
#pragma unroll
        for (int i = 0; i < 8; ++i)
            glds16p<K2_WNT>(wvoff, wsrc(i >> 2, cw * 4 + (i & 3)), lds0 + K2_SF + (i >> 2) * 16384 + (cw * 4 + (i & 3)) * 1024);
        {
            const u32x4 z4 = {0u, 0u, 0u, 0u};
            if (lane < 2) *(LAS u32x4*)(cv + CV_HB + lane * 16) = z4;
#pragma unroll
            for (int i = 0; i < 4; ++i) {
                const int off = 32 + i * 1024 + lane * 16;
                const u32x4 hv = *(const LAS u32x4*)(cv + CV_HA + off);
                const unsigned pv = *(const LAS unsigned*)(cv + CV_HA + off - 4);
                u32x4 sh;
                sh[0] = __builtin_amdgcn_alignbit(hv[0], pv, 16);
                sh[1] = __builtin_amdgcn_alignbit(hv[1], hv[0], 16);
                sh[2] = __builtin_amdgcn_alignbit(hv[2], hv[1], 16);
                sh[3] = __builtin_amdgcn_alignbit(hv[3], hv[2], 16);
                *(LAS u32x4*)(cv + CV_HB + off) = sh;
            }
        }
        WAIT_BAR(8);
        DG(8);
        issue(7);
        auto warm23 = [&]() {
#pragma unroll
            for (int i = 0; i < 8; ++i)
                glds16p<K2_WNT>(wvoff, wsrc(idx >> 4, 32 + cw * 8 + i), lds0 + K2_SCR + cw * 1024);
        };
        auto chunk1 = [&]() {
#pragma unroll
            for (int i = 0; i < 8; ++i)
                glds16p<K2_WNT>(wvoff, wsrc(i >> 2, 16 + cw * 4 + (i & 3)), lds0 + (i >> 2) * 16384 + (cw * 4 + (i & 3)) * 1024);
        };
        warm23();
        WAIT_BAR(8);
        DG(11);
        if (cw == 0) { poll_ge(cnt + b * 64, BK1 ? 32u : 64u); lds_add(fl + F_KGO, lane); }
        else lds_wait_ge(fl + F_KGO, 1u);
        {
            u32x4 t[4];
#pragma unroll
            for (int i = 0; i < 4; ++i) t[i] = ld_sc1s(rs, (unsigned)WS_Q + (xcd * 32 + (lane >> 3)) * 16384 + (idx * 4 + cw) * 128 + (lane & 7) * 16, i * 131072);
#pragma unroll
            for (int i = 0; i < 4; ++i) *(LAS u32x4*)(cv + CV_KV + 480 + i * 1024 + lane * 16) = t[i];
        }
        const int r1 = lane & 15, q = lane >> 4;
        const int toff = (r1 & 1) ? CV_HA + 2 * (16 + r1 + 9 - 8 * q) : CV_HB + 2 * (16 + r1 + 10 - 8 * q);
        const int boff = CV_KV + 448 + 32 * r1 + 16 * q;
        f32x4 y[8];
        u32x4 tf[8];
        bf16x8 bf[8];
        const lds_ptr bp = cv + boff - 448;
        lds_ptr tp[4];
#pragma unroll
        for (int i = 0; i < 4; ++i) { tp[i] = cv + toff + 1024 * i; asm volatile("" : "+v"(tp[i])); }
#define LDT(bb, a, hf) do { const LAS unsigned* t_ = (const LAS unsigned*)(tp[(a) >> 1] + 64 * (bb) + 512 * ((a) & 1) + 8 * (hf)); \
            tf[a][2 * (hf)] = t_[0]; tf[a][2 * (hf) + 1] = t_[1]; SBAR(); } while (0)
#define LDB(bb, a) do { bf[a] = lds_rd128(bp + 448 - 64 * (bb) + 512 * (a)); SBAR(); } while (0)
#define CM(a, ww) do { asm volatile("v_mfma_f32_16x16x32_bf16 %0, %1, %2, %0" : "+v"(y[(a) + (ww)]) : "v"(tf[a]), "v"(bf[ww])); SBAR(); } while (0)
#define CM0(a, ww) do { asm volatile("v_mfma_f32_16x16x32_bf16 %0, %1, %2, 0" : "=&v"(y[(a) + (ww)]) : "v"(tf[a]), "v"(bf[ww])); SBAR(); } while (0)
#define PC() do { asm volatile(PACE_OUT); SBAR(); } while (0)
#pragma unroll
        for (int a = 0; a < 8; ++a) { if (a < 5) { LDT(0, a, 0); LDT(0, a, 1); } LDB(0, 7 - a); }
        chunk1();
#pragma unroll
        for (int i = 0; i < 4; ++i)
            glds16p<K2_QNT>((unsigned)(idx * 64 + cw * 16 + (lane >> 4)) * 2048u + xcd * 256 + (lane & 15) * 16, Aq + i * 8192, lds0 + K2_SCR + cw * 1024);
        RAW_BAR();
        asm volatile("s_waitcnt vmcnt(%0)" :: "n"(AORD == 2 ? 12 : 4) : "memory");
        lds_add(fl + C_S1, lane);
#define ZFL() PC()
        __builtin_amdgcn_s_setprio(AUX_PRIO);
        const float dch = (ch & 1) ? bfhi(dd) : bflo(dd);
        const lds_ptr kdp = cv + CV_KV + 480 + 32 * r1 + 16 * (q >> 1) + ((q & 1) ? 0 : 8);
        float kd[8][4];
        u32x2 kk;
        unsigned kc0, kc1, zo[8][2];
#define KD_RD(t) do { kk = *(const LAS u32x2*)(kdp + 512 * (t)); SBAR(); } while (0)
#define KD_UP0(t) do { kd[t][0] = bfhi(kk[1]); kd[t][1] = bflo(kk[1]); SBAR(); } while (0)
#define KD_UP1(t) do { kd[t][2] = bfhi(kk[0]); kd[t][3] = bflo(kk[0]); SBAR(); } while (0)
#define KD_MU0(t) do { kd[t][0] = mul_sv(dch, kd[t][0]); kd[t][1] = mul_sv(dch, kd[t][1]); SBAR(); } while (0)
#define KD_MU1(t) do { kd[t][2] = mul_sv(dch, kd[t][2]); kd[t][3] = mul_sv(dch, kd[t][3]); SBAR(); } while (0)
#define KD_CV(t) do { kc0 = cvtpk(kd[t][0], kd[t][1]); kc1 = cvtpk(kd[t][2], kd[t][3]); SBAR(); } while (0)
#define KD_X0(t) do { kd[t][0] = bflo(kc0); kd[t][1] = bfhi(kc0); SBAR(); } while (0)
#define KD_X1(t) do { kd[t][2] = bflo(kc1); kd[t][3] = bfhi(kc1); SBAR(); } while (0)
#define ZF0(t) do { kc0 = cvtpk(y[t][0], y[t][1]); kc1 = cvtpk(y[t][2], y[t][3]); SBAR(); } while (0)
#define ZF1(t) do { y[t][0] = bflo(kc0); y[t][1] = bfhi(kc0); SBAR(); } while (0)
#define ZF2(t) do { y[t][2] = bflo(kc1); y[t][3] = bfhi(kc1); SBAR(); } while (0)
#define ZF3(t) do { y[t][0] = add_vv(y[t][0], kd[t][0]); y[t][1] = add_vv(y[t][1], kd[t][1]); SBAR(); } while (0)
#define ZF4(t) do { y[t][2] = add_vv(y[t][2], kd[t][2]); y[t][3] = add_vv(y[t][3], kd[t][3]); SBAR(); } while (0)
#define ZF5(t) do { zo[t][0] = nz(cvtpk(y[t][0], y[t][1])); zo[t][1] = nz(cvtpk(y[t][2], y[t][3])); SBAR(); } while (0)
#define ZST(pr) do { auto s0 = __builtin_amdgcn_permlane16_swap(zo[2 * (pr)][0], zo[2 * (pr) + 1][0], false, false); \
            auto s1 = __builtin_amdgcn_permlane16_swap(zo[2 * (pr)][1], zo[2 * (pr) + 1][1], false, false); \
            st_sc1(u32x4{s0[0], s1[0], s0[1], s1[1]}, rs, (unsigned)WS_ZT + ch * 4096 + (pr) * 1024 + voff); SBAR(); } while (0)
#define W3(i) do { if ((i) == 0) PC(); } while (0)
#define HF() do { lds_add(fl + C_HF, lane); SBAR(); } while (0)
        CM0(0, 7); LDB(1, 7); CM0(0, 6); LDT(0, 5, 0); CM0(0, 5); LDT(0, 5, 1); CM0(0, 4); LDT(0, 6, 0); CM0(0, 3); LDT(0, 6, 1); CM0(0, 2); LDT(0, 7, 0); CM0(0, 1); LDT(0, 7, 1); CM0(0, 0); KD_RD(0);
        CM(1, 6); LDB(1, 6); CM(1, 5); LDT(1, 0, 0); CM(1, 4); LDT(1, 0, 1); CM(1, 3); PC(); CM(1, 2); PC(); CM(1, 1); KD_UP0(0); CM(1, 0); KD_UP1(0);
        CM(2, 5); LDB(1, 5); CM(2, 4); LDT(1, 1, 0); CM(2, 3); LDT(1, 1, 1); CM(2, 2); KD_MU0(0); CM(2, 1); KD_MU1(0); CM(2, 0); KD_CV(0);
        CM(3, 4); LDB(1, 4); CM(3, 3); LDT(1, 2, 0); CM(3, 2); LDT(1, 2, 1); CM(3, 1); KD_X0(0); CM(3, 0); KD_X1(0);
        CM(4, 3); LDB(1, 3); CM(4, 2); LDT(1, 3, 0); CM(4, 1); LDT(1, 3, 1); CM(4, 0); KD_RD(1);
        CM(5, 2); LDB(1, 2); CM(5, 1); LDT(1, 4, 0); CM(5, 0); LDT(1, 4, 1);
        CM(6, 1); LDB(1, 1); CM(6, 0); PC();
        CM(7, 0); LDB(1, 0);
        DGV(6);
        CM(0, 7); LDB(2, 7); CM(0, 6); LDT(1, 5, 0); CM(0, 5); LDT(1, 5, 1); CM(0, 4); LDT(1, 6, 0); CM(0, 3); LDT(1, 6, 1); CM(0, 2); LDT(1, 7, 0); CM(0, 1); LDT(1, 7, 1); CM(0, 0); PC();
        CM(1, 6); LDB(2, 6); CM(1, 5); LDT(2, 0, 0); CM(1, 4); LDT(2, 0, 1); CM(1, 3); KD_UP0(1); CM(1, 2); KD_UP1(1); CM(1, 1); KD_MU0(1); CM(1, 0); KD_MU1(1);
        CM(2, 5); LDB(2, 5); CM(2, 4); LDT(2, 1, 0); CM(2, 3); LDT(2, 1, 1); CM(2, 2); KD_CV(1); CM(2, 1); KD_X0(1); CM(2, 0); KD_X1(1);
        CM(3, 4); LDB(2, 4); CM(3, 3); LDT(2, 2, 0); CM(3, 2); LDT(2, 2, 1); CM(3, 1); KD_RD(2); CM(3, 0); PC();
        CM(4, 3); LDB(2, 3); CM(4, 2); LDT(2, 3, 0); CM(4, 1); LDT(2, 3, 1); CM(4, 0); PC();
        CM(5, 2); LDB(2, 2); CM(5, 1); LDT(2, 4, 0); CM(5, 0); LDT(2, 4, 1);
        CM(6, 1); LDB(2, 1); CM(6, 0); KD_UP0(2);
        CM(7, 0); LDB(2, 0);
        DGV(8);
        CM(0, 7); LDB(3, 7); CM(0, 6); LDT(2, 5, 0); CM(0, 5); LDT(2, 5, 1); CM(0, 4); LDT(2, 6, 0); CM(0, 3); LDT(2, 6, 1); CM(0, 2); LDT(2, 7, 0); CM(0, 1); LDT(2, 7, 1); CM(0, 0); KD_UP1(2);
        CM(1, 6); LDB(3, 6); CM(1, 5); LDT(3, 0, 0); CM(1, 4); LDT(3, 0, 1); CM(1, 3); KD_MU0(2); CM(1, 2); KD_MU1(2); CM(1, 1); KD_CV(2); CM(1, 0); KD_X0(2);
        CM(2, 5); LDB(3, 5); CM(2, 4); LDT(3, 1, 0); CM(2, 3); LDT(3, 1, 1); CM(2, 2); KD_X1(2); CM(2, 1); KD_RD(3); CM(2, 0); PC();
        CM(3, 4); LDB(3, 4); CM(3, 3); LDT(3, 2, 0); CM(3, 2); LDT(3, 2, 1); CM(3, 1); PC(); CM(3, 0); KD_UP0(3);
        CM(4, 3); LDB(3, 3); CM(4, 2); LDT(3, 3, 0); CM(4, 1); LDT(3, 3, 1); CM(4, 0); KD_UP1(3);
        CM(5, 2); LDB(3, 2); CM(5, 1); LDT(3, 4, 0); CM(5, 0); LDT(3, 4, 1);
        CM(6, 1); LDB(3, 1); CM(6, 0); KD_MU0(3);
        CM(7, 0); LDB(3, 0);
        DGV(12);
        CM(0, 7); LDB(4, 7); CM(0, 6); LDT(3, 5, 0); CM(0, 5); LDT(3, 5, 1); CM(0, 4); LDT(3, 6, 0); CM(0, 3); LDT(3, 6, 1); CM(0, 2); LDT(3, 7, 0); CM(0, 1); LDT(3, 7, 1); CM(0, 0); KD_MU1(3);
        CM(1, 6); LDB(4, 6); CM(1, 5); LDT(4, 0, 0); CM(1, 4); LDT(4, 0, 1); CM(1, 3); KD_CV(3); CM(1, 2); KD_X0(3); CM(1, 1); KD_X1(3); CM(1, 0); KD_RD(4);
        CM(2, 5); LDB(4, 5); CM(2, 4); LDT(4, 1, 0); CM(2, 3); LDT(4, 1, 1); CM(2, 2); PC(); CM(2, 1); PC(); CM(2, 0); KD_UP0(4);
        CM(3, 4); LDB(4, 4); CM(3, 3); LDT(4, 2, 0); CM(3, 2); LDT(4, 2, 1); CM(3, 1); KD_UP1(4); CM(3, 0); KD_MU0(4);
        CM(4, 3); LDB(4, 3); CM(4, 2); LDT(4, 3, 0); CM(4, 1); LDT(4, 3, 1); CM(4, 0); KD_MU1(4);
        CM(5, 2); LDB(4, 2); CM(5, 1); LDT(4, 4, 0); CM(5, 0); LDT(4, 4, 1);
        CM(6, 1); LDB(4, 1); CM(6, 0); KD_CV(4);
        CM(7, 0); LDB(4, 0);
        DGV(9);
        CM(0, 7); LDB(5, 7); CM(0, 6); LDT(4, 5, 0); CM(0, 5); LDT(4, 5, 1); CM(0, 4); LDT(4, 6, 0); CM(0, 3); LDT(4, 6, 1); CM(0, 2); LDT(4, 7, 0); CM(0, 1); LDT(4, 7, 1); CM(0, 0); KD_X0(4);
        CM(1, 6); LDB(5, 6); CM(1, 5); LDT(5, 0, 0); CM(1, 4); LDT(5, 0, 1); CM(1, 3); KD_X1(4); CM(1, 2); KD_RD(5); CM(1, 1); PC(); CM(1, 0); PC();
        CM(2, 5); LDB(5, 5); CM(2, 4); LDT(5, 1, 0); CM(2, 3); LDT(5, 1, 1); CM(2, 2); KD_UP0(5); CM(2, 1); KD_UP1(5); CM(2, 0); KD_MU0(5);
        CM(3, 4); LDB(5, 4); CM(3, 3); LDT(5, 2, 0); CM(3, 2); LDT(5, 2, 1); CM(3, 1); KD_MU1(5); CM(3, 0); KD_CV(5);
        CM(4, 3); LDB(5, 3); CM(4, 2); LDT(5, 3, 0); CM(4, 1); LDT(5, 3, 1); CM(4, 0); KD_X0(5);
        CM(5, 2); LDB(5, 2); CM(5, 1); LDT(5, 4, 0); CM(5, 0); LDT(5, 4, 1);
        CM(6, 1); LDB(5, 1); CM(6, 0); KD_X1(5);
        CM(7, 0); LDB(5, 0);
        DGV(11);
        CM(0, 7); LDB(6, 7); CM(0, 6); LDT(5, 5, 0); CM(0, 5); LDT(5, 5, 1); CM(0, 4); LDT(5, 6, 0); CM(0, 3); LDT(5, 6, 1); CM(0, 2); LDT(5, 7, 0); CM(0, 1); LDT(5, 7, 1); CM(0, 0); KD_RD(6);
        CM(1, 6); LDB(6, 6); CM(1, 5); LDT(6, 0, 0); CM(1, 4); LDT(6, 0, 1); CM(1, 3); PC(); CM(1, 2); PC(); CM(1, 1); KD_UP0(6); CM(1, 0); KD_UP1(6);
        CM(2, 5); LDB(6, 5); CM(2, 4); LDT(6, 1, 0); CM(2, 3); LDT(6, 1, 1); CM(2, 2); KD_MU0(6); CM(2, 1); KD_MU1(6); CM(2, 0); KD_CV(6);
        CM(3, 4); LDB(6, 4); CM(3, 3); LDT(6, 2, 0); CM(3, 2); LDT(6, 2, 1); CM(3, 1); KD_X0(6); CM(3, 0); KD_X1(6);
        CM(4, 3); LDB(6, 3); CM(4, 2); LDT(6, 3, 0); CM(4, 1); LDT(6, 3, 1); CM(4, 0); KD_RD(7);
        CM(5, 2); LDB(6, 2); CM(5, 1); LDT(6, 4, 0); CM(5, 0); LDT(6, 4, 1);
        CM(6, 1); LDB(6, 1); CM(6, 0); PC();
        CM(7, 0); LDB(6, 0);
        DGV(7);
        CM(0, 7); LDB(7, 7); CM(0, 6); LDT(6, 5, 0); CM(0, 5); LDT(6, 5, 1); CM(0, 4); LDT(6, 6, 0); CM(0, 3); LDT(6, 6, 1); CM(0, 2); LDT(6, 7, 0); CM(0, 1); LDT(6, 7, 1); CM(0, 0); PC();
        CM(1, 6); LDB(7, 6); CM(1, 5); LDT(7, 0, 0); CM(1, 4); LDT(7, 0, 1); CM(1, 3); KD_UP0(7); CM(1, 2); KD_UP1(7); CM(1, 1); KD_MU0(7); CM(1, 0); KD_MU1(7);
        CM(2, 5); LDB(7, 5); CM(2, 4); LDT(7, 1, 0); CM(2, 3); LDT(7, 1, 1); CM(2, 2); KD_CV(7); CM(2, 1); KD_X0(7); CM(2, 0); KD_X1(7);
        CM(3, 4); LDB(7, 4); CM(3, 3); LDT(7, 2, 0); CM(3, 2); LDT(7, 2, 1); CM(3, 1); PC(); CM(3, 0); PC();
        CM(4, 3); LDB(7, 3); CM(4, 2); LDT(7, 3, 0); CM(4, 1); LDT(7, 3, 1); CM(4, 0); PC();
        CM(5, 2); LDB(7, 2); CM(5, 1); LDT(7, 4, 0); CM(5, 0); LDT(7, 4, 1);
        CM(6, 1); LDB(7, 1); CM(6, 0); PC();
        CM(7, 0); LDB(7, 0);
        DGV(3);
        CM(0, 3); LDT(7, 5, 0); CM(0, 2); LDT(7, 5, 1); CM(1, 2); LDT(7, 6, 0); CM(1, 1); LDT(7, 6, 1); CM(2, 1); LDT(7, 7, 0); CM(2, 0); LDT(7, 7, 1);
        CM(3, 0); HF(); CM(0, 1); ZFL(); CM(0, 0); W3(0); CM(1, 0); ZF0(2); W3(1); CM(0, 5); ZF1(2); W3(2); CM(0, 4); ZF2(2); W3(3);
        CM(1, 4); ZF3(2); W3(4); CM(1, 3); ZF4(2); W3(5); CM(2, 3); ZF5(2); W3(6); CM(2, 2); ZF0(3); W3(7); CM(3, 2); ZF1(3);  CM(3, 1); ZF2(3);
        CM(4, 1); ZF3(3);  CM(4, 0); ZF4(3);  CM(5, 0); ZF5(3);  CM(0, 7); ZST(1);  CM(0, 6); ZF0(0);  CM(1, 6); ZF1(0);
        CM(1, 5); ZF2(0);  CM(2, 5); ZF3(0);  CM(2, 4); ZF4(0);  CM(3, 4); ZF5(0);  CM(3, 3); ZF0(1);  CM(4, 3); ZF1(1);
        CM(4, 2); ZF2(1);  CM(5, 2); ZF3(1);  CM(5, 1); ZF4(1);  CM(6, 1); ZF5(1);  CM(6, 0); ZST(0);  CM(7, 0); ZF0(4);
        DG(4);
        ZF1(4); ZF2(4); ZF3(4); ZF4(4); ZF5(4); ZF0(5); ZF1(5); ZF2(5); ZF3(5); ZF4(5); ZF5(5); ZST(2); ZF0(6); ZF1(6); ZF2(6); ZF3(6); ZF4(6); ZF5(6); ZF0(7); ZF1(7); ZF2(7); ZF3(7); ZF4(7); ZF5(7); ZST(3);
        DG(10);
        __builtin_amdgcn_s_setprio(3);
        while ((unsigned)__builtin_amdgcn_readfirstlane(*(volatile LAS unsigned*)(fl + C_OL)) < ZL_GATE) __builtin_amdgcn_s_sleep(4);
        CFENCE();
        const int arow = idx * 64 + (cw >> 1) * 32 + rg * 4, acol = xcd * 256 + (cw & 1) * 128 + cq * 16;
        u32x4 qr[4];
#pragma unroll
        for (int i = 0; i < 4; ++i) qr[i] = gld128((unsigned)(arow * 2048 + acol), Aq + i * 2048);
        {
            const lds_ptr tr = cv + CV_KV + 480;
            u32x4 t[4];
            const int ck = (cw >> 1) * 4 + ((lane ^ ((lane >> 5) * 2)) & 3);
            const unsigned zb = (unsigned)WS_ZT + (unsigned)(xcd * 128 + (cw & 1) * 64 + (lane >> 4) * 4) * 4096u + (idx >> 3) * 1024 +
                                ((idx & 3) * 4 + ((idx >> 2) & 1) * 16 + (ck >> 1) + (ck & 1) * 32) * 16;
#pragma unroll 1
            for (unsigned spins = 0; spins < (1u << 20); ++spins) {
#pragma unroll
                for (int i = 0; i < 4; ++i) t[i] = ld_sc1s(rs, zb + ((((lane >> 2) & 3) ^ i) << 12), i * 65536);
                if (!missing(mx4(mx4(mx8(t[0], t[1]), t[2]), t[3]))) break;
                __builtin_amdgcn_s_sleep(Z_SLEEP);
            }
            DG(13);
            __builtin_amdgcn_s_setprio(ZE_PRIO);
#pragma unroll
            for (int i = 0; i < 4; ++i) *(LAS u32x4*)(tr + i * 1024 + lane * 16) = t[i];
#pragma unroll
            for (int i = 0; i < 4; ++i) asm volatile("" : "+v"(qr[i]));
            u32x2 zc[8];
#pragma unroll
            for (int c = 0; c < 8; ++c)
                zc[c] = *(const LAS u32x2*)(tr + cq * 512 + (c >> 2) * 256 + ((((c & 3) * 4 + (rg >> 1)) ^ (cq * 2)) << 4) + (rg & 1) * 8);
#pragma unroll
            for (int i = 0; i < 4; ++i) {
                u32x4 o;
#pragma unroll
                for (int c = 0; c < 4; ++c) {
                    const unsigned z0 = zc[2 * c][i >> 1], z1 = zc[2 * c + 1][i >> 1];
                    o[c] = cvtpk(mul_vv(bflo(qr[i][c]), (i & 1) ? bfhi(z0) : bflo(z0)), mul_vv(bfhi(qr[i][c]), (i & 1) ? bfhi(z1) : bflo(z1)));
                }
                __builtin_amdgcn_raw_buffer_store_b128(o, ro, arow * 4096 + acol, i * 4096, OUT_AUX);
            }
        }
        asm volatile(".Lcp_k2z:");
        DG_END(14);
        return;
    }

    const int half = w >> 2;
    const int TA = qb * 8 + (w & 3) * 2 + half, TB = TA ^ 1;
    bf16x8 sel;
    {
        const short one = (((lane >> 2) & 1) == ((lane >> 4) & 1) && (lane & 11) == 0) ? (short)0x3f80 : (short)0;
#pragma unroll
        for (int i = 0; i < 8; ++i) sel[i] = SPM ? ((lane & 15) == (i & 4) ? (short)0x3f80 : (short)0) : one;
    }
    int sidx = (int)0xdddddddd;
#define LO8(x) __builtin_bit_cast(bfx16_t, __builtin_shufflevector(x, x, 0, 1, 2, 3, 4, 5, 6, 7))
#define HI8(x) __builtin_bit_cast(bfx16_t, __builtin_shufflevector(x, x, 8, 9, 10, 11, 12, 13, 14, 15))
#define SPV(v, p, o) __builtin_amdgcn_smfmac_f32_32x32x32_bf16(__builtin_bit_cast(bfx8_t, v), p, o, sidx, 0, 0)
#define SSUM(p, l) __builtin_amdgcn_smfmac_f32_16x16x64_bf16(__builtin_bit_cast(bfx8_t, sel), p, l, sidx, 0, 0)
    f32x16 oA = f32x16{}, oB = f32x16{};
    f32x4 lA = f32x4{}, lB = f32x4{};
    f32x16 negm, sA, sB;
    lds_ptr lp = lds + half * 16384 + lane * 16;
    bf16x8 k0[2], k1[2], v0[2], v1[2];
    u32x4 p0, p1 = {0u, 0u, 0u, 0u};
#define EX(X, i) X[i] = __builtin_amdgcn_exp2f(X[i])
    lds_ptr qpa = lds + K2_SF + (TA & 7) * 2048 + lane * 16, qpb = lds + K2_SF + (TB & 7) * 2048 + lane * 16;
    asm volatile("" : "+v"(qpa), "+v"(qpb), "+v"(lp), "+v"(sel), "+v"(oA), "+v"(oB), "+v"(lA), "+v"(lB), "+v"(sidx));
    WAIT_BAR(0);
    DG(3);
    const bf16x8 qa0 = lds_rd128(qpa), qb0 = lds_rd128(qpb);
    const bf16x8 x0 = lds_rd128(lds + lane * 16), x1 = lds_rd128(lds + lane * 16 + 1024);
    const bf16x8 qa1 = lds_rd128(qpa + 1024), qb1 = lds_rd128(qpb + 1024);
    {
        k0[0] = lds_rd128(lp); k1[0] = lds_rd128(lp + 1024);
        v0[0] = lds_rd128(lp + 2048); v1[0] = lds_rd128(lp + 3072);
        sA = MFMA32(x0, qa0, f32x16{});
        sB = MFMA32(x0, qb0, f32x16{});
        sA = MFMA32(x1, qa1, sA);
        sB = MFMA32(x1, qb1, sB);
        float m = fmx(sA[0], sA[1]), m2 = fmx(sB[0], sB[1]);
#pragma unroll
        for (int i = 2; i < 16; i += 2) m = fmx(fmx(m, sA[i]), sA[i + 1]);
#pragma unroll
        for (int i = 2; i < 16; i += 2) m2 = fmx(fmx(m2, sB[i]), sB[i + 1]);
        m = fmx(m, m2);
        auto rr = __builtin_amdgcn_permlane32_swap(__float_as_uint(m), __float_as_uint(m), false, false);
        m = fmx(__uint_as_float(rr[0]), __uint_as_float(rr[1]));
#pragma unroll
        for (int i = 0; i < 16; ++i) negm[i] = -m;
        sA = MFMA32(k0[0], qa0, negm);
        sA = MFMA32(k1[0], qa1, sA);
        v1[1] = v1[0];
#pragma unroll
        for (int i = 8; i < 16; ++i) sB[i] = 0.f;
        asm volatile("" : "+v"(sB));
        SBAR();
        RAW_BAR();
        SBAR();
        EX(sA, 0); EX(sA, 1);
    }
    DG(10);
    HS(5); HC(5);
#define SUM16(l, p) asm volatile("v_mfma_f32_16x16x32_bf16 %0, %1, %2, %0" : "+v"(l) : "v"(sel), "v"(p))
#define HALF(C, oC, lC, N, qx, qy, ka, kb, oP, lP, vprev, vcur0, PRE) do { \
        oP = SPV(vprev, HI8(N), oP); SBAR(); \
        PRE; EX(C, 2); EX(C, 3); EX(C, 4); SBAR(); \
        lP = SSUM(HI8(N), lP); SBAR(); \
        EX(C, 5); SBAR(); \
        N = MFMA32(ka, qx, negm); SBAR(); \
        EX(C, 6); EX(C, 7); EX(C, 8); SBAR(); \
        N = MFMA32(kb, qy, N); SBAR(); \
        EX(C, 9); EX(C, 10); EX(C, 11); SBAR(); \
        oC = SPV(vcur0, LO8(C), oC); SBAR(); \
        EX(C, 12); EX(C, 13); EX(C, 14); SBAR(); \
        lC = SSUM(LO8(C), lC); SBAR(); \
        EX(C, 15); EX(N, 0); EX(N, 1); SBAR(); \
    } while (0)
#define RD4(Y, nx) do { v1[Y] = lds_rd128((nx) + 3072); v0[Y] = lds_rd128((nx) + 2048); \
        k1[Y] = lds_rd128((nx) + 1024); k0[Y] = lds_rd128(nx); } while (0)
#define KT(X, nx, PRE0) do { \
        HALF(sA, oA, lA, sB, qb0, qb1, k0[X], k1[X], oB, lB, v1[X ^ 1], v0[X], PRE0; RD4(X ^ 1, nx)); \
        HALF(sB, oB, lB, sA, qa0, qa1, k0[X ^ 1], k1[X ^ 1], oA, lA, v1[X], v0[X], (void)0); \
    } while (0)
#define RING_BAR() do { unsigned long long sv_; \
        asm volatile("s_cmp_eq_u32 %1, 7\n\ts_cbranch_scc1 1f\n\ts_barrier\n1:\n\ts_mov_b64 %0, exec\n\ts_mov_b64 exec, 1\n\tds_add_u32 %2, %3\n\ts_mov_b64 exec, %0" \
                     : "=&s"(sv_) : "s"(st), "v"(fl + C_LD), "v"(1u) : "memory", "scc"); } while (0)
    SBAR();
#pragma unroll 1
    for (int st = 0; st < 8; ++st) {
        const lds_ptr cur = lp + (st & 1) * K2_SLOT;
        const lds_ptr nxt = lp + ((st + 1) & 1) * K2_SLOT;
        PRIO(2);
        KT(0, cur + 4096, (void)0);
        PRIO(1);
        KT(1, cur + 8192, (void)0);
        PRIO(0);
        KT(0, cur + 12288, (void)0);
        PRIO(3);
        KT(1, nxt, RAW_BAR(); SBAR());
    }
    oB = SPV(v1[1], HI8(sB), oB);
    SBAR();
    lB = SSUM(HI8(sB), lB);
    SBAR();
    DG(4);
    HS(6); HC(6);
    {
        const lds_ptr xo = lds + K2_SLOT + w * 4096 + lane * 16;
        *(LAS float*)(lds + K2_LX + w * 256 + lane * 4) = lB[0];
#pragma unroll
        for (int i = 0; i < 4; ++i) *(LAS f32x4*)(xo + i * 1024) = f32x4{oB[4 * i], oB[4 * i + 1], oB[4 * i + 2], oB[4 * i + 3]};
        lds_add(fl + F_X + w * 4, lane);
        lds_add(fl + C_END + half * 4, lane);
        lds_wait_ge(fl + F_X + (w ^ 4) * 4, 1u);
        const lds_ptr xi = lds + K2_SLOT + (w ^ 4) * 4096 + lane * 16;
        f32x4 t[4];
        const float lx = *(const LAS float*)(lds + K2_LX + (w ^ 4) * 256 + (lane & 31) * 4);
        { auto rr = __builtin_amdgcn_permlane32_swap(__float_as_uint(lA[0]), __float_as_uint(lA[0]), false, false); lA[0] = __uint_as_float(rr[0]); }
#pragma unroll
        for (int i = 0; i < 4; ++i) t[i] = *(const LAS f32x4*)(xi + i * 1024);
        SBAR();
        lA[0] += lx;
#pragma unroll
        for (int i = 0; i < 16; ++i) { float o = oA[i]; acc_vv(o, t[i >> 2][i & 3]); oA[i] = o; }
    }
    DG(9);
    const float l = lA[0];
    u32x4 on0, on1;
    auto finish = [&](float ls) {
        float rl = __builtin_amdgcn_rcpf(ls);
        asm volatile("s_nop 1" : "+v"(rl));
        f32x16 o;
#pragma unroll
        for (int i = 0; i < 16; ++i) o[i] = mul_vv(oA[i], rl);
        on0 = nz4(pack8n(o, 0)); on1 = nz4(pack8n(o, 1));
    };
    finish(l);
    {
        float t0, t1;
        asm("v_mul_f32_e32 %0, 0, %2\n\tv_mul_f32_e32 %1, 0, %3\n\tv_fmac_f32_e32 %0, 0, %4\n\tv_fmac_f32_e32 %1, 0, %5\n\tv_fmac_f32_e32 %0, 0, %6\n\tv_fmac_f32_e32 %1, 0, %7\n\tv_fmac_f32_e32 %0, 0, %8\n\tv_fmac_f32_e32 %1, 0, %9\n\t"
            "v_fmac_f32_e32 %0, 0, %10\n\tv_fmac_f32_e32 %1, 0, %11\n\tv_fmac_f32_e32 %0, 0, %12\n\tv_fmac_f32_e32 %1, 0, %13\n\tv_fmac_f32_e32 %0, 0, %14\n\tv_fmac_f32_e32 %1, 0, %15\n\tv_fmac_f32_e32 %0, 0, %16\n\tv_fmac_f32_e32 %1, 0, %17"
            : "=&v"(t0), "=&v"(t1) : "v"(oA[0]), "v"(oA[1]), "v"(oA[2]), "v"(oA[3]), "v"(oA[4]), "v"(oA[5]), "v"(oA[6]), "v"(oA[7]), "v"(oA[8]), "v"(oA[9]), "v"(oA[10]), "v"(oA[11]), "v"(oA[12]), "v"(oA[13]), "v"(oA[14]), "v"(oA[15]));
        const bool ok = (l <= 1.0e30f) && (l >= 1.0e-18f) && (t0 + t1 == 0.f);
        if (__builtin_expect(__any(!ok), 0)) {
            float m = -INFINITY, ls = 0.f;
            oA = f32x16{};
#pragma unroll 1
            for (int t = 0; t < 64; ++t) {
                const char* g = kvg + t * 4096 + lane * 16;
                const bf16x8 a0 = *(const bf16x8*)g, a1 = *(const bf16x8*)(g + 1024);
                const bf16x8 b0 = *(const bf16x8*)(g + 2048), b1 = *(const bf16x8*)(g + 3072);
                f32x16 s = MFMA32(a0, qa0, f32x16{});
                s = MFMA32(a1, qa1, s);
                float tm = s[0];
#pragma unroll
                for (int i = 1; i < 16; ++i) tm = __builtin_fmaxf(tm, s[i]);
                auto rr = __builtin_amdgcn_permlane32_swap(__float_as_uint(tm), __float_as_uint(tm), false, false);
                tm = __builtin_fmaxf(__uint_as_float(rr[0]), __uint_as_float(rr[1]));
                const float mn = __builtin_fmaxf(m, tm);
                const float alpha = __builtin_amdgcn_exp2f(m - mn);
                m = mn;
                float ps = 0.f;
#pragma unroll
                for (int i = 0; i < 16; ++i) { s[i] = bfhi(__float_as_uint(__builtin_amdgcn_exp2f(s[i] - mn))); ps += s[i]; oA[i] *= alpha; }
                ls = ls * alpha + ps;
                oA = SPV(b0, LO8(s), oA);
                oA = SPV(b1, HI8(s), oA);
            }
            auto rr = __builtin_amdgcn_permlane32_swap(__float_as_uint(ls), __float_as_uint(ls), false, false);
            asm volatile("s_nop 7\n\ts_nop 7" : "+v"(oA));
            finish(__uint_as_float(rr[0]) + __uint_as_float(rr[1]));
        }
    }
    u32x4 w3[4];
    const char* s2 = wsrc(half ^ 1, 32 + (w & 3) * 4);
    const char* s3 = wsrc(half, 48 + (w & 3) * 4);
    auto ldw2 = [&]() {
#pragma unroll
        for (int i = 0; i < 4; ++i)
            glds16p<K2_WNT>(wvoff, s2 + i * 16384, lds0 + K2_SLOT + ((w ^ 4) * 4 + i) * 1024);
    };
    auto ldw3 = [&]() {
#pragma unroll
        for (int i = 0; i < 4; ++i) w3[i] = gld128(wvoff, s3 + i * 16384);
    };
    {
        const unsigned og = (unsigned)WS_O + (unsigned)(TA * 32 + h) * 2048u + lane * 16;
        st_sc1(on0, rs, og);
        st_sc1(on1, rs, og + 1024);
        lds_add(fl + C_S1, lane);
        lds_wait_ge(fl + C_S1, 12u);
        DG(5);
        ldw2();
        ldw3();
    }
    {
        const int half = w & 1;
        const int T3 = tt * 4 + (w >> 1);
        const unsigned og = (unsigned)WS_O + (unsigned)T3 * 65536u + half * 32768 + lane * 16;
        const int r = lane & 31, hh = lane >> 5;
        const int g = lane >> 4, q = (lane & 15) >> 2, p = lane & 3;
        int woff[2];
#pragma unroll
        for (int n = 0; n < 2; ++n) {
            const int c = 4 * (n ^ half) + 2 * (p & 1) + (g & 1);
            woff[n] = (4 * hh + q) * 128 + ((c ^ ((q >> 1) << 2)) << 4) + (p >> 1) * 8;
        }
        f32x16 acc[2] = {f32x16{}, f32x16{}};
        lds_ptr wl[3][2] = {{lds + half * 16384 + woff[0], lds + half * 16384 + woff[1]},
                            {lds + K2_SF + half * 16384 + woff[0], lds + K2_SF + half * 16384 + woff[1]},
                            {lds + K2_CV + half * 2 * CV_BYTES + woff[0], lds + K2_CV + half * 2 * CV_BYTES + woff[1]}};
        asm volatile("" : "+v"(wl[0][0]), "+v"(wl[0][1]), "+v"(wl[1][0]), "+v"(wl[1][1]), "+v"(wl[2][0]), "+v"(wl[2][1]));
#define MS(m) ((m) < 64 - 2 * TL ? (m) >> 1 : 32 - TL + ((m) - (64 - 2 * TL)) % TL)
#define MN(m) ((m) < 64 - 2 * TL ? (m) & 1 : (m) < 64 - TL)
        constexpr int WR = 2 * WD + 2;
        s16x4 wa[WR], wb[WR];
#define LDW(m) do { const int s_ = MS(m), f_ = s_ < 8 ? 1 : s_ < 24 ? 0 : 2; \
            const int o_ = f_ == 2 ? ((s_ >> 2) & 1) * CV_BYTES + (s_ & 3) * 2048 : (f_ ? 0 : ((s_ >> 3) - 1) * K2_SLOT) + (s_ & 7) * 2048; \
            wa[(m) % WR] = lds_tr(wl[f_][MN(m)] + o_); wb[(m) % WR] = lds_tr(wl[f_][MN(m)] + o_ + 1024); SBAR(); } while (0)
#pragma unroll
        for (int m = 0; m < 2 * WD; ++m) LDW(m);
        u32x4 of[OWIN];
#define LDO(i) do { of[(i) % OWIN] = ld_sc1s(rs, og, (((i) + krot) & 31) * 1024); SBAR(); } while (0)
        for (unsigned spins = 0; spins < (1u << 20); ++spins) {
#pragma unroll
            for (int i = 0; i < OW0; ++i) LDO(i);
            unsigned m = mx8(of[0], of[1]);
#pragma unroll
            for (int i = 2; i < OW0; ++i) m = mx4(m, of[i]);
            if (!missing(m)) break;
            DGC(12);
        }
        lds_add(fl + C_S2, lane);
#pragma unroll
        for (int i = OW0; i < OWIN; ++i) LDO(i);
        DG(6);
        __builtin_amdgcn_s_setprio(P3_PRIO);
        SBAR();
        unsigned fv = 0;
#define FLAG_RD(a) do { fv = *(volatile LAS unsigned*)(a); SBAR(); } while (0)
#define FLAG_CHK(a, n) do { if (__builtin_expect((unsigned)__builtin_amdgcn_readfirstlane(fv) < (n), 0)) lds_wait_ge(a, n); CFENCE(); SBAR(); } while (0)
        lds_ptr xo = lds + K2_SF + w * 4096 + lane * 16, xi = lds + K2_SF + (w ^ 1) * 4096 + lane * 16;
        int bo = (T3 * 32 + (lane & 15)) * 4096 + 2048 + nt * 128 + half * 64 + g * 16;
        lds_ptr w3d = lds + K2_CV + (w >> 1) * CV_BYTES + (w & 1) * 4096 + lane * 16;
        asm volatile("" : "+v"(xo), "+v"(xi), "+v"(bo), "+v"(w3d));
        f32x4 xt[4];
        constexpr int M3 = TL > 8 ? 64 - 2 * TL + TL - 8 - 2 * WD : 48 - 2 * WD;
        sfor(std::make_integer_sequence<int, 64>{}, [&](auto mc) __attribute__((always_inline)) {
            constexpr int m = decltype(mc)::value, s = MS(m), n = MN(m);
            constexpr bool first = m < 64 - 2 * TL ? n == 0 : n == 1;
            constexpr bool last = m < 64 - 2 * TL ? n == 1 : n == 0;
            acc[n] = MFMA32(cat8(wa[m % WR], wb[m % WR]), PB(of[s % OWIN]), acc[n]);
            SBAR();
            if constexpr (m == 32 - 2 * WD) FLAG_CHK(fl + C_S2, 8u);
            if constexpr (m == M3) FLAG_CHK(fl + C_S3, 8u);
            if constexpr (m + 2 * WD < 64) LDW(m + 2 * WD);
            if constexpr (first && (s & 1) && s + 1 >= OW0 && s + 1 < 32) {
                if (__builtin_expect(missing(mx8(of[(s + 1) % OWIN], of[(s + 2) % OWIN])), 0)) {
#pragma unroll 1
                    for (unsigned spins = 0; spins < (1u << 20); ++spins) {
                        __builtin_amdgcn_s_sleep(2);
                        LDO(s + 1); LDO(s + 2);
                        DGC(13);
                        if (!missing(mx8(of[(s + 1) % OWIN], of[(s + 2) % OWIN]))) break;
                    }
                }
            }
            if constexpr (last && s + OWIN < 32) LDO(s + OWIN);
            if constexpr (LORD && m == S2M) { asm volatile("s_waitcnt vmcnt(%0)" :: "n"(4 + (m + 1) / 2) : "memory"); lds_add(fl + C_S2, lane); }
            if constexpr (LORD && m == W3M) asm volatile("s_waitcnt vmcnt(%0)" :: "n"((m + 1) / 2 < 16 ? (m + 1) / 2 : 16) : "memory");
            if constexpr (m == 15 - 2 * WD) lds_add(fl + C_U0, lane);
            if constexpr (m == 29 - 2 * WD) FLAG_RD(fl + C_S2);
            if constexpr (m == W3M - 3) FLAG_RD(fl + C_HF);
            if constexpr (m == W3M) FLAG_CHK(fl + C_HF, 4u);
            if constexpr (m >= W3M && m < W3M + 8 && ((m - W3M) & 1) == 0) {
                asm volatile("" : "+v"(w3[((m - W3M) >> 1) & 3]));
                *(LAS u32x4*)(w3d + (((m - W3M) >> 1) & 3) * 1024) = w3[((m - W3M) >> 1) & 3];
            }
            if constexpr (m == W3M + 6) lds_add(fl + C_S3, lane);
            if constexpr (m == M3 - 3) FLAG_RD(fl + C_S3);
            if constexpr (m == 64 - TL - 3) FLAG_RD(fl + C_U0);
            if constexpr (m == 64 - TL) FLAG_CHK(fl + C_U0, 8u);
            if constexpr (m == 64 - TL + 1 || m == 64 - TL + 2) {
                constexpr int i = 2 * (m - (64 - TL + 1));
                *(LAS f32x4*)(xo + i * 1024) = f32x4{acc[1][4 * i], acc[1][4 * i + 1], acc[1][4 * i + 2], acc[1][4 * i + 3]};
                *(LAS f32x4*)(xo + i * 1024 + 1024) = f32x4{acc[1][4 * i + 4], acc[1][4 * i + 5], acc[1][4 * i + 6], acc[1][4 * i + 7]};
            }
            if constexpr (m == 64 - TL + 2) lds_add(fl + F_R + w * 4, lane);
            if constexpr (m == XR_AT - 2) FLAG_RD(fl + F_R + (w ^ 1) * 4);
            if constexpr (m == XR_AT) {
                FLAG_CHK(fl + F_R + (w ^ 1) * 4, 1u);
#pragma unroll
                for (int i = 0; i < 4; ++i) xt[i] = *(const LAS f32x4*)(xi + i * 1024);
            }
            if constexpr (ZL_GATE && last && s + OWIN == ZL_AT) lds_add(fl + C_OL, lane);
            if constexpr (m == 2 * P3_T1 + 1) __builtin_amdgcn_s_setprio(P3_PRIO - 1);
            SBAR();
            if constexpr (m == 31) DG(11);
            if constexpr (m == 47) DG(14);
        });
        asm volatile("s_nop 11" : "+v"(acc[0]));
        DG(7);
#pragma unroll
        for (int i = 0; i < 16; ++i) { float o = xt[i >> 2][i & 3]; acc_vv(o, acc[0][i]); acc[1][i] = o; }
        u32x4 pa = pack8n(acc[1], 0), pb = pack8n(acc[1], 1);
#pragma unroll
        for (int i = 0; i < 4; ++i) {
            auto sw = __builtin_amdgcn_permlane16_swap(pa[i], pb[i], false, false);
            pa[i] = sw[0]; pb[i] = sw[1];
        }
        asm volatile("" :: "v"(acc[0]));
        __builtin_amdgcn_raw_buffer_store_b128(pa, ro, bo, 0, OUT_AUX);
        __builtin_amdgcn_raw_buffer_store_b128(pb, ro, bo, 65536, OUT_AUX);
    }
    DG_END(8);
    if (b == HB && w == 0) {
        DRAIN();
        HS(7);
        const unsigned* hp_ = (const unsigned*)(ws + (30u << 20));
        const unsigned hs0 = __builtin_amdgcn_readfirstlane(hp_[0]), hs1 = __builtin_amdgcn_readfirstlane(hp_[1]), hs2 = __builtin_amdgcn_readfirstlane(hp_[2]), hcy = __builtin_amdgcn_readfirstlane(hp_[3]);
        unsigned iv = (HQ);
        iv = iv < 20000u ? iv : 20000u;
        while ((unsigned)__builtin_amdgcn_s_memrealtime() - hs7 < iv) __builtin_amdgcn_s_sleep(1);
    }
}

extern "C" void kernel_launch(void* const* d_in, const int* in_sizes, int n_in,
                              void* d_out, int out_size, void* d_ws, size_t ws_size,
                              hipStream_t stream) {
    const void* A_q = d_in[0];
    const void* A_k = d_in[1];
    const void* A_v = d_in[2];
    const void* A_shift = d_in[3];
    const void* A_ssmk = d_in[4];
    const void* A_D = d_in[5];
    const void* B_x = d_in[6];
    const void* B_wqkv = d_in[7];
    const void* B_wo = d_in[8];
    char* ws = (char*)d_ws;
    k1_qkv<<<256, 512, 0, stream>>>((const char*)B_x, (const char*)B_wqkv, ws, (const char*)B_wo, (const char*)A_q,
                                    (const char*)A_k, (const char*)A_v, (const char*)A_ssmk);
    k2_attn<<<256, 768, 0, stream>>>(ws, (const char*)A_q, (const char*)A_k, (const char*)A_v, (const char*)A_shift,
                                     (const char*)A_ssmk, (const unsigned*)A_D, (const char*)B_wo, (char*)d_out);
}
```

```cpp
#include <hip/hip_runtime.h>
#include <hip/hip_bf16.h>
#include <stdint.h>
#include <utility>

typedef short bf16x8 __attribute__((ext_vector_type(8)));
typedef short s16x4 __attribute__((ext_vector_type(4)));
typedef float f32x16 __attribute__((ext_vector_type(16)));
typedef float f32x4 __attribute__((ext_vector_type(4)));
typedef float f32x2 __attribute__((ext_vector_type(2)));
typedef unsigned u32x4 __attribute__((ext_vector_type(4)));
typedef unsigned u32x2 __attribute__((ext_vector_type(2)));
typedef __bf16 bf16x2_t __attribute__((ext_vector_type(2)));

#define LAS __attribute__((address_space(3)))
typedef LAS char* lds_ptr;
#define DI __device__ __forceinline__
#define SBAR() __builtin_amdgcn_sched_barrier(0)
#define MFMA32(a, b, c) __builtin_amdgcn_mfma_f32_32x32x16_bf16((a), (b), (c), 0, 0, 0)
#define MFMA16(a, b, c) __builtin_amdgcn_mfma_f32_16x16x32_bf16((a), (b), (c), 0, 0, 0)
typedef __bf16 bfx8_t __attribute__((ext_vector_type(8)));
typedef __bf16 bfx16_t __attribute__((ext_vector_type(16)));
typedef float f32x8 __attribute__((ext_vector_type(8)));
#define SPM 1
#define WAIT_BAR(N) asm volatile("s_waitcnt vmcnt(" #N ") lgkmcnt(0)\n\ts_barrier" ::: "memory")
#define LGKM_BAR() asm volatile("s_waitcnt lgkmcnt(0)\n\ts_barrier" ::: "memory")

#define DG_DECL(K)
#define DG(i)
#define DG_END(i)
#define DGC(i)

#define HB 0
#define HQ (400)
constexpr int S_LEN = 2048, DM = 1024, NH = 32;

#define KQ 1
#define OQ 0
#define QT(h, T) (KQ ? (h) * 64 + (T) : (T) * 32 + (h))
constexpr size_t WS_Q = 0;
constexpr size_t WS_KV = 4u << 20;
constexpr size_t WS_O = OQ ? WS_Q : 12u << 20;
constexpr size_t WS_KVR = 16u << 20;
#define ZK 0
constexpr size_t WS_ZT = ZK ? WS_KVR : 20u << 20;
constexpr size_t WS_CNT = 24u << 20;

template <int... I, class F> DI void sfor(std::integer_sequence<int, I...>, F&& f) { (f(std::integral_constant<int, I>{}), ...); }
DI unsigned cvtpk(float lo, float hi) {
    f32x2 v = {lo, hi};
    return __builtin_bit_cast(unsigned, __builtin_convertvector(v, bf16x2_t));
}
DI float bflo(unsigned u) { return __uint_as_float(u << 16); }
DI float bfhi(unsigned u) { return __uint_as_float(u & 0xffff0000u); }

DI void glds16(unsigned voff, const void* sbase, unsigned lds_dst) {
    unsigned sv;
    asm volatile("s_mov_b32 %0, m0\n\ts_mov_b32 m0, %3\n\ts_nop 0\n\tglobal_load_lds_dwordx4 %1, %2\n\ts_mov_b32 m0, %0"
                 : "=&s"(sv) : "v"(voff), "s"(sbase), "s"(lds_dst) : "memory");
}
template <int NT> DI void glds16p(unsigned voff, const void* sbase, unsigned lds_dst) {
    if constexpr (NT) {
        unsigned sv;
        asm volatile("s_mov_b32 %0, m0\n\ts_mov_b32 m0, %3\n\ts_nop 0\n\tglobal_load_lds_dwordx4 %1, %2 nt\n\ts_mov_b32 m0, %0"
                     : "=&s"(sv) : "v"(voff), "s"(sbase), "s"(lds_dst) : "memory");
    } else glds16(voff, sbase, lds_dst);
}
#define K1_XNT 0
#define K1_WNT 0
#define K2_SNT 1
#define K2_QNT 0
#define K2_WNT 0
#define OUT_AUX 16
#define K1_SAUX 16
DI bf16x8 lds_rd128(lds_ptr p) { return *(const LAS bf16x8*)p; }
DI s16x4 lds_tr(lds_ptr p) {
    return __builtin_bit_cast(s16x4, __builtin_amdgcn_ds_read_tr16_b64_v4i16((LAS s16x4*)p));
}
DI bf16x8 cat8(s16x4 a, s16x4 b) { return __builtin_shufflevector(a, b, 0, 1, 2, 3, 4, 5, 6, 7); }
DI u32x4 pack8(const f32x16& x, int s, float sc) {
    u32x4 p;
    p[0] = cvtpk(x[8 * s + 0] * sc, x[8 * s + 1] * sc);
    p[1] = cvtpk(x[8 * s + 2] * sc, x[8 * s + 3] * sc);
    p[2] = cvtpk(x[8 * s + 4] * sc, x[8 * s + 5] * sc);
    p[3] = cvtpk(x[8 * s + 6] * sc, x[8 * s + 7] * sc);
    return p;
}
DI u32x4 pack8n(const f32x16& x, int s) {
    u32x4 p;
    p[0] = cvtpk(x[8 * s + 0], x[8 * s + 1]);
    p[1] = cvtpk(x[8 * s + 2], x[8 * s + 3]);
    p[2] = cvtpk(x[8 * s + 4], x[8 * s + 5]);
    p[3] = cvtpk(x[8 * s + 6], x[8 * s + 7]);
    return p;
}
DI void rnd2(float& a, float& b) { unsigned u = cvtpk(a, b); a = bflo(u); b = bfhi(u); }

#define CONV_PACE()
#define RAW_BAR() asm volatile("s_barrier" ::: "memory")
DI unsigned gld32(unsigned voff, const void* sbase) {
    unsigned r;
    asm volatile("global_load_dword %0, %1, %2" : "=v"(r) : "v"(voff), "s"(sbase) : "memory");
    return r;
}
DI u32x2 gld64(unsigned voff, const void* sbase) {
    u32x2 r;
    asm volatile("global_load_dwordx2 %0, %1, %2" : "=v"(r) : "v"(voff), "s"(sbase) : "memory");
    return r;
}
DI float mul_sv(float s, float v) { float r; asm("v_mul_f32_e32 %0, %1, %2" : "=v"(r) : "s"(s), "v"(v)); return r; }
DI float mul_vv(float a, float b) { float r; asm("v_mul_f32_e32 %0, %1, %2" : "=v"(r) : "v"(a), "v"(b)); return r; }
DI float add_vv(float a, float b) { float r; asm("v_add_f32_e32 %0, %1, %2" : "=v"(r) : "v"(a), "v"(b)); return r; }
DI void acc_vv(float& a, float b) { asm("v_add_f32_e32 %0, %0, %1" : "+v"(a) : "v"(b)); }
DI void fmac_vv(float& acc, float a, float b) { asm("v_fmac_f32_e32 %0, %1, %2" : "+v"(acc) : "v"(a), "v"(b)); }
DI void fmac_sv(float& acc, float s, float v) { asm("v_fmac_f32_e32 %0, %1, %2" : "+v"(acc) : "s"(s), "v"(v)); }
typedef __attribute__((address_space(1))) unsigned gu32;
#define RLX_AGENT __ATOMIC_RELAXED, __HIP_MEMORY_SCOPE_AGENT
#define CFENCE() asm volatile("" ::: "memory")
#define DRAIN() asm volatile("s_waitcnt vmcnt(0)" ::: "memory")
constexpr int AUX_SC1 = 16;
DI u32x4 ld_sc1(__amdgpu_buffer_rsrc_t rs, unsigned off) { return __builtin_amdgcn_raw_buffer_load_b128(rs, (int)off, 0, AUX_SC1); }
DI u32x4 ld_sc1s(__amdgpu_buffer_rsrc_t rs, unsigned off, int soff) { return __builtin_amdgcn_raw_buffer_load_b128(rs, (int)off, soff, AUX_SC1); }
DI void st_sc1(u32x4 v, __amdgpu_buffer_rsrc_t rs, unsigned off) { __builtin_amdgcn_raw_buffer_store_b128(v, rs, (int)off, 0, AUX_SC1); }
constexpr unsigned EMPTY = 0xffffffffu;
DI unsigned nz(unsigned x) { return x < EMPTY - 1u ? x : EMPTY - 1u; }
DI u32x4 nz4(u32x4 v) { return u32x4{nz(v[0]), nz(v[1]), nz(v[2]), nz(v[3])}; }
DI float fmx(float a, float b) { return __builtin_elementwise_maximum(a, b); }
DI unsigned umax(unsigned a, unsigned b) { return a > b ? a : b; }
DI unsigned umax3(unsigned a, unsigned b, unsigned c) { return umax(umax(a, b), c); }
DI unsigned mx4(unsigned m, u32x4 v) { return umax3(umax3(m, v[0], v[1]), v[2], v[3]); }
DI unsigned mx8(u32x4 a, u32x4 b) { return umax(umax3(umax3(umax3(a[0], a[1], a[2]), a[3], b[0]), b[1], b[2]), b[3]); }
DI bool missing(unsigned m) { return __builtin_amdgcn_ballot_w64(m == EMPTY) != 0; }
DI unsigned lds_add_rtn(lds_ptr p, int lane) {
    unsigned old = 0;
    CFENCE();
    if (lane == 0) old = __hip_atomic_fetch_add((LAS unsigned*)p, 1u, __ATOMIC_RELAXED, __HIP_MEMORY_SCOPE_WORKGROUP);
    old = __builtin_amdgcn_readfirstlane(old);
    CFENCE();
    return old;
}
DI void lds_add(lds_ptr p, int lane) {
    unsigned long long sv;
    asm volatile("s_mov_b64 %0, exec\n\ts_mov_b64 exec, 1\n\tds_add_u32 %1, %2\n\ts_mov_b64 exec, %0"
                 : "=&s"(sv) : "v"(p), "v"(1u) : "memory");
}
DI void lds_wait_ge(lds_ptr p, unsigned v) {
    CFENCE();
    while ((unsigned)__builtin_amdgcn_readfirstlane(*(volatile LAS unsigned*)p) < v) __builtin_amdgcn_s_sleep(1);
    CFENCE();
}
DI void poll_ge(gu32* word, unsigned want) {
    CFENCE();
    for (unsigned spins = 0; spins < (1u << 22); ++spins) {
        if ((unsigned)__builtin_amdgcn_readfirstlane(__hip_atomic_load(word, RLX_AGENT)) >= want) break;
        __builtin_amdgcn_s_sleep(1);
    }
    __builtin_amdgcn_fence(__ATOMIC_ACQUIRE, "wavefront");
    CFENCE();
}
#define PB(x) __builtin_bit_cast(bf16x8, x)
#define PRIO(x) do { __builtin_amdgcn_s_setprio(x); SBAR(); } while (0)
DI u32x4 gld128(unsigned voff, const void* sbase) {
    u32x4 r;
    asm volatile("global_load_dwordx4 %0, %1, %2" : "=v"(r) : "v"(voff), "s"(sbase) : "memory");
    return r;
}

#define K1_UNR 15
#define K1_LA 5
#define K1_ROT ((tb & 3) * 4)
#define K1_PF 0
#define K1_PG 0
#define K1_TW 0
#define CPF 3
#define CPF_POS 1
#define PG1_AT 0
#define PG2_AT 2
#define PG3_AT 6
#define K1_WB(n) WAIT_BAR(1)
#define PF1_AT -1
#define PF1_LO 4
#define PF1_N 4
#define PF2_AT 1
#define PF2_LO 8
#define PF2_N 4
#define PF3_AT 5
#define PF3_LO 12
#define PF3_N 4
#define K1_KR ((kt + rot) & 15)
#define K1_ST(v, rs, off) __builtin_amdgcn_raw_buffer_store_b128(v, rs, (int)(off), 0, K1_SAUX)
constexpr int K1_STAGE = 40960, K1U = K1_UNR;
__global__ __launch_bounds__(512, 2) void k1_qkv(const char* __restrict__ X, const char* __restrict__ W,
                                                 char* __restrict__ ws) {
    __shared__ __attribute__((aligned(16))) char lds_raw[4 * K1_STAGE];
    unsigned long long ka0, ka1, kax;
    asm volatile("s_memtime %0\n\ts_load_dwordx2 %2, %3, 0\n\ts_waitcnt lgkmcnt(0)\n\ts_memtime %1\n\ts_waitcnt lgkmcnt(0)" : "=&s"(ka0), "=&s"(ka1), "=&s"(kax) : "s"(__builtin_amdgcn_kernarg_segment_ptr()) : "memory");
    const lds_ptr lds = (lds_ptr)lds_raw;
    const unsigned lds0 = (unsigned)(uintptr_t)lds;
    const int tid = threadIdx.x, lane = tid & 63;
    const int w = __builtin_amdgcn_readfirstlane(tid >> 6);
    const int b = blockIdx.x, xcd = b & 7, idx = b >> 3;
    const int hp = (xcd >> 1) * 4 + (idx & 3), tb = (xcd & 1) * 8 + (idx >> 2);
    DG_DECL(0)

    const int lw = w & 3;
    const int rot = K1_ROT;
    unsigned voffX[4], voffW[2];
#pragma unroll
    for (int pp = 0; pp < 4; ++pp) {
        const int row = 32 * lw + 8 * pp + (lane >> 3);
        const int ch = (lane & 7) ^ ((row >> 1) & 7);
        voffX[pp] = (unsigned)(tb * 128 + row) * 2048u + ch * 16;
    }
#pragma unroll
    for (int pp = 0; pp < 2; ++pp) {
        const int krow = 16 * lw + 8 * pp + (lane >> 3);
        const int ch = (lane & 7) ^ (((lane >> 4) & 1) << 2);
        voffW[pp] = (unsigned)krow * 6144u + ch * 16;
    }
    auto issue = [&](int kt, int lo, int hi) {
        const int kr = K1_KR;
        const char* xb = X + kr * 128;
        const char* wb = W + (size_t)kr * (64 * 6144) + hp * 128;
        const unsigned st = lds0 + (kt & 3) * K1_STAGE;
#pragma unroll
        for (int pp = 0; pp < 4; ++pp)
            if (pp >= lo && pp < hi) glds16p<K1_XNT>(voffX[pp], xb, st + (4 * lw + pp) * 1024);
#pragma unroll
        for (int sec = 0; sec < 3; ++sec)
#pragma unroll
            for (int pp = 0; pp < 2; ++pp)
                if (4 + 2 * sec + pp >= lo && 4 + 2 * sec + pp < hi)
                    glds16p<K1_WNT>(voffW[pp], wb + sec * 2048, st + 16384 + sec * 8192 + (2 * lw + pp) * 1024);
    };
    DG(13);
    unsigned cpf;
    auto codepf = [&]() {
        unsigned long long pc; unsigned lo, hi;
        asm volatile("s_getpc_b64 %0\n.Lcp_k1a:\n\ts_mov_b32 %1, %3\n\ts_mov_b32 %2, %4" : "=s"(pc), "=s"(lo), "=s"(hi) : "n"(0), "n"(0));
        if (CPF & 1) lo = 0; else asm volatile("s_mov_b32 %0, .Lcp_k2a-.Lcp_k1a" : "=s"(lo));
        if (CPF & 2) asm volatile("s_mov_b32 %0, .Lcp_k2z-.Lcp_k1a" : "=s"(hi)); else asm volatile("s_mov_b32 %0, .Lcp_k1z-.Lcp_k1a" : "=s"(hi));
        unsigned off = lo + (unsigned)(idx + 32 * (lane & 7) + 256 * w) * 64u;
        off = off < hi ? off : hi;
        asm volatile("global_load_dword %0, %1, %2" : "=v"(cpf) : "v"(off), "s"(pc) : "memory");
    };
    if (w < 4) { issue(0, 0, 5); issue(1, 0, 5); codepf(); }
    else { issue(0, 5, 10); issue(1, 5, 10); }
    DG(14);
    int w2 = w;
    asm volatile("" : "+s"(w2));

    if (w2 >= 4) {
#define WAITN_BAR(N) asm volatile("s_waitcnt vmcnt(%0) lgkmcnt(0)\n\ts_barrier" :: "n"(N) : "memory")
        issue(2, 0, K1_LA);
        asm volatile("s_waitcnt vmcnt(%0)" :: "n"(5 + K1_LA) : "memory");
#pragma unroll 1
        for (int kt = 0; kt < 13; ++kt) {
            WAITN_BAR(10 + K1_LA);
            issue(kt + 2, K1_LA, 10);
            issue(kt + 3, 0, K1_LA);
        }
        WAITN_BAR(10 + K1_LA);
        issue(15, K1_LA, 10);
        WAIT_BAR(10);
        WAIT_BAR(0);
        if (lw == 0 && lane < 2) *(unsigned*)(ws + WS_CNT + b * 256 + lane * 128) = 0u;
        DG_END(5);
        return;
    }

    const int wm = w >> 1, wn = w & 1;
    const int r = lane & 31, hh = lane >> 5;
    const int g = lane >> 4, q = (lane & 15) >> 2, p = lane & 3;
    const int xoff0 = (64 * wm + r) * 128 + ((hh ^ ((r >> 1) & 7)) << 4);
    const int woff0 = 16384 + (8 * hh + q) * 128 + (((4 * wn + 2 * (g & 1) + (p >> 1)) ^ ((q >> 1) << 2)) << 4) + (p & 1) * 8;

    f32x16 acc[2][3];
#pragma unroll
    for (int m = 0; m < 2; ++m)
#pragma unroll
        for (int s = 0; s < 3; ++s) acc[m][s] = f32x16{};
    bf16x8 xf[2][2];
    s16x4 wl[2][3], wh[2][3];
#define K1_WF(f, sec) cat8(wl[f][sec], wh[f][sec])
#define K1_STEP(F, st, t, BAR_HERE) do { \
        const lds_ptr xp_ = (st) + (xoff0 ^ ((t) << 5)); const lds_ptr wp_ = (st) + woff0 + (t) * 2048; \
        acc[0][0] = MFMA32(K1_WF(F, 0), xf[F][0], acc[0][0]); SBAR(); \
        if (BAR_HERE) { K1_WB(BAR_HERE); SBAR(); } \
        xf[F ^ 1][0] = lds_rd128(xp_); wl[F ^ 1][0] = lds_tr(wp_); wh[F ^ 1][0] = lds_tr(wp_ + 512); SBAR(); \
        acc[0][1] = MFMA32(K1_WF(F, 1), xf[F][0], acc[0][1]); SBAR(); \
        wl[F ^ 1][1] = lds_tr(wp_ + 8192); wh[F ^ 1][1] = lds_tr(wp_ + 8192 + 512); SBAR(); \
        acc[0][2] = MFMA32(xf[F][0], K1_WF(F, 2), acc[0][2]); SBAR(); \
        wl[F ^ 1][2] = lds_tr(wp_ + 16384); wh[F ^ 1][2] = lds_tr(wp_ + 16384 + 512); xf[F ^ 1][1] = lds_rd128(xp_ + 4096); SBAR(); \
        acc[1][0] = MFMA32(K1_WF(F, 0), xf[F][1], acc[1][0]); SBAR(); \
        acc[1][1] = MFMA32(K1_WF(F, 1), xf[F][1], acc[1][1]); \
        acc[1][2] = MFMA32(xf[F][1], K1_WF(F, 2), acc[1][2]); SBAR(); \
    } while (0)

    WAIT_BAR(6);
    DG(3);
    {
        const lds_ptr xp_ = lds + xoff0; const lds_ptr wp_ = lds + woff0;
        xf[0][0] = lds_rd128(xp_); xf[0][1] = lds_rd128(xp_ + 4096);
#pragma unroll
        for (int sec = 0; sec < 3; ++sec) { wl[0][sec] = lds_tr(wp_ + sec * 8192); wh[0][sec] = lds_tr(wp_ + sec * 8192 + 512); }
    }
    SBAR();
#pragma unroll K1U
    for (int kt = 0; kt < 15; ++kt) {
        const lds_ptr st = lds + (kt & 3) * K1_STAGE;
        const lds_ptr sn = lds + ((kt + 1) & 3) * K1_STAGE;
        K1_STEP(0, st, 1, false);
        K1_STEP(1, st, 2, false);
        K1_STEP(0, st, 3, false);
        K1_STEP(1, sn, 0, kt + 1);
    }
    {
        const lds_ptr st = lds + 3 * K1_STAGE;
        K1_STEP(0, st, 1, false);
        K1_STEP(1, st, 2, false);
        K1_STEP(0, st, 3, false);
#pragma unroll
        for (int m = 0; m < 2; ++m) {
            acc[m][1] = MFMA32(K1_WF(1, 1), xf[1][m], acc[m][1]);
            acc[m][2] = MFMA32(xf[1][m], K1_WF(1, 2), acc[m][2]);
            acc[m][0] = MFMA32(K1_WF(1, 0), xf[1][m], acc[m][0]);
        }
    }
    DG(4);
    asm volatile("s_waitcnt vmcnt(0)\n.Lcp_k1z:" :: "v"(cpf));
    {
        if (b == HB && tid == 0) { unsigned* hp_ = (unsigned*)(ws + (30u << 20)); hp_[0] = (unsigned)(ka1 - ka0); }
        const int h = 2 * hp + wn;
        const float qscale = 0.17677669529663687f * 1.4426950408889634f;
        const __amdgpu_buffer_rsrc_t rs = __builtin_amdgcn_make_buffer_rsrc(ws, 0, 32 << 20, 0x00020000);
#pragma unroll
        for (int m = 0; m < 2; ++m) {
            const int T = tb * 4 + wm * 2 + m;
            const unsigned dq = (unsigned)WS_Q + QT(h, T) * 2048 + lane * 16;
            const unsigned dk = (unsigned)WS_KV + (h * 64 + T) * 4096 + lane * 16;
            const unsigned dkk = SPM ? (unsigned)WS_KV + (h * 64 + T) * 4096 + ((lane & 51) | ((lane & 4) << 1) | ((lane & 8) >> 1)) * 16 : dk;
            K1_ST(pack8n(acc[m][1], 0), rs, dkk);
            K1_ST(pack8n(acc[m][1], 1), rs, dkk + 1024);
            K1_ST(pack8n(acc[m][2], 0), rs, dk + 2048);
            K1_ST(pack8n(acc[m][2], 1), rs, dk + 3072);
            K1_ST(pack8(acc[m][0], 0, qscale), rs, dq);
            K1_ST(pack8(acc[m][0], 1, qscale), rs, dq + 1024);
        }
    }
    DG_END(5);
}

constexpr int K2_SLOT = 32768;
constexpr int K2_SF = 65536;
constexpr int K2_CV = 98304;
constexpr int CV_HA = 0, CV_HB = 4160, CV_KV = 8320, CV_BYTES = 8320 + 480 + 4096;
constexpr int K2_LX = K2_CV + 4 * CV_BYTES;
constexpr int K2_FL = K2_LX + 2048;
constexpr int K2_SCR = K2_FL + 256;
constexpr int K2_PAD = K2_SCR + 4096;
constexpr int K2_LDS = K2_PAD + 1024;
#define NB8 0
#define WW 0
constexpr int F_X = 0, F_R = 32, F_W2 = 64, C_OST = 68, C_S1 = 76, C_S2 = 80, C_S3 = 128, C_ZST = 88, F_ZGO = 92,
              C_KST = 96, C_U0 = 100, C_LD = 108, F_KGO = 116, C_END = 120, C_OL = 72, C_HF = 84;
#define NG_IN 0
#define GATE_WORD C_END
#define GATE_VAL 0
#define X_NZ 4
#define X_NW3 8
#define ZP_GATE 4u
#define ZL_GATE 4
#define ZL_AT 31
#define Z_SLEEP0 0
#define O_SLEEP 0
#define Z_SLEEP 8
#define OWIN 16
#define OW0 8
#define WD 2
#define W3_AT 10
#define X_NOLDW 0
#define X_NOLDO 0
#define X_NOCHK 0
#define KROT (nt * 2)
#define NG_LAST 0
#define PACE_IN "s_nop 7"
#define PACE_OUT "s_nop 1"
#define P3_PRIO 1
#define TOP_SLEEP 2
#define A_AUX 16
#define B_AUX 16
#define B_LDS 0
#define TL 8
#define XR_AT 62
#define W3_AUX 0
#define W3M 13
#define A16 1
#define SEL4 2
#define KV_DOT 1
#define ZE_PRIO 3
#define P3_T1 99
#define P3_T2 99
#define AUX_PRIO 2

__global__ __launch_bounds__(768, 3) void k2_attn(char* __restrict__ ws, const char* __restrict__ Aq,
                                                  const char* __restrict__ Ak, const char* __restrict__ Av,
                                                  const char* __restrict__ shiftk, const char* __restrict__ ssmk,
                                                  const unsigned* __restrict__ ssmD, const char* __restrict__ Wo,
                                                  char* __restrict__ out) {
    __shared__ __attribute__((aligned(16))) char lds_raw[K2_LDS];
    unsigned long long kb0, kb1, kbx;
    asm volatile("s_memtime %0\n\ts_load_dwordx2 %2, %3, 0\n\ts_waitcnt lgkmcnt(0)\n\ts_memtime %1\n\ts_waitcnt lgkmcnt(0)" : "=&s"(kb0), "=&s"(kb1), "=&s"(kbx) : "s"(__builtin_amdgcn_kernarg_segment_ptr()) : "memory");
    const unsigned kbd = (unsigned)(kb1 - kb0);
    const lds_ptr lds = (lds_ptr)lds_raw;
    const lds_ptr fl = lds + K2_FL;
    const unsigned lds0 = (unsigned)(uintptr_t)lds;
    const int tid = threadIdx.x, lane = tid & 63;
    const int w = __builtin_amdgcn_readfirstlane(tid >> 6);
    const int b = blockIdx.x, xcd = b & 7, idx = b >> 3;
    const int h = xcd * 4 + (idx >> 3), qb = idx & 7;
    const int tt = xcd * 2 + (idx >> 4), nt = idx & 15;
    const char* kvg = ws + WS_KV + (size_t)h * 64 * 4096;
    const __amdgpu_buffer_rsrc_t rs = __builtin_amdgcn_make_buffer_rsrc(ws, 0, 32 << 20, 0x00020000);
    const __amdgpu_buffer_rsrc_t ro = __builtin_amdgcn_make_buffer_rsrc(out, 0, 8 << 20, 0x00020000);
    gu32* const cnt = (gu32*)(ws + WS_CNT);
    auto signal = [&]() {
        (void)__hip_atomic_fetch_add(cnt + ((lane & 31) * 8 + (lane < 32 ? xcd : qb)) * 64, 1u, RLX_AGENT);
    };
    const unsigned wvoff = (unsigned)(lane >> 3) * 2048u + nt * 128 + (((lane & 7) ^ (((lane >> 4) & 1) << 2)) << 4);
    const int krot = KROT;
    auto wsrc = [&](int hf, int lp) { return Wo + (size_t)(hf * 64 + ((lp + krot * 2) & 63)) * 16384; };
    DG_DECL(1)
    const unsigned voff = lane * 16;
    const int cw = w & 3;
    auto issue = [&](int st) {
        const char* src = kvg + (st * 4 + cw) * 4096;
        const unsigned dst = lds0 + (st & 1) * K2_SLOT + cw * 4096;
#pragma unroll
        for (int i = 0; i < 4; ++i) glds16(voff, src + i * 1024, dst + i * 1024);
#pragma unroll
        for (int i = 0; i < 4; ++i) glds16(voff, src + 131072 + i * 1024, dst + 16384 + i * 1024);
    };
    asm volatile(".Lcp_k2a:");
    if (w >= 8) {
        __builtin_amdgcn_s_sleep(TOP_SLEEP);
#pragma unroll
        for (int i = 2; i < 8; ++i)
            glds16(voff, kvg + (i & 1) * 131072 + (i >> 1) * 4096 + cw * 1024, lds0 + (i & 1) * 16384 + (i >> 1) * 4096 + cw * 1024);
        RAW_BAR();
        issue(1);
        WAIT_BAR(8);
    } else {
        const int tq = cw * 2 + (w >> 2);
        const char* qg = ws + WS_Q + (size_t)QT(h, qb * 8 + tq) * 2048;
        glds16(voff, kvg + (w >> 2) * 131072 + cw * 1024, lds0 + (w >> 2) * 16384 + cw * 1024);
        glds16(voff, qg, lds0 + K2_SF + tq * 2048);
        glds16(voff, qg + 1024, lds0 + K2_SF + tq * 2048 + 1024);
    }
    int w2 = w;
    asm volatile("" : "+s"(w2));

    if (w2 >= 8) {
        __builtin_amdgcn_s_setprio(3);
        const int ch = xcd * 128 + idx * 4 + cw;
        const lds_ptr cv = lds + K2_CV + cw * CV_BYTES;
        {
            const u32x4 z4 = {0u, 0u, 0u, 0u};
            if (lane < 2) *(LAS u32x4*)(cv + CV_HA + lane * 16) = z4;
            if (lane < 30) *(LAS u32x4*)(cv + CV_KV + lane * 16) = z4;
            if (cw == 0 && lane < 16) *(LAS u32x4*)(fl + lane * 16) = z4;
        }
        const int c0 = xcd * 128 + cw * 32, cq = lane & 7, rg = lane >> 3;
        const int row0 = idx * 64 + rg * 8;
        const unsigned roff = (unsigned)row0 * 2048u + (c0 + cq * 4) * 2;
        const unsigned toffs = (unsigned)(c0 + (lane >> 3)) * 4096u + idx * 128;
        u32x4 sk[2];
        sk[0] = gld128((unsigned)(c0 + cq * 4) * 8u, shiftk); sk[1] = gld128((unsigned)(c0 + cq * 4) * 8u, shiftk + 16);
        unsigned dd = gld32((unsigned)(ch >> 1) * 4u, ssmD);
        auto stage = [&](const char* src, int dst) {
            const unsigned sbase = (unsigned)(idx * 64 + cw * 16 + (lane >> 4)) * 2048u + xcd * 256;
#pragma unroll
            for (int i = 0; i < 4; ++i)
                glds16p<K2_SNT>(sbase + (((lane & 15) ^ (((2 * cw + (i >> 1)) & 3) << 2)) << 4), src + i * 8192, lds0 + dst + cw * 4096 + i * 1024);
        };
        auto blank = [&](unsigned off) {
            const u32x4 e4 = {EMPTY, EMPTY, EMPTY, EMPTY};
            CFENCE();
            st_sc1(e4, rs, off + voff); st_sc1(e4, rs, off + 1024 + voff);
            CFENCE();
        };
        WAIT_BAR(3);
        DG(12);
        issue(2);
        stage(Ak, K2_SF);
        glds16((unsigned)(idx * 64 - (idx ? 4 : 0) + (lane >> 4)) * 2048u + xcd * 256 + (((lane & 15) ^ 12) << 4), Ak, lds0 + K2_SCR + cw * 1024);
        blank((unsigned)WS_O + (unsigned)((qb * 8 + cw * 2) * 32 + h) * 2048u);
        auto warm = [&](int g) {
#pragma unroll
            for (int i = 0; i < 4; ++i) glds16(wvoff, wsrc(idx >> 4, 16 + g * 16 + cw * 4 + i), lds0 + K2_PAD);
        };
        WAIT_BAR(7);
        DG(9);
        issue(3);
        stage(Av, K2_SF + 16384);
        blank((unsigned)WS_O + (unsigned)((qb * 8 + cw * 2 + 1) * 32 + h) * 2048u);
        WAIT_BAR(6);
        DG(3);
        issue(4);
#pragma unroll
        for (int i = 0; i < 4; ++i) glds16p<K2_SNT>(voff, ssmk + (size_t)ch * 4096 + i * 1024, lds0 + K2_CV + cw * CV_BYTES + CV_HA + 32 + i * 1024);
        blank((unsigned)WS_ZT + ch * 4096); blank((unsigned)WS_ZT + ch * 4096 + 2048);
        WAIT_BAR(8);
        DG(5);
        issue(5);
        SBAR();
        asm volatile("" : "+v"(sk[0]), "+v"(sk[1]), "+v"(dd));
        dd = __builtin_amdgcn_readfirstlane(dd);
        {
            u32x2 kr[11], vr[8];
            lds_ptr kb = lds + K2_SF + rg * 2048 + ((cw ^ (rg & 3)) << 6) + cq * 8;
            asm volatile("" : "+v"(kb));
            const lds_ptr hb = lds + (rg ? K2_SF + rg * 2048 - 768 : K2_SCR + cw * 1024 + 256) + ((cw ^ ((rg + 3) & 3)) << 6) + cq * 8;
#pragma unroll
            for (int i = 0; i < 3; ++i) kr[i] = *(const LAS u32x2*)(hb + i * 256);
#pragma unroll
            for (int i = 0; i < 8; ++i) kr[3 + i] = *(const LAS u32x2*)(kb + i * 256);
#pragma unroll
            for (int i = 0; i < 8; ++i) vr[i] = *(const LAS u32x2*)(kb + 16384 + i * 256);
            if (row0 == 0) { kr[0] = u32x2{0u, 0u}; kr[1] = u32x2{0u, 0u}; kr[2] = u32x2{0u, 0u}; }
            const lds_ptr tb = cv + CV_KV + 480;
#pragma unroll
            for (int c = 0; c < 4; ++c) {
                const unsigned s01 = sk[c >> 1][(c & 1) * 2], s23 = sk[c >> 1][(c & 1) * 2 + 1];
                unsigned kp[10];
                float kv[8], vf[8];
#pragma unroll
                for (int j = 0; j < 10; ++j) kp[j] = __builtin_amdgcn_perm(kr[j][c >> 1], kr[j + 1][c >> 1], (c & 1) ? 0x07060302u : 0x05040100u);
                SBAR();
#pragma unroll
                for (int i = 0; i < 8; ++i) asm("v_dot2_f32_bf16 %0, %1, %2, 0" : "=v"(kv[i]) : "v"(kp[i]), "v"(s23));
                SBAR();
#pragma unroll
                for (int i = 0; i < 8; ++i) asm("v_dot2_f32_bf16 %0, %1, %2, %0" : "+v"(kv[i]) : "v"(kp[i + 2]), "v"(s01));
                SBAR();
#pragma unroll
                for (int i = 0; i < 8; ++i) vf[i] = (c & 1) ? bfhi(vr[i][c >> 1]) : bflo(vr[i][c >> 1]);
                SBAR();
#pragma unroll
                for (int i = 0; i < 8; ++i) kv[i] = mul_vv(kv[i], vf[i]);
                u32x4 o4;
#pragma unroll
                for (int i = 0; i < 4; ++i) o4[i] = cvtpk(kv[7 - 2 * i], kv[6 - 2 * i]);
                *(LAS u32x4*)(tb + (cq * 4 + c) * 128 + ((rg ^ cq) << 4)) = o4;
            }
#pragma unroll
            for (int i = 0; i < 4; ++i) {
                const u32x4 t = *(const LAS u32x4*)(tb + i * 1024 + lane * 16);
                st_sc1(t, rs, (unsigned)WS_Q + (xcd * 32 + idx) * 16384 + cw * 4096 + i * 1024 + (lane >> 3) * 128 + (((lane & 7) ^ (2 * i + (lane >> 5))) << 4));
            }
        }
        CFENCE();
        DG(7);
        WAIT_BAR(4);
        DG(6);
        issue(6);
        asm volatile("s_waitcnt vmcnt(8)" ::: "memory");
        if (lds_add_rtn(fl + C_KST, lane) == 3u) signal();
#pragma unroll
        for (int i = 0; i < 8; ++i)
            glds16p<K2_WNT>(wvoff, wsrc(i >> 2, cw * 4 + (i & 3)), lds0 + K2_SF + (i >> 2) * 16384 + (cw * 4 + (i & 3)) * 1024);
        {
            const u32x4 z4 = {0u, 0u, 0u, 0u};
            if (lane < 2) *(LAS u32x4*)(cv + CV_HB + lane * 16) = z4;
#pragma unroll
            for (int i = 0; i < 4; ++i) {
                const int off = 32 + i * 1024 + lane * 16;
                const u32x4 hv = *(const LAS u32x4*)(cv + CV_HA + off);
                const unsigned pv = *(const LAS unsigned*)(cv + CV_HA + off - 4);
                u32x4 sh;
                sh[0] = __builtin_amdgcn_alignbit(hv[0], pv, 16);
                sh[1] = __builtin_amdgcn_alignbit(hv[1], hv[0], 16);
                sh[2] = __builtin_amdgcn_alignbit(hv[2], hv[1], 16);
                sh[3] = __builtin_amdgcn_alignbit(hv[3], hv[2], 16);
                *(LAS u32x4*)(cv + CV_HB + off) = sh;
            }
        }
        WAIT_BAR(8);
        DG(8);
        issue(7);
#pragma unroll
        for (int i = 0; i < 8; ++i)
            glds16p<K2_WNT>(wvoff, wsrc(idx >> 4, 32 + cw * 8 + i), lds0 + K2_SCR + cw * 1024);
        WAIT_BAR(8);
        DG(11);
        if (cw == 0) { poll_ge(cnt + b * 64, 64u); lds_add(fl + F_KGO, lane); }
        else lds_wait_ge(fl + F_KGO, 1u);
        {
            u32x4 t[4];
#pragma unroll
            for (int i = 0; i < 4; ++i) t[i] = ld_sc1s(rs, (unsigned)WS_Q + (xcd * 32 + (lane >> 3)) * 16384 + (idx * 4 + cw) * 128 + (lane & 7) * 16, i * 131072);
#pragma unroll
            for (int i = 0; i < 4; ++i) *(LAS u32x4*)(cv + CV_KV + 480 + i * 1024 + lane * 16) = t[i];
        }
        const int r1 = lane & 15, q = lane >> 4;
        const int toff = (r1 & 1) ? CV_HA + 2 * (16 + r1 + 9 - 8 * q) : CV_HB + 2 * (16 + r1 + 10 - 8 * q);
        const int boff = CV_KV + 448 + 32 * r1 + 16 * q;
        f32x4 y[8];
        u32x4 tf[8];
        bf16x8 bf[8];
        const lds_ptr bp = cv + boff - 448;
        lds_ptr tp[4];
#pragma unroll
        for (int i = 0; i < 4; ++i) { tp[i] = cv + toff + 1024 * i; asm volatile("" : "+v"(tp[i])); }
#define LDT(bb, a, hf) do { const LAS unsigned* t_ = (const LAS unsigned*)(tp[(a) >> 1] + 64 * (bb) + 512 * ((a) & 1) + 8 * (hf)); \
            tf[a][2 * (hf)] = t_[0]; tf[a][2 * (hf) + 1] = t_[1]; SBAR(); } while (0)
#define LDB(bb, a) do { bf[a] = lds_rd128(bp + 448 - 64 * (bb) + 512 * (a)); SBAR(); } while (0)
#define CM(a, ww) do { asm volatile("v_mfma_f32_16x16x32_bf16 %0, %1, %2, %0" : "+v"(y[(a) + (ww)]) : "v"(tf[a]), "v"(bf[ww])); SBAR(); } while (0)
#define CM0(a, ww) do { asm volatile("v_mfma_f32_16x16x32_bf16 %0, %1, %2, 0" : "=&v"(y[(a) + (ww)]) : "v"(tf[a]), "v"(bf[ww])); SBAR(); } while (0)
#define PC() do { asm volatile(PACE_OUT); SBAR(); } while (0)
#pragma unroll
        for (int a = 0; a < 8; ++a) { if (a < 5) { LDT(0, a, 0); LDT(0, a, 1); } LDB(0, 7 - a); }
#pragma unroll
        for (int i = 0; i < 8; ++i)
            glds16p<K2_WNT>(wvoff, wsrc(i >> 2, 16 + cw * 4 + (i & 3)), lds0 + (i >> 2) * 16384 + (cw * 4 + (i & 3)) * 1024);
#pragma unroll
        for (int i = 0; i < 4; ++i)
            glds16p<K2_QNT>((unsigned)(idx * 64 + cw * 16 + (lane >> 4)) * 2048u + xcd * 256 + (lane & 15) * 16, Aq + i * 8192, lds0 + K2_SCR + cw * 1024);
        RAW_BAR();
        asm volatile("s_waitcnt vmcnt(4)" ::: "memory");
        lds_add(fl + C_S1, lane);
#define ZFL() PC()
        __builtin_amdgcn_s_setprio(AUX_PRIO);
        const float dch = (ch & 1) ? bfhi(dd) : bflo(dd);
        const lds_ptr kdp = cv + CV_KV + 480 + 32 * r1 + 16 * (q >> 1) + ((q & 1) ? 0 : 8);
        float kd[8][4];
        u32x2 kk;
        unsigned kc0, kc1, zo[8][2];
#define KD_RD(t) do { kk = *(const LAS u32x2*)(kdp + 512 * (t)); SBAR(); } while (0)
#define KD_UP0(t) do { kd[t][0] = bfhi(kk[1]); kd[t][1] = bflo(kk[1]); SBAR(); } while (0)
#define KD_UP1(t) do { kd[t][2] = bfhi(kk[0]); kd[t][3] = bflo(kk[0]); SBAR(); } while (0)
#define KD_MU0(t) do { kd[t][0] = mul_sv(dch, kd[t][0]); kd[t][1] = mul_sv(dch, kd[t][1]); SBAR(); } while (0)
#define KD_MU1(t) do { kd[t][2] = mul_sv(dch, kd[t][2]); kd[t][3] = mul_sv(dch, kd[t][3]); SBAR(); } while (0)
#define KD_CV(t) do { kc0 = cvtpk(kd[t][0], kd[t][1]); kc1 = cvtpk(kd[t][2], kd[t][3]); SBAR(); } while (0)
#define KD_X0(t) do { kd[t][0] = bflo(kc0); kd[t][1] = bfhi(kc0); SBAR(); } while (0)
#define KD_X1(t) do { kd[t][2] = bflo(kc1); kd[t][3] = bfhi(kc1); SBAR(); } while (0)
#define ZF0(t) do { kc0 = cvtpk(y[t][0], y[t][1]); kc1 = cvtpk(y[t][2], y[t][3]); SBAR(); } while (0)
#define ZF1(t) do { y[t][0] = bflo(kc0); y[t][1] = bfhi(kc0); SBAR(); } while (0)
#define ZF2(t) do { y[t][2] = bflo(kc1); y[t][3] = bfhi(kc1); SBAR(); } while (0)
#define ZF3(t) do { y[t][0] = add_vv(y[t][0], kd[t][0]); y[t][1] = add_vv(y[t][1], kd[t][1]); SBAR(); } while (0)
#define ZF4(t) do { y[t][2] = add_vv(y[t][2], kd[t][2]); y[t][3] = add_vv(y[t][3], kd[t][3]); SBAR(); } while (0)
#define ZF5(t) do { zo[t][0] = nz(cvtpk(y[t][0], y[t][1])); zo[t][1] = nz(cvtpk(y[t][2], y[t][3])); SBAR(); } while (0)
#define ZST(pr) do { auto s0 = __builtin_amdgcn_permlane16_swap(zo[2 * (pr)][0], zo[2 * (pr) + 1][0], false, false); \
            auto s1 = __builtin_amdgcn_permlane16_swap(zo[2 * (pr)][1], zo[2 * (pr) + 1][1], false, false); \
            st_sc1(u32x4{s0[0], s1[0], s0[1], s1[1]}, rs, (unsigned)WS_ZT + ch * 4096 + (pr) * 1024 + voff); SBAR(); } while (0)
#define W3(i) do { if ((i) == 0) PC(); } while (0)
#define HF() do { lds_add(fl + C_HF, lane); SBAR(); } while (0)
        CM0(0, 7); LDB(1, 7); CM0(0, 6); LDT(0, 5, 0); CM0(0, 5); LDT(0, 5, 1); CM0(0, 4); LDT(0, 6, 0); CM0(0, 3); LDT(0, 6, 1); CM0(0, 2); LDT(0, 7, 0); CM0(0, 1); LDT(0, 7, 1); CM0(0, 0); KD_RD(0);
        CM(1, 6); LDB(1, 6); CM(1, 5); LDT(1, 0, 0); CM(1, 4); LDT(1, 0, 1); CM(1, 3); PC(); CM(1, 2); PC(); CM(1, 1); KD_UP0(0); CM(1, 0); KD_UP1(0);
        CM(2, 5); LDB(1, 5); CM(2, 4); LDT(1, 1, 0); CM(2, 3); LDT(1, 1, 1); CM(2, 2); KD_MU0(0); CM(2, 1); KD_MU1(0); CM(2, 0); KD_CV(0);
        CM(3, 4); LDB(1, 4); CM(3, 3); LDT(1, 2, 0); CM(3, 2); LDT(1, 2, 1); CM(3, 1); KD_X0(0); CM(3, 0); KD_X1(0);
        CM(4, 3); LDB(1, 3); CM(4, 2); LDT(1, 3, 0); CM(4, 1); LDT(1, 3, 1); CM(4, 0); KD_RD(1);
        CM(5, 2); LDB(1, 2); CM(5, 1); LDT(1, 4, 0); CM(5, 0); LDT(1, 4, 1);
        CM(6, 1); LDB(1, 1); CM(6, 0); PC();
        CM(7, 0); LDB(1, 0);
        CM(0, 7); LDB(2, 7); CM(0, 6); LDT(1, 5, 0); CM(0, 5); LDT(1, 5, 1); CM(0, 4); LDT(1, 6, 0); CM(0, 3); LDT(1, 6, 1); CM(0, 2); LDT(1, 7, 0); CM(0, 1); LDT(1, 7, 1); CM(0, 0); PC();
        CM(1, 6); LDB(2, 6); CM(1, 5); LDT(2, 0, 0); CM(1, 4); LDT(2, 0, 1); CM(1, 3); KD_UP0(1); CM(1, 2); KD_UP1(1); CM(1, 1); KD_MU0(1); CM(1, 0); KD_MU1(1);
        CM(2, 5); LDB(2, 5); CM(2, 4); LDT(2, 1, 0); CM(2, 3); LDT(2, 1, 1); CM(2, 2); KD_CV(1); CM(2, 1); KD_X0(1); CM(2, 0); KD_X1(1);
        CM(3, 4); LDB(2, 4); CM(3, 3); LDT(2, 2, 0); CM(3, 2); LDT(2, 2, 1); CM(3, 1); KD_RD(2); CM(3, 0); PC();
        CM(4, 3); LDB(2, 3); CM(4, 2); LDT(2, 3, 0); CM(4, 1); LDT(2, 3, 1); CM(4, 0); PC();
        CM(5, 2); LDB(2, 2); CM(5, 1); LDT(2, 4, 0); CM(5, 0); LDT(2, 4, 1);
        CM(6, 1); LDB(2, 1); CM(6, 0); KD_UP0(2);
        CM(7, 0); LDB(2, 0);
        CM(0, 7); LDB(3, 7); CM(0, 6); LDT(2, 5, 0); CM(0, 5); LDT(2, 5, 1); CM(0, 4); LDT(2, 6, 0); CM(0, 3); LDT(2, 6, 1); CM(0, 2); LDT(2, 7, 0); CM(0, 1); LDT(2, 7, 1); CM(0, 0); KD_UP1(2);
        CM(1, 6); LDB(3, 6); CM(1, 5); LDT(3, 0, 0); CM(1, 4); LDT(3, 0, 1); CM(1, 3); KD_MU0(2); CM(1, 2); KD_MU1(2); CM(1, 1); KD_CV(2); CM(1, 0); KD_X0(2);
        CM(2, 5); LDB(3, 5); CM(2, 4); LDT(3, 1, 0); CM(2, 3); LDT(3, 1, 1); CM(2, 2); KD_X1(2); CM(2, 1); KD_RD(3); CM(2, 0); PC();
        CM(3, 4); LDB(3, 4); CM(3, 3); LDT(3, 2, 0); CM(3, 2); LDT(3, 2, 1); CM(3, 1); PC(); CM(3, 0); KD_UP0(3);
        CM(4, 3); LDB(3, 3); CM(4, 2); LDT(3, 3, 0); CM(4, 1); LDT(3, 3, 1); CM(4, 0); KD_UP1(3);
        CM(5, 2); LDB(3, 2); CM(5, 1); LDT(3, 4, 0); CM(5, 0); LDT(3, 4, 1);
        CM(6, 1); LDB(3, 1); CM(6, 0); KD_MU0(3);
        CM(7, 0); LDB(3, 0);
        CM(0, 7); LDB(4, 7); CM(0, 6); LDT(3, 5, 0); CM(0, 5); LDT(3, 5, 1); CM(0, 4); LDT(3, 6, 0); CM(0, 3); LDT(3, 6, 1); CM(0, 2); LDT(3, 7, 0); CM(0, 1); LDT(3, 7, 1); CM(0, 0); KD_MU1(3);
        CM(1, 6); LDB(4, 6); CM(1, 5); LDT(4, 0, 0); CM(1, 4); LDT(4, 0, 1); CM(1, 3); KD_CV(3); CM(1, 2); KD_X0(3); CM(1, 1); KD_X1(3); CM(1, 0); KD_RD(4);
        CM(2, 5); LDB(4, 5); CM(2, 4); LDT(4, 1, 0); CM(2, 3); LDT(4, 1, 1); CM(2, 2); PC(); CM(2, 1); PC(); CM(2, 0); KD_UP0(4);
        CM(3, 4); LDB(4, 4); CM(3, 3); LDT(4, 2, 0); CM(3, 2); LDT(4, 2, 1); CM(3, 1); KD_UP1(4); CM(3, 0); KD_MU0(4);
        CM(4, 3); LDB(4, 3); CM(4, 2); LDT(4, 3, 0); CM(4, 1); LDT(4, 3, 1); CM(4, 0); KD_MU1(4);
        CM(5, 2); LDB(4, 2); CM(5, 1); LDT(4, 4, 0); CM(5, 0); LDT(4, 4, 1);
        CM(6, 1); LDB(4, 1); CM(6, 0); KD_CV(4);
        CM(7, 0); LDB(4, 0);
        CM(0, 7); LDB(5, 7); CM(0, 6); LDT(4, 5, 0); CM(0, 5); LDT(4, 5, 1); CM(0, 4); LDT(4, 6, 0); CM(0, 3); LDT(4, 6, 1); CM(0, 2); LDT(4, 7, 0); CM(0, 1); LDT(4, 7, 1); CM(0, 0); KD_X0(4);
        CM(1, 6); LDB(5, 6); CM(1, 5); LDT(5, 0, 0); CM(1, 4); LDT(5, 0, 1); CM(1, 3); KD_X1(4); CM(1, 2); KD_RD(5); CM(1, 1); PC(); CM(1, 0); PC();
        CM(2, 5); LDB(5, 5); CM(2, 4); LDT(5, 1, 0); CM(2, 3); LDT(5, 1, 1); CM(2, 2); KD_UP0(5); CM(2, 1); KD_UP1(5); CM(2, 0); KD_MU0(5);
        CM(3, 4); LDB(5, 4); CM(3, 3); LDT(5, 2, 0); CM(3, 2); LDT(5, 2, 1); CM(3, 1); KD_MU1(5); CM(3, 0); KD_CV(5);
        CM(4, 3); LDB(5, 3); CM(4, 2); LDT(5, 3, 0); CM(4, 1); LDT(5, 3, 1); CM(4, 0); KD_X0(5);
        CM(5, 2); LDB(5, 2); CM(5, 1); LDT(5, 4, 0); CM(5, 0); LDT(5, 4, 1);
        CM(6, 1); LDB(5, 1); CM(6, 0); KD_X1(5);
        CM(7, 0); LDB(5, 0);
        CM(0, 7); LDB(6, 7); CM(0, 6); LDT(5, 5, 0); CM(0, 5); LDT(5, 5, 1); CM(0, 4); LDT(5, 6, 0); CM(0, 3); LDT(5, 6, 1); CM(0, 2); LDT(5, 7, 0); CM(0, 1); LDT(5, 7, 1); CM(0, 0); KD_RD(6);
        CM(1, 6); LDB(6, 6); CM(1, 5); LDT(6, 0, 0); CM(1, 4); LDT(6, 0, 1); CM(1, 3); PC(); CM(1, 2); PC(); CM(1, 1); KD_UP0(6); CM(1, 0); KD_UP1(6);
        CM(2, 5); LDB(6, 5); CM(2, 4); LDT(6, 1, 0); CM(2, 3); LDT(6, 1, 1); CM(2, 2); KD_MU0(6); CM(2, 1); KD_MU1(6); CM(2, 0); KD_CV(6);
        CM(3, 4); LDB(6, 4); CM(3, 3); LDT(6, 2, 0); CM(3, 2); LDT(6, 2, 1); CM(3, 1); KD_X0(6); CM(3, 0); KD_X1(6);
        CM(4, 3); LDB(6, 3); CM(4, 2); LDT(6, 3, 0); CM(4, 1); LDT(6, 3, 1); CM(4, 0); KD_RD(7);
        CM(5, 2); LDB(6, 2); CM(5, 1); LDT(6, 4, 0); CM(5, 0); LDT(6, 4, 1);
        CM(6, 1); LDB(6, 1); CM(6, 0); PC();
        CM(7, 0); LDB(6, 0);
        CM(0, 7); LDB(7, 7); CM(0, 6); LDT(6, 5, 0); CM(0, 5); LDT(6, 5, 1); CM(0, 4); LDT(6, 6, 0); CM(0, 3); LDT(6, 6, 1); CM(0, 2); LDT(6, 7, 0); CM(0, 1); LDT(6, 7, 1); CM(0, 0); PC();
        CM(1, 6); LDB(7, 6); CM(1, 5); LDT(7, 0, 0); CM(1, 4); LDT(7, 0, 1); CM(1, 3); KD_UP0(7); CM(1, 2); KD_UP1(7); CM(1, 1); KD_MU0(7); CM(1, 0); KD_MU1(7);
        CM(2, 5); LDB(7, 5); CM(2, 4); LDT(7, 1, 0); CM(2, 3); LDT(7, 1, 1); CM(2, 2); KD_CV(7); CM(2, 1); KD_X0(7); CM(2, 0); KD_X1(7);
        CM(3, 4); LDB(7, 4); CM(3, 3); LDT(7, 2, 0); CM(3, 2); LDT(7, 2, 1); CM(3, 1); PC(); CM(3, 0); PC();
        CM(4, 3); LDB(7, 3); CM(4, 2); LDT(7, 3, 0); CM(4, 1); LDT(7, 3, 1); CM(4, 0); PC();
        CM(5, 2); LDB(7, 2); CM(5, 1); LDT(7, 4, 0); CM(5, 0); LDT(7, 4, 1);
        CM(6, 1); LDB(7, 1); CM(6, 0); PC();
        CM(7, 0); LDB(7, 0);
        CM(0, 3); LDT(7, 5, 0); CM(0, 2); LDT(7, 5, 1); CM(1, 2); LDT(7, 6, 0); CM(1, 1); LDT(7, 6, 1); CM(2, 1); LDT(7, 7, 0); CM(2, 0); LDT(7, 7, 1);
        CM(3, 0); HF(); CM(0, 1); ZFL(); CM(0, 0); W3(0); CM(1, 0); ZF0(2); W3(1); CM(0, 5); ZF1(2); W3(2); CM(0, 4); ZF2(2); W3(3);
        CM(1, 4); ZF3(2); W3(4); CM(1, 3); ZF4(2); W3(5); CM(2, 3); ZF5(2); W3(6); CM(2, 2); ZF0(3); W3(7); CM(3, 2); ZF1(3);  CM(3, 1); ZF2(3);
        CM(4, 1); ZF3(3);  CM(4, 0); ZF4(3);  CM(5, 0); ZF5(3);  CM(0, 7); ZST(1);  CM(0, 6); ZF0(0);  CM(1, 6); ZF1(0);
        CM(1, 5); ZF2(0);  CM(2, 5); ZF3(0);  CM(2, 4); ZF4(0);  CM(3, 4); ZF5(0);  CM(3, 3); ZF0(1);  CM(4, 3); ZF1(1);
        CM(4, 2); ZF2(1);  CM(5, 2); ZF3(1);  CM(5, 1); ZF4(1);  CM(6, 1); ZF5(1);  CM(6, 0); ZST(0);  CM(7, 0); ZF0(4);
        DG(4);
        ZF1(4); ZF2(4); ZF3(4); ZF4(4); ZF5(4); ZF0(5); ZF1(5); ZF2(5); ZF3(5); ZF4(5); ZF5(5); ZST(2); ZF0(6); ZF1(6); ZF2(6); ZF3(6); ZF4(6); ZF5(6); ZF0(7); ZF1(7); ZF2(7); ZF3(7); ZF4(7); ZF5(7); ZST(3);
        DG(10);
        __builtin_amdgcn_s_setprio(3);
        while ((unsigned)__builtin_amdgcn_readfirstlane(*(volatile LAS unsigned*)(fl + C_OL)) < ZL_GATE) __builtin_amdgcn_s_sleep(4);
        CFENCE();
        const int arow = idx * 64 + (cw >> 1) * 32 + rg * 4, acol = xcd * 256 + (cw & 1) * 128 + cq * 16;
        u32x4 qr[4];
#pragma unroll
        for (int i = 0; i < 4; ++i) qr[i] = gld128((unsigned)(arow * 2048 + acol), Aq + i * 2048);
        {
            const lds_ptr tr = cv + CV_KV + 480;
            u32x4 t[4];
            const int ck = (cw >> 1) * 4 + ((lane ^ ((lane >> 5) * 2)) & 3);
            const unsigned zb = (unsigned)WS_ZT + (unsigned)(xcd * 128 + (cw & 1) * 64 + (lane >> 4) * 4) * 4096u + (idx >> 3) * 1024 +
                                ((idx & 3) * 4 + ((idx >> 2) & 1) * 16 + (ck >> 1) + (ck & 1) * 32) * 16;
#pragma unroll 1
            for (unsigned spins = 0; spins < (1u << 20); ++spins) {
#pragma unroll
                for (int i = 0; i < 4; ++i) t[i] = ld_sc1s(rs, zb + ((((lane >> 2) & 3) ^ i) << 12), i * 65536);
                if (!missing(mx4(mx4(mx8(t[0], t[1]), t[2]), t[3]))) break;
                __builtin_amdgcn_s_sleep(Z_SLEEP);
            }
            DG(13);
            __builtin_amdgcn_s_setprio(ZE_PRIO);
#pragma unroll
            for (int i = 0; i < 4; ++i) *(LAS u32x4*)(tr + i * 1024 + lane * 16) = t[i];
#pragma unroll
            for (int i = 0; i < 4; ++i) asm volatile("" : "+v"(qr[i]));
            u32x2 zc[8];
#pragma unroll
            for (int c = 0; c < 8; ++c)
                zc[c] = *(const LAS u32x2*)(tr + cq * 512 + (c >> 2) * 256 + ((((c & 3) * 4 + (rg >> 1)) ^ (cq * 2)) << 4) + (rg & 1) * 8);
#pragma unroll
            for (int i = 0; i < 4; ++i) {
                u32x4 o;
#pragma unroll
                for (int c = 0; c < 4; ++c) {
                    const unsigned z0 = zc[2 * c][i >> 1], z1 = zc[2 * c + 1][i >> 1];
                    o[c] = cvtpk(mul_vv(bflo(qr[i][c]), (i & 1) ? bfhi(z0) : bflo(z0)), mul_vv(bfhi(qr[i][c]), (i & 1) ? bfhi(z1) : bflo(z1)));
                }
                __builtin_amdgcn_raw_buffer_store_b128(o, ro, arow * 4096 + acol, i * 4096, OUT_AUX);
            }
        }
        asm volatile(".Lcp_k2z:");
        DG_END(14);
        return;
    }

    const int half = w >> 2;
    const int TA = qb * 8 + (w & 3) * 2 + half, TB = TA ^ 1;
    bf16x8 sel;
    {
        const short one = (((lane >> 2) & 1) == ((lane >> 4) & 1) && (lane & 11) == 0) ? (short)0x3f80 : (short)0;
#pragma unroll
        for (int i = 0; i < 8; ++i) sel[i] = SPM ? ((lane & 15) == (i & 4) ? (short)0x3f80 : (short)0) : one;
    }
    int sidx = (int)0xdddddddd;
#define LO8(x) __builtin_bit_cast(bfx16_t, __builtin_shufflevector(x, x, 0, 1, 2, 3, 4, 5, 6, 7))
#define HI8(x) __builtin_bit_cast(bfx16_t, __builtin_shufflevector(x, x, 8, 9, 10, 11, 12, 13, 14, 15))
#define SPV(v, p, o) __builtin_amdgcn_smfmac_f32_32x32x32_bf16(__builtin_bit_cast(bfx8_t, v), p, o, sidx, 0, 0)
#define SSUM(p, l) __builtin_amdgcn_smfmac_f32_16x16x64_bf16(__builtin_bit_cast(bfx8_t, sel), p, l, sidx, 0, 0)
    f32x16 oA = f32x16{}, oB = f32x16{};
    f32x4 lA = f32x4{}, lB = f32x4{};
    f32x16 negm, sA, sB;
    lds_ptr lp = lds + half * 16384 + lane * 16;
    bf16x8 k0[2], k1[2], v0[2], v1[2];
    u32x4 p0, p1 = {0u, 0u, 0u, 0u};
#define EX(X, i) X[i] = __builtin_amdgcn_exp2f(X[i])
    lds_ptr qpa = lds + K2_SF + (TA & 7) * 2048 + lane * 16, qpb = lds + K2_SF + (TB & 7) * 2048 + lane * 16;
    asm volatile("" : "+v"(qpa), "+v"(qpb), "+v"(lp), "+v"(sel), "+v"(oA), "+v"(oB), "+v"(lA), "+v"(lB), "+v"(sidx));
    WAIT_BAR(0);
    DG(3);
    const bf16x8 qa0 = lds_rd128(qpa), qb0 = lds_rd128(qpb);
    const bf16x8 x0 = lds_rd128(lds + lane * 16), x1 = lds_rd128(lds + lane * 16 + 1024);
    const bf16x8 qa1 = lds_rd128(qpa + 1024), qb1 = lds_rd128(qpb + 1024);
    {
        k0[0] = lds_rd128(lp); k1[0] = lds_rd128(lp + 1024);
        v0[0] = lds_rd128(lp + 2048); v1[0] = lds_rd128(lp + 3072);
        sA = MFMA32(x0, qa0, f32x16{});
        sB = MFMA32(x0, qb0, f32x16{});
        sA = MFMA32(x1, qa1, sA);
        sB = MFMA32(x1, qb1, sB);
        float m = fmx(sA[0], sA[1]), m2 = fmx(sB[0], sB[1]);
#pragma unroll
        for (int i = 2; i < 16; i += 2) m = fmx(fmx(m, sA[i]), sA[i + 1]);
#pragma unroll
        for (int i = 2; i < 16; i += 2) m2 = fmx(fmx(m2, sB[i]), sB[i + 1]);
        m = fmx(m, m2);
        auto rr = __builtin_amdgcn_permlane32_swap(__float_as_uint(m), __float_as_uint(m), false, false);
        m = fmx(__uint_as_float(rr[0]), __uint_as_float(rr[1]));
#pragma unroll
        for (int i = 0; i < 16; ++i) negm[i] = -m;
        sA = MFMA32(k0[0], qa0, negm);
        sA = MFMA32(k1[0], qa1, sA);
        v1[1] = v1[0];
#pragma unroll
        for (int i = 8; i < 16; ++i) sB[i] = 0.f;
        asm volatile("" : "+v"(sB));
        SBAR();
        RAW_BAR();
        SBAR();
        EX(sA, 0); EX(sA, 1);
    }
    DG(10);
#define SUM16(l, p) asm volatile("v_mfma_f32_16x16x32_bf16 %0, %1, %2, %0" : "+v"(l) : "v"(sel), "v"(p))
#define HALF(C, oC, lC, N, qx, qy, ka, kb, oP, lP, vprev, vcur0, PRE) do { \
        oP = SPV(vprev, HI8(N), oP); SBAR(); \
        PRE; EX(C, 2); EX(C, 3); EX(C, 4); SBAR(); \
        lP = SSUM(HI8(N), lP); SBAR(); \
        EX(C, 5); SBAR(); \
        N = MFMA32(ka, qx, negm); SBAR(); \
        EX(C, 6); EX(C, 7); EX(C, 8); SBAR(); \
        N = MFMA32(kb, qy, N); SBAR(); \
        EX(C, 9); EX(C, 10); EX(C, 11); SBAR(); \
        oC = SPV(vcur0, LO8(C), oC); SBAR(); \
        EX(C, 12); EX(C, 13); EX(C, 14); SBAR(); \
        lC = SSUM(LO8(C), lC); SBAR(); \
        EX(C, 15); EX(N, 0); EX(N, 1); SBAR(); \
    } while (0)
#define RD4(Y, nx) do { v1[Y] = lds_rd128((nx) + 3072); v0[Y] = lds_rd128((nx) + 2048); \
        k1[Y] = lds_rd128((nx) + 1024); k0[Y] = lds_rd128(nx); } while (0)
#define KT(X, nx, PRE0) do { \
        HALF(sA, oA, lA, sB, qb0, qb1, k0[X], k1[X], oB, lB, v1[X ^ 1], v0[X], PRE0; RD4(X ^ 1, nx)); \
        HALF(sB, oB, lB, sA, qa0, qa1, k0[X ^ 1], k1[X ^ 1], oA, lA, v1[X], v0[X], (void)0); \
    } while (0)
#define RING_BAR() do { unsigned long long sv_; \
        asm volatile("s_cmp_eq_u32 %1, 7\n\ts_cbranch_scc1 1f\n\ts_barrier\n1:\n\ts_mov_b64 %0, exec\n\ts_mov_b64 exec, 1\n\tds_add_u32 %2, %3\n\ts_mov_b64 exec, %0" \
                     : "=&s"(sv_) : "s"(st), "v"(fl + C_LD), "v"(1u) : "memory", "scc"); } while (0)
    SBAR();
#pragma unroll 1
    for (int st = 0; st < 8; ++st) {
        const lds_ptr cur = lp + (st & 1) * K2_SLOT;
        const lds_ptr nxt = lp + ((st + 1) & 1) * K2_SLOT;
        PRIO(2);
        KT(0, cur + 4096, (void)0);
        PRIO(1);
        KT(1, cur + 8192, (void)0);
        PRIO(0);
        KT(0, cur + 12288, (void)0);
        PRIO(3);
        KT(1, nxt, RAW_BAR(); SBAR());
    }
    oB = SPV(v1[1], HI8(sB), oB);
    SBAR();
    lB = SSUM(HI8(sB), lB);
    SBAR();
    DG(4);
    {
        const lds_ptr xo = lds + K2_SLOT + w * 4096 + lane * 16;
        *(LAS float*)(lds + K2_LX + w * 256 + lane * 4) = lB[0];
#pragma unroll
        for (int i = 0; i < 4; ++i) *(LAS f32x4*)(xo + i * 1024) = f32x4{oB[4 * i], oB[4 * i + 1], oB[4 * i + 2], oB[4 * i + 3]};
        lds_add(fl + F_X + w * 4, lane);
        lds_add(fl + C_END + half * 4, lane);
        lds_wait_ge(fl + F_X + (w ^ 4) * 4, 1u);
        const lds_ptr xi = lds + K2_SLOT + (w ^ 4) * 4096 + lane * 16;
        f32x4 t[4];
        const float lx = *(const LAS float*)(lds + K2_LX + (w ^ 4) * 256 + (lane & 31) * 4);
        { auto rr = __builtin_amdgcn_permlane32_swap(__float_as_uint(lA[0]), __float_as_uint(lA[0]), false, false); lA[0] = __uint_as_float(rr[0]); }
#pragma unroll
        for (int i = 0; i < 4; ++i) t[i] = *(const LAS f32x4*)(xi + i * 1024);
        SBAR();
        lA[0] += lx;
#pragma unroll
        for (int i = 0; i < 16; ++i) { float o = oA[i]; acc_vv(o, t[i >> 2][i & 3]); oA[i] = o; }
    }
    DG(9);
    const float l = lA[0];
    u32x4 on0, on1;
    auto finish = [&](float ls) {
        float rl = __builtin_amdgcn_rcpf(ls);
        asm volatile("s_nop 1" : "+v"(rl));
        f32x16 o;
#pragma unroll
        for (int i = 0; i < 16; ++i) o[i] = mul_vv(oA[i], rl);
        on0 = nz4(pack8n(o, 0)); on1 = nz4(pack8n(o, 1));
    };
    finish(l);
    {
        float t0, t1;
        asm("v_mul_f32_e32 %0, 0, %2\n\tv_mul_f32_e32 %1, 0, %3\n\tv_fmac_f32_e32 %0, 0, %4\n\tv_fmac_f32_e32 %1, 0, %5\n\tv_fmac_f32_e32 %0, 0, %6\n\tv_fmac_f32_e32 %1, 0, %7\n\tv_fmac_f32_e32 %0, 0, %8\n\tv_fmac_f32_e32 %1, 0, %9\n\t"
            "v_fmac_f32_e32 %0, 0, %10\n\tv_fmac_f32_e32 %1, 0, %11\n\tv_fmac_f32_e32 %0, 0, %12\n\tv_fmac_f32_e32 %1, 0, %13\n\tv_fmac_f32_e32 %0, 0, %14\n\tv_fmac_f32_e32 %1, 0, %15\n\tv_fmac_f32_e32 %0, 0, %16\n\tv_fmac_f32_e32 %1, 0, %17"
            : "=&v"(t0), "=&v"(t1) : "v"(oA[0]), "v"(oA[1]), "v"(oA[2]), "v"(oA[3]), "v"(oA[4]), "v"(oA[5]), "v"(oA[6]), "v"(oA[7]), "v"(oA[8]), "v"(oA[9]), "v"(oA[10]), "v"(oA[11]), "v"(oA[12]), "v"(oA[13]), "v"(oA[14]), "v"(oA[15]));
        const bool ok = (l <= 1.0e30f) && (l >= 1.0e-18f) && (t0 + t1 == 0.f);
        if (__builtin_expect(__any(!ok), 0)) {
            float m = -INFINITY, ls = 0.f;
            oA = f32x16{};
#pragma unroll 1
            for (int t = 0; t < 64; ++t) {
                const char* g = kvg + t * 4096 + lane * 16;
                const bf16x8 a0 = *(const bf16x8*)g, a1 = *(const bf16x8*)(g + 1024);
                const bf16x8 b0 = *(const bf16x8*)(g + 2048), b1 = *(const bf16x8*)(g + 3072);
                f32x16 s = MFMA32(a0, qa0, f32x16{});
                s = MFMA32(a1, qa1, s);
                float tm = s[0];
#pragma unroll
                for (int i = 1; i < 16; ++i) tm = __builtin_fmaxf(tm, s[i]);
                auto rr = __builtin_amdgcn_permlane32_swap(__float_as_uint(tm), __float_as_uint(tm), false, false);
                tm = __builtin_fmaxf(__uint_as_float(rr[0]), __uint_as_float(rr[1]));
                const float mn = __builtin_fmaxf(m, tm);
                const float alpha = __builtin_amdgcn_exp2f(m - mn);
                m = mn;
                float ps = 0.f;
#pragma unroll
                for (int i = 0; i < 16; ++i) { s[i] = bfhi(__float_as_uint(__builtin_amdgcn_exp2f(s[i] - mn))); ps += s[i]; oA[i] *= alpha; }
                ls = ls * alpha + ps;
                oA = SPV(b0, LO8(s), oA);
                oA = SPV(b1, HI8(s), oA);
            }
            auto rr = __builtin_amdgcn_permlane32_swap(__float_as_uint(ls), __float_as_uint(ls), false, false);
            asm volatile("s_nop 7\n\ts_nop 7" : "+v"(oA));
            finish(__uint_as_float(rr[0]) + __uint_as_float(rr[1]));
        }
    }
    u32x4 w3[4];
    {
        const unsigned og = (unsigned)WS_O + (unsigned)(TA * 32 + h) * 2048u + lane * 16;
        st_sc1(on0, rs, og);
        st_sc1(on1, rs, og + 1024);
        lds_add(fl + C_S1, lane);
        const char* s2 = wsrc(half ^ 1, 32 + (w & 3) * 4);
        const char* s3 = wsrc(half, 48 + (w & 3) * 4);
        lds_wait_ge(fl + C_S1, 12u);
        DG(5);
#pragma unroll
        for (int i = 0; i < 4; ++i)
            glds16p<K2_WNT>(wvoff, s2 + i * 16384, lds0 + K2_SLOT + ((w ^ 4) * 4 + i) * 1024);
#pragma unroll
        for (int i = 0; i < 4; ++i) w3[i] = gld128(wvoff, s3 + i * 16384);
    }
    {
        const int half = w & 1;
        const int T3 = tt * 4 + (w >> 1);
        const unsigned og = (unsigned)WS_O + (unsigned)T3 * 65536u + half * 32768 + lane * 16;
        const int r = lane & 31, hh = lane >> 5;
        const int g = lane >> 4, q = (lane & 15) >> 2, p = lane & 3;
        int woff[2];
#pragma unroll
        for (int n = 0; n < 2; ++n) {
            const int c = 4 * (n ^ half) + 2 * (p & 1) + (g & 1);
            woff[n] = (4 * hh + q) * 128 + ((c ^ ((q >> 1) << 2)) << 4) + (p >> 1) * 8;
        }
        f32x16 acc[2] = {f32x16{}, f32x16{}};
        lds_ptr wl[3][2] = {{lds + half * 16384 + woff[0], lds + half * 16384 + woff[1]},
                            {lds + K2_SF + half * 16384 + woff[0], lds + K2_SF + half * 16384 + woff[1]},
                            {lds + K2_CV + half * 2 * CV_BYTES + woff[0], lds + K2_CV + half * 2 * CV_BYTES + woff[1]}};
        asm volatile("" : "+v"(wl[0][0]), "+v"(wl[0][1]), "+v"(wl[1][0]), "+v"(wl[1][1]), "+v"(wl[2][0]), "+v"(wl[2][1]));
#define MS(m) ((m) < 64 - 2 * TL ? (m) >> 1 : 32 - TL + ((m) - (64 - 2 * TL)) % TL)
#define MN(m) ((m) < 64 - 2 * TL ? (m) & 1 : (m) < 64 - TL)
        constexpr int WR = 2 * WD + 2;
        s16x4 wa[WR], wb[WR];
#define LDW(m) do { const int s_ = MS(m), f_ = s_ < 8 ? 1 : s_ < 24 ? 0 : 2; \
            const int o_ = f_ == 2 ? ((s_ >> 2) & 1) * CV_BYTES + (s_ & 3) * 2048 : (f_ ? 0 : ((s_ >> 3) - 1) * K2_SLOT) + (s_ & 7) * 2048; \
            wa[(m) % WR] = lds_tr(wl[f_][MN(m)] + o_); wb[(m) % WR] = lds_tr(wl[f_][MN(m)] + o_ + 1024); SBAR(); } while (0)
#pragma unroll
        for (int m = 0; m < 2 * WD; ++m) LDW(m);
        u32x4 of[OWIN];
#define LDO(i) do { of[(i) % OWIN] = ld_sc1s(rs, og, (((i) + krot) & 31) * 1024); SBAR(); } while (0)
        for (unsigned spins = 0; spins < (1u << 20); ++spins) {
#pragma unroll
            for (int i = 0; i < OW0; ++i) LDO(i);
            unsigned m = mx8(of[0], of[1]);
#pragma unroll
            for (int i = 2; i < OW0; ++i) m = mx4(m, of[i]);
            if (!missing(m)) break;
            DGC(12);
        }
        lds_add(fl + C_S2, lane);
#pragma unroll
        for (int i = OW0; i < OWIN; ++i) LDO(i);
        DG(6);
        __builtin_amdgcn_s_setprio(P3_PRIO);
        SBAR();
        unsigned fv = 0;
#define FLAG_RD(a) do { fv = *(volatile LAS unsigned*)(a); SBAR(); } while (0)
#define FLAG_CHK(a, n) do { if (__builtin_expect((unsigned)__builtin_amdgcn_readfirstlane(fv) < (n), 0)) lds_wait_ge(a, n); CFENCE(); SBAR(); } while (0)
        lds_ptr xo = lds + K2_SF + w * 4096 + lane * 16, xi = lds + K2_SF + (w ^ 1) * 4096 + lane * 16;
        int bo = (T3 * 32 + (lane & 15)) * 4096 + 2048 + nt * 128 + half * 64 + g * 16;
        lds_ptr w3d = lds + K2_CV + (w >> 1) * CV_BYTES + (w & 1) * 4096 + lane * 16;
        asm volatile("" : "+v"(xo), "+v"(xi), "+v"(bo), "+v"(w3d));
        f32x4 xt[4];
        constexpr int M3 = TL > 8 ? 64 - 2 * TL + TL - 8 - 2 * WD : 48 - 2 * WD;
        sfor(std::make_integer_sequence<int, 64>{}, [&](auto mc) __attribute__((always_inline)) {
            constexpr int m = decltype(mc)::value, s = MS(m), n = MN(m);
            constexpr bool first = m < 64 - 2 * TL ? n == 0 : n == 1;
            constexpr bool last = m < 64 - 2 * TL ? n == 1 : n == 0;
            acc[n] = MFMA32(cat8(wa[m % WR], wb[m % WR]), PB(of[s % OWIN]), acc[n]);
            SBAR();
            if constexpr (m == 32 - 2 * WD) FLAG_CHK(fl + C_S2, 8u);
            if constexpr (m == M3) FLAG_CHK(fl + C_S3, 8u);
            if constexpr (m + 2 * WD < 64) LDW(m + 2 * WD);
            if constexpr (first && (s & 1) && s + 1 >= OW0 && s + 1 < 32) {
                if (__builtin_expect(missing(mx8(of[(s + 1) % OWIN], of[(s + 2) % OWIN])), 0)) {
#pragma unroll 1
                    for (unsigned spins = 0; spins < (1u << 20); ++spins) {
                        __builtin_amdgcn_s_sleep(2);
                        LDO(s + 1); LDO(s + 2);
                        DGC(13);
                        if (!missing(mx8(of[(s + 1) % OWIN], of[(s + 2) % OWIN]))) break;
                    }
                }
            }
            if constexpr (last && s + OWIN < 32) LDO(s + OWIN);
            if constexpr (m == 15 - 2 * WD) lds_add(fl + C_U0, lane);
            if constexpr (m == 29 - 2 * WD) FLAG_RD(fl + C_S2);
            if constexpr (m == W3M - 3) FLAG_RD(fl + C_HF);
            if constexpr (m == W3M) FLAG_CHK(fl + C_HF, 4u);
            if constexpr (m >= W3M && m < W3M + 8 && ((m - W3M) & 1) == 0) {
                asm volatile("" : "+v"(w3[((m - W3M) >> 1) & 3]));
                *(LAS u32x4*)(w3d + (((m - W3M) >> 1) & 3) * 1024) = w3[((m - W3M) >> 1) & 3];
            }
            if constexpr (m == W3M + 6) lds_add(fl + C_S3, lane);
            if constexpr (m == M3 - 3) FLAG_RD(fl + C_S3);
            if constexpr (m == 64 - TL - 3) FLAG_RD(fl + C_U0);
            if constexpr (m == 64 - TL) FLAG_CHK(fl + C_U0, 8u);
            if constexpr (m == 64 - TL + 1 || m == 64 - TL + 2) {
                constexpr int i = 2 * (m - (64 - TL + 1));
                *(LAS f32x4*)(xo + i * 1024) = f32x4{acc[1][4 * i], acc[1][4 * i + 1], acc[1][4 * i + 2], acc[1][4 * i + 3]};
                *(LAS f32x4*)(xo + i * 1024 + 1024) = f32x4{acc[1][4 * i + 4], acc[1][4 * i + 5], acc[1][4 * i + 6], acc[1][4 * i + 7]};
            }
            if constexpr (m == 64 - TL + 2) lds_add(fl + F_R + w * 4, lane);
            if constexpr (m == XR_AT - 2) FLAG_RD(fl + F_R + (w ^ 1) * 4);
            if constexpr (m == XR_AT) {
                FLAG_CHK(fl + F_R + (w ^ 1) * 4, 1u);
#pragma unroll
                for (int i = 0; i < 4; ++i) xt[i] = *(const LAS f32x4*)(xi + i * 1024);
            }
            if constexpr (ZL_GATE && last && s + OWIN == ZL_AT) lds_add(fl + C_OL, lane);
            if constexpr (m == 2 * P3_T1 + 1) __builtin_amdgcn_s_setprio(P3_PRIO - 1);
            SBAR();
            if constexpr (m == 31) DG(11);
            if constexpr (m == 47) DG(14);
        });
        asm volatile("s_nop 11" : "+v"(acc[0]));
        DG(7);
#pragma unroll
        for (int i = 0; i < 16; ++i) { float o = xt[i >> 2][i & 3]; acc_vv(o, acc[0][i]); acc[1][i] = o; }
        u32x4 pa = pack8n(acc[1], 0), pb = pack8n(acc[1], 1);
#pragma unroll
        for (int i = 0; i < 4; ++i) {
            auto sw = __builtin_amdgcn_permlane16_swap(pa[i], pb[i], false, false);
            pa[i] = sw[0]; pb[i] = sw[1];
        }
        asm volatile("" :: "v"(acc[0]));
        __builtin_amdgcn_raw_buffer_store_b128(pa, ro, bo, 0, OUT_AUX);
        __builtin_amdgcn_raw_buffer_store_b128(pb, ro, bo, 65536, OUT_AUX);
    }
    if (b == HB && w == 0) {
        DRAIN();
        const unsigned hs7 = (unsigned)__builtin_amdgcn_s_memrealtime();
        const unsigned* hp_ = (const unsigned*)(ws + (30u << 20));
        const unsigned kad = __builtin_amdgcn_readfirstlane(hp_[0]);
        unsigned iv = (HQ);
        iv = iv < 20000u ? iv : 20000u;
        while ((unsigned)__builtin_amdgcn_s_memrealtime() - hs7 < iv) __builtin_amdgcn_s_sleep(1);
    }
    DG_END(8);
}

extern "C" void kernel_launch(void* const* d_in, const int* in_sizes, int n_in,
                              void* d_out, int out_size, void* d_ws, size_t ws_size,
                              hipStream_t stream) {
    const void* A_q = d_in[0];
    const void* A_k = d_in[1];
    const void* A_v = d_in[2];
    const void* A_shift = d_in[3];
    const void* A_ssmk = d_in[4];
    const void* A_D = d_in[5];
    const void* B_x = d_in[6];
    const void* B_wqkv = d_in[7];
    const void* B_wo = d_in[8];
    char* ws = (char*)d_ws;
    k1_qkv<<<256, 512, 0, stream>>>((const char*)B_x, (const char*)B_wqkv, ws);
    k2_attn<<<256, 768, 0, stream>>>(ws, (const char*)A_q, (const char*)A_k, (const char*)A_v, (const char*)A_shift,
                                     (const char*)A_ssmk, (const unsigned*)A_D, (const char*)B_wo, (char*)d_out);
}
```

```cpp
#include <hip/hip_runtime.h>
#include <hip/hip_bf16.h>
#include <stdint.h>
#include <utility>

typedef short bf16x8 __attribute__((ext_vector_type(8)));
typedef short s16x4 __attribute__((ext_vector_type(4)));
typedef float f32x16 __attribute__((ext_vector_type(16)));
typedef float f32x4 __attribute__((ext_vector_type(4)));
typedef float f32x2 __attribute__((ext_vector_type(2)));
typedef unsigned u32x4 __attribute__((ext_vector_type(4)));
typedef unsigned u32x2 __attribute__((ext_vector_type(2)));
typedef __bf16 bf16x2_t __attribute__((ext_vector_type(2)));

#define LAS __attribute__((address_space(3)))
typedef LAS char* lds_ptr;
#define DI __device__ __forceinline__
#define SBAR() __builtin_amdgcn_sched_barrier(0)
#define MFMA32(a, b, c) __builtin_amdgcn_mfma_f32_32x32x16_bf16((a), (b), (c), 0, 0, 0)
#define MFMA16(a, b, c) __builtin_amdgcn_mfma_f32_16x16x32_bf16((a), (b), (c), 0, 0, 0)
typedef __bf16 bfx8_t __attribute__((ext_vector_type(8)));
typedef __bf16 bfx16_t __attribute__((ext_vector_type(16)));
typedef float f32x8 __attribute__((ext_vector_type(8)));
#define SPM 1
#define WAIT_BAR(N) asm volatile("s_waitcnt vmcnt(" #N ") lgkmcnt(0)\n\ts_barrier" ::: "memory")
#define LGKM_BAR() asm volatile("s_waitcnt lgkmcnt(0)\n\ts_barrier" ::: "memory")

#define DG_DECL(K)
#define DG(i)
#define DG_END(i)
#define DGC(i)

#define DGV(i)
constexpr int S_LEN = 2048, DM = 1024, NH = 32;

#define KQ 1
#define OQ 0
#define QT(h, T) (KQ ? (h) * 64 + (T) : (T) * 32 + (h))
constexpr size_t WS_Q = 0;
constexpr size_t WS_KV = 4u << 20;
constexpr size_t WS_O = OQ ? WS_Q : 12u << 20;
constexpr size_t WS_KVR = 16u << 20;
#define ZK 0
constexpr size_t WS_ZT = ZK ? WS_KVR : 20u << 20;
constexpr size_t WS_CNT = 24u << 20;

template <int... I, class F> DI void sfor(std::integer_sequence<int, I...>, F&& f) { (f(std::integral_constant<int, I>{}), ...); }
DI unsigned cvtpk(float lo, float hi) {
    f32x2 v = {lo, hi};
    return __builtin_bit_cast(unsigned, __builtin_convertvector(v, bf16x2_t));
}
DI float bflo(unsigned u) { return __uint_as_float(u << 16); }
DI float bfhi(unsigned u) { return __uint_as_float(u & 0xffff0000u); }

DI void glds16(unsigned voff, const void* sbase, unsigned lds_dst) {
    unsigned sv;
    asm volatile("s_mov_b32 %0, m0\n\ts_mov_b32 m0, %3\n\ts_nop 0\n\tglobal_load_lds_dwordx4 %1, %2\n\ts_mov_b32 m0, %0"
                 : "=&s"(sv) : "v"(voff), "s"(sbase), "s"(lds_dst) : "memory");
}
template <int NT> DI void glds16p(unsigned voff, const void* sbase, unsigned lds_dst) {
    if constexpr (NT) {
        unsigned sv;
        asm volatile("s_mov_b32 %0, m0\n\ts_mov_b32 m0, %3\n\ts_nop 0\n\tglobal_load_lds_dwordx4 %1, %2 nt\n\ts_mov_b32 m0, %0"
                     : "=&s"(sv) : "v"(voff), "s"(sbase), "s"(lds_dst) : "memory");
    } else glds16(voff, sbase, lds_dst);
}
#define K1_XNT 0
#define K1_WNT 0
#define K2_SNT 1
#define K2_QNT 0
#define K2_WNT 0
#define OUT_AUX 16
#define K1_SAUX 16
DI bf16x8 lds_rd128(lds_ptr p) { return *(const LAS bf16x8*)p; }
DI s16x4 lds_tr(lds_ptr p) {
    return __builtin_bit_cast(s16x4, __builtin_amdgcn_ds_read_tr16_b64_v4i16((LAS s16x4*)p));
}
DI bf16x8 cat8(s16x4 a, s16x4 b) { return __builtin_shufflevector(a, b, 0, 1, 2, 3, 4, 5, 6, 7); }
DI u32x4 pack8(const f32x16& x, int s, float sc) {
    u32x4 p;
    p[0] = cvtpk(x[8 * s + 0] * sc, x[8 * s + 1] * sc);
    p[1] = cvtpk(x[8 * s + 2] * sc, x[8 * s + 3] * sc);
    p[2] = cvtpk(x[8 * s + 4] * sc, x[8 * s + 5] * sc);
    p[3] = cvtpk(x[8 * s + 6] * sc, x[8 * s + 7] * sc);
    return p;
}
DI u32x4 pack8n(const f32x16& x, int s) {
    u32x4 p;
    p[0] = cvtpk(x[8 * s + 0], x[8 * s + 1]);
    p[1] = cvtpk(x[8 * s + 2], x[8 * s + 3]);
    p[2] = cvtpk(x[8 * s + 4], x[8 * s + 5]);
    p[3] = cvtpk(x[8 * s + 6], x[8 * s + 7]);
    return p;
}
DI void rnd2(float& a, float& b) { unsigned u = cvtpk(a, b); a = bflo(u); b = bfhi(u); }

#define CONV_PACE()
#define RAW_BAR() asm volatile("s_barrier" ::: "memory")
DI unsigned gld32(unsigned voff, const void* sbase) {
    unsigned r;
    asm volatile("global_load_dword %0, %1, %2" : "=v"(r) : "v"(voff), "s"(sbase) : "memory");
    return r;
}
DI u32x2 gld64(unsigned voff, const void* sbase) {
    u32x2 r;
    asm volatile("global_load_dwordx2 %0, %1, %2" : "=v"(r) : "v"(voff), "s"(sbase) : "memory");
    return r;
}
DI float mul_sv(float s, float v) { float r; asm("v_mul_f32_e32 %0, %1, %2" : "=v"(r) : "s"(s), "v"(v)); return r; }
DI float mul_vv(float a, float b) { float r; asm("v_mul_f32_e32 %0, %1, %2" : "=v"(r) : "v"(a), "v"(b)); return r; }
DI float add_vv(float a, float b) { float r; asm("v_add_f32_e32 %0, %1, %2" : "=v"(r) : "v"(a), "v"(b)); return r; }
DI void acc_vv(float& a, float b) { asm("v_add_f32_e32 %0, %0, %1" : "+v"(a) : "v"(b)); }
DI void fmac_vv(float& acc, float a, float b) { asm("v_fmac_f32_e32 %0, %1, %2" : "+v"(acc) : "v"(a), "v"(b)); }
DI void fmac_sv(float& acc, float s, float v) { asm("v_fmac_f32_e32 %0, %1, %2" : "+v"(acc) : "s"(s), "v"(v)); }
typedef __attribute__((address_space(1))) unsigned gu32;
#define RLX_AGENT __ATOMIC_RELAXED, __HIP_MEMORY_SCOPE_AGENT
#define CFENCE() asm volatile("" ::: "memory")
#define DRAIN() asm volatile("s_waitcnt vmcnt(0)" ::: "memory")
constexpr int AUX_SC1 = 16;
DI u32x4 ld_sc1(__amdgpu_buffer_rsrc_t rs, unsigned off) { return __builtin_amdgcn_raw_buffer_load_b128(rs, (int)off, 0, AUX_SC1); }
DI u32x4 ld_sc1s(__amdgpu_buffer_rsrc_t rs, unsigned off, int soff) { return __builtin_amdgcn_raw_buffer_load_b128(rs, (int)off, soff, AUX_SC1); }
DI void st_sc1(u32x4 v, __amdgpu_buffer_rsrc_t rs, unsigned off) { __builtin_amdgcn_raw_buffer_store_b128(v, rs, (int)off, 0, AUX_SC1); }
constexpr unsigned EMPTY = 0xffffffffu;
DI unsigned nz(unsigned x) { return x < EMPTY - 1u ? x : EMPTY - 1u; }
DI u32x4 nz4(u32x4 v) { return u32x4{nz(v[0]), nz(v[1]), nz(v[2]), nz(v[3])}; }
DI float fmx(float a, float b) { return __builtin_elementwise_maximum(a, b); }
DI unsigned umax(unsigned a, unsigned b) { return a > b ? a : b; }
DI unsigned umax3(unsigned a, unsigned b, unsigned c) { return umax(umax(a, b), c); }
DI unsigned mx4(unsigned m, u32x4 v) { return umax3(umax3(m, v[0], v[1]), v[2], v[3]); }
DI unsigned mx8(u32x4 a, u32x4 b) { return umax(umax3(umax3(umax3(a[0], a[1], a[2]), a[3], b[0]), b[1], b[2]), b[3]); }
DI bool missing(unsigned m) { return __builtin_amdgcn_ballot_w64(m == EMPTY) != 0; }
DI unsigned lds_add_rtn(lds_ptr p, int lane) {
    unsigned old = 0;
    CFENCE();
    if (lane == 0) old = __hip_atomic_fetch_add((LAS unsigned*)p, 1u, __ATOMIC_RELAXED, __HIP_MEMORY_SCOPE_WORKGROUP);
    old = __builtin_amdgcn_readfirstlane(old);
    CFENCE();
    return old;
}
DI void lds_add(lds_ptr p, int lane) {
    unsigned long long sv;
    asm volatile("s_mov_b64 %0, exec\n\ts_mov_b64 exec, 1\n\tds_add_u32 %1, %2\n\ts_mov_b64 exec, %0"
                 : "=&s"(sv) : "v"(p), "v"(1u) : "memory");
}
DI void lds_wait_ge(lds_ptr p, unsigned v) {
    CFENCE();
    while ((unsigned)__builtin_amdgcn_readfirstlane(*(volatile LAS unsigned*)p) < v) __builtin_amdgcn_s_sleep(1);
    CFENCE();
}
DI void poll_ge(gu32* word, unsigned want) {
    CFENCE();
    for (unsigned spins = 0; spins < (1u << 22); ++spins) {
        if ((unsigned)__builtin_amdgcn_readfirstlane(__hip_atomic_load(word, RLX_AGENT)) >= want) break;
        __builtin_amdgcn_s_sleep(1);
    }
    __builtin_amdgcn_fence(__ATOMIC_ACQUIRE, "wavefront");
    CFENCE();
}
#define PB(x) __builtin_bit_cast(bf16x8, x)
#define PRIO(x) do { __builtin_amdgcn_s_setprio(x); SBAR(); } while (0)
DI u32x4 gld128(unsigned voff, const void* sbase) {
    u32x4 r;
    asm volatile("global_load_dwordx4 %0, %1, %2" : "=v"(r) : "v"(voff), "s"(sbase) : "memory");
    return r;
}

#define K1_UNR 15
#define K1_LA 5
#define K1_ROT ((tb & 3) * 4)
#define K1_PF 0
#define K1_PG 0
#define K1_TW 0
#define CPF 3
#define PK2 0
#define BK1 0
#define PKC 0
#define PKC_AT 6
#define PK2_AT 13
#define CPF_POS 1
#define PG1_AT 0
#define PG2_AT 2
#define PG3_AT 6
#define K1_WB(n) WAIT_BAR(1)
#define PF1_AT -1
#define PF1_LO 4
#define PF1_N 4
#define PF2_AT 1
#define PF2_LO 8
#define PF2_N 4
#define PF3_AT 5
#define PF3_LO 12
#define PF3_N 4
#define K1_KR ((kt + rot) & 15)
#define K1_ST(v, rs, off) __builtin_amdgcn_raw_buffer_store_b128(v, rs, (int)(off), 0, K1_SAUX)
constexpr int K1_STAGE = 40960, K1U = K1_UNR;
__global__ __launch_bounds__(512, 2) void k1_qkv(const char* __restrict__ X, const char* __restrict__ W,
                                                 char* __restrict__ ws, const char* __restrict__ Wo,
                                                 const char* __restrict__ Aq, const char* __restrict__ Ak,
                                                 const char* __restrict__ Av, const char* __restrict__ ssmk) {
    __shared__ __attribute__((aligned(16))) char lds_raw[4 * K1_STAGE];
    const lds_ptr lds = (lds_ptr)lds_raw;
    const unsigned lds0 = (unsigned)(uintptr_t)lds;
    const int tid = threadIdx.x, lane = tid & 63;
    const int w = __builtin_amdgcn_readfirstlane(tid >> 6);
    const int b = blockIdx.x, xcd = b & 7, idx = b >> 3;
    const int hp = (xcd >> 1) * 4 + (idx & 3), tb = (xcd & 1) * 8 + (idx >> 2);
    DG_DECL(0)

    const int lw = w & 3;
    const int rot = K1_ROT;
    unsigned voffX[4], voffW[2];
#pragma unroll
    for (int pp = 0; pp < 4; ++pp) {
        const int row = 32 * lw + 8 * pp + (lane >> 3);
        const int ch = (lane & 7) ^ ((row >> 1) & 7);
        voffX[pp] = (unsigned)(tb * 128 + row) * 2048u + ch * 16;
    }
#pragma unroll
    for (int pp = 0; pp < 2; ++pp) {
        const int krow = 16 * lw + 8 * pp + (lane >> 3);
        const int ch = (lane & 7) ^ (((lane >> 4) & 1) << 2);
        voffW[pp] = (unsigned)krow * 6144u + ch * 16;
    }
    auto issue = [&](int kt, int lo, int hi) {
        const int kr = K1_KR;
        const char* xb = X + kr * 128;
        const char* wb = W + (size_t)kr * (64 * 6144) + hp * 128;
        const unsigned st = lds0 + (kt & 3) * K1_STAGE;
#pragma unroll
        for (int pp = 0; pp < 4; ++pp)
            if (pp >= lo && pp < hi) glds16p<K1_XNT>(voffX[pp], xb, st + (4 * lw + pp) * 1024);
#pragma unroll
        for (int sec = 0; sec < 3; ++sec)
#pragma unroll
            for (int pp = 0; pp < 2; ++pp)
                if (4 + 2 * sec + pp >= lo && 4 + 2 * sec + pp < hi)
                    glds16p<K1_WNT>(voffW[pp], wb + sec * 2048, st + 16384 + sec * 8192 + (2 * lw + pp) * 1024);
    };
    DG(13);
    unsigned cpf;
    auto codepf = [&]() {
        unsigned long long pc; unsigned lo, hi;
        asm volatile("s_getpc_b64 %0\n.Lcp_k1a:\n\ts_mov_b32 %1, %3\n\ts_mov_b32 %2, %4" : "=s"(pc), "=s"(lo), "=s"(hi) : "n"(0), "n"(0));
        if (CPF & 1) lo = 0; else asm volatile("s_mov_b32 %0, .Lcp_k2a-.Lcp_k1a" : "=s"(lo));
        if (CPF & 2) asm volatile("s_mov_b32 %0, .Lcp_k2z-.Lcp_k1a" : "=s"(hi)); else asm volatile("s_mov_b32 %0, .Lcp_k1z-.Lcp_k1a" : "=s"(hi));
        unsigned off = lo + (unsigned)(idx + 32 * (lane & 7) + 256 * w) * 64u;
        off = off < hi ? off : hi;
        asm volatile("global_load_dword %0, %1, %2" : "=v"(cpf) : "v"(off), "s"(pc) : "memory");
    };
    if (w < 4) { issue(0, 0, 5); issue(1, 0, 5); codepf(); }
    else { issue(0, 5, 10); issue(1, 5, 10); }
    DG(14);
    int w2 = w;
    asm volatile("" : "+s"(w2));

    if (w2 >= 4) {
#define WAITN_BAR(N) asm volatile("s_waitcnt vmcnt(%0) lgkmcnt(0)\n\ts_barrier" :: "n"(N) : "memory")
        issue(2, 0, K1_LA);
        asm volatile("s_waitcnt vmcnt(%0)" :: "n"(5 + K1_LA) : "memory");
#pragma unroll 1
        for (int kt = 0; kt < 13; ++kt) {
            WAITN_BAR(10 + K1_LA);
            issue(kt + 2, K1_LA, 10);
            issue(kt + 3, 0, K1_LA);
        }
        WAITN_BAR(10 + K1_LA);
        issue(15, K1_LA, 10);
        WAIT_BAR(10);
        WAIT_BAR(0);
        if (lw == 0 && lane < 2) *(unsigned*)(ws + WS_CNT + b * 256 + lane * 128) = 0u;
        DG_END(5);
        return;
    }

    const int wm = w >> 1, wn = w & 1;
    const int r = lane & 31, hh = lane >> 5;
    const int g = lane >> 4, q = (lane & 15) >> 2, p = lane & 3;
    const int xoff0 = (64 * wm + r) * 128 + ((hh ^ ((r >> 1) & 7)) << 4);
    const int woff0 = 16384 + (8 * hh + q) * 128 + (((4 * wn + 2 * (g & 1) + (p >> 1)) ^ ((q >> 1) << 2)) << 4) + (p & 1) * 8;

    f32x16 acc[2][3];
#pragma unroll
    for (int m = 0; m < 2; ++m)
#pragma unroll
        for (int s = 0; s < 3; ++s) acc[m][s] = f32x16{};
    bf16x8 xf[2][2];
    s16x4 wl[2][3], wh[2][3];
#define K1_WF(f, sec) cat8(wl[f][sec], wh[f][sec])
#define K1_STEP(F, st, t, BAR_HERE) do { \
        const lds_ptr xp_ = (st) + (xoff0 ^ ((t) << 5)); const lds_ptr wp_ = (st) + woff0 + (t) * 2048; \
        acc[0][0] = MFMA32(K1_WF(F, 0), xf[F][0], acc[0][0]); SBAR(); \
        if (BAR_HERE) { K1_WB(BAR_HERE); SBAR(); } \
        xf[F ^ 1][0] = lds_rd128(xp_); wl[F ^ 1][0] = lds_tr(wp_); wh[F ^ 1][0] = lds_tr(wp_ + 512); SBAR(); \
        acc[0][1] = MFMA32(K1_WF(F, 1), xf[F][0], acc[0][1]); SBAR(); \
        wl[F ^ 1][1] = lds_tr(wp_ + 8192); wh[F ^ 1][1] = lds_tr(wp_ + 8192 + 512); SBAR(); \
        acc[0][2] = MFMA32(xf[F][0], K1_WF(F, 2), acc[0][2]); SBAR(); \
        wl[F ^ 1][2] = lds_tr(wp_ + 16384); wh[F ^ 1][2] = lds_tr(wp_ + 16384 + 512); xf[F ^ 1][1] = lds_rd128(xp_ + 4096); SBAR(); \
        acc[1][0] = MFMA32(K1_WF(F, 0), xf[F][1], acc[1][0]); SBAR(); \
        acc[1][1] = MFMA32(K1_WF(F, 1), xf[F][1], acc[1][1]); \
        acc[1][2] = MFMA32(xf[F][1], K1_WF(F, 2), acc[1][2]); SBAR(); \
    } while (0)

    WAIT_BAR(6);
    DG(3);
    {
        const lds_ptr xp_ = lds + xoff0; const lds_ptr wp_ = lds + woff0;
        xf[0][0] = lds_rd128(xp_); xf[0][1] = lds_rd128(xp_ + 4096);
#pragma unroll
        for (int sec = 0; sec < 3; ++sec) { wl[0][sec] = lds_tr(wp_ + sec * 8192); wh[0][sec] = lds_tr(wp_ + sec * 8192 + 512); }
    }
    SBAR();
#pragma unroll K1U
    for (int kt = 0; kt < 15; ++kt) {
        const lds_ptr st = lds + (kt & 3) * K1_STAGE;
        const lds_ptr sn = lds + ((kt + 1) & 3) * K1_STAGE;
        K1_STEP(0, st, 1, false);
        K1_STEP(1, st, 2, false);
        K1_STEP(0, st, 3, false);
        K1_STEP(1, sn, 0, kt + 1);
    }
    {
        const lds_ptr st = lds + 3 * K1_STAGE;
        K1_STEP(0, st, 1, false);
        K1_STEP(1, st, 2, false);
        K1_STEP(0, st, 3, false);
#pragma unroll
        for (int m = 0; m < 2; ++m) {
            acc[m][1] = MFMA32(K1_WF(1, 1), xf[1][m], acc[m][1]);
            acc[m][2] = MFMA32(xf[1][m], K1_WF(1, 2), acc[m][2]);
            acc[m][0] = MFMA32(K1_WF(1, 0), xf[1][m], acc[m][0]);
        }
    }
    DG(4);
    asm volatile(".Lcp_k1z:");
    {
        const int h = 2 * hp + wn;
        const float qscale = 0.17677669529663687f * 1.4426950408889634f;
        const __amdgpu_buffer_rsrc_t rs = __builtin_amdgcn_make_buffer_rsrc(ws, 0, 32 << 20, 0x00020000);
#pragma unroll
        for (int m = 0; m < 2; ++m) {
            const int T = tb * 4 + wm * 2 + m;
            const unsigned dq = (unsigned)WS_Q + QT(h, T) * 2048 + lane * 16;
            const unsigned dk = (unsigned)WS_KV + (h * 64 + T) * 4096 + lane * 16;
            const unsigned dkk = SPM ? (unsigned)WS_KV + (h * 64 + T) * 4096 + ((lane & 51) | ((lane & 4) << 1) | ((lane & 8) >> 1)) * 16 : dk;
            K1_ST(pack8n(acc[m][1], 0), rs, dkk);
            K1_ST(pack8n(acc[m][1], 1), rs, dkk + 1024);
            K1_ST(pack8n(acc[m][2], 0), rs, dk + 2048);
            K1_ST(pack8n(acc[m][2], 1), rs, dk + 3072);
            K1_ST(pack8(acc[m][0], 0, qscale), rs, dq);
            K1_ST(pack8(acc[m][0], 1, qscale), rs, dq + 1024);
        }
    }
    DG(5);
    asm volatile("s_waitcnt vmcnt(0)" :: "v"(cpf));
    DG_END(5);
}

constexpr int K2_SLOT = 32768;
constexpr int K2_SF = 65536;
constexpr int K2_CV = 98304;
constexpr int CV_HA = 0, CV_HB = 4160, CV_KV = 8320, CV_BYTES = 8320 + 480 + 4096;
constexpr int K2_LX = K2_CV + 4 * CV_BYTES;
constexpr int K2_FL = K2_LX + 2048;
constexpr int K2_SCR = K2_FL + 256;
constexpr int K2_PAD = K2_SCR + 4096;
constexpr int K2_LDS = K2_PAD + 1024;
#define NB8 0
#define WW 0
constexpr int F_X = 0, F_R = 32, F_W2 = 64, C_OST = 68, C_S1 = 76, C_S2 = 80, C_S3 = 128, C_ZST = 88, F_ZGO = 92,
              C_KST = 96, C_U0 = 100, C_LD = 108, F_KGO = 116, C_END = 120, C_OL = 72, C_HF = 84;
#define NG_IN 0
#define GATE_WORD C_END
#define GATE_VAL 0
#define X_NZ 4
#define X_NW3 8
#define ZP_GATE 4u
#define ZL_GATE 4
#define ZL_AT 31
#define Z_SLEEP0 0
#define O_SLEEP 0
#define Z_SLEEP 8
#define OWIN 16
#define OW0 8
#define OWR 8
#define WD 2
#define W3_AT 10
#define X_NOLDW 0
#define X_NOLDO 0
#define X_NOCHK 0
#define KROT (nt * 2)
#define NG_LAST 0
#define PACE_IN "s_nop 7"
#define PACE_OUT "s_nop 1"
#define P3_PRIO 1
#define TOP_SLEEP 2
#define A_AUX 16
#define B_AUX 16
#define B_LDS 0
#define TL 8
#define XR_AT 62
#define W3_AUX 0
#define LORD 0
#define AORD 0
#define S2M 17
#define W3M (LORD ? 23 : 13)
#define A16 1
#define SEL4 2
#define KV_DOT 1
#define ZE_PRIO 3
#define P3_T1 99
#define P3_T2 99
#define AUX_PRIO 2

__global__ __launch_bounds__(768, 3) void k2_attn(char* __restrict__ ws, const char* __restrict__ Aq,
                                                  const char* __restrict__ Ak, const char* __restrict__ Av,
                                                  const char* __restrict__ shiftk, const char* __restrict__ ssmk,
                                                  const unsigned* __restrict__ ssmD, const char* __restrict__ Wo,
                                                  char* __restrict__ out) {
    __shared__ __attribute__((aligned(16))) char lds_raw[K2_LDS];
    const lds_ptr lds = (lds_ptr)lds_raw;
    const lds_ptr fl = lds + K2_FL;
    const unsigned lds0 = (unsigned)(uintptr_t)lds;
    const int tid = threadIdx.x, lane = tid & 63;
    const int w = __builtin_amdgcn_readfirstlane(tid >> 6);
    const int b = blockIdx.x, xcd = b & 7, idx = b >> 3;
    const int h = xcd * 4 + (idx >> 3), qb = idx & 7;
    const int tt = xcd * 2 + (idx >> 4), nt = idx & 15;
    const char* kvg = ws + WS_KV + (size_t)h * 64 * 4096;
    const __amdgpu_buffer_rsrc_t rs = __builtin_amdgcn_make_buffer_rsrc(ws, 0, 32 << 20, 0x00020000);
    const __amdgpu_buffer_rsrc_t ro = __builtin_amdgcn_make_buffer_rsrc(out, 0, 8 << 20, 0x00020000);
    gu32* const cnt = (gu32*)(ws + WS_CNT);
    auto signal = [&]() {
        if (!BK1 || lane < 32) (void)__hip_atomic_fetch_add(cnt + ((lane & 31) * 8 + (lane < 32 ? xcd : qb)) * 64, 1u, RLX_AGENT);
    };
    const unsigned wvoff = (unsigned)(lane >> 3) * 2048u + nt * 128 + (((lane & 7) ^ (((lane >> 4) & 1) << 2)) << 4);
    const int krot = KROT;
    auto wsrc = [&](int hf, int lp) { return Wo + (size_t)(hf * 64 + ((lp + krot * 2) & 63)) * 16384; };
    DG_DECL(1)
    const unsigned voff = lane * 16;
    const int cw = w & 3;
    auto issue = [&](int st) {
        const char* src = kvg + (st * 4 + cw) * 4096;
        const unsigned dst = lds0 + (st & 1) * K2_SLOT + cw * 4096;
#pragma unroll
        for (int i = 0; i < 4; ++i) glds16(voff, src + i * 1024, dst + i * 1024);
#pragma unroll
        for (int i = 0; i < 4; ++i) glds16(voff, src + 131072 + i * 1024, dst + 16384 + i * 1024);
    };
    asm volatile(".Lcp_k2a:");
    if (w >= 8) {
        __builtin_amdgcn_s_sleep(TOP_SLEEP);
#pragma unroll
        for (int i = 2; i < 8; ++i)
            glds16(voff, kvg + (i & 1) * 131072 + (i >> 1) * 4096 + cw * 1024, lds0 + (i & 1) * 16384 + (i >> 1) * 4096 + cw * 1024);
        RAW_BAR();
        issue(1);
        WAIT_BAR(8);
    } else {
        const int tq = cw * 2 + (w >> 2);
        const char* qg = ws + WS_Q + (size_t)QT(h, qb * 8 + tq) * 2048;
        glds16(voff, kvg + (w >> 2) * 131072 + cw * 1024, lds0 + (w >> 2) * 16384 + cw * 1024);
        glds16(voff, qg, lds0 + K2_SF + tq * 2048);
        glds16(voff, qg + 1024, lds0 + K2_SF + tq * 2048 + 1024);
    }
    int w2 = w;
    asm volatile("" : "+s"(w2));

    if (w2 >= 8) {
        __builtin_amdgcn_s_setprio(3);
        const int ch = xcd * 128 + idx * 4 + cw;
        const lds_ptr cv = lds + K2_CV + cw * CV_BYTES;
        {
            const u32x4 z4 = {0u, 0u, 0u, 0u};
            if (lane < 2) *(LAS u32x4*)(cv + CV_HA + lane * 16) = z4;
            if (lane < 30) *(LAS u32x4*)(cv + CV_KV + lane * 16) = z4;
            if (cw == 0 && lane < 16) *(LAS u32x4*)(fl + lane * 16) = z4;
        }
        const int c0 = xcd * 128 + cw * 32, cq = lane & 7, rg = lane >> 3;
        const int row0 = idx * 64 + rg * 8;
        const unsigned roff = (unsigned)row0 * 2048u + (c0 + cq * 4) * 2;
        const unsigned toffs = (unsigned)(c0 + (lane >> 3)) * 4096u + idx * 128;
        u32x4 sk[2];
        sk[0] = gld128((unsigned)(c0 + cq * 4) * 8u, shiftk); sk[1] = gld128((unsigned)(c0 + cq * 4) * 8u, shiftk + 16);
        unsigned dd = gld32((unsigned)(ch >> 1) * 4u, ssmD);
        auto stage = [&](const char* src, int dst) {
            const unsigned sbase = (unsigned)(idx * 64 + cw * 16 + (lane >> 4)) * 2048u + xcd * 256;
#pragma unroll
            for (int i = 0; i < 4; ++i)
                glds16p<K2_SNT>(sbase + (((lane & 15) ^ (((2 * cw + (i >> 1)) & 3) << 2)) << 4), src + i * 8192, lds0 + dst + cw * 4096 + i * 1024);
        };
        auto blank = [&](unsigned off) {
            const u32x4 e4 = {EMPTY, EMPTY, EMPTY, EMPTY};
            CFENCE();
            if (!BK1) { st_sc1(e4, rs, off + voff); st_sc1(e4, rs, off + 1024 + voff); }
            CFENCE();
        };
        WAIT_BAR(3);
        DG(12);
        issue(2);
        stage(Ak, K2_SF);
        glds16((unsigned)(idx * 64 - (idx ? 4 : 0) + (lane >> 4)) * 2048u + xcd * 256 + (((lane & 15) ^ 12) << 4), Ak, lds0 + K2_SCR + cw * 1024);
        blank((unsigned)WS_O + (unsigned)((qb * 8 + cw * 2) * 32 + h) * 2048u);
        auto warm = [&](int g) {
#pragma unroll
            for (int i = 0; i < 4; ++i) glds16(wvoff, wsrc(idx >> 4, 16 + g * 16 + cw * 4 + i), lds0 + K2_PAD);
        };
        WAITN_BAR(BK1 ? 5 : 7);
        DG(9);
        issue(3);
        stage(Av, K2_SF + 16384);
        blank((unsigned)WS_O + (unsigned)((qb * 8 + cw * 2 + 1) * 32 + h) * 2048u);
        WAITN_BAR(BK1 ? 4 : 6);
        DG(3);
        issue(4);
#pragma unroll
        for (int i = 0; i < 4; ++i) glds16p<K2_SNT>(voff, ssmk + (size_t)ch * 4096 + i * 1024, lds0 + K2_CV + cw * CV_BYTES + CV_HA + 32 + i * 1024);
        blank((unsigned)WS_ZT + ch * 4096); blank((unsigned)WS_ZT + ch * 4096 + 2048);
        WAITN_BAR(BK1 ? 4 : 8);
        DG(5);
        issue(5);
        SBAR();
        asm volatile("" : "+v"(sk[0]), "+v"(sk[1]), "+v"(dd));
        dd = __builtin_amdgcn_readfirstlane(dd);
        {
            u32x2 kr[11], vr[8];
            lds_ptr kb = lds + K2_SF + rg * 2048 + ((cw ^ (rg & 3)) << 6) + cq * 8;
            asm volatile("" : "+v"(kb));
            const lds_ptr hb = lds + (rg ? K2_SF + rg * 2048 - 768 : K2_SCR + cw * 1024 + 256) + ((cw ^ ((rg + 3) & 3)) << 6) + cq * 8;
#pragma unroll
            for (int i = 0; i < 3; ++i) kr[i] = *(const LAS u32x2*)(hb + i * 256);
#pragma unroll
            for (int i = 0; i < 8; ++i) kr[3 + i] = *(const LAS u32x2*)(kb + i * 256);
#pragma unroll
            for (int i = 0; i < 8; ++i) vr[i] = *(const LAS u32x2*)(kb + 16384 + i * 256);
            if (row0 == 0) { kr[0] = u32x2{0u, 0u}; kr[1] = u32x2{0u, 0u}; kr[2] = u32x2{0u, 0u}; }
            const lds_ptr tb = cv + CV_KV + 480;
#pragma unroll
            for (int c = 0; c < 4; ++c) {
                const unsigned s01 = sk[c >> 1][(c & 1) * 2], s23 = sk[c >> 1][(c & 1) * 2 + 1];
                unsigned kp[10];
                float kv[8], vf[8];
#pragma unroll
                for (int j = 0; j < 10; ++j) kp[j] = __builtin_amdgcn_perm(kr[j][c >> 1], kr[j + 1][c >> 1], (c & 1) ? 0x07060302u : 0x05040100u);
                SBAR();
#pragma unroll
                for (int i = 0; i < 8; ++i) asm("v_dot2_f32_bf16 %0, %1, %2, 0" : "=v"(kv[i]) : "v"(kp[i]), "v"(s23));
                SBAR();
#pragma unroll
                for (int i = 0; i < 8; ++i) asm("v_dot2_f32_bf16 %0, %1, %2, %0" : "+v"(kv[i]) : "v"(kp[i + 2]), "v"(s01));
                SBAR();
#pragma unroll
                for (int i = 0; i < 8; ++i) vf[i] = (c & 1) ? bfhi(vr[i][c >> 1]) : bflo(vr[i][c >> 1]);
                SBAR();
#pragma unroll
                for (int i = 0; i < 8; ++i) kv[i] = mul_vv(kv[i], vf[i]);
                u32x4 o4;
#pragma unroll
                for (int i = 0; i < 4; ++i) o4[i] = cvtpk(kv[7 - 2 * i], kv[6 - 2 * i]);
                *(LAS u32x4*)(tb + (cq * 4 + c) * 128 + ((rg ^ cq) << 4)) = o4;
            }
#pragma unroll
            for (int i = 0; i < 4; ++i) {
                const u32x4 t = *(const LAS u32x4*)(tb + i * 1024 + lane * 16);
                st_sc1(t, rs, (unsigned)WS_Q + (xcd * 32 + idx) * 16384 + cw * 4096 + i * 1024 + (lane >> 3) * 128 + (((lane & 7) ^ (2 * i + (lane >> 5))) << 4));
            }
        }
        CFENCE();
        DG(7);
        WAIT_BAR(4);
        DG(6);
        issue(6);
        asm volatile("s_waitcnt vmcnt(8)" ::: "memory");
        if (lds_add_rtn(fl + C_KST, lane) == 3u) signal();
#pragma unroll
        for (int i = 0; i < 8; ++i)
            glds16p<K2_WNT>(wvoff, wsrc(i >> 2, cw * 4 + (i & 3)), lds0 + K2_SF + (i >> 2) * 16384 + (cw * 4 + (i & 3)) * 1024);
        {
            const u32x4 z4 = {0u, 0u, 0u, 0u};
            if (lane < 2) *(LAS u32x4*)(cv + CV_HB + lane * 16) = z4;
#pragma unroll
            for (int i = 0; i < 4; ++i) {
                const int off = 32 + i * 1024 + lane * 16;
                const u32x4 hv = *(const LAS u32x4*)(cv + CV_HA + off);
                const unsigned pv = *(const LAS unsigned*)(cv + CV_HA + off - 4);
                u32x4 sh;
                sh[0] = __builtin_amdgcn_alignbit(hv[0], pv, 16);
                sh[1] = __builtin_amdgcn_alignbit(hv[1], hv[0], 16);
                sh[2] = __builtin_amdgcn_alignbit(hv[2], hv[1], 16);
                sh[3] = __builtin_amdgcn_alignbit(hv[3], hv[2], 16);
                *(LAS u32x4*)(cv + CV_HB + off) = sh;
            }
        }
        WAIT_BAR(8);
        DG(8);
        issue(7);
        auto warm23 = [&]() {
#pragma unroll
            for (int i = 0; i < 8; ++i)
                glds16p<K2_WNT>(wvoff, wsrc(idx >> 4, 32 + cw * 8 + i), lds0 + K2_SCR + cw * 1024);
        };
        auto chunk1 = [&]() {
#pragma unroll
            for (int i = 0; i < 8; ++i)
                glds16p<K2_WNT>(wvoff, wsrc(i >> 2, 16 + cw * 4 + (i & 3)), lds0 + (i >> 2) * 16384 + (cw * 4 + (i & 3)) * 1024);
        };
        warm23();
        WAIT_BAR(8);
        DG(11);
        if (cw == 0) { poll_ge(cnt + b * 64, BK1 ? 32u : 64u); lds_add(fl + F_KGO, lane); }
        else lds_wait_ge(fl + F_KGO, 1u);
        {
            u32x4 t[4];
#pragma unroll
            for (int i = 0; i < 4; ++i) t[i] = ld_sc1s(rs, (unsigned)WS_Q + (xcd * 32 + (lane >> 3)) * 16384 + (idx * 4 + cw) * 128 + (lane & 7) * 16, i * 131072);
#pragma unroll
            for (int i = 0; i < 4; ++i) *(LAS u32x4*)(cv + CV_KV + 480 + i * 1024 + lane * 16) = t[i];
        }
        const int r1 = lane & 15, q = lane >> 4;
        const int toff = (r1 & 1) ? CV_HA + 2 * (16 + r1 + 9 - 8 * q) : CV_HB + 2 * (16 + r1 + 10 - 8 * q);
        const int boff = CV_KV + 448 + 32 * r1 + 16 * q;
        f32x4 y[8];
        u32x4 tf[8];
        bf16x8 bf[8];
        const lds_ptr bp = cv + boff - 448;
        lds_ptr tp[4];
#pragma unroll
        for (int i = 0; i < 4; ++i) { tp[i] = cv + toff + 1024 * i; asm volatile("" : "+v"(tp[i])); }
#define LDT(bb, a, hf) do { const LAS unsigned* t_ = (const LAS unsigned*)(tp[(a) >> 1] + 64 * (bb) + 512 * ((a) & 1) + 8 * (hf)); \
            tf[a][2 * (hf)] = t_[0]; tf[a][2 * (hf) + 1] = t_[1]; SBAR(); } while (0)
#define LDB(bb, a) do { bf[a] = lds_rd128(bp + 448 - 64 * (bb) + 512 * (a)); SBAR(); } while (0)
#define CM(a, ww) do { asm volatile("v_mfma_f32_16x16x32_bf16 %0, %1, %2, %0" : "+v"(y[(a) + (ww)]) : "v"(tf[a]), "v"(bf[ww])); SBAR(); } while (0)
#define CM0(a, ww) do { asm volatile("v_mfma_f32_16x16x32_bf16 %0, %1, %2, 0" : "=&v"(y[(a) + (ww)]) : "v"(tf[a]), "v"(bf[ww])); SBAR(); } while (0)
#define PC() do { asm volatile(PACE_OUT); SBAR(); } while (0)
#pragma unroll
        for (int a = 0; a < 8; ++a) { if (a < 5) { LDT(0, a, 0); LDT(0, a, 1); } LDB(0, 7 - a); }
        chunk1();
#pragma unroll
        for (int i = 0; i < 4; ++i)
            glds16p<K2_QNT>((unsigned)(idx * 64 + cw * 16 + (lane >> 4)) * 2048u + xcd * 256 + (lane & 15) * 16, Aq + i * 8192, lds0 + K2_SCR + cw * 1024);
        RAW_BAR();
        asm volatile("s_waitcnt vmcnt(%0)" :: "n"(AORD == 2 ? 12 : 4) : "memory");
        lds_add(fl + C_S1, lane);
#define ZFL() PC()
        __builtin_amdgcn_s_setprio(AUX_PRIO);
        const float dch = (ch & 1) ? bfhi(dd) : bflo(dd);
        const lds_ptr kdp = cv + CV_KV + 480 + 32 * r1 + 16 * (q >> 1) + ((q & 1) ? 0 : 8);
        float kd[8][4];
        u32x2 kk;
        unsigned kc0, kc1, zo[8][2];
#define KD_RD(t) do { kk = *(const LAS u32x2*)(kdp + 512 * (t)); SBAR(); } while (0)
#define KD_UP0(t) do { kd[t][0] = bfhi(kk[1]); kd[t][1] = bflo(kk[1]); SBAR(); } while (0)
#define KD_UP1(t) do { kd[t][2] = bfhi(kk[0]); kd[t][3] = bflo(kk[0]); SBAR(); } while (0)
#define KD_MU0(t) do { kd[t][0] = mul_sv(dch, kd[t][0]); kd[t][1] = mul_sv(dch, kd[t][1]); SBAR(); } while (0)
#define KD_MU1(t) do { kd[t][2] = mul_sv(dch, kd[t][2]); kd[t][3] = mul_sv(dch, kd[t][3]); SBAR(); } while (0)
#define KD_CV(t) do { kc0 = cvtpk(kd[t][0], kd[t][1]); kc1 = cvtpk(kd[t][2], kd[t][3]); SBAR(); } while (0)
#define KD_X0(t) do { kd[t][0] = bflo(kc0); kd[t][1] = bfhi(kc0); SBAR(); } while (0)
#define KD_X1(t) do { kd[t][2] = bflo(kc1); kd[t][3] = bfhi(kc1); SBAR(); } while (0)
#define ZF0(t) do { kc0 = cvtpk(y[t][0], y[t][1]); kc1 = cvtpk(y[t][2], y[t][3]); SBAR(); } while (0)
#define ZF1(t) do { y[t][0] = bflo(kc0); y[t][1] = bfhi(kc0); SBAR(); } while (0)
#define ZF2(t) do { y[t][2] = bflo(kc1); y[t][3] = bfhi(kc1); SBAR(); } while (0)
#define ZF3(t) do { y[t][0] = add_vv(y[t][0], kd[t][0]); y[t][1] = add_vv(y[t][1], kd[t][1]); SBAR(); } while (0)
#define ZF4(t) do { y[t][2] = add_vv(y[t][2], kd[t][2]); y[t][3] = add_vv(y[t][3], kd[t][3]); SBAR(); } while (0)
#define ZF5(t) do { zo[t][0] = nz(cvtpk(y[t][0], y[t][1])); zo[t][1] = nz(cvtpk(y[t][2], y[t][3])); SBAR(); } while (0)
#define ZST(pr) do { auto s0 = __builtin_amdgcn_permlane16_swap(zo[2 * (pr)][0], zo[2 * (pr) + 1][0], false, false); \
            auto s1 = __builtin_amdgcn_permlane16_swap(zo[2 * (pr)][1], zo[2 * (pr) + 1][1], false, false); \
            st_sc1(u32x4{s0[0], s1[0], s0[1], s1[1]}, rs, (unsigned)WS_ZT + ch * 4096 + (pr) * 1024 + voff); SBAR(); } while (0)
#define W3(i) do { if ((i) == 0) PC(); } while (0)
#define HF() do { lds_add(fl + C_HF, lane); SBAR(); } while (0)
        CM0(0, 7); LDB(1, 7); CM0(0, 6); LDT(0, 5, 0); CM0(0, 5); LDT(0, 5, 1); CM0(0, 4); LDT(0, 6, 0); CM0(0, 3); LDT(0, 6, 1); CM0(0, 2); LDT(0, 7, 0); CM0(0, 1); LDT(0, 7, 1); CM0(0, 0); KD_RD(0);
        CM(1, 6); LDB(1, 6); CM(1, 5); LDT(1, 0, 0); CM(1, 4); LDT(1, 0, 1); CM(1, 3); PC(); CM(1, 2); PC(); CM(1, 1); KD_UP0(0); CM(1, 0); KD_UP1(0);
        CM(2, 5); LDB(1, 5); CM(2, 4); LDT(1, 1, 0); CM(2, 3); LDT(1, 1, 1); CM(2, 2); KD_MU0(0); CM(2, 1); KD_MU1(0); CM(2, 0); KD_CV(0);
        CM(3, 4); LDB(1, 4); CM(3, 3); LDT(1, 2, 0); CM(3, 2); LDT(1, 2, 1); CM(3, 1); KD_X0(0); CM(3, 0); KD_X1(0);
        CM(4, 3); LDB(1, 3); CM(4, 2); LDT(1, 3, 0); CM(4, 1); LDT(1, 3, 1); CM(4, 0); KD_RD(1);
        CM(5, 2); LDB(1, 2); CM(5, 1); LDT(1, 4, 0); CM(5, 0); LDT(1, 4, 1);
        CM(6, 1); LDB(1, 1); CM(6, 0); PC();
        CM(7, 0); LDB(1, 0);
        DGV(6);
        CM(0, 7); LDB(2, 7); CM(0, 6); LDT(1, 5, 0); CM(0, 5); LDT(1, 5, 1); CM(0, 4); LDT(1, 6, 0); CM(0, 3); LDT(1, 6, 1); CM(0, 2); LDT(1, 7, 0); CM(0, 1); LDT(1, 7, 1); CM(0, 0); PC();
        CM(1, 6); LDB(2, 6); CM(1, 5); LDT(2, 0, 0); CM(1, 4); LDT(2, 0, 1); CM(1, 3); KD_UP0(1); CM(1, 2); KD_UP1(1); CM(1, 1); KD_MU0(1); CM(1, 0); KD_MU1(1);
        CM(2, 5); LDB(2, 5); CM(2, 4); LDT(2, 1, 0); CM(2, 3); LDT(2, 1, 1); CM(2, 2); KD_CV(1); CM(2, 1); KD_X0(1); CM(2, 0); KD_X1(1);
        CM(3, 4); LDB(2, 4); CM(3, 3); LDT(2, 2, 0); CM(3, 2); LDT(2, 2, 1); CM(3, 1); KD_RD(2); CM(3, 0); PC();
        CM(4, 3); LDB(2, 3); CM(4, 2); LDT(2, 3, 0); CM(4, 1); LDT(2, 3, 1); CM(4, 0); PC();
        CM(5, 2); LDB(2, 2); CM(5, 1); LDT(2, 4, 0); CM(5, 0); LDT(2, 4, 1);
        CM(6, 1); LDB(2, 1); CM(6, 0); KD_UP0(2);
        CM(7, 0); LDB(2, 0);
        DGV(8);
        CM(0, 7); LDB(3, 7); CM(0, 6); LDT(2, 5, 0); CM(0, 5); LDT(2, 5, 1); CM(0, 4); LDT(2, 6, 0); CM(0, 3); LDT(2, 6, 1); CM(0, 2); LDT(2, 7, 0); CM(0, 1); LDT(2, 7, 1); CM(0, 0); KD_UP1(2);
        CM(1, 6); LDB(3, 6); CM(1, 5); LDT(3, 0, 0); CM(1, 4); LDT(3, 0, 1); CM(1, 3); KD_MU0(2); CM(1, 2); KD_MU1(2); CM(1, 1); KD_CV(2); CM(1, 0); KD_X0(2);
        CM(2, 5); LDB(3, 5); CM(2, 4); LDT(3, 1, 0); CM(2, 3); LDT(3, 1, 1); CM(2, 2); KD_X1(2); CM(2, 1); KD_RD(3); CM(2, 0); PC();
        CM(3, 4); LDB(3, 4); CM(3, 3); LDT(3, 2, 0); CM(3, 2); LDT(3, 2, 1); CM(3, 1); PC(); CM(3, 0); KD_UP0(3);
        CM(4, 3); LDB(3, 3); CM(4, 2); LDT(3, 3, 0); CM(4, 1); LDT(3, 3, 1); CM(4, 0); KD_UP1(3);
        CM(5, 2); LDB(3, 2); CM(5, 1); LDT(3, 4, 0); CM(5, 0); LDT(3, 4, 1);
        CM(6, 1); LDB(3, 1); CM(6, 0); KD_MU0(3);
        CM(7, 0); LDB(3, 0);
        DGV(12);
        CM(0, 7); LDB(4, 7); CM(0, 6); LDT(3, 5, 0); CM(0, 5); LDT(3, 5, 1); CM(0, 4); LDT(3, 6, 0); CM(0, 3); LDT(3, 6, 1); CM(0, 2); LDT(3, 7, 0); CM(0, 1); LDT(3, 7, 1); CM(0, 0); KD_MU1(3);
        CM(1, 6); LDB(4, 6); CM(1, 5); LDT(4, 0, 0); CM(1, 4); LDT(4, 0, 1); CM(1, 3); KD_CV(3); CM(1, 2); KD_X0(3); CM(1, 1); KD_X1(3); CM(1, 0); KD_RD(4);
        CM(2, 5); LDB(4, 5); CM(2, 4); LDT(4, 1, 0); CM(2, 3); LDT(4, 1, 1); CM(2, 2); PC(); CM(2, 1); PC(); CM(2, 0); KD_UP0(4);
        CM(3, 4); LDB(4, 4); CM(3, 3); LDT(4, 2, 0); CM(3, 2); LDT(4, 2, 1); CM(3, 1); KD_UP1(4); CM(3, 0); KD_MU0(4);
        CM(4, 3); LDB(4, 3); CM(4, 2); LDT(4, 3, 0); CM(4, 1); LDT(4, 3, 1); CM(4, 0); KD_MU1(4);
        CM(5, 2); LDB(4, 2); CM(5, 1); LDT(4, 4, 0); CM(5, 0); LDT(4, 4, 1);
        CM(6, 1); LDB(4, 1); CM(6, 0); KD_CV(4);
        CM(7, 0); LDB(4, 0);
        DGV(9);
        CM(0, 7); LDB(5, 7); CM(0, 6); LDT(4, 5, 0); CM(0, 5); LDT(4, 5, 1); CM(0, 4); LDT(4, 6, 0); CM(0, 3); LDT(4, 6, 1); CM(0, 2); LDT(4, 7, 0); CM(0, 1); LDT(4, 7, 1); CM(0, 0); KD_X0(4);
        CM(1, 6); LDB(5, 6); CM(1, 5); LDT(5, 0, 0); CM(1, 4); LDT(5, 0, 1); CM(1, 3); KD_X1(4); CM(1, 2); KD_RD(5); CM(1, 1); PC(); CM(1, 0); PC();
        CM(2, 5); LDB(5, 5); CM(2, 4); LDT(5, 1, 0); CM(2, 3); LDT(5, 1, 1); CM(2, 2); KD_UP0(5); CM(2, 1); KD_UP1(5); CM(2, 0); KD_MU0(5);
        CM(3, 4); LDB(5, 4); CM(3, 3); LDT(5, 2, 0); CM(3, 2); LDT(5, 2, 1); CM(3, 1); KD_MU1(5); CM(3, 0); KD_CV(5);
        CM(4, 3); LDB(5, 3); CM(4, 2); LDT(5, 3, 0); CM(4, 1); LDT(5, 3, 1); CM(4, 0); KD_X0(5);
        CM(5, 2); LDB(5, 2); CM(5, 1); LDT(5, 4, 0); CM(5, 0); LDT(5, 4, 1);
        CM(6, 1); LDB(5, 1); CM(6, 0); KD_X1(5);
        CM(7, 0); LDB(5, 0);
        DGV(11);
        CM(0, 7); LDB(6, 7); CM(0, 6); LDT(5, 5, 0); CM(0, 5); LDT(5, 5, 1); CM(0, 4); LDT(5, 6, 0); CM(0, 3); LDT(5, 6, 1); CM(0, 2); LDT(5, 7, 0); CM(0, 1); LDT(5, 7, 1); CM(0, 0); KD_RD(6);
        CM(1, 6); LDB(6, 6); CM(1, 5); LDT(6, 0, 0); CM(1, 4); LDT(6, 0, 1); CM(1, 3); PC(); CM(1, 2); PC(); CM(1, 1); KD_UP0(6); CM(1, 0); KD_UP1(6);
        CM(2, 5); LDB(6, 5); CM(2, 4); LDT(6, 1, 0); CM(2, 3); LDT(6, 1, 1); CM(2, 2); KD_MU0(6); CM(2, 1); KD_MU1(6); CM(2, 0); KD_CV(6);
        CM(3, 4); LDB(6, 4); CM(3, 3); LDT(6, 2, 0); CM(3, 2); LDT(6, 2, 1); CM(3, 1); KD_X0(6); CM(3, 0); KD_X1(6);
        CM(4, 3); LDB(6, 3); CM(4, 2); LDT(6, 3, 0); CM(4, 1); LDT(6, 3, 1); CM(4, 0); KD_RD(7);
        CM(5, 2); LDB(6, 2); CM(5, 1); LDT(6, 4, 0); CM(5, 0); LDT(6, 4, 1);
        CM(6, 1); LDB(6, 1); CM(6, 0); PC();
        CM(7, 0); LDB(6, 0);
        DGV(7);
        CM(0, 7); LDB(7, 7); CM(0, 6); LDT(6, 5, 0); CM(0, 5); LDT(6, 5, 1); CM(0, 4); LDT(6, 6, 0); CM(0, 3); LDT(6, 6, 1); CM(0, 2); LDT(6, 7, 0); CM(0, 1); LDT(6, 7, 1); CM(0, 0); PC();
        CM(1, 6); LDB(7, 6); CM(1, 5); LDT(7, 0, 0); CM(1, 4); LDT(7, 0, 1); CM(1, 3); KD_UP0(7); CM(1, 2); KD_UP1(7); CM(1, 1); KD_MU0(7); CM(1, 0); KD_MU1(7);
        CM(2, 5); LDB(7, 5); CM(2, 4); LDT(7, 1, 0); CM(2, 3); LDT(7, 1, 1); CM(2, 2); KD_CV(7); CM(2, 1); KD_X0(7); CM(2, 0); KD_X1(7);
        CM(3, 4); LDB(7, 4); CM(3, 3); LDT(7, 2, 0); CM(3, 2); LDT(7, 2, 1); CM(3, 1); PC(); CM(3, 0); PC();
        CM(4, 3); LDB(7, 3); CM(4, 2); LDT(7, 3, 0); CM(4, 1); LDT(7, 3, 1); CM(4, 0); PC();
        CM(5, 2); LDB(7, 2); CM(5, 1); LDT(7, 4, 0); CM(5, 0); LDT(7, 4, 1);
        CM(6, 1); LDB(7, 1); CM(6, 0); PC();
        CM(7, 0); LDB(7, 0);
        DGV(3);
        CM(0, 3); LDT(7, 5, 0); CM(0, 2); LDT(7, 5, 1); CM(1, 2); LDT(7, 6, 0); CM(1, 1); LDT(7, 6, 1); CM(2, 1); LDT(7, 7, 0); CM(2, 0); LDT(7, 7, 1);
        CM(3, 0); HF(); CM(0, 1); ZFL(); CM(0, 0); W3(0); CM(1, 0); ZF0(2); W3(1); CM(0, 5); ZF1(2); W3(2); CM(0, 4); ZF2(2); W3(3);
        CM(1, 4); ZF3(2); W3(4); CM(1, 3); ZF4(2); W3(5); CM(2, 3); ZF5(2); W3(6); CM(2, 2); ZF0(3); W3(7); CM(3, 2); ZF1(3);  CM(3, 1); ZF2(3);
        CM(4, 1); ZF3(3);  CM(4, 0); ZF4(3);  CM(5, 0); ZF5(3);  CM(0, 7); ZST(1);  CM(0, 6); ZF0(0);  CM(1, 6); ZF1(0);
        CM(1, 5); ZF2(0);  CM(2, 5); ZF3(0);  CM(2, 4); ZF4(0);  CM(3, 4); ZF5(0);  CM(3, 3); ZF0(1);  CM(4, 3); ZF1(1);
        CM(4, 2); ZF2(1);  CM(5, 2); ZF3(1);  CM(5, 1); ZF4(1);  CM(6, 1); ZF5(1);  CM(6, 0); ZST(0);  CM(7, 0); ZF0(4);
        DG(4);
        ZF1(4); ZF2(4); ZF3(4); ZF4(4); ZF5(4); ZF0(5); ZF1(5); ZF2(5); ZF3(5); ZF4(5); ZF5(5); ZST(2); ZF0(6); ZF1(6); ZF2(6); ZF3(6); ZF4(6); ZF5(6); ZF0(7); ZF1(7); ZF2(7); ZF3(7); ZF4(7); ZF5(7); ZST(3);
        DG(10);
        __builtin_amdgcn_s_setprio(3);
        while ((unsigned)__builtin_amdgcn_readfirstlane(*(volatile LAS unsigned*)(fl + C_OL)) < ZL_GATE) __builtin_amdgcn_s_sleep(4);
        CFENCE();
        const int arow = idx * 64 + (cw >> 1) * 32 + rg * 4, acol = xcd * 256 + (cw & 1) * 128 + cq * 16;
        u32x4 qr[4];
#pragma unroll
        for (int i = 0; i < 4; ++i) qr[i] = gld128((unsigned)(arow * 2048 + acol), Aq + i * 2048);
        {
            const lds_ptr tr = cv + CV_KV + 480;
            u32x4 t[4];
            const int ck = (cw >> 1) * 4 + ((lane ^ ((lane >> 5) * 2)) & 3);
            const unsigned zb = (unsigned)WS_ZT + (unsigned)(xcd * 128 + (cw & 1) * 64 + (lane >> 4) * 4) * 4096u + (idx >> 3) * 1024 +
                                ((idx & 3) * 4 + ((idx >> 2) & 1) * 16 + (ck >> 1) + (ck & 1) * 32) * 16;
#pragma unroll 1
            for (unsigned spins = 0; spins < (1u << 20); ++spins) {
#pragma unroll
                for (int i = 0; i < 4; ++i) t[i] = ld_sc1s(rs, zb + ((((lane >> 2) & 3) ^ i) << 12), i * 65536);
                if (!missing(mx4(mx4(mx8(t[0], t[1]), t[2]), t[3]))) break;
                __builtin_amdgcn_s_sleep(Z_SLEEP);
            }
            DG(13);
            __builtin_amdgcn_s_setprio(ZE_PRIO);
#pragma unroll
            for (int i = 0; i < 4; ++i) *(LAS u32x4*)(tr + i * 1024 + lane * 16) = t[i];
#pragma unroll
            for (int i = 0; i < 4; ++i) asm volatile("" : "+v"(qr[i]));
            u32x2 zc[8];
#pragma unroll
            for (int c = 0; c < 8; ++c)
                zc[c] = *(const LAS u32x2*)(tr + cq * 512 + (c >> 2) * 256 + ((((c & 3) * 4 + (rg >> 1)) ^ (cq * 2)) << 4) + (rg & 1) * 8);
#pragma unroll
            for (int i = 0; i < 4; ++i) {
                u32x4 o;
#pragma unroll
                for (int c = 0; c < 4; ++c) {
                    const unsigned z0 = zc[2 * c][i >> 1], z1 = zc[2 * c + 1][i >> 1];
                    o[c] = cvtpk(mul_vv(bflo(qr[i][c]), (i & 1) ? bfhi(z0) : bflo(z0)), mul_vv(bfhi(qr[i][c]), (i & 1) ? bfhi(z1) : bflo(z1)));
                }
                __builtin_amdgcn_raw_buffer_store_b128(o, ro, arow * 4096 + acol, i * 4096, OUT_AUX);
            }
        }
        asm volatile(".Lcp_k2z:");
        DG_END(14);
        return;
    }

    const int half = w >> 2;
    const int TA = qb * 8 + (w & 3) * 2 + half, TB = TA ^ 1;
    bf16x8 sel;
    {
        const short one = (((lane >> 2) & 1) == ((lane >> 4) & 1) && (lane & 11) == 0) ? (short)0x3f80 : (short)0;
#pragma unroll
        for (int i = 0; i < 8; ++i) sel[i] = SPM ? ((lane & 15) == (i & 4) ? (short)0x3f80 : (short)0) : one;
    }
    int sidx = (int)0xdddddddd;
#define LO8(x) __builtin_bit_cast(bfx16_t, __builtin_shufflevector(x, x, 0, 1, 2, 3, 4, 5, 6, 7))
#define HI8(x) __builtin_bit_cast(bfx16_t, __builtin_shufflevector(x, x, 8, 9, 10, 11, 12, 13, 14, 15))
#define SPV(v, p, o) __builtin_amdgcn_smfmac_f32_32x32x32_bf16(__builtin_bit_cast(bfx8_t, v), p, o, sidx, 0, 0)
#define SSUM(p, l) __builtin_amdgcn_smfmac_f32_16x16x64_bf16(__builtin_bit_cast(bfx8_t, sel), p, l, sidx, 0, 0)
    f32x16 oA = f32x16{}, oB = f32x16{};
    f32x4 lA = f32x4{}, lB = f32x4{};
    f32x16 negm, sA, sB;
    lds_ptr lp = lds + half * 16384 + lane * 16;
    bf16x8 k0[2], k1[2], v0[2], v1[2];
    u32x4 p0, p1 = {0u, 0u, 0u, 0u};
#define EX(X, i) X[i] = __builtin_amdgcn_exp2f(X[i])
    lds_ptr qpa = lds + K2_SF + (TA & 7) * 2048 + lane * 16, qpb = lds + K2_SF + (TB & 7) * 2048 + lane * 16;
    asm volatile("" : "+v"(qpa), "+v"(qpb), "+v"(lp), "+v"(sel), "+v"(oA), "+v"(oB), "+v"(lA), "+v"(lB), "+v"(sidx));
    WAIT_BAR(0);
    DG(3);
    const bf16x8 qa0 = lds_rd128(qpa), qb0 = lds_rd128(qpb);
    const bf16x8 x0 = lds_rd128(lds + lane * 16), x1 = lds_rd128(lds + lane * 16 + 1024);
    const bf16x8 qa1 = lds_rd128(qpa + 1024), qb1 = lds_rd128(qpb + 1024);
    {
        k0[0] = lds_rd128(lp); k1[0] = lds_rd128(lp + 1024);
        v0[0] = lds_rd128(lp + 2048); v1[0] = lds_rd128(lp + 3072);
        sA = MFMA32(x0, qa0, f32x16{});
        sB = MFMA32(x0, qb0, f32x16{});
        sA = MFMA32(x1, qa1, sA);
        sB = MFMA32(x1, qb1, sB);
        float m = fmx(sA[0], sA[1]), m2 = fmx(sB[0], sB[1]);
#pragma unroll
        for (int i = 2; i < 16; i += 2) m = fmx(fmx(m, sA[i]), sA[i + 1]);
#pragma unroll
        for (int i = 2; i < 16; i += 2) m2 = fmx(fmx(m2, sB[i]), sB[i + 1]);
        m = fmx(m, m2);
        auto rr = __builtin_amdgcn_permlane32_swap(__float_as_uint(m), __float_as_uint(m), false, false);
        m = fmx(__uint_as_float(rr[0]), __uint_as_float(rr[1]));
#pragma unroll
        for (int i = 0; i < 16; ++i) negm[i] = -m;
        sA = MFMA32(k0[0], qa0, negm);
        sA = MFMA32(k1[0], qa1, sA);
        v1[1] = v1[0];
#pragma unroll
        for (int i = 8; i < 16; ++i) sB[i] = 0.f;
        asm volatile("" : "+v"(sB));
        SBAR();
        RAW_BAR();
        SBAR();
        EX(sA, 0); EX(sA, 1);
    }
    DG(10);
#define SUM16(l, p) asm volatile("v_mfma_f32_16x16x32_bf16 %0, %1, %2, %0" : "+v"(l) : "v"(sel), "v"(p))
#define HALF(C, oC, lC, N, qx, qy, ka, kb, oP, lP, vprev, vcur0, PRE) do { \
        oP = SPV(vprev, HI8(N), oP); SBAR(); \
        PRE; EX(C, 2); EX(C, 3); EX(C, 4); SBAR(); \
        lP = SSUM(HI8(N), lP); SBAR(); \
        EX(C, 5); SBAR(); \
        N = MFMA32(ka, qx, negm); SBAR(); \
        EX(C, 6); EX(C, 7); EX(C, 8); SBAR(); \
        N = MFMA32(kb, qy, N); SBAR(); \
        EX(C, 9); EX(C, 10); EX(C, 11); SBAR(); \
        oC = SPV(vcur0, LO8(C), oC); SBAR(); \
        EX(C, 12); EX(C, 13); EX(C, 14); SBAR(); \
        lC = SSUM(LO8(C), lC); SBAR(); \
        EX(C, 15); EX(N, 0); EX(N, 1); SBAR(); \
    } while (0)
#define RD4(Y, nx) do { v1[Y] = lds_rd128((nx) + 3072); v0[Y] = lds_rd128((nx) + 2048); \
        k1[Y] = lds_rd128((nx) + 1024); k0[Y] = lds_rd128(nx); } while (0)
#define KT(X, nx, PRE0) do { \
        HALF(sA, oA, lA, sB, qb0, qb1, k0[X], k1[X], oB, lB, v1[X ^ 1], v0[X], PRE0; RD4(X ^ 1, nx)); \
        HALF(sB, oB, lB, sA, qa0, qa1, k0[X ^ 1], k1[X ^ 1], oA, lA, v1[X], v0[X], (void)0); \
    } while (0)
#define RING_BAR() do { unsigned long long sv_; \
        asm volatile("s_cmp_eq_u32 %1, 7\n\ts_cbranch_scc1 1f\n\ts_barrier\n1:\n\ts_mov_b64 %0, exec\n\ts_mov_b64 exec, 1\n\tds_add_u32 %2, %3\n\ts_mov_b64 exec, %0" \
                     : "=&s"(sv_) : "s"(st), "v"(fl + C_LD), "v"(1u) : "memory", "scc"); } while (0)
    SBAR();
#pragma unroll 1
    for (int st = 0; st < 8; ++st) {
        const lds_ptr cur = lp + (st & 1) * K2_SLOT;
        const lds_ptr nxt = lp + ((st + 1) & 1) * K2_SLOT;
        PRIO(2);
        KT(0, cur + 4096, (void)0);
        PRIO(1);
        KT(1, cur + 8192, (void)0);
        PRIO(0);
        KT(0, cur + 12288, (void)0);
        PRIO(3);
        KT(1, nxt, RAW_BAR(); SBAR());
    }
    oB = SPV(v1[1], HI8(sB), oB);
    SBAR();
    lB = SSUM(HI8(sB), lB);
    SBAR();
    DG(4);
    {
        const lds_ptr xo = lds + K2_SLOT + w * 4096 + lane * 16;
        *(LAS float*)(lds + K2_LX + w * 256 + lane * 4) = lB[0];
#pragma unroll
        for (int i = 0; i < 4; ++i) *(LAS f32x4*)(xo + i * 1024) = f32x4{oB[4 * i], oB[4 * i + 1], oB[4 * i + 2], oB[4 * i + 3]};
        lds_add(fl + F_X + w * 4, lane);
        lds_add(fl + C_END + half * 4, lane);
        lds_wait_ge(fl + F_X + (w ^ 4) * 4, 1u);
        const lds_ptr xi = lds + K2_SLOT + (w ^ 4) * 4096 + lane * 16;
        f32x4 t[4];
        const float lx = *(const LAS float*)(lds + K2_LX + (w ^ 4) * 256 + (lane & 31) * 4);
        { auto rr = __builtin_amdgcn_permlane32_swap(__float_as_uint(lA[0]), __float_as_uint(lA[0]), false, false); lA[0] = __uint_as_float(rr[0]); }
#pragma unroll
        for (int i = 0; i < 4; ++i) t[i] = *(const LAS f32x4*)(xi + i * 1024);
        SBAR();
        lA[0] += lx;
#pragma unroll
        for (int i = 0; i < 16; ++i) { float o = oA[i]; acc_vv(o, t[i >> 2][i & 3]); oA[i] = o; }
    }
    DG(9);
    const float l = lA[0];
    u32x4 on0, on1;
    auto finish = [&](float ls) {
        float rl = __builtin_amdgcn_rcpf(ls);
        asm volatile("s_nop 1" : "+v"(rl));
        f32x16 o;
#pragma unroll
        for (int i = 0; i < 16; ++i) o[i] = mul_vv(oA[i], rl);
        on0 = nz4(pack8n(o, 0)); on1 = nz4(pack8n(o, 1));
    };
    finish(l);
    {
        float t0, t1;
        asm("v_mul_f32_e32 %0, 0, %2\n\tv_mul_f32_e32 %1, 0, %3\n\tv_fmac_f32_e32 %0, 0, %4\n\tv_fmac_f32_e32 %1, 0, %5\n\tv_fmac_f32_e32 %0, 0, %6\n\tv_fmac_f32_e32 %1, 0, %7\n\tv_fmac_f32_e32 %0, 0, %8\n\tv_fmac_f32_e32 %1, 0, %9\n\t"
            "v_fmac_f32_e32 %0, 0, %10\n\tv_fmac_f32_e32 %1, 0, %11\n\tv_fmac_f32_e32 %0, 0, %12\n\tv_fmac_f32_e32 %1, 0, %13\n\tv_fmac_f32_e32 %0, 0, %14\n\tv_fmac_f32_e32 %1, 0, %15\n\tv_fmac_f32_e32 %0, 0, %16\n\tv_fmac_f32_e32 %1, 0, %17"
            : "=&v"(t0), "=&v"(t1) : "v"(oA[0]), "v"(oA[1]), "v"(oA[2]), "v"(oA[3]), "v"(oA[4]), "v"(oA[5]), "v"(oA[6]), "v"(oA[7]), "v"(oA[8]), "v"(oA[9]), "v"(oA[10]), "v"(oA[11]), "v"(oA[12]), "v"(oA[13]), "v"(oA[14]), "v"(oA[15]));
        const bool ok = (l <= 1.0e30f) && (l >= 1.0e-18f) && (t0 + t1 == 0.f);
        if (__builtin_expect(__any(!ok), 0)) {
            float m = -INFINITY, ls = 0.f;
            oA = f32x16{};
#pragma unroll 1
            for (int t = 0; t < 64; ++t) {
                const char* g = kvg + t * 4096 + lane * 16;
                const bf16x8 a0 = *(const bf16x8*)g, a1 = *(const bf16x8*)(g + 1024);
                const bf16x8 b0 = *(const bf16x8*)(g + 2048), b1 = *(const bf16x8*)(g + 3072);
                f32x16 s = MFMA32(a0, qa0, f32x16{});
                s = MFMA32(a1, qa1, s);
                float tm = s[0];
#pragma unroll
                for (int i = 1; i < 16; ++i) tm = __builtin_fmaxf(tm, s[i]);
                auto rr = __builtin_amdgcn_permlane32_swap(__float_as_uint(tm), __float_as_uint(tm), false, false);
                tm = __builtin_fmaxf(__uint_as_float(rr[0]), __uint_as_float(rr[1]));
                const float mn = __builtin_fmaxf(m, tm);
                const float alpha = __builtin_amdgcn_exp2f(m - mn);
                m = mn;
                float ps = 0.f;
#pragma unroll
                for (int i = 0; i < 16; ++i) { s[i] = bfhi(__float_as_uint(__builtin_amdgcn_exp2f(s[i] - mn))); ps += s[i]; oA[i] *= alpha; }
                ls = ls * alpha + ps;
                oA = SPV(b0, LO8(s), oA);
                oA = SPV(b1, HI8(s), oA);
            }
            auto rr = __builtin_amdgcn_permlane32_swap(__float_as_uint(ls), __float_as_uint(ls), false, false);
            asm volatile("s_nop 7\n\ts_nop 7" : "+v"(oA));
            finish(__uint_as_float(rr[0]) + __uint_as_float(rr[1]));
        }
    }
    u32x4 w3[4];
    const char* s2 = wsrc(half ^ 1, 32 + (w & 3) * 4);
    const char* s3 = wsrc(half, 48 + (w & 3) * 4);
    auto ldw2 = [&]() {
#pragma unroll
        for (int i = 0; i < 4; ++i)
            glds16p<K2_WNT>(wvoff, s2 + i * 16384, lds0 + K2_SLOT + ((w ^ 4) * 4 + i) * 1024);
    };
    auto ldw3 = [&]() {
#pragma unroll
        for (int i = 0; i < 4; ++i) w3[i] = gld128(wvoff, s3 + i * 16384);
    };
    {
        const unsigned og = (unsigned)WS_O + (unsigned)(TA * 32 + h) * 2048u + lane * 16;
        st_sc1(on0, rs, og);
        st_sc1(on1, rs, og + 1024);
        lds_add(fl + C_S1, lane);
        lds_wait_ge(fl + C_S1, 12u);
        DG(5);
        ldw2();
        ldw3();
    }
    {
        const int half = w & 1;
        const int T3 = tt * 4 + (w >> 1);
        const unsigned og = (unsigned)WS_O + (unsigned)T3 * 65536u + half * 32768 + lane * 16;
        const int r = lane & 31, hh = lane >> 5;
        const int g = lane >> 4, q = (lane & 15) >> 2, p = lane & 3;
        int woff[2];
#pragma unroll
        for (int n = 0; n < 2; ++n) {
            const int c = 4 * (n ^ half) + 2 * (p & 1) + (g & 1);
            woff[n] = (4 * hh + q) * 128 + ((c ^ ((q >> 1) << 2)) << 4) + (p >> 1) * 8;
        }
        f32x16 acc[2] = {f32x16{}, f32x16{}};
        lds_ptr wl[3][2] = {{lds + half * 16384 + woff[0], lds + half * 16384 + woff[1]},
                            {lds + K2_SF + half * 16384 + woff[0], lds + K2_SF + half * 16384 + woff[1]},
                            {lds + K2_CV + half * 2 * CV_BYTES + woff[0], lds + K2_CV + half * 2 * CV_BYTES + woff[1]}};
        asm volatile("" : "+v"(wl[0][0]), "+v"(wl[0][1]), "+v"(wl[1][0]), "+v"(wl[1][1]), "+v"(wl[2][0]), "+v"(wl[2][1]));
#define MS(m) ((m) < 64 - 2 * TL ? (m) >> 1 : 32 - TL + ((m) - (64 - 2 * TL)) % TL)
#define MN(m) ((m) < 64 - 2 * TL ? (m) & 1 : (m) < 64 - TL)
        constexpr int WR = 2 * WD + 2;
        s16x4 wa[WR], wb[WR];
#define LDW(m) do { const int s_ = MS(m), f_ = s_ < 8 ? 1 : s_ < 24 ? 0 : 2; \
            const int o_ = f_ == 2 ? ((s_ >> 2) & 1) * CV_BYTES + (s_ & 3) * 2048 : (f_ ? 0 : ((s_ >> 3) - 1) * K2_SLOT) + (s_ & 7) * 2048; \
            wa[(m) % WR] = lds_tr(wl[f_][MN(m)] + o_); wb[(m) % WR] = lds_tr(wl[f_][MN(m)] + o_ + 1024); SBAR(); } while (0)
#pragma unroll
        for (int m = 0; m < 2 * WD; ++m) LDW(m);
        u32x4 of[OWIN];
#define LDO(i) do { of[(i) % OWIN] = ld_sc1s(rs, og, (((i) + krot) & 31) * 1024); SBAR(); } while (0)
        for (unsigned spins = 0; spins < (1u << 20); ++spins) {
#pragma unroll
            for (int i = 0; i < OW0; ++i) LDO(i);
            unsigned m = mx8(of[0], of[1]);
#pragma unroll
            for (int i = 2; i < OW0; ++i) m = mx4(m, of[i]);
            if (!missing(m)) break;
            DGC(12);
        }
        lds_add(fl + C_S2, lane);
#pragma unroll
        for (int i = OW0; i < OWIN; ++i) LDO(i);
        DG(6);
        __builtin_amdgcn_s_setprio(P3_PRIO);
        SBAR();
        unsigned fv = 0;
#define FLAG_RD(a) do { fv = *(volatile LAS unsigned*)(a); SBAR(); } while (0)
#define FLAG_CHK(a, n) do { if (__builtin_expect((unsigned)__builtin_amdgcn_readfirstlane(fv) < (n), 0)) lds_wait_ge(a, n); CFENCE(); SBAR(); } while (0)
        lds_ptr xo = lds + K2_SF + w * 4096 + lane * 16, xi = lds + K2_SF + (w ^ 1) * 4096 + lane * 16;
        int bo = (T3 * 32 + (lane & 15)) * 4096 + 2048 + nt * 128 + half * 64 + g * 16;
        lds_ptr w3d = lds + K2_CV + (w >> 1) * CV_BYTES + (w & 1) * 4096 + lane * 16;
        asm volatile("" : "+v"(xo), "+v"(xi), "+v"(bo), "+v"(w3d));
        f32x4 xt[4];
        constexpr int M3 = TL > 8 ? 64 - 2 * TL + TL - 8 - 2 * WD : 48 - 2 * WD;
        sfor(std::make_integer_sequence<int, 64>{}, [&](auto mc) __attribute__((always_inline)) {
            constexpr int m = decltype(mc)::value, s = MS(m), n = MN(m);
            constexpr bool first = m < 64 - 2 * TL ? n == 0 : n == 1;
            constexpr bool last = m < 64 - 2 * TL ? n == 1 : n == 0;
            acc[n] = MFMA32(cat8(wa[m % WR], wb[m % WR]), PB(of[s % OWIN]), acc[n]);
            SBAR();
            if constexpr (m == 32 - 2 * WD) FLAG_CHK(fl + C_S2, 8u);
            if constexpr (m == M3) FLAG_CHK(fl + C_S3, 8u);
            if constexpr (m + 2 * WD < 64) LDW(m + 2 * WD);
            if constexpr (first && (s & 1) && s + 1 >= OW0 && s + 1 < 32) {
                if (__builtin_expect(missing(mx8(of[(s + 1) % OWIN], of[(s + 2) % OWIN])), 0)) {
#pragma unroll 1
                    for (unsigned spins = 0; spins < (1u << 20); ++spins) {
                        __builtin_amdgcn_s_sleep(2);
                        LDO(s + 1); LDO(s + 2);
                        DGC(13);
                        if (!missing(mx8(of[(s + 1) % OWIN], of[(s + 2) % OWIN]))) break;
                    }
                }
            }
            if constexpr (last && s + OWIN < 32) LDO(s + OWIN);
            if constexpr (LORD && m == S2M) { asm volatile("s_waitcnt vmcnt(%0)" :: "n"(4 + (m + 1) / 2) : "memory"); lds_add(fl + C_S2, lane); }
            if constexpr (LORD && m == W3M) asm volatile("s_waitcnt vmcnt(%0)" :: "n"((m + 1) / 2 < 16 ? (m + 1) / 2 : 16) : "memory");
            if constexpr (m == 15 - 2 * WD) lds_add(fl + C_U0, lane);
            if constexpr (m == 29 - 2 * WD) FLAG_RD(fl + C_S2);
            if constexpr (m == W3M - 3) FLAG_RD(fl + C_HF);
            if constexpr (m == W3M) FLAG_CHK(fl + C_HF, 4u);
            if constexpr (m >= W3M && m < W3M + 8 && ((m - W3M) & 1) == 0) {
                asm volatile("" : "+v"(w3[((m - W3M) >> 1) & 3]));
                *(LAS u32x4*)(w3d + (((m - W3M) >> 1) & 3) * 1024) = w3[((m - W3M) >> 1) & 3];
            }
            if constexpr (m == W3M + 6) lds_add(fl + C_S3, lane);
            if constexpr (m == M3 - 3) FLAG_RD(fl + C_S3);
            if constexpr (m == 64 - TL - 3) FLAG_RD(fl + C_U0);
            if constexpr (m == 64 - TL) FLAG_CHK(fl + C_U0, 8u);
            if constexpr (m == 64 - TL + 1 || m == 64 - TL + 2) {
                constexpr int i = 2 * (m - (64 - TL + 1));
                *(LAS f32x4*)(xo + i * 1024) = f32x4{acc[1][4 * i], acc[1][4 * i + 1], acc[1][4 * i + 2], acc[1][4 * i + 3]};
                *(LAS f32x4*)(xo + i * 1024 + 1024) = f32x4{acc[1][4 * i + 4], acc[1][4 * i + 5], acc[1][4 * i + 6], acc[1][4 * i + 7]};
            }
            if constexpr (m == 64 - TL + 2) lds_add(fl + F_R + w * 4, lane);
            if constexpr (m == XR_AT - 2) FLAG_RD(fl + F_R + (w ^ 1) * 4);
            if constexpr (m == XR_AT) {
                FLAG_CHK(fl + F_R + (w ^ 1) * 4, 1u);
#pragma unroll
                for (int i = 0; i < 4; ++i) xt[i] = *(const LAS f32x4*)(xi + i * 1024);
            }
            if constexpr (ZL_GATE && last && s + OWIN == ZL_AT) lds_add(fl + C_OL, lane);
            if constexpr (m == 2 * P3_T1 + 1) __builtin_amdgcn_s_setprio(P3_PRIO - 1);
            SBAR();
            if constexpr (m == 31) DG(11);
            if constexpr (m == 47) DG(14);
        });
        asm volatile("s_nop 11" : "+v"(acc[0]));
        DG(7);
#pragma unroll
        for (int i = 0; i < 16; ++i) { float o = xt[i >> 2][i & 3]; acc_vv(o, acc[0][i]); acc[1][i] = o; }
        u32x4 pa = pack8n(acc[1], 0), pb = pack8n(acc[1], 1);
#pragma unroll
        for (int i = 0; i < 4; ++i) {
            auto sw = __builtin_amdgcn_permlane16_swap(pa[i], pb[i], false, false);
            pa[i] = sw[0]; pb[i] = sw[1];
        }
        asm volatile("" :: "v"(acc[0]));
        __builtin_amdgcn_raw_buffer_store_b128(pa, ro, bo, 0, OUT_AUX);
        __builtin_amdgcn_raw_buffer_store_b128(pb, ro, bo, 65536, OUT_AUX);
    }
    DG_END(8);
}

extern "C" void kernel_launch(void* const* d_in, const int* in_sizes, int n_in,
                              void* d_out, int out_size, void* d_ws, size_t ws_size,
                              hipStream_t stream) {
    const void* A_q = d_in[0];
    const void* A_k = d_in[1];
    const void* A_v = d_in[2];
    const void* A_shift = d_in[3];
    const void* A_ssmk = d_in[4];
    const void* A_D = d_in[5];
    const void* B_x = d_in[6];
    const void* B_wqkv = d_in[7];
    const void* B_wo = d_in[8];
    char* ws = (char*)d_ws;
    k1_qkv<<<256, 512, 0, stream>>>((const char*)B_x, (const char*)B_wqkv, ws, (const char*)B_wo, (const char*)A_q,
                                    (const char*)A_k, (const char*)A_v, (const char*)A_ssmk);
    k2_attn<<<256, 768, 0, stream>>>(ws, (const char*)A_q, (const char*)A_k, (const char*)A_v, (const char*)A_shift,
                                     (const char*)A_ssmk, (const unsigned*)A_D, (const char*)B_wo, (char*)d_out);
}
```

```cpp
#include <hip/hip_runtime.h>
#include <hip/hip_bf16.h>
#include <stdint.h>
#include <utility>

typedef short bf16x8 __attribute__((ext_vector_type(8)));
typedef short s16x4 __attribute__((ext_vector_type(4)));
typedef float f32x16 __attribute__((ext_vector_type(16)));
typedef float f32x4 __attribute__((ext_vector_type(4)));
typedef float f32x2 __attribute__((ext_vector_type(2)));
typedef unsigned u32x4 __attribute__((ext_vector_type(4)));
typedef unsigned u32x2 __attribute__((ext_vector_type(2)));
typedef __bf16 bf16x2_t __attribute__((ext_vector_type(2)));

#define LAS __attribute__((address_space(3)))
typedef LAS char* lds_ptr;
#define DI __device__ __forceinline__
#define SBAR() __builtin_amdgcn_sched_barrier(0)
#define MFMA32(a, b, c) __builtin_amdgcn_mfma_f32_32x32x16_bf16((a), (b), (c), 0, 0, 0)
#define MFMA16(a, b, c) __builtin_amdgcn_mfma_f32_16x16x32_bf16((a), (b), (c), 0, 0, 0)
typedef __bf16 bfx8_t __attribute__((ext_vector_type(8)));
typedef __bf16 bfx16_t __attribute__((ext_vector_type(16)));
typedef float f32x8 __attribute__((ext_vector_type(8)));
#define SPM 1
#define WAIT_BAR(N) asm volatile("s_waitcnt vmcnt(" #N ") lgkmcnt(0)\n\ts_barrier" ::: "memory")
#define LGKM_BAR() asm volatile("s_waitcnt lgkmcnt(0)\n\ts_barrier" ::: "memory")

#define DG_DECL(K)
#define DG(i)
#define DG_END(i)
#define DGC(i)

#define HB 0
#define HW 0
#define HQ (400)
constexpr int S_LEN = 2048, DM = 1024, NH = 32;

#define KQ 1
#define OQ 0
#define QT(h, T) (KQ ? (h) * 64 + (T) : (T) * 32 + (h))
constexpr size_t WS_Q = 0;
constexpr size_t WS_KV = 4u << 20;
constexpr size_t WS_O = OQ ? WS_Q : 12u << 20;
constexpr size_t WS_KVR = 16u << 20;
#define ZK 0
constexpr size_t WS_ZT = ZK ? WS_KVR : 20u << 20;
constexpr size_t WS_CNT = 24u << 20;

template <int... I, class F> DI void sfor(std::integer_sequence<int, I...>, F&& f) { (f(std::integral_constant<int, I>{}), ...); }
DI unsigned cvtpk(float lo, float hi) {
    f32x2 v = {lo, hi};
    return __builtin_bit_cast(unsigned, __builtin_convertvector(v, bf16x2_t));
}
DI float bflo(unsigned u) { return __uint_as_float(u << 16); }
DI float bfhi(unsigned u) { return __uint_as_float(u & 0xffff0000u); }

DI void glds16(unsigned voff, const void* sbase, unsigned lds_dst) {
    unsigned sv;
    asm volatile("s_mov_b32 %0, m0\n\ts_mov_b32 m0, %3\n\ts_nop 0\n\tglobal_load_lds_dwordx4 %1, %2\n\ts_mov_b32 m0, %0"
                 : "=&s"(sv) : "v"(voff), "s"(sbase), "s"(lds_dst) : "memory");
}
template <int NT> DI void glds16p(unsigned voff, const void* sbase, unsigned lds_dst) {
    if constexpr (NT) {
        unsigned sv;
        asm volatile("s_mov_b32 %0, m0\n\ts_mov_b32 m0, %3\n\ts_nop 0\n\tglobal_load_lds_dwordx4 %1, %2 nt\n\ts_mov_b32 m0, %0"
                     : "=&s"(sv) : "v"(voff), "s"(sbase), "s"(lds_dst) : "memory");
    } else glds16(voff, sbase, lds_dst);
}
#define K1_XNT 0
#define K1_WNT 0
#define K2_SNT 1
#define K2_QNT 0
#define K2_WNT 0
#define OUT_AUX 16
#define K1_SAUX 16
DI bf16x8 lds_rd128(lds_ptr p) { return *(const LAS bf16x8*)p; }
DI s16x4 lds_tr(lds_ptr p) {
    return __builtin_bit_cast(s16x4, __builtin_amdgcn_ds_read_tr16_b64_v4i16((LAS s16x4*)p));
}
DI bf16x8 cat8(s16x4 a, s16x4 b) { return __builtin_shufflevector(a, b, 0, 1, 2, 3, 4, 5, 6, 7); }
DI u32x4 pack8(const f32x16& x, int s, float sc) {
    u32x4 p;
    p[0] = cvtpk(x[8 * s + 0] * sc, x[8 * s + 1] * sc);
    p[1] = cvtpk(x[8 * s + 2] * sc, x[8 * s + 3] * sc);
    p[2] = cvtpk(x[8 * s + 4] * sc, x[8 * s + 5] * sc);
    p[3] = cvtpk(x[8 * s + 6] * sc, x[8 * s + 7] * sc);
    return p;
}
DI u32x4 pack8n(const f32x16& x, int s) {
    u32x4 p;
    p[0] = cvtpk(x[8 * s + 0], x[8 * s + 1]);
    p[1] = cvtpk(x[8 * s + 2], x[8 * s + 3]);
    p[2] = cvtpk(x[8 * s + 4], x[8 * s + 5]);
    p[3] = cvtpk(x[8 * s + 6], x[8 * s + 7]);
    return p;
}
DI void rnd2(float& a, float& b) { unsigned u = cvtpk(a, b); a = bflo(u); b = bfhi(u); }

#define CONV_PACE()
#define RAW_BAR() asm volatile("s_barrier" ::: "memory")
DI unsigned gld32(unsigned voff, const void* sbase) {
    unsigned r;
    asm volatile("global_load_dword %0, %1, %2" : "=v"(r) : "v"(voff), "s"(sbase) : "memory");
    return r;
}
DI u32x2 gld64(unsigned voff, const void* sbase) {
    u32x2 r;
    asm volatile("global_load_dwordx2 %0, %1, %2" : "=v"(r) : "v"(voff), "s"(sbase) : "memory");
    return r;
}
DI float mul_sv(float s, float v) { float r; asm("v_mul_f32_e32 %0, %1, %2" : "=v"(r) : "s"(s), "v"(v)); return r; }
DI float mul_vv(float a, float b) { float r; asm("v_mul_f32_e32 %0, %1, %2" : "=v"(r) : "v"(a), "v"(b)); return r; }
DI float add_vv(float a, float b) { float r; asm("v_add_f32_e32 %0, %1, %2" : "=v"(r) : "v"(a), "v"(b)); return r; }
DI void acc_vv(float& a, float b) { asm("v_add_f32_e32 %0, %0, %1" : "+v"(a) : "v"(b)); }
DI void fmac_vv(float& acc, float a, float b) { asm("v_fmac_f32_e32 %0, %1, %2" : "+v"(acc) : "v"(a), "v"(b)); }
DI void fmac_sv(float& acc, float s, float v) { asm("v_fmac_f32_e32 %0, %1, %2" : "+v"(acc) : "s"(s), "v"(v)); }
typedef __attribute__((address_space(1))) unsigned gu32;
#define RLX_AGENT __ATOMIC_RELAXED, __HIP_MEMORY_SCOPE_AGENT
#define CFENCE() asm volatile("" ::: "memory")
#define DRAIN() asm volatile("s_waitcnt vmcnt(0)" ::: "memory")
constexpr int AUX_SC1 = 16;
DI u32x4 ld_sc1(__amdgpu_buffer_rsrc_t rs, unsigned off) { return __builtin_amdgcn_raw_buffer_load_b128(rs, (int)off, 0, AUX_SC1); }
DI u32x4 ld_sc1s(__amdgpu_buffer_rsrc_t rs, unsigned off, int soff) { return __builtin_amdgcn_raw_buffer_load_b128(rs, (int)off, soff, AUX_SC1); }
DI void st_sc1(u32x4 v, __amdgpu_buffer_rsrc_t rs, unsigned off) { __builtin_amdgcn_raw_buffer_store_b128(v, rs, (int)off, 0, AUX_SC1); }
constexpr unsigned EMPTY = 0xffffffffu;
DI unsigned nz(unsigned x) { return x < EMPTY - 1u ? x : EMPTY - 1u; }
DI u32x4 nz4(u32x4 v) { return u32x4{nz(v[0]), nz(v[1]), nz(v[2]), nz(v[3])}; }
DI float fmx(float a, float b) { return __builtin_elementwise_maximum(a, b); }
DI unsigned umax(unsigned a, unsigned b) { return a > b ? a : b; }
DI unsigned umax3(unsigned a, unsigned b, unsigned c) { return umax(umax(a, b), c); }
DI unsigned mx4(unsigned m, u32x4 v) { return umax3(umax3(m, v[0], v[1]), v[2], v[3]); }
DI unsigned mx8(u32x4 a, u32x4 b) { return umax(umax3(umax3(umax3(a[0], a[1], a[2]), a[3], b[0]), b[1], b[2]), b[3]); }
DI bool missing(unsigned m) { return __builtin_amdgcn_ballot_w64(m == EMPTY) != 0; }
DI unsigned lds_add_rtn(lds_ptr p, int lane) {
    unsigned old = 0;
    CFENCE();
    if (lane == 0) old = __hip_atomic_fetch_add((LAS unsigned*)p, 1u, __ATOMIC_RELAXED, __HIP_MEMORY_SCOPE_WORKGROUP);
    old = __builtin_amdgcn_readfirstlane(old);
    CFENCE();
    return old;
}
DI void lds_add(lds_ptr p, int lane) {
    unsigned long long sv;
    asm volatile("s_mov_b64 %0, exec\n\ts_mov_b64 exec, 1\n\tds_add_u32 %1, %2\n\ts_mov_b64 exec, %0"
                 : "=&s"(sv) : "v"(p), "v"(1u) : "memory");
}
DI void lds_wait_ge(lds_ptr p, unsigned v) {
    CFENCE();
    while ((unsigned)__builtin_amdgcn_readfirstlane(*(volatile LAS unsigned*)p) < v) __builtin_amdgcn_s_sleep(1);
    CFENCE();
}
DI void poll_ge(gu32* word, unsigned want) {
    CFENCE();
    for (unsigned spins = 0; spins < (1u << 22); ++spins) {
        if ((unsigned)__builtin_amdgcn_readfirstlane(__hip_atomic_load(word, RLX_AGENT)) >= want) break;
        __builtin_amdgcn_s_sleep(1);
    }
    __builtin_amdgcn_fence(__ATOMIC_ACQUIRE, "wavefront");
    CFENCE();
}
#define PB(x) __builtin_bit_cast(bf16x8, x)
#define PRIO(x) do { __builtin_amdgcn_s_setprio(x); SBAR(); } while (0)
DI u32x4 gld128(unsigned voff, const void* sbase) {
    u32x4 r;
    asm volatile("global_load_dwordx4 %0, %1, %2" : "=v"(r) : "v"(voff), "s"(sbase) : "memory");
    return r;
}

#define K1_UNR 15
#define K1_LA 5
#define K1_ROT ((tb & 3) * 4)
#define K1_PF 0
#define K1_PG 0
#define K1_TW 0
#define CPF 3
#define CPF_POS 1
#define PG1_AT 0
#define PG2_AT 2
#define PG3_AT 6
#define K1_WB(n) WAIT_BAR(1)
#define PF1_AT -1
#define PF1_LO 4
#define PF1_N 4
#define PF2_AT 1
#define PF2_LO 8
#define PF2_N 4
#define PF3_AT 5
#define PF3_LO 12
#define PF3_N 4
#define K1_KR ((kt + rot) & 15)
#define K1_ST(v, rs, off) __builtin_amdgcn_raw_buffer_store_b128(v, rs, (int)(off), 0, K1_SAUX)
constexpr int K1_STAGE = 40960, K1U = K1_UNR;
__global__ __launch_bounds__(512, 2) void k1_qkv(const char* __restrict__ X, const char* __restrict__ W,
                                                 char* __restrict__ ws) {
    __shared__ __attribute__((aligned(16))) char lds_raw[4 * K1_STAGE];
    const lds_ptr lds = (lds_ptr)lds_raw;
    const unsigned lds0 = (unsigned)(uintptr_t)lds;
    const int tid = threadIdx.x, lane = tid & 63;
    const int w = __builtin_amdgcn_readfirstlane(tid >> 6);
    const int b = blockIdx.x, xcd = b & 7, idx = b >> 3;
    const int hp = (xcd >> 1) * 4 + (idx & 3), tb = (xcd & 1) * 8 + (idx >> 2);
    DG_DECL(0)

    const int lw = w & 3;
    const int rot = K1_ROT;
    unsigned voffX[4], voffW[2];
#pragma unroll
    for (int pp = 0; pp < 4; ++pp) {
        const int row = 32 * lw + 8 * pp + (lane >> 3);
        const int ch = (lane & 7) ^ ((row >> 1) & 7);
        voffX[pp] = (unsigned)(tb * 128 + row) * 2048u + ch * 16;
    }
#pragma unroll
    for (int pp = 0; pp < 2; ++pp) {
        const int krow = 16 * lw + 8 * pp + (lane >> 3);
        const int ch = (lane & 7) ^ (((lane >> 4) & 1) << 2);
        voffW[pp] = (unsigned)krow * 6144u + ch * 16;
    }
    auto issue = [&](int kt, int lo, int hi) {
        const int kr = K1_KR;
        const char* xb = X + kr * 128;
        const char* wb = W + (size_t)kr * (64 * 6144) + hp * 128;
        const unsigned st = lds0 + (kt & 3) * K1_STAGE;
#pragma unroll
        for (int pp = 0; pp < 4; ++pp)
            if (pp >= lo && pp < hi) glds16p<K1_XNT>(voffX[pp], xb, st + (4 * lw + pp) * 1024);
#pragma unroll
        for (int sec = 0; sec < 3; ++sec)
#pragma unroll
            for (int pp = 0; pp < 2; ++pp)
                if (4 + 2 * sec + pp >= lo && 4 + 2 * sec + pp < hi)
                    glds16p<K1_WNT>(voffW[pp], wb + sec * 2048, st + 16384 + sec * 8192 + (2 * lw + pp) * 1024);
    };
    DG(13);
    unsigned cpf;
    auto codepf = [&]() {
        unsigned long long pc; unsigned lo, hi;
        asm volatile("s_getpc_b64 %0\n.Lcp_k1a:\n\ts_mov_b32 %1, %3\n\ts_mov_b32 %2, %4" : "=s"(pc), "=s"(lo), "=s"(hi) : "n"(0), "n"(0));
        if (CPF & 1) lo = 0; else asm volatile("s_mov_b32 %0, .Lcp_k2a-.Lcp_k1a" : "=s"(lo));
        if (CPF & 2) asm volatile("s_mov_b32 %0, .Lcp_k2z-.Lcp_k1a" : "=s"(hi)); else asm volatile("s_mov_b32 %0, .Lcp_k1z-.Lcp_k1a" : "=s"(hi));
        unsigned off = lo + (unsigned)(idx + 32 * (lane & 7) + 256 * w) * 64u;
        off = off < hi ? off : hi;
        asm volatile("global_load_dword %0, %1, %2" : "=v"(cpf) : "v"(off), "s"(pc) : "memory");
    };
    if (w < 4) { issue(0, 0, 5); issue(1, 0, 5); codepf(); }
    else { issue(0, 5, 10); issue(1, 5, 10); }
    DG(14);
    int w2 = w;
    asm volatile("" : "+s"(w2));

    if (w2 >= 4) {
#define WAITN_BAR(N) asm volatile("s_waitcnt vmcnt(%0) lgkmcnt(0)\n\ts_barrier" :: "n"(N) : "memory")
        issue(2, 0, K1_LA);
        asm volatile("s_waitcnt vmcnt(%0)" :: "n"(5 + K1_LA) : "memory");
#pragma unroll 1
        for (int kt = 0; kt < 13; ++kt) {
            WAITN_BAR(10 + K1_LA);
            issue(kt + 2, K1_LA, 10);
            issue(kt + 3, 0, K1_LA);
        }
        WAITN_BAR(10 + K1_LA);
        issue(15, K1_LA, 10);
        WAIT_BAR(10);
        WAIT_BAR(0);
        if (lw == 0 && lane < 2) *(unsigned*)(ws + WS_CNT + b * 256 + lane * 128) = 0u;
        DG_END(5);
        return;
    }

    const int wm = w >> 1, wn = w & 1;
    const int r = lane & 31, hh = lane >> 5;
    const int g = lane >> 4, q = (lane & 15) >> 2, p = lane & 3;
    const int xoff0 = (64 * wm + r) * 128 + ((hh ^ ((r >> 1) & 7)) << 4);
    const int woff0 = 16384 + (8 * hh + q) * 128 + (((4 * wn + 2 * (g & 1) + (p >> 1)) ^ ((q >> 1) << 2)) << 4) + (p & 1) * 8;

    f32x16 acc[2][3];
#pragma unroll
    for (int m = 0; m < 2; ++m)
#pragma unroll
        for (int s = 0; s < 3; ++s) acc[m][s] = f32x16{};
    bf16x8 xf[2][2];
    s16x4 wl[2][3], wh[2][3];
#define K1_WF(f, sec) cat8(wl[f][sec], wh[f][sec])
#define K1_STEP(F, st, t, BAR_HERE) do { \
        const lds_ptr xp_ = (st) + (xoff0 ^ ((t) << 5)); const lds_ptr wp_ = (st) + woff0 + (t) * 2048; \
        acc[0][0] = MFMA32(K1_WF(F, 0), xf[F][0], acc[0][0]); SBAR(); \
        if (BAR_HERE) { K1_WB(BAR_HERE); SBAR(); } \
        xf[F ^ 1][0] = lds_rd128(xp_); wl[F ^ 1][0] = lds_tr(wp_); wh[F ^ 1][0] = lds_tr(wp_ + 512); SBAR(); \
        acc[0][1] = MFMA32(K1_WF(F, 1), xf[F][0], acc[0][1]); SBAR(); \
        wl[F ^ 1][1] = lds_tr(wp_ + 8192); wh[F ^ 1][1] = lds_tr(wp_ + 8192 + 512); SBAR(); \
        acc[0][2] = MFMA32(xf[F][0], K1_WF(F, 2), acc[0][2]); SBAR(); \
        wl[F ^ 1][2] = lds_tr(wp_ + 16384); wh[F ^ 1][2] = lds_tr(wp_ + 16384 + 512); xf[F ^ 1][1] = lds_rd128(xp_ + 4096); SBAR(); \
        acc[1][0] = MFMA32(K1_WF(F, 0), xf[F][1], acc[1][0]); SBAR(); \
        acc[1][1] = MFMA32(K1_WF(F, 1), xf[F][1], acc[1][1]); \
        acc[1][2] = MFMA32(xf[F][1], K1_WF(F, 2), acc[1][2]); SBAR(); \
    } while (0)

    WAIT_BAR(6);
    DG(3);
    {
        const lds_ptr xp_ = lds + xoff0; const lds_ptr wp_ = lds + woff0;
        xf[0][0] = lds_rd128(xp_); xf[0][1] = lds_rd128(xp_ + 4096);
#pragma unroll
        for (int sec = 0; sec < 3; ++sec) { wl[0][sec] = lds_tr(wp_ + sec * 8192); wh[0][sec] = lds_tr(wp_ + sec * 8192 + 512); }
    }
    SBAR();
#pragma unroll K1U
    for (int kt = 0; kt < 15; ++kt) {
        const lds_ptr st = lds + (kt & 3) * K1_STAGE;
        const lds_ptr sn = lds + ((kt + 1) & 3) * K1_STAGE;
        K1_STEP(0, st, 1, false);
        K1_STEP(1, st, 2, false);
        K1_STEP(0, st, 3, false);
        K1_STEP(1, sn, 0, kt + 1);
    }
    {
        const lds_ptr st = lds + 3 * K1_STAGE;
        K1_STEP(0, st, 1, false);
        K1_STEP(1, st, 2, false);
        K1_STEP(0, st, 3, false);
#pragma unroll
        for (int m = 0; m < 2; ++m) {
            acc[m][1] = MFMA32(K1_WF(1, 1), xf[1][m], acc[m][1]);
            acc[m][2] = MFMA32(xf[1][m], K1_WF(1, 2), acc[m][2]);
            acc[m][0] = MFMA32(K1_WF(1, 0), xf[1][m], acc[m][0]);
        }
    }
    SBAR(); unsigned long long ta0; asm volatile("s_memtime %0\n\ts_waitcnt lgkmcnt(0)" : "=s"(ta0) :: "memory"); SBAR();
    DG(4);
    asm volatile("s_waitcnt vmcnt(0)\n.Lcp_k1z:" :: "v"(cpf));
    {
        const int h = 2 * hp + wn;
        const float qscale = 0.17677669529663687f * 1.4426950408889634f;
        const __amdgpu_buffer_rsrc_t rs = __builtin_amdgcn_make_buffer_rsrc(ws, 0, 32 << 20, 0x00020000);
#pragma unroll
        for (int m = 0; m < 2; ++m) {
            const int T = tb * 4 + wm * 2 + m;
            const unsigned dq = (unsigned)WS_Q + QT(h, T) * 2048 + lane * 16;
            const unsigned dk = (unsigned)WS_KV + (h * 64 + T) * 4096 + lane * 16;
            const unsigned dkk = SPM ? (unsigned)WS_KV + (h * 64 + T) * 4096 + ((lane & 51) | ((lane & 4) << 1) | ((lane & 8) >> 1)) * 16 : dk;
            K1_ST(pack8n(acc[m][1], 0), rs, dkk);
            K1_ST(pack8n(acc[m][1], 1), rs, dkk + 1024);
            K1_ST(pack8n(acc[m][2], 0), rs, dk + 2048);
            K1_ST(pack8n(acc[m][2], 1), rs, dk + 3072);
            K1_ST(pack8(acc[m][0], 0, qscale), rs, dq);
            K1_ST(pack8(acc[m][0], 1, qscale), rs, dq + 1024);
        }
    }
    SBAR(); unsigned long long ta1; asm volatile("s_memtime %0\n\ts_waitcnt lgkmcnt(0)" : "=s"(ta1) :: "memory"); SBAR();
    DRAIN();
    SBAR(); unsigned long long ta2; asm volatile("s_memtime %0\n\ts_waitcnt lgkmcnt(0)" : "=s"(ta2) :: "memory"); SBAR();
    if (b == HB && tid == 0) { unsigned* hp_ = (unsigned*)(ws + (30u << 20)); hp_[0] = (unsigned)(ta1 - ta0); hp_[1] = (unsigned)(ta2 - ta1); }
    DG_END(5);
}

constexpr int K2_SLOT = 32768;
constexpr int K2_SF = 65536;
constexpr int K2_CV = 98304;
constexpr int CV_HA = 0, CV_HB = 4160, CV_KV = 8320, CV_BYTES = 8320 + 480 + 4096;
constexpr int K2_LX = K2_CV + 4 * CV_BYTES;
constexpr int K2_FL = K2_LX + 2048;
constexpr int K2_SCR = K2_FL + 256;
constexpr int K2_PAD = K2_SCR + 4096;
constexpr int K2_LDS = K2_PAD + 1024;
#define NB8 0
#define WW 0
constexpr int F_X = 0, F_R = 32, F_W2 = 64, C_OST = 68, C_S1 = 76, C_S2 = 80, C_S3 = 128, C_ZST = 88, F_ZGO = 92,
              C_KST = 96, C_U0 = 100, C_LD = 108, F_KGO = 116, C_END = 120, C_OL = 72, C_HF = 84;
#define NG_IN 0
#define GATE_WORD C_END
#define GATE_VAL 0
#define X_NZ 4
#define X_NW3 8
#define ZP_GATE 4u
#define ZL_GATE 4
#define ZL_AT 31
#define Z_SLEEP0 0
#define O_SLEEP 0
#define Z_SLEEP 8
#define OWIN 16
#define OW0 8
#define WD 2
#define W3_AT 10
#define X_NOLDW 0
#define X_NOLDO 0
#define X_NOCHK 0
#define KROT (nt * 2)
#define NG_LAST 0
#define PACE_IN "s_nop 7"
#define PACE_OUT "s_nop 1"
#define P3_PRIO 1
#define TOP_SLEEP 2
#define A_AUX 16
#define B_AUX 16
#define B_LDS 0
#define TL 8
#define XR_AT 62
#define W3_AUX 0
#define W3M 13
#define A16 1
#define SEL4 2
#define KV_DOT 1
#define ZE_PRIO 3
#define P3_T1 99
#define P3_T2 99
#define AUX_PRIO 2

__global__ __launch_bounds__(768, 3) void k2_attn(char* __restrict__ ws, const char* __restrict__ Aq,
                                                  const char* __restrict__ Ak, const char* __restrict__ Av,
                                                  const char* __restrict__ shiftk, const char* __restrict__ ssmk,
                                                  const unsigned* __restrict__ ssmD, const char* __restrict__ Wo,
                                                  char* __restrict__ out) {
    __shared__ __attribute__((aligned(16))) char lds_raw[K2_LDS];
    const lds_ptr lds = (lds_ptr)lds_raw;
    const lds_ptr fl = lds + K2_FL;
    const unsigned lds0 = (unsigned)(uintptr_t)lds;
    const int tid = threadIdx.x, lane = tid & 63;
    const int w = __builtin_amdgcn_readfirstlane(tid >> 6);
    const int b = blockIdx.x, xcd = b & 7, idx = b >> 3;
    const int h = xcd * 4 + (idx >> 3), qb = idx & 7;
    const int tt = xcd * 2 + (idx >> 4), nt = idx & 15;
    const char* kvg = ws + WS_KV + (size_t)h * 64 * 4096;
    const __amdgpu_buffer_rsrc_t rs = __builtin_amdgcn_make_buffer_rsrc(ws, 0, 32 << 20, 0x00020000);
    const __amdgpu_buffer_rsrc_t ro = __builtin_amdgcn_make_buffer_rsrc(out, 0, 8 << 20, 0x00020000);
    gu32* const cnt = (gu32*)(ws + WS_CNT);
    auto signal = [&]() {
        (void)__hip_atomic_fetch_add(cnt + ((lane & 31) * 8 + (lane < 32 ? xcd : qb)) * 64, 1u, RLX_AGENT);
    };
    const unsigned wvoff = (unsigned)(lane >> 3) * 2048u + nt * 128 + (((lane & 7) ^ (((lane >> 4) & 1) << 2)) << 4);
    const int krot = KROT;
    auto wsrc = [&](int hf, int lp) { return Wo + (size_t)(hf * 64 + ((lp + krot * 2) & 63)) * 16384; };
    DG_DECL(1)
    const unsigned voff = lane * 16;
    const int cw = w & 3;
    auto issue = [&](int st) {
        const char* src = kvg + (st * 4 + cw) * 4096;
        const unsigned dst = lds0 + (st & 1) * K2_SLOT + cw * 4096;
#pragma unroll
        for (int i = 0; i < 4; ++i) glds16(voff, src + i * 1024, dst + i * 1024);
#pragma unroll
        for (int i = 0; i < 4; ++i) glds16(voff, src + 131072 + i * 1024, dst + 16384 + i * 1024);
    };
    asm volatile(".Lcp_k2a:");
    if (w >= 8) {
        __builtin_amdgcn_s_sleep(TOP_SLEEP);
#pragma unroll
        for (int i = 2; i < 8; ++i)
            glds16(voff, kvg + (i & 1) * 131072 + (i >> 1) * 4096 + cw * 1024, lds0 + (i & 1) * 16384 + (i >> 1) * 4096 + cw * 1024);
        RAW_BAR();
        issue(1);
        WAIT_BAR(8);
    } else {
        const int tq = cw * 2 + (w >> 2);
        const char* qg = ws + WS_Q + (size_t)QT(h, qb * 8 + tq) * 2048;
        glds16(voff, kvg + (w >> 2) * 131072 + cw * 1024, lds0 + (w >> 2) * 16384 + cw * 1024);
        glds16(voff, qg, lds0 + K2_SF + tq * 2048);
        glds16(voff, qg + 1024, lds0 + K2_SF + tq * 2048 + 1024);
    }
    int w2 = w;
    asm volatile("" : "+s"(w2));

    if (w2 >= 8) {
        __builtin_amdgcn_s_setprio(3);
        const int ch = xcd * 128 + idx * 4 + cw;
        const lds_ptr cv = lds + K2_CV + cw * CV_BYTES;
        {
            const u32x4 z4 = {0u, 0u, 0u, 0u};
            if (lane < 2) *(LAS u32x4*)(cv + CV_HA + lane * 16) = z4;
            if (lane < 30) *(LAS u32x4*)(cv + CV_KV + lane * 16) = z4;
            if (cw == 0 && lane < 16) *(LAS u32x4*)(fl + lane * 16) = z4;
        }
        const int c0 = xcd * 128 + cw * 32, cq = lane & 7, rg = lane >> 3;
        const int row0 = idx * 64 + rg * 8;
        const unsigned roff = (unsigned)row0 * 2048u + (c0 + cq * 4) * 2;
        const unsigned toffs = (unsigned)(c0 + (lane >> 3)) * 4096u + idx * 128;
        u32x4 sk[2];
        sk[0] = gld128((unsigned)(c0 + cq * 4) * 8u, shiftk); sk[1] = gld128((unsigned)(c0 + cq * 4) * 8u, shiftk + 16);
        unsigned dd = gld32((unsigned)(ch >> 1) * 4u, ssmD);
        auto stage = [&](const char* src, int dst) {
            const unsigned sbase = (unsigned)(idx * 64 + cw * 16 + (lane >> 4)) * 2048u + xcd * 256;
#pragma unroll
            for (int i = 0; i < 4; ++i)
                glds16p<K2_SNT>(sbase + (((lane & 15) ^ (((2 * cw + (i >> 1)) & 3) << 2)) << 4), src + i * 8192, lds0 + dst + cw * 4096 + i * 1024);
        };
        auto blank = [&](unsigned off) {
            const u32x4 e4 = {EMPTY, EMPTY, EMPTY, EMPTY};
            CFENCE();
            st_sc1(e4, rs, off + voff); st_sc1(e4, rs, off + 1024 + voff);
            CFENCE();
        };
        WAIT_BAR(3);
        DG(12);
        issue(2);
        stage(Ak, K2_SF);
        glds16((unsigned)(idx * 64 - (idx ? 4 : 0) + (lane >> 4)) * 2048u + xcd * 256 + (((lane & 15) ^ 12) << 4), Ak, lds0 + K2_SCR + cw * 1024);
        blank((unsigned)WS_O + (unsigned)((qb * 8 + cw * 2) * 32 + h) * 2048u);
        auto warm = [&](int g) {
#pragma unroll
            for (int i = 0; i < 4; ++i) glds16(wvoff, wsrc(idx >> 4, 16 + g * 16 + cw * 4 + i), lds0 + K2_PAD);
        };
        WAIT_BAR(7);
        DG(9);
        issue(3);
        stage(Av, K2_SF + 16384);
        blank((unsigned)WS_O + (unsigned)((qb * 8 + cw * 2 + 1) * 32 + h) * 2048u);
        WAIT_BAR(6);
        DG(3);
        issue(4);
#pragma unroll
        for (int i = 0; i < 4; ++i) glds16p<K2_SNT>(voff, ssmk + (size_t)ch * 4096 + i * 1024, lds0 + K2_CV + cw * CV_BYTES + CV_HA + 32 + i * 1024);
        blank((unsigned)WS_ZT + ch * 4096); blank((unsigned)WS_ZT + ch * 4096 + 2048);
        WAIT_BAR(8);
        DG(5);
        issue(5);
        SBAR();
        asm volatile("" : "+v"(sk[0]), "+v"(sk[1]), "+v"(dd));
        dd = __builtin_amdgcn_readfirstlane(dd);
        {
            u32x2 kr[11], vr[8];
            lds_ptr kb = lds + K2_SF + rg * 2048 + ((cw ^ (rg & 3)) << 6) + cq * 8;
            asm volatile("" : "+v"(kb));
            const lds_ptr hb = lds + (rg ? K2_SF + rg * 2048 - 768 : K2_SCR + cw * 1024 + 256) + ((cw ^ ((rg + 3) & 3)) << 6) + cq * 8;
#pragma unroll
            for (int i = 0; i < 3; ++i) kr[i] = *(const LAS u32x2*)(hb + i * 256);
#pragma unroll
            for (int i = 0; i < 8; ++i) kr[3 + i] = *(const LAS u32x2*)(kb + i * 256);
#pragma unroll
            for (int i = 0; i < 8; ++i) vr[i] = *(const LAS u32x2*)(kb + 16384 + i * 256);
            if (row0 == 0) { kr[0] = u32x2{0u, 0u}; kr[1] = u32x2{0u, 0u}; kr[2] = u32x2{0u, 0u}; }
            const lds_ptr tb = cv + CV_KV + 480;
#pragma unroll
            for (int c = 0; c < 4; ++c) {
                const unsigned s01 = sk[c >> 1][(c & 1) * 2], s23 = sk[c >> 1][(c & 1) * 2 + 1];
                unsigned kp[10];
                float kv[8], vf[8];
#pragma unroll
                for (int j = 0; j < 10; ++j) kp[j] = __builtin_amdgcn_perm(kr[j][c >> 1], kr[j + 1][c >> 1], (c & 1) ? 0x07060302u : 0x05040100u);
                SBAR();
#pragma unroll
                for (int i = 0; i < 8; ++i) asm("v_dot2_f32_bf16 %0, %1, %2, 0" : "=v"(kv[i]) : "v"(kp[i]), "v"(s23));
                SBAR();
#pragma unroll
                for (int i = 0; i < 8; ++i) asm("v_dot2_f32_bf16 %0, %1, %2, %0" : "+v"(kv[i]) : "v"(kp[i + 2]), "v"(s01));
                SBAR();
#pragma unroll
                for (int i = 0; i < 8; ++i) vf[i] = (c & 1) ? bfhi(vr[i][c >> 1]) : bflo(vr[i][c >> 1]);
                SBAR();
#pragma unroll
                for (int i = 0; i < 8; ++i) kv[i] = mul_vv(kv[i], vf[i]);
                u32x4 o4;
#pragma unroll
                for (int i = 0; i < 4; ++i) o4[i] = cvtpk(kv[7 - 2 * i], kv[6 - 2 * i]);
                *(LAS u32x4*)(tb + (cq * 4 + c) * 128 + ((rg ^ cq) << 4)) = o4;
            }
#pragma unroll
            for (int i = 0; i < 4; ++i) {
                const u32x4 t = *(const LAS u32x4*)(tb + i * 1024 + lane * 16);
                st_sc1(t, rs, (unsigned)WS_Q + (xcd * 32 + idx) * 16384 + cw * 4096 + i * 1024 + (lane >> 3) * 128 + (((lane & 7) ^ (2 * i + (lane >> 5))) << 4));
            }
        }
        CFENCE();
        DG(7);
        WAIT_BAR(4);
        DG(6);
        issue(6);
        asm volatile("s_waitcnt vmcnt(8)" ::: "memory");
        if (lds_add_rtn(fl + C_KST, lane) == 3u) signal();
#pragma unroll
        for (int i = 0; i < 8; ++i)
            glds16p<K2_WNT>(wvoff, wsrc(i >> 2, cw * 4 + (i & 3)), lds0 + K2_SF + (i >> 2) * 16384 + (cw * 4 + (i & 3)) * 1024);
        {
            const u32x4 z4 = {0u, 0u, 0u, 0u};
            if (lane < 2) *(LAS u32x4*)(cv + CV_HB + lane * 16) = z4;
#pragma unroll
            for (int i = 0; i < 4; ++i) {
                const int off = 32 + i * 1024 + lane * 16;
                const u32x4 hv = *(const LAS u32x4*)(cv + CV_HA + off);
                const unsigned pv = *(const LAS unsigned*)(cv + CV_HA + off - 4);
                u32x4 sh;
                sh[0] = __builtin_amdgcn_alignbit(hv[0], pv, 16);
                sh[1] = __builtin_amdgcn_alignbit(hv[1], hv[0], 16);
                sh[2] = __builtin_amdgcn_alignbit(hv[2], hv[1], 16);
                sh[3] = __builtin_amdgcn_alignbit(hv[3], hv[2], 16);
                *(LAS u32x4*)(cv + CV_HB + off) = sh;
            }
        }
        WAIT_BAR(8);
        DG(8);
        issue(7);
#pragma unroll
        for (int i = 0; i < 8; ++i)
            glds16p<K2_WNT>(wvoff, wsrc(idx >> 4, 32 + cw * 8 + i), lds0 + K2_SCR + cw * 1024);
        WAIT_BAR(8);
        DG(11);
        if (cw == 0) { poll_ge(cnt + b * 64, 64u); lds_add(fl + F_KGO, lane); }
        else lds_wait_ge(fl + F_KGO, 1u);
        {
            u32x4 t[4];
#pragma unroll
            for (int i = 0; i < 4; ++i) t[i] = ld_sc1s(rs, (unsigned)WS_Q + (xcd * 32 + (lane >> 3)) * 16384 + (idx * 4 + cw) * 128 + (lane & 7) * 16, i * 131072);
#pragma unroll
            for (int i = 0; i < 4; ++i) *(LAS u32x4*)(cv + CV_KV + 480 + i * 1024 + lane * 16) = t[i];
        }
        const int r1 = lane & 15, q = lane >> 4;
        const int toff = (r1 & 1) ? CV_HA + 2 * (16 + r1 + 9 - 8 * q) : CV_HB + 2 * (16 + r1 + 10 - 8 * q);
        const int boff = CV_KV + 448 + 32 * r1 + 16 * q;
        f32x4 y[8];
        u32x4 tf[8];
        bf16x8 bf[8];
        const lds_ptr bp = cv + boff - 448;
        lds_ptr tp[4];
#pragma unroll
        for (int i = 0; i < 4; ++i) { tp[i] = cv + toff + 1024 * i; asm volatile("" : "+v"(tp[i])); }
#define LDT(bb, a, hf) do { const LAS unsigned* t_ = (const LAS unsigned*)(tp[(a) >> 1] + 64 * (bb) + 512 * ((a) & 1) + 8 * (hf)); \
            tf[a][2 * (hf)] = t_[0]; tf[a][2 * (hf) + 1] = t_[1]; SBAR(); } while (0)
#define LDB(bb, a) do { bf[a] = lds_rd128(bp + 448 - 64 * (bb) + 512 * (a)); SBAR(); } while (0)
#define CM(a, ww) do { asm volatile("v_mfma_f32_16x16x32_bf16 %0, %1, %2, %0" : "+v"(y[(a) + (ww)]) : "v"(tf[a]), "v"(bf[ww])); SBAR(); } while (0)
#define CM0(a, ww) do { asm volatile("v_mfma_f32_16x16x32_bf16 %0, %1, %2, 0" : "=&v"(y[(a) + (ww)]) : "v"(tf[a]), "v"(bf[ww])); SBAR(); } while (0)
#define PC() do { asm volatile(PACE_OUT); SBAR(); } while (0)
#pragma unroll
        for (int a = 0; a < 8; ++a) { if (a < 5) { LDT(0, a, 0); LDT(0, a, 1); } LDB(0, 7 - a); }
#pragma unroll
        for (int i = 0; i < 8; ++i)
            glds16p<K2_WNT>(wvoff, wsrc(i >> 2, 16 + cw * 4 + (i & 3)), lds0 + (i >> 2) * 16384 + (cw * 4 + (i & 3)) * 1024);
#pragma unroll
        for (int i = 0; i < 4; ++i)
            glds16p<K2_QNT>((unsigned)(idx * 64 + cw * 16 + (lane >> 4)) * 2048u + xcd * 256 + (lane & 15) * 16, Aq + i * 8192, lds0 + K2_SCR + cw * 1024);
        RAW_BAR();
        asm volatile("s_waitcnt vmcnt(4)" ::: "memory");
        lds_add(fl + C_S1, lane);
#define ZFL() PC()
        __builtin_amdgcn_s_setprio(AUX_PRIO);
        const float dch = (ch & 1) ? bfhi(dd) : bflo(dd);
        const lds_ptr kdp = cv + CV_KV + 480 + 32 * r1 + 16 * (q >> 1) + ((q & 1) ? 0 : 8);
        float kd[8][4];
        u32x2 kk;
        unsigned kc0, kc1, zo[8][2];
#define KD_RD(t) do { kk = *(const LAS u32x2*)(kdp + 512 * (t)); SBAR(); } while (0)
#define KD_UP0(t) do { kd[t][0] = bfhi(kk[1]); kd[t][1] = bflo(kk[1]); SBAR(); } while (0)
#define KD_UP1(t) do { kd[t][2] = bfhi(kk[0]); kd[t][3] = bflo(kk[0]); SBAR(); } while (0)
#define KD_MU0(t) do { kd[t][0] = mul_sv(dch, kd[t][0]); kd[t][1] = mul_sv(dch, kd[t][1]); SBAR(); } while (0)
#define KD_MU1(t) do { kd[t][2] = mul_sv(dch, kd[t][2]); kd[t][3] = mul_sv(dch, kd[t][3]); SBAR(); } while (0)
#define KD_CV(t) do { kc0 = cvtpk(kd[t][0], kd[t][1]); kc1 = cvtpk(kd[t][2], kd[t][3]); SBAR(); } while (0)
#define KD_X0(t) do { kd[t][0] = bflo(kc0); kd[t][1] = bfhi(kc0); SBAR(); } while (0)
#define KD_X1(t) do { kd[t][2] = bflo(kc1); kd[t][3] = bfhi(kc1); SBAR(); } while (0)
#define ZF0(t) do { kc0 = cvtpk(y[t][0], y[t][1]); kc1 = cvtpk(y[t][2], y[t][3]); SBAR(); } while (0)
#define ZF1(t) do { y[t][0] = bflo(kc0); y[t][1] = bfhi(kc0); SBAR(); } while (0)
#define ZF2(t) do { y[t][2] = bflo(kc1); y[t][3] = bfhi(kc1); SBAR(); } while (0)
#define ZF3(t) do { y[t][0] = add_vv(y[t][0], kd[t][0]); y[t][1] = add_vv(y[t][1], kd[t][1]); SBAR(); } while (0)
#define ZF4(t) do { y[t][2] = add_vv(y[t][2], kd[t][2]); y[t][3] = add_vv(y[t][3], kd[t][3]); SBAR(); } while (0)
#define ZF5(t) do { zo[t][0] = nz(cvtpk(y[t][0], y[t][1])); zo[t][1] = nz(cvtpk(y[t][2], y[t][3])); SBAR(); } while (0)
#define ZST(pr) do { auto s0 = __builtin_amdgcn_permlane16_swap(zo[2 * (pr)][0], zo[2 * (pr) + 1][0], false, false); \
            auto s1 = __builtin_amdgcn_permlane16_swap(zo[2 * (pr)][1], zo[2 * (pr) + 1][1], false, false); \
            st_sc1(u32x4{s0[0], s1[0], s0[1], s1[1]}, rs, (unsigned)WS_ZT + ch * 4096 + (pr) * 1024 + voff); SBAR(); } while (0)
#define W3(i) do { if ((i) == 0) PC(); } while (0)
#define HF() do { lds_add(fl + C_HF, lane); SBAR(); } while (0)
        CM0(0, 7); LDB(1, 7); CM0(0, 6); LDT(0, 5, 0); CM0(0, 5); LDT(0, 5, 1); CM0(0, 4); LDT(0, 6, 0); CM0(0, 3); LDT(0, 6, 1); CM0(0, 2); LDT(0, 7, 0); CM0(0, 1); LDT(0, 7, 1); CM0(0, 0); KD_RD(0);
        CM(1, 6); LDB(1, 6); CM(1, 5); LDT(1, 0, 0); CM(1, 4); LDT(1, 0, 1); CM(1, 3); PC(); CM(1, 2); PC(); CM(1, 1); KD_UP0(0); CM(1, 0); KD_UP1(0);
        CM(2, 5); LDB(1, 5); CM(2, 4); LDT(1, 1, 0); CM(2, 3); LDT(1, 1, 1); CM(2, 2); KD_MU0(0); CM(2, 1); KD_MU1(0); CM(2, 0); KD_CV(0);
        CM(3, 4); LDB(1, 4); CM(3, 3); LDT(1, 2, 0); CM(3, 2); LDT(1, 2, 1); CM(3, 1); KD_X0(0); CM(3, 0); KD_X1(0);
        CM(4, 3); LDB(1, 3); CM(4, 2); LDT(1, 3, 0); CM(4, 1); LDT(1, 3, 1); CM(4, 0); KD_RD(1);
        CM(5, 2); LDB(1, 2); CM(5, 1); LDT(1, 4, 0); CM(5, 0); LDT(1, 4, 1);
        CM(6, 1); LDB(1, 1); CM(6, 0); PC();
        CM(7, 0); LDB(1, 0);
        CM(0, 7); LDB(2, 7); CM(0, 6); LDT(1, 5, 0); CM(0, 5); LDT(1, 5, 1); CM(0, 4); LDT(1, 6, 0); CM(0, 3); LDT(1, 6, 1); CM(0, 2); LDT(1, 7, 0); CM(0, 1); LDT(1, 7, 1); CM(0, 0); PC();
        CM(1, 6); LDB(2, 6); CM(1, 5); LDT(2, 0, 0); CM(1, 4); LDT(2, 0, 1); CM(1, 3); KD_UP0(1); CM(1, 2); KD_UP1(1); CM(1, 1); KD_MU0(1); CM(1, 0); KD_MU1(1);
        CM(2, 5); LDB(2, 5); CM(2, 4); LDT(2, 1, 0); CM(2, 3); LDT(2, 1, 1); CM(2, 2); KD_CV(1); CM(2, 1); KD_X0(1); CM(2, 0); KD_X1(1);
        CM(3, 4); LDB(2, 4); CM(3, 3); LDT(2, 2, 0); CM(3, 2); LDT(2, 2, 1); CM(3, 1); KD_RD(2); CM(3, 0); PC();
        CM(4, 3); LDB(2, 3); CM(4, 2); LDT(2, 3, 0); CM(4, 1); LDT(2, 3, 1); CM(4, 0); PC();
        CM(5, 2); LDB(2, 2); CM(5, 1); LDT(2, 4, 0); CM(5, 0); LDT(2, 4, 1);
        CM(6, 1); LDB(2, 1); CM(6, 0); KD_UP0(2);
        CM(7, 0); LDB(2, 0);
        CM(0, 7); LDB(3, 7); CM(0, 6); LDT(2, 5, 0); CM(0, 5); LDT(2, 5, 1); CM(0, 4); LDT(2, 6, 0); CM(0, 3); LDT(2, 6, 1); CM(0, 2); LDT(2, 7, 0); CM(0, 1); LDT(2, 7, 1); CM(0, 0); KD_UP1(2);
        CM(1, 6); LDB(3, 6); CM(1, 5); LDT(3, 0, 0); CM(1, 4); LDT(3, 0, 1); CM(1, 3); KD_MU0(2); CM(1, 2); KD_MU1(2); CM(1, 1); KD_CV(2); CM(1, 0); KD_X0(2);
        CM(2, 5); LDB(3, 5); CM(2, 4); LDT(3, 1, 0); CM(2, 3); LDT(3, 1, 1); CM(2, 2); KD_X1(2); CM(2, 1); KD_RD(3); CM(2, 0); PC();
        CM(3, 4); LDB(3, 4); CM(3, 3); LDT(3, 2, 0); CM(3, 2); LDT(3, 2, 1); CM(3, 1); PC(); CM(3, 0); KD_UP0(3);
        CM(4, 3); LDB(3, 3); CM(4, 2); LDT(3, 3, 0); CM(4, 1); LDT(3, 3, 1); CM(4, 0); KD_UP1(3);
        CM(5, 2); LDB(3, 2); CM(5, 1); LDT(3, 4, 0); CM(5, 0); LDT(3, 4, 1);
        CM(6, 1); LDB(3, 1); CM(6, 0); KD_MU0(3);
        CM(7, 0); LDB(3, 0);
        CM(0, 7); LDB(4, 7); CM(0, 6); LDT(3, 5, 0); CM(0, 5); LDT(3, 5, 1); CM(0, 4); LDT(3, 6, 0); CM(0, 3); LDT(3, 6, 1); CM(0, 2); LDT(3, 7, 0); CM(0, 1); LDT(3, 7, 1); CM(0, 0); KD_MU1(3);
        CM(1, 6); LDB(4, 6); CM(1, 5); LDT(4, 0, 0); CM(1, 4); LDT(4, 0, 1); CM(1, 3); KD_CV(3); CM(1, 2); KD_X0(3); CM(1, 1); KD_X1(3); CM(1, 0); KD_RD(4);
        CM(2, 5); LDB(4, 5); CM(2, 4); LDT(4, 1, 0); CM(2, 3); LDT(4, 1, 1); CM(2, 2); PC(); CM(2, 1); PC(); CM(2, 0); KD_UP0(4);
        CM(3, 4); LDB(4, 4); CM(3, 3); LDT(4, 2, 0); CM(3, 2); LDT(4, 2, 1); CM(3, 1); KD_UP1(4); CM(3, 0); KD_MU0(4);
        CM(4, 3); LDB(4, 3); CM(4, 2); LDT(4, 3, 0); CM(4, 1); LDT(4, 3, 1); CM(4, 0); KD_MU1(4);
        CM(5, 2); LDB(4, 2); CM(5, 1); LDT(4, 4, 0); CM(5, 0); LDT(4, 4, 1);
        CM(6, 1); LDB(4, 1); CM(6, 0); KD_CV(4);
        CM(7, 0); LDB(4, 0);
        CM(0, 7); LDB(5, 7); CM(0, 6); LDT(4, 5, 0); CM(0, 5); LDT(4, 5, 1); CM(0, 4); LDT(4, 6, 0); CM(0, 3); LDT(4, 6, 1); CM(0, 2); LDT(4, 7, 0); CM(0, 1); LDT(4, 7, 1); CM(0, 0); KD_X0(4);
        CM(1, 6); LDB(5, 6); CM(1, 5); LDT(5, 0, 0); CM(1, 4); LDT(5, 0, 1); CM(1, 3); KD_X1(4); CM(1, 2); KD_RD(5); CM(1, 1); PC(); CM(1, 0); PC();
        CM(2, 5); LDB(5, 5); CM(2, 4); LDT(5, 1, 0); CM(2, 3); LDT(5, 1, 1); CM(2, 2); KD_UP0(5); CM(2, 1); KD_UP1(5); CM(2, 0); KD_MU0(5);
        CM(3, 4); LDB(5, 4); CM(3, 3); LDT(5, 2, 0); CM(3, 2); LDT(5, 2, 1); CM(3, 1); KD_MU1(5); CM(3, 0); KD_CV(5);
        CM(4, 3); LDB(5, 3); CM(4, 2); LDT(5, 3, 0); CM(4, 1); LDT(5, 3, 1); CM(4, 0); KD_X0(5);
        CM(5, 2); LDB(5, 2); CM(5, 1); LDT(5, 4, 0); CM(5, 0); LDT(5, 4, 1);
        CM(6, 1); LDB(5, 1); CM(6, 0); KD_X1(5);
        CM(7, 0); LDB(5, 0);
        CM(0, 7); LDB(6, 7); CM(0, 6); LDT(5, 5, 0); CM(0, 5); LDT(5, 5, 1); CM(0, 4); LDT(5, 6, 0); CM(0, 3); LDT(5, 6, 1); CM(0, 2); LDT(5, 7, 0); CM(0, 1); LDT(5, 7, 1); CM(0, 0); KD_RD(6);
        CM(1, 6); LDB(6, 6); CM(1, 5); LDT(6, 0, 0); CM(1, 4); LDT(6, 0, 1); CM(1, 3); PC(); CM(1, 2); PC(); CM(1, 1); KD_UP0(6); CM(1, 0); KD_UP1(6);
        CM(2, 5); LDB(6, 5); CM(2, 4); LDT(6, 1, 0); CM(2, 3); LDT(6, 1, 1); CM(2, 2); KD_MU0(6); CM(2, 1); KD_MU1(6); CM(2, 0); KD_CV(6);
        CM(3, 4); LDB(6, 4); CM(3, 3); LDT(6, 2, 0); CM(3, 2); LDT(6, 2, 1); CM(3, 1); KD_X0(6); CM(3, 0); KD_X1(6);
        CM(4, 3); LDB(6, 3); CM(4, 2); LDT(6, 3, 0); CM(4, 1); LDT(6, 3, 1); CM(4, 0); KD_RD(7);
        CM(5, 2); LDB(6, 2); CM(5, 1); LDT(6, 4, 0); CM(5, 0); LDT(6, 4, 1);
        CM(6, 1); LDB(6, 1); CM(6, 0); PC();
        CM(7, 0); LDB(6, 0);
        CM(0, 7); LDB(7, 7); CM(0, 6); LDT(6, 5, 0); CM(0, 5); LDT(6, 5, 1); CM(0, 4); LDT(6, 6, 0); CM(0, 3); LDT(6, 6, 1); CM(0, 2); LDT(6, 7, 0); CM(0, 1); LDT(6, 7, 1); CM(0, 0); PC();
        CM(1, 6); LDB(7, 6); CM(1, 5); LDT(7, 0, 0); CM(1, 4); LDT(7, 0, 1); CM(1, 3); KD_UP0(7); CM(1, 2); KD_UP1(7); CM(1, 1); KD_MU0(7); CM(1, 0); KD_MU1(7);
        CM(2, 5); LDB(7, 5); CM(2, 4); LDT(7, 1, 0); CM(2, 3); LDT(7, 1, 1); CM(2, 2); KD_CV(7); CM(2, 1); KD_X0(7); CM(2, 0); KD_X1(7);
        CM(3, 4); LDB(7, 4); CM(3, 3); LDT(7, 2, 0); CM(3, 2); LDT(7, 2, 1); CM(3, 1); PC(); CM(3, 0); PC();
        CM(4, 3); LDB(7, 3); CM(4, 2); LDT(7, 3, 0); CM(4, 1); LDT(7, 3, 1); CM(4, 0); PC();
        CM(5, 2); LDB(7, 2); CM(5, 1); LDT(7, 4, 0); CM(5, 0); LDT(7, 4, 1);
        CM(6, 1); LDB(7, 1); CM(6, 0); PC();
        CM(7, 0); LDB(7, 0);
        CM(0, 3); LDT(7, 5, 0); CM(0, 2); LDT(7, 5, 1); CM(1, 2); LDT(7, 6, 0); CM(1, 1); LDT(7, 6, 1); CM(2, 1); LDT(7, 7, 0); CM(2, 0); LDT(7, 7, 1);
        CM(3, 0); HF(); CM(0, 1); ZFL(); CM(0, 0); W3(0); CM(1, 0); ZF0(2); W3(1); CM(0, 5); ZF1(2); W3(2); CM(0, 4); ZF2(2); W3(3);
        CM(1, 4); ZF3(2); W3(4); CM(1, 3); ZF4(2); W3(5); CM(2, 3); ZF5(2); W3(6); CM(2, 2); ZF0(3); W3(7); CM(3, 2); ZF1(3);  CM(3, 1); ZF2(3);
        CM(4, 1); ZF3(3);  CM(4, 0); ZF4(3);  CM(5, 0); ZF5(3);  CM(0, 7); ZST(1);  CM(0, 6); ZF0(0);  CM(1, 6); ZF1(0);
        CM(1, 5); ZF2(0);  CM(2, 5); ZF3(0);  CM(2, 4); ZF4(0);  CM(3, 4); ZF5(0);  CM(3, 3); ZF0(1);  CM(4, 3); ZF1(1);
        CM(4, 2); ZF2(1);  CM(5, 2); ZF3(1);  CM(5, 1); ZF4(1);  CM(6, 1); ZF5(1);  CM(6, 0); ZST(0);  CM(7, 0); ZF0(4);
        DG(4);
        ZF1(4); ZF2(4); ZF3(4); ZF4(4); ZF5(4); ZF0(5); ZF1(5); ZF2(5); ZF3(5); ZF4(5); ZF5(5); ZST(2); ZF0(6); ZF1(6); ZF2(6); ZF3(6); ZF4(6); ZF5(6); ZF0(7); ZF1(7); ZF2(7); ZF3(7); ZF4(7); ZF5(7); ZST(3);
        DG(10);
        __builtin_amdgcn_s_setprio(3);
        while ((unsigned)__builtin_amdgcn_readfirstlane(*(volatile LAS unsigned*)(fl + C_OL)) < ZL_GATE) __builtin_amdgcn_s_sleep(4);
        CFENCE();
        const int arow = idx * 64 + (cw >> 1) * 32 + rg * 4, acol = xcd * 256 + (cw & 1) * 128 + cq * 16;
        u32x4 qr[4];
#pragma unroll
        for (int i = 0; i < 4; ++i) qr[i] = gld128((unsigned)(arow * 2048 + acol), Aq + i * 2048);
        {
            const lds_ptr tr = cv + CV_KV + 480;
            u32x4 t[4];
            const int ck = (cw >> 1) * 4 + ((lane ^ ((lane >> 5) * 2)) & 3);
            const unsigned zb = (unsigned)WS_ZT + (unsigned)(xcd * 128 + (cw & 1) * 64 + (lane >> 4) * 4) * 4096u + (idx >> 3) * 1024 +
                                ((idx & 3) * 4 + ((idx >> 2) & 1) * 16 + (ck >> 1) + (ck & 1) * 32) * 16;
#pragma unroll 1
            for (unsigned spins = 0; spins < (1u << 20); ++spins) {
#pragma unroll
                for (int i = 0; i < 4; ++i) t[i] = ld_sc1s(rs, zb + ((((lane >> 2) & 3) ^ i) << 12), i * 65536);
                if (!missing(mx4(mx4(mx8(t[0], t[1]), t[2]), t[3]))) break;
                __builtin_amdgcn_s_sleep(Z_SLEEP);
            }
            DG(13);
            __builtin_amdgcn_s_setprio(ZE_PRIO);
#pragma unroll
            for (int i = 0; i < 4; ++i) *(LAS u32x4*)(tr + i * 1024 + lane * 16) = t[i];
#pragma unroll
            for (int i = 0; i < 4; ++i) asm volatile("" : "+v"(qr[i]));
            u32x2 zc[8];
#pragma unroll
            for (int c = 0; c < 8; ++c)
                zc[c] = *(const LAS u32x2*)(tr + cq * 512 + (c >> 2) * 256 + ((((c & 3) * 4 + (rg >> 1)) ^ (cq * 2)) << 4) + (rg & 1) * 8);
#pragma unroll
            for (int i = 0; i < 4; ++i) {
                u32x4 o;
#pragma unroll
                for (int c = 0; c < 4; ++c) {
                    const unsigned z0 = zc[2 * c][i >> 1], z1 = zc[2 * c + 1][i >> 1];
                    o[c] = cvtpk(mul_vv(bflo(qr[i][c]), (i & 1) ? bfhi(z0) : bflo(z0)), mul_vv(bfhi(qr[i][c]), (i & 1) ? bfhi(z1) : bflo(z1)));
                }
                __builtin_amdgcn_raw_buffer_store_b128(o, ro, arow * 4096 + acol, i * 4096, OUT_AUX);
            }
        }
        asm volatile(".Lcp_k2z:");
        DG_END(14);
        return;
    }

    const int half = w >> 2;
    const int TA = qb * 8 + (w & 3) * 2 + half, TB = TA ^ 1;
    bf16x8 sel;
    {
        const short one = (((lane >> 2) & 1) == ((lane >> 4) & 1) && (lane & 11) == 0) ? (short)0x3f80 : (short)0;
#pragma unroll
        for (int i = 0; i < 8; ++i) sel[i] = SPM ? ((lane & 15) == (i & 4) ? (short)0x3f80 : (short)0) : one;
    }
    int sidx = (int)0xdddddddd;
#define LO8(x) __builtin_bit_cast(bfx16_t, __builtin_shufflevector(x, x, 0, 1, 2, 3, 4, 5, 6, 7))
#define HI8(x) __builtin_bit_cast(bfx16_t, __builtin_shufflevector(x, x, 8, 9, 10, 11, 12, 13, 14, 15))
#define SPV(v, p, o) __builtin_amdgcn_smfmac_f32_32x32x32_bf16(__builtin_bit_cast(bfx8_t, v), p, o, sidx, 0, 0)
#define SSUM(p, l) __builtin_amdgcn_smfmac_f32_16x16x64_bf16(__builtin_bit_cast(bfx8_t, sel), p, l, sidx, 0, 0)
    f32x16 oA = f32x16{}, oB = f32x16{};
    f32x4 lA = f32x4{}, lB = f32x4{};
    f32x16 negm, sA, sB;
    lds_ptr lp = lds + half * 16384 + lane * 16;
    bf16x8 k0[2], k1[2], v0[2], v1[2];
    u32x4 p0, p1 = {0u, 0u, 0u, 0u};
#define EX(X, i) X[i] = __builtin_amdgcn_exp2f(X[i])
    lds_ptr qpa = lds + K2_SF + (TA & 7) * 2048 + lane * 16, qpb = lds + K2_SF + (TB & 7) * 2048 + lane * 16;
    asm volatile("" : "+v"(qpa), "+v"(qpb), "+v"(lp), "+v"(sel), "+v"(oA), "+v"(oB), "+v"(lA), "+v"(lB), "+v"(sidx));
    WAIT_BAR(0);
    DG(3);
    const bf16x8 qa0 = lds_rd128(qpa), qb0 = lds_rd128(qpb);
    const bf16x8 x0 = lds_rd128(lds + lane * 16), x1 = lds_rd128(lds + lane * 16 + 1024);
    const bf16x8 qa1 = lds_rd128(qpa + 1024), qb1 = lds_rd128(qpb + 1024);
    {
        k0[0] = lds_rd128(lp); k1[0] = lds_rd128(lp + 1024);
        v0[0] = lds_rd128(lp + 2048); v1[0] = lds_rd128(lp + 3072);
        sA = MFMA32(x0, qa0, f32x16{});
        sB = MFMA32(x0, qb0, f32x16{});
        sA = MFMA32(x1, qa1, sA);
        sB = MFMA32(x1, qb1, sB);
        float m = fmx(sA[0], sA[1]), m2 = fmx(sB[0], sB[1]);
#pragma unroll
        for (int i = 2; i < 16; i += 2) m = fmx(fmx(m, sA[i]), sA[i + 1]);
#pragma unroll
        for (int i = 2; i < 16; i += 2) m2 = fmx(fmx(m2, sB[i]), sB[i + 1]);
        m = fmx(m, m2);
        auto rr = __builtin_amdgcn_permlane32_swap(__float_as_uint(m), __float_as_uint(m), false, false);
        m = fmx(__uint_as_float(rr[0]), __uint_as_float(rr[1]));
#pragma unroll
        for (int i = 0; i < 16; ++i) negm[i] = -m;
        sA = MFMA32(k0[0], qa0, negm);
        sA = MFMA32(k1[0], qa1, sA);
        v1[1] = v1[0];
#pragma unroll
        for (int i = 8; i < 16; ++i) sB[i] = 0.f;
        asm volatile("" : "+v"(sB));
        SBAR();
        RAW_BAR();
        SBAR();
        EX(sA, 0); EX(sA, 1);
    }
    DG(10);
#define SUM16(l, p) asm volatile("v_mfma_f32_16x16x32_bf16 %0, %1, %2, %0" : "+v"(l) : "v"(sel), "v"(p))
#define HALF(C, oC, lC, N, qx, qy, ka, kb, oP, lP, vprev, vcur0, PRE) do { \
        oP = SPV(vprev, HI8(N), oP); SBAR(); \
        PRE; EX(C, 2); EX(C, 3); EX(C, 4); SBAR(); \
        lP = SSUM(HI8(N), lP); SBAR(); \
        EX(C, 5); SBAR(); \
        N = MFMA32(ka, qx, negm); SBAR(); \
        EX(C, 6); EX(C, 7); EX(C, 8); SBAR(); \
        N = MFMA32(kb, qy, N); SBAR(); \
        EX(C, 9); EX(C, 10); EX(C, 11); SBAR(); \
        oC = SPV(vcur0, LO8(C), oC); SBAR(); \
        EX(C, 12); EX(C, 13); EX(C, 14); SBAR(); \
        lC = SSUM(LO8(C), lC); SBAR(); \
        EX(C, 15); EX(N, 0); EX(N, 1); SBAR(); \
    } while (0)
#define RD4(Y, nx) do { v1[Y] = lds_rd128((nx) + 3072); v0[Y] = lds_rd128((nx) + 2048); \
        k1[Y] = lds_rd128((nx) + 1024); k0[Y] = lds_rd128(nx); } while (0)
#define KT(X, nx, PRE0) do { \
        HALF(sA, oA, lA, sB, qb0, qb1, k0[X], k1[X], oB, lB, v1[X ^ 1], v0[X], PRE0; RD4(X ^ 1, nx)); \
        HALF(sB, oB, lB, sA, qa0, qa1, k0[X ^ 1], k1[X ^ 1], oA, lA, v1[X], v0[X], (void)0); \
    } while (0)
#define RING_BAR() do { unsigned long long sv_; \
        asm volatile("s_cmp_eq_u32 %1, 7\n\ts_cbranch_scc1 1f\n\ts_barrier\n1:\n\ts_mov_b64 %0, exec\n\ts_mov_b64 exec, 1\n\tds_add_u32 %2, %3\n\ts_mov_b64 exec, %0" \
                     : "=&s"(sv_) : "s"(st), "v"(fl + C_LD), "v"(1u) : "memory", "scc"); } while (0)
    SBAR();
#pragma unroll 1
    for (int st = 0; st < 8; ++st) {
        const lds_ptr cur = lp + (st & 1) * K2_SLOT;
        const lds_ptr nxt = lp + ((st + 1) & 1) * K2_SLOT;
        PRIO(2);
        KT(0, cur + 4096, (void)0);
        PRIO(1);
        KT(1, cur + 8192, (void)0);
        PRIO(0);
        KT(0, cur + 12288, (void)0);
        PRIO(3);
        KT(1, nxt, RAW_BAR(); SBAR());
    }
    oB = SPV(v1[1], HI8(sB), oB);
    SBAR();
    lB = SSUM(HI8(sB), lB);
    SBAR();
    DG(4);
    {
        const lds_ptr xo = lds + K2_SLOT + w * 4096 + lane * 16;
        *(LAS float*)(lds + K2_LX + w * 256 + lane * 4) = lB[0];
#pragma unroll
        for (int i = 0; i < 4; ++i) *(LAS f32x4*)(xo + i * 1024) = f32x4{oB[4 * i], oB[4 * i + 1], oB[4 * i + 2], oB[4 * i + 3]};
        lds_add(fl + F_X + w * 4, lane);
        lds_add(fl + C_END + half * 4, lane);
        lds_wait_ge(fl + F_X + (w ^ 4) * 4, 1u);
        const lds_ptr xi = lds + K2_SLOT + (w ^ 4) * 4096 + lane * 16;
        f32x4 t[4];
        const float lx = *(const LAS float*)(lds + K2_LX + (w ^ 4) * 256 + (lane & 31) * 4);
        { auto rr = __builtin_amdgcn_permlane32_swap(__float_as_uint(lA[0]), __float_as_uint(lA[0]), false, false); lA[0] = __uint_as_float(rr[0]); }
#pragma unroll
        for (int i = 0; i < 4; ++i) t[i] = *(const LAS f32x4*)(xi + i * 1024);
        SBAR();
        lA[0] += lx;
#pragma unroll
        for (int i = 0; i < 16; ++i) { float o = oA[i]; acc_vv(o, t[i >> 2][i & 3]); oA[i] = o; }
    }
    DG(9);
    const float l = lA[0];
    u32x4 on0, on1;
    auto finish = [&](float ls) {
        float rl = __builtin_amdgcn_rcpf(ls);
        asm volatile("s_nop 1" : "+v"(rl));
        f32x16 o;
#pragma unroll
        for (int i = 0; i < 16; ++i) o[i] = mul_vv(oA[i], rl);
        on0 = nz4(pack8n(o, 0)); on1 = nz4(pack8n(o, 1));
    };
    finish(l);
    {
        float t0, t1;
        asm("v_mul_f32_e32 %0, 0, %2\n\tv_mul_f32_e32 %1, 0, %3\n\tv_fmac_f32_e32 %0, 0, %4\n\tv_fmac_f32_e32 %1, 0, %5\n\tv_fmac_f32_e32 %0, 0, %6\n\tv_fmac_f32_e32 %1, 0, %7\n\tv_fmac_f32_e32 %0, 0, %8\n\tv_fmac_f32_e32 %1, 0, %9\n\t"
            "v_fmac_f32_e32 %0, 0, %10\n\tv_fmac_f32_e32 %1, 0, %11\n\tv_fmac_f32_e32 %0, 0, %12\n\tv_fmac_f32_e32 %1, 0, %13\n\tv_fmac_f32_e32 %0, 0, %14\n\tv_fmac_f32_e32 %1, 0, %15\n\tv_fmac_f32_e32 %0, 0, %16\n\tv_fmac_f32_e32 %1, 0, %17"
            : "=&v"(t0), "=&v"(t1) : "v"(oA[0]), "v"(oA[1]), "v"(oA[2]), "v"(oA[3]), "v"(oA[4]), "v"(oA[5]), "v"(oA[6]), "v"(oA[7]), "v"(oA[8]), "v"(oA[9]), "v"(oA[10]), "v"(oA[11]), "v"(oA[12]), "v"(oA[13]), "v"(oA[14]), "v"(oA[15]));
        const bool ok = (l <= 1.0e30f) && (l >= 1.0e-18f) && (t0 + t1 == 0.f);
        if (__builtin_expect(__any(!ok), 0)) {
            float m = -INFINITY, ls = 0.f;
            oA = f32x16{};
#pragma unroll 1
            for (int t = 0; t < 64; ++t) {
                const char* g = kvg + t * 4096 + lane * 16;
                const bf16x8 a0 = *(const bf16x8*)g, a1 = *(const bf16x8*)(g + 1024);
                const bf16x8 b0 = *(const bf16x8*)(g + 2048), b1 = *(const bf16x8*)(g + 3072);
                f32x16 s = MFMA32(a0, qa0, f32x16{});
                s = MFMA32(a1, qa1, s);
                float tm = s[0];
#pragma unroll
                for (int i = 1; i < 16; ++i) tm = __builtin_fmaxf(tm, s[i]);
                auto rr = __builtin_amdgcn_permlane32_swap(__float_as_uint(tm), __float_as_uint(tm), false, false);
                tm = __builtin_fmaxf(__uint_as_float(rr[0]), __uint_as_float(rr[1]));
                const float mn = __builtin_fmaxf(m, tm);
                const float alpha = __builtin_amdgcn_exp2f(m - mn);
                m = mn;
                float ps = 0.f;
#pragma unroll
                for (int i = 0; i < 16; ++i) { s[i] = bfhi(__float_as_uint(__builtin_amdgcn_exp2f(s[i] - mn))); ps += s[i]; oA[i] *= alpha; }
                ls = ls * alpha + ps;
                oA = SPV(b0, LO8(s), oA);
                oA = SPV(b1, HI8(s), oA);
            }
            auto rr = __builtin_amdgcn_permlane32_swap(__float_as_uint(ls), __float_as_uint(ls), false, false);
            asm volatile("s_nop 7\n\ts_nop 7" : "+v"(oA));
            finish(__uint_as_float(rr[0]) + __uint_as_float(rr[1]));
        }
    }
    u32x4 w3[4];
    {
        const unsigned og = (unsigned)WS_O + (unsigned)(TA * 32 + h) * 2048u + lane * 16;
        st_sc1(on0, rs, og);
        st_sc1(on1, rs, og + 1024);
        lds_add(fl + C_S1, lane);
        const char* s2 = wsrc(half ^ 1, 32 + (w & 3) * 4);
        const char* s3 = wsrc(half, 48 + (w & 3) * 4);
        lds_wait_ge(fl + C_S1, 12u);
        DG(5);
#pragma unroll
        for (int i = 0; i < 4; ++i)
            glds16p<K2_WNT>(wvoff, s2 + i * 16384, lds0 + K2_SLOT + ((w ^ 4) * 4 + i) * 1024);
#pragma unroll
        for (int i = 0; i < 4; ++i) w3[i] = gld128(wvoff, s3 + i * 16384);
    }
    {
        const int half = w & 1;
        const int T3 = tt * 4 + (w >> 1);
        const unsigned og = (unsigned)WS_O + (unsigned)T3 * 65536u + half * 32768 + lane * 16;
        const int r = lane & 31, hh = lane >> 5;
        const int g = lane >> 4, q = (lane & 15) >> 2, p = lane & 3;
        int woff[2];
#pragma unroll
        for (int n = 0; n < 2; ++n) {
            const int c = 4 * (n ^ half) + 2 * (p & 1) + (g & 1);
            woff[n] = (4 * hh + q) * 128 + ((c ^ ((q >> 1) << 2)) << 4) + (p >> 1) * 8;
        }
        f32x16 acc[2] = {f32x16{}, f32x16{}};
        lds_ptr wl[3][2] = {{lds + half * 16384 + woff[0], lds + half * 16384 + woff[1]},
                            {lds + K2_SF + half * 16384 + woff[0], lds + K2_SF + half * 16384 + woff[1]},
                            {lds + K2_CV + half * 2 * CV_BYTES + woff[0], lds + K2_CV + half * 2 * CV_BYTES + woff[1]}};
        asm volatile("" : "+v"(wl[0][0]), "+v"(wl[0][1]), "+v"(wl[1][0]), "+v"(wl[1][1]), "+v"(wl[2][0]), "+v"(wl[2][1]));
#define MS(m) ((m) < 64 - 2 * TL ? (m) >> 1 : 32 - TL + ((m) - (64 - 2 * TL)) % TL)
#define MN(m) ((m) < 64 - 2 * TL ? (m) & 1 : (m) < 64 - TL)
        constexpr int WR = 2 * WD + 2;
        s16x4 wa[WR], wb[WR];
#define LDW(m) do { const int s_ = MS(m), f_ = s_ < 8 ? 1 : s_ < 24 ? 0 : 2; \
            const int o_ = f_ == 2 ? ((s_ >> 2) & 1) * CV_BYTES + (s_ & 3) * 2048 : (f_ ? 0 : ((s_ >> 3) - 1) * K2_SLOT) + (s_ & 7) * 2048; \
            wa[(m) % WR] = lds_tr(wl[f_][MN(m)] + o_); wb[(m) % WR] = lds_tr(wl[f_][MN(m)] + o_ + 1024); SBAR(); } while (0)
#pragma unroll
        for (int m = 0; m < 2 * WD; ++m) LDW(m);
        u32x4 of[OWIN];
#define LDO(i) do { of[(i) % OWIN] = ld_sc1s(rs, og, (((i) + krot) & 31) * 1024); SBAR(); } while (0)
        for (unsigned spins = 0; spins < (1u << 20); ++spins) {
#pragma unroll
            for (int i = 0; i < OW0; ++i) LDO(i);
            unsigned m = mx8(of[0], of[1]);
#pragma unroll
            for (int i = 2; i < OW0; ++i) m = mx4(m, of[i]);
            if (!missing(m)) break;
            DGC(12);
        }
        lds_add(fl + C_S2, lane);
#pragma unroll
        for (int i = OW0; i < OWIN; ++i) LDO(i);
        DG(6);
        __builtin_amdgcn_s_setprio(P3_PRIO);
        SBAR();
        unsigned fv = 0;
#define FLAG_RD(a) do { fv = *(volatile LAS unsigned*)(a); SBAR(); } while (0)
#define FLAG_CHK(a, n) do { if (__builtin_expect((unsigned)__builtin_amdgcn_readfirstlane(fv) < (n), 0)) lds_wait_ge(a, n); CFENCE(); SBAR(); } while (0)
        lds_ptr xo = lds + K2_SF + w * 4096 + lane * 16, xi = lds + K2_SF + (w ^ 1) * 4096 + lane * 16;
        int bo = (T3 * 32 + (lane & 15)) * 4096 + 2048 + nt * 128 + half * 64 + g * 16;
        lds_ptr w3d = lds + K2_CV + (w >> 1) * CV_BYTES + (w & 1) * 4096 + lane * 16;
        asm volatile("" : "+v"(xo), "+v"(xi), "+v"(bo), "+v"(w3d));
        f32x4 xt[4];
        constexpr int M3 = TL > 8 ? 64 - 2 * TL + TL - 8 - 2 * WD : 48 - 2 * WD;
        sfor(std::make_integer_sequence<int, 64>{}, [&](auto mc) __attribute__((always_inline)) {
            constexpr int m = decltype(mc)::value, s = MS(m), n = MN(m);
            constexpr bool first = m < 64 - 2 * TL ? n == 0 : n == 1;
            constexpr bool last = m < 64 - 2 * TL ? n == 1 : n == 0;
            acc[n] = MFMA32(cat8(wa[m % WR], wb[m % WR]), PB(of[s % OWIN]), acc[n]);
            SBAR();
            if constexpr (m == 32 - 2 * WD) FLAG_CHK(fl + C_S2, 8u);
            if constexpr (m == M3) FLAG_CHK(fl + C_S3, 8u);
            if constexpr (m + 2 * WD < 64) LDW(m + 2 * WD);
            if constexpr (first && (s & 1) && s + 1 >= OW0 && s + 1 < 32) {
                if (__builtin_expect(missing(mx8(of[(s + 1) % OWIN], of[(s + 2) % OWIN])), 0)) {
#pragma unroll 1
                    for (unsigned spins = 0; spins < (1u << 20); ++spins) {
                        __builtin_amdgcn_s_sleep(2);
                        LDO(s + 1); LDO(s + 2);
                        DGC(13);
                        if (!missing(mx8(of[(s + 1) % OWIN], of[(s + 2) % OWIN]))) break;
                    }
                }
            }
            if constexpr (last && s + OWIN < 32) LDO(s + OWIN);
            if constexpr (m == 15 - 2 * WD) lds_add(fl + C_U0, lane);
            if constexpr (m == 29 - 2 * WD) FLAG_RD(fl + C_S2);
            if constexpr (m == W3M - 3) FLAG_RD(fl + C_HF);
            if constexpr (m == W3M) FLAG_CHK(fl + C_HF, 4u);
            if constexpr (m >= W3M && m < W3M + 8 && ((m - W3M) & 1) == 0) {
                asm volatile("" : "+v"(w3[((m - W3M) >> 1) & 3]));
                *(LAS u32x4*)(w3d + (((m - W3M) >> 1) & 3) * 1024) = w3[((m - W3M) >> 1) & 3];
            }
            if constexpr (m == W3M + 6) lds_add(fl + C_S3, lane);
            if constexpr (m == M3 - 3) FLAG_RD(fl + C_S3);
            if constexpr (m == 64 - TL - 3) FLAG_RD(fl + C_U0);
            if constexpr (m == 64 - TL) FLAG_CHK(fl + C_U0, 8u);
            if constexpr (m == 64 - TL + 1 || m == 64 - TL + 2) {
                constexpr int i = 2 * (m - (64 - TL + 1));
                *(LAS f32x4*)(xo + i * 1024) = f32x4{acc[1][4 * i], acc[1][4 * i + 1], acc[1][4 * i + 2], acc[1][4 * i + 3]};
                *(LAS f32x4*)(xo + i * 1024 + 1024) = f32x4{acc[1][4 * i + 4], acc[1][4 * i + 5], acc[1][4 * i + 6], acc[1][4 * i + 7]};
            }
            if constexpr (m == 64 - TL + 2) lds_add(fl + F_R + w * 4, lane);
            if constexpr (m == XR_AT - 2) FLAG_RD(fl + F_R + (w ^ 1) * 4);
            if constexpr (m == XR_AT) {
                FLAG_CHK(fl + F_R + (w ^ 1) * 4, 1u);
#pragma unroll
                for (int i = 0; i < 4; ++i) xt[i] = *(const LAS f32x4*)(xi + i * 1024);
            }
            if constexpr (ZL_GATE && last && s + OWIN == ZL_AT) lds_add(fl + C_OL, lane);
            if constexpr (m == 2 * P3_T1 + 1) __builtin_amdgcn_s_setprio(P3_PRIO - 1);
            SBAR();
            if constexpr (m == 31) DG(11);
            if constexpr (m == 47) DG(14);
        });
        asm volatile("s_nop 11" : "+v"(acc[0]));
        DG(7);
#pragma unroll
        for (int i = 0; i < 16; ++i) { float o = xt[i >> 2][i & 3]; acc_vv(o, acc[0][i]); acc[1][i] = o; }
        u32x4 pa = pack8n(acc[1], 0), pb = pack8n(acc[1], 1);
#pragma unroll
        for (int i = 0; i < 4; ++i) {
            auto sw = __builtin_amdgcn_permlane16_swap(pa[i], pb[i], false, false);
            pa[i] = sw[0]; pb[i] = sw[1];
        }
        asm volatile("" :: "v"(acc[0]));
        __builtin_amdgcn_raw_buffer_store_b128(pa, ro, bo, 0, OUT_AUX);
        __builtin_amdgcn_raw_buffer_store_b128(pb, ro, bo, 65536, OUT_AUX);
    }
    if (b == HB && w == HW) {
        SBAR(); unsigned long long tb0; asm volatile("s_memtime %0\n\ts_waitcnt lgkmcnt(0)" : "=s"(tb0) :: "memory"); SBAR();

        DRAIN();
        SBAR(); unsigned long long tb1; asm volatile("s_memtime %0\n\ts_waitcnt lgkmcnt(0)" : "=s"(tb1) :: "memory"); SBAR();

        const unsigned hs7 = (unsigned)__builtin_amdgcn_s_memrealtime();
        const unsigned* hp_ = (const unsigned*)(ws + (30u << 20));
        const unsigned kad = __builtin_amdgcn_readfirstlane(hp_[0]), kae = __builtin_amdgcn_readfirstlane(hp_[1]), kbd = (unsigned)(tb1 - tb0);
        unsigned iv = (HQ);
        iv = iv < 20000u ? iv : 20000u;
        while ((unsigned)__builtin_amdgcn_s_memrealtime() - hs7 < iv) __builtin_amdgcn_s_sleep(1);
    }
    DG_END(8);
}

extern "C" void kernel_launch(void* const* d_in, const int* in_sizes, int n_in,
                              void* d_out, int out_size, void* d_ws, size_t ws_size,
                              hipStream_t stream) {
    const void* A_q = d_in[0];
    const void* A_k = d_in[1];
    const void* A_v = d_in[2];
    const void* A_shift = d_in[3];
    const void* A_ssmk = d_in[4];
    const void* A_D = d_in[5];
    const void* B_x = d_in[6];
    const void* B_wqkv = d_in[7];
    const void* B_wo = d_in[8];
    char* ws = (char*)d_ws;
    k1_qkv<<<256, 512, 0, stream>>>((const char*)B_x, (const char*)B_wqkv, ws);
    k2_attn<<<256, 768, 0, stream>>>(ws, (const char*)A_q, (const char*)A_k, (const char*)A_v, (const char*)A_shift,
                                     (const char*)A_ssmk, (const unsigned*)A_D, (const char*)B_wo, (char*)d_out);
}
```

```cpp
#include <hip/hip_runtime.h>
#include <hip/hip_bf16.h>
#include <stdint.h>
#include <utility>

typedef short bf16x8 __attribute__((ext_vector_type(8)));
typedef short s16x4 __attribute__((ext_vector_type(4)));
typedef float f32x16 __attribute__((ext_vector_type(16)));
typedef float f32x4 __attribute__((ext_vector_type(4)));
typedef float f32x2 __attribute__((ext_vector_type(2)));
typedef unsigned u32x4 __attribute__((ext_vector_type(4)));
typedef unsigned u32x2 __attribute__((ext_vector_type(2)));
typedef __bf16 bf16x2_t __attribute__((ext_vector_type(2)));

#define LAS __attribute__((address_space(3)))
typedef LAS char* lds_ptr;
#define DI __device__ __forceinline__
#define SBAR() __builtin_amdgcn_sched_barrier(0)
#define MFMA32(a, b, c) __builtin_amdgcn_mfma_f32_32x32x16_bf16((a), (b), (c), 0, 0, 0)
#define MFMA16(a, b, c) __builtin_amdgcn_mfma_f32_16x16x32_bf16((a), (b), (c), 0, 0, 0)
typedef __bf16 bfx8_t __attribute__((ext_vector_type(8)));
typedef __bf16 bfx16_t __attribute__((ext_vector_type(16)));
typedef float f32x8 __attribute__((ext_vector_type(8)));
#define SPM 1
#define WAIT_BAR(N) asm volatile("s_waitcnt vmcnt(" #N ") lgkmcnt(0)\n\ts_barrier" ::: "memory")
#define LGKM_BAR() asm volatile("s_waitcnt lgkmcnt(0)\n\ts_barrier" ::: "memory")

#define DG_DECL(K)
#define DG(i)
#define DG_END(i)
#define DGC(i)

#define DGV(i)
constexpr int S_LEN = 2048, DM = 1024, NH = 32;

#define KQ 1
#define OQ 0
#define QT(h, T) (KQ ? (h) * 64 + (T) : (T) * 32 + (h))
constexpr size_t WS_Q = 0;
constexpr size_t WS_KV = 4u << 20;
constexpr size_t WS_O = OQ ? WS_Q : 12u << 20;
constexpr size_t WS_KVR = 16u << 20;
#define ZK 0
constexpr size_t WS_ZT = ZK ? WS_KVR : 20u << 20;
constexpr size_t WS_CNT = 24u << 20;

template <int... I, class F> DI void sfor(std::integer_sequence<int, I...>, F&& f) { (f(std::integral_constant<int, I>{}), ...); }
DI unsigned cvtpk(float lo, float hi) {
    f32x2 v = {lo, hi};
    return __builtin_bit_cast(unsigned, __builtin_convertvector(v, bf16x2_t));
}
DI float bflo(unsigned u) { return __uint_as_float(u << 16); }
DI float bfhi(unsigned u) { return __uint_as_float(u & 0xffff0000u); }

DI void glds16(unsigned voff, const void* sbase, unsigned lds_dst) {
    unsigned sv;
    asm volatile("s_mov_b32 %0, m0\n\ts_mov_b32 m0, %3\n\ts_nop 0\n\tglobal_load_lds_dwordx4 %1, %2\n\ts_mov_b32 m0, %0"
                 : "=&s"(sv) : "v"(voff), "s"(sbase), "s"(lds_dst) : "memory");
}
template <int NT> DI void glds16p(unsigned voff, const void* sbase, unsigned lds_dst) {
    if constexpr (NT) {
        unsigned sv;
        asm volatile("s_mov_b32 %0, m0\n\ts_mov_b32 m0, %3\n\ts_nop 0\n\tglobal_load_lds_dwordx4 %1, %2 nt\n\ts_mov_b32 m0, %0"
                     : "=&s"(sv) : "v"(voff), "s"(sbase), "s"(lds_dst) : "memory");
    } else glds16(voff, sbase, lds_dst);
}
#define K1_XNT 0
#define K1_WNT 0
#define K2_SNT 1
#define K2_QNT 0
#define K2_WNT 0
#define OUT_AUX 16
#define K1_SAUX 16
DI bf16x8 lds_rd128(lds_ptr p) { return *(const LAS bf16x8*)p; }
DI s16x4 lds_tr(lds_ptr p) {
    return __builtin_bit_cast(s16x4, __builtin_amdgcn_ds_read_tr16_b64_v4i16((LAS s16x4*)p));
}
DI bf16x8 cat8(s16x4 a, s16x4 b) { return __builtin_shufflevector(a, b, 0, 1, 2, 3, 4, 5, 6, 7); }
DI u32x4 pack8(const f32x16& x, int s, float sc) {
    u32x4 p;
    p[0] = cvtpk(x[8 * s + 0] * sc, x[8 * s + 1] * sc);
    p[1] = cvtpk(x[8 * s + 2] * sc, x[8 * s + 3] * sc);
    p[2] = cvtpk(x[8 * s + 4] * sc, x[8 * s + 5] * sc);
    p[3] = cvtpk(x[8 * s + 6] * sc, x[8 * s + 7] * sc);
    return p;
}
DI u32x4 pack8n(const f32x16& x, int s) {
    u32x4 p;
    p[0] = cvtpk(x[8 * s + 0], x[8 * s + 1]);
    p[1] = cvtpk(x[8 * s + 2], x[8 * s + 3]);
    p[2] = cvtpk(x[8 * s + 4], x[8 * s + 5]);
    p[3] = cvtpk(x[8 * s + 6], x[8 * s + 7]);
    return p;
}
DI void rnd2(float& a, float& b) { unsigned u = cvtpk(a, b); a = bflo(u); b = bfhi(u); }

#define CONV_PACE()
#define RAW_BAR() asm volatile("s_barrier" ::: "memory")
DI unsigned gld32(unsigned voff, const void* sbase) {
    unsigned r;
    asm volatile("global_load_dword %0, %1, %2" : "=v"(r) : "v"(voff), "s"(sbase) : "memory");
    return r;
}
DI u32x2 gld64(unsigned voff, const void* sbase) {
    u32x2 r;
    asm volatile("global_load_dwordx2 %0, %1, %2" : "=v"(r) : "v"(voff), "s"(sbase) : "memory");
    return r;
}
DI float mul_sv(float s, float v) { float r; asm("v_mul_f32_e32 %0, %1, %2" : "=v"(r) : "s"(s), "v"(v)); return r; }
DI float mul_vv(float a, float b) { float r; asm("v_mul_f32_e32 %0, %1, %2" : "=v"(r) : "v"(a), "v"(b)); return r; }
DI float add_vv(float a, float b) { float r; asm("v_add_f32_e32 %0, %1, %2" : "=v"(r) : "v"(a), "v"(b)); return r; }
DI void acc_vv(float& a, float b) { asm("v_add_f32_e32 %0, %0, %1" : "+v"(a) : "v"(b)); }
DI void fmac_vv(float& acc, float a, float b) { asm("v_fmac_f32_e32 %0, %1, %2" : "+v"(acc) : "v"(a), "v"(b)); }
DI void fmac_sv(float& acc, float s, float v) { asm("v_fmac_f32_e32 %0, %1, %2" : "+v"(acc) : "s"(s), "v"(v)); }
typedef __attribute__((address_space(1))) unsigned gu32;
#define RLX_AGENT __ATOMIC_RELAXED, __HIP_MEMORY_SCOPE_AGENT
#define CFENCE() asm volatile("" ::: "memory")
#define DRAIN() asm volatile("s_waitcnt vmcnt(0)" ::: "memory")
constexpr int AUX_SC1 = 16;
DI u32x4 ld_sc1(__amdgpu_buffer_rsrc_t rs, unsigned off) { return __builtin_amdgcn_raw_buffer_load_b128(rs, (int)off, 0, AUX_SC1); }
DI u32x4 ld_sc1s(__amdgpu_buffer_rsrc_t rs, unsigned off, int soff) { return __builtin_amdgcn_raw_buffer_load_b128(rs, (int)off, soff, AUX_SC1); }
DI void st_sc1(u32x4 v, __amdgpu_buffer_rsrc_t rs, unsigned off) { __builtin_amdgcn_raw_buffer_store_b128(v, rs, (int)off, 0, AUX_SC1); }
constexpr unsigned EMPTY = 0xffffffffu;
DI unsigned nz(unsigned x) { return x < EMPTY - 1u ? x : EMPTY - 1u; }
DI u32x4 nz4(u32x4 v) { return u32x4{nz(v[0]), nz(v[1]), nz(v[2]), nz(v[3])}; }
DI float fmx(float a, float b) { return __builtin_elementwise_maximum(a, b); }
DI unsigned umax(unsigned a, unsigned b) { return a > b ? a : b; }
DI unsigned umax3(unsigned a, unsigned b, unsigned c) { return umax(umax(a, b), c); }
DI unsigned mx4(unsigned m, u32x4 v) { return umax3(umax3(m, v[0], v[1]), v[2], v[3]); }
DI unsigned mx8(u32x4 a, u32x4 b) { return umax(umax3(umax3(umax3(a[0], a[1], a[2]), a[3], b[0]), b[1], b[2]), b[3]); }
DI bool missing(unsigned m) { return __builtin_amdgcn_ballot_w64(m == EMPTY) != 0; }
DI unsigned lds_add_rtn(lds_ptr p, int lane) {
    unsigned old = 0;
    CFENCE();
    if (lane == 0) old = __hip_atomic_fetch_add((LAS unsigned*)p, 1u, __ATOMIC_RELAXED, __HIP_MEMORY_SCOPE_WORKGROUP);
    old = __builtin_amdgcn_readfirstlane(old);
    CFENCE();
    return old;
}
DI void lds_add(lds_ptr p, int lane) {
    unsigned long long sv;
    asm volatile("s_mov_b64 %0, exec\n\ts_mov_b64 exec, 1\n\tds_add_u32 %1, %2\n\ts_mov_b64 exec, %0"
                 : "=&s"(sv) : "v"(p), "v"(1u) : "memory");
}
DI void lds_wait_ge(lds_ptr p, unsigned v) {
    CFENCE();
    while ((unsigned)__builtin_amdgcn_readfirstlane(*(volatile LAS unsigned*)p) < v) __builtin_amdgcn_s_sleep(1);
    CFENCE();
}
DI void poll_ge(gu32* word, unsigned want) {
    CFENCE();
    for (unsigned spins = 0; spins < (1u << 22); ++spins) {
        if ((unsigned)__builtin_amdgcn_readfirstlane(__hip_atomic_load(word, RLX_AGENT)) >= want) break;
        __builtin_amdgcn_s_sleep(1);
    }
    __builtin_amdgcn_fence(__ATOMIC_ACQUIRE, "wavefront");
    CFENCE();
}
#define PB(x) __builtin_bit_cast(bf16x8, x)
#define PRIO(x) do { __builtin_amdgcn_s_setprio(x); SBAR(); } while (0)
DI u32x4 gld128(unsigned voff, const void* sbase) {
    u32x4 r;
    asm volatile("global_load_dwordx4 %0, %1, %2" : "=v"(r) : "v"(voff), "s"(sbase) : "memory");
    return r;
}

#define K1_UNR 15
#define K1_LA 5
#define K1_ROT ((tb & 3) * 4)
#define K1_PF 0
#define K1_PG 0
#define K1_TW 0
#define CPF 3
#define SKD 1
#define PK2 0
#define BK1 0
#define PKC 0
#define PKC_AT 6
#define PK2_AT 13
#define CPF_POS 1
#define PG1_AT 0
#define PG2_AT 2
#define PG3_AT 6
#define K1_WB(n) WAIT_BAR(1)
#define PF1_AT -1
#define PF1_LO 4
#define PF1_N 4
#define PF2_AT 1
#define PF2_LO 8
#define PF2_N 4
#define PF3_AT 5
#define PF3_LO 12
#define PF3_N 4
#define K1_KR ((kt + rot) & 15)
#define K1_ST(v, rs, off) __builtin_amdgcn_raw_buffer_store_b128(v, rs, (int)(off), 0, K1_SAUX)
constexpr int K1_STAGE = 40960, K1U = K1_UNR;
__global__ __launch_bounds__(512, 2) void k1_qkv(const char* __restrict__ X, const char* __restrict__ W,
                                                 char* __restrict__ ws, const char* __restrict__ Wo,
                                                 const char* __restrict__ Aq, const char* __restrict__ Ak,
                                                 const char* __restrict__ Av, const char* __restrict__ ssmk) {
    __shared__ __attribute__((aligned(16))) char lds_raw[4 * K1_STAGE];
    const lds_ptr lds = (lds_ptr)lds_raw;
    const unsigned lds0 = (unsigned)(uintptr_t)lds;
    const int tid = threadIdx.x, lane = tid & 63;
    const int w = __builtin_amdgcn_readfirstlane(tid >> 6);
    const int b = blockIdx.x, xcd = b & 7, idx = b >> 3;
    const int hp = (xcd >> 1) * 4 + (idx & 3), tb = (xcd & 1) * 8 + (idx >> 2);
    DG_DECL(0)

    const int lw = w & 3;
    const int rot = K1_ROT;
    unsigned voffX[4], voffW[2];
#pragma unroll
    for (int pp = 0; pp < 4; ++pp) {
        const int row = 32 * lw + 8 * pp + (lane >> 3);
        const int ch = (lane & 7) ^ ((row >> 1) & 7);
        voffX[pp] = (unsigned)(tb * 128 + row) * 2048u + ch * 16;
    }
#pragma unroll
    for (int pp = 0; pp < 2; ++pp) {
        const int krow = 16 * lw + 8 * pp + (lane >> 3);
        const int ch = (lane & 7) ^ (((lane >> 4) & 1) << 2);
        voffW[pp] = (unsigned)krow * 6144u + ch * 16;
    }
    auto issue = [&](int kt, int lo, int hi) {
        const int kr = K1_KR;
        const char* xb = X + kr * 128;
        const char* wb = W + (size_t)kr * (64 * 6144) + hp * 128;
        const unsigned st = lds0 + (kt & 3) * K1_STAGE;
#pragma unroll
        for (int pp = 0; pp < 4; ++pp)
            if (pp >= lo && pp < hi) glds16p<K1_XNT>(voffX[pp], xb, st + (4 * lw + pp) * 1024);
#pragma unroll
        for (int sec = 0; sec < 3; ++sec)
#pragma unroll
            for (int pp = 0; pp < 2; ++pp)
                if (4 + 2 * sec + pp >= lo && 4 + 2 * sec + pp < hi)
                    glds16p<K1_WNT>(voffW[pp], wb + sec * 2048, st + 16384 + sec * 8192 + (2 * lw + pp) * 1024);
    };
    DG(13);
    unsigned cpf;
    auto codepf = [&]() {
        unsigned long long pc; unsigned lo, hi;
        asm volatile("s_getpc_b64 %0\n.Lcp_k1a:\n\ts_mov_b32 %1, %3\n\ts_mov_b32 %2, %4" : "=s"(pc), "=s"(lo), "=s"(hi) : "n"(0), "n"(0));
        if (CPF & 1) lo = 0; else asm volatile("s_mov_b32 %0, .Lcp_k2a-.Lcp_k1a" : "=s"(lo));
        if (CPF & 2) asm volatile("s_mov_b32 %0, .Lcp_k2z-.Lcp_k1a" : "=s"(hi)); else asm volatile("s_mov_b32 %0, .Lcp_k1z-.Lcp_k1a" : "=s"(hi));
        unsigned off = lo + (unsigned)(idx + 32 * (lane & 7) + 256 * w) * 64u;
        off = off < hi ? off : hi;
        asm volatile("global_load_dword %0, %1, %2" : "=v"(cpf) : "v"(off), "s"(pc) : "memory");
    };
    if (w < 4) { issue(0, 0, 5); issue(1, 0, 5); codepf(); }
    else { issue(0, 5, 10); issue(1, 5, 10); }
    DG(14);
    int w2 = w;
    asm volatile("" : "+s"(w2));

    if (w2 >= 4) {
#define WAITN_BAR(N) asm volatile("s_waitcnt vmcnt(%0) lgkmcnt(0)\n\ts_barrier" :: "n"(N) : "memory")
        issue(2, 0, K1_LA);
        asm volatile("s_waitcnt vmcnt(%0)" :: "n"(5 + K1_LA) : "memory");
#pragma unroll 1
        for (int kt = 0; kt < 13; ++kt) {
            WAITN_BAR(10 + K1_LA);
            issue(kt + 2, K1_LA, 10);
            issue(kt + 3, 0, K1_LA);
        }
        WAITN_BAR(10 + K1_LA);
        issue(15, K1_LA, 10);
        WAIT_BAR(10);
        WAIT_BAR(0);
        if (lw == 0 && lane < 2) *(unsigned*)(ws + WS_CNT + b * 256 + lane * 128) = 0u;
        DG_END(5);
        return;
    }

    const int wm = w >> 1, wn = w & 1;
    const int r = lane & 31, hh = lane >> 5;
    const int g = lane >> 4, q = (lane & 15) >> 2, p = lane & 3;
    const int xoff0 = (64 * wm + r) * 128 + ((hh ^ ((r >> 1) & 7)) << 4);
    const int woff0 = 16384 + (8 * hh + q) * 128 + (((4 * wn + 2 * (g & 1) + (p >> 1)) ^ ((q >> 1) << 2)) << 4) + (p & 1) * 8;

    f32x16 acc[2][3];
#pragma unroll
    for (int m = 0; m < 2; ++m)
#pragma unroll
        for (int s = 0; s < 3; ++s) acc[m][s] = f32x16{};
    bf16x8 xf[2][2];
    s16x4 wl[2][3], wh[2][3];
#define K1_WF(f, sec) cat8(wl[f][sec], wh[f][sec])
#define K1_STEP(F, st, t, BAR_HERE) do { \
        const lds_ptr xp_ = (st) + (xoff0 ^ ((t) << 5)); const lds_ptr wp_ = (st) + woff0 + (t) * 2048; \
        acc[0][0] = MFMA32(K1_WF(F, 0), xf[F][0], acc[0][0]); SBAR(); \
        if (BAR_HERE) { K1_WB(BAR_HERE); SBAR(); } \
        xf[F ^ 1][0] = lds_rd128(xp_); wl[F ^ 1][0] = lds_tr(wp_); wh[F ^ 1][0] = lds_tr(wp_ + 512); SBAR(); \
        acc[0][1] = MFMA32(K1_WF(F, 1), xf[F][0], acc[0][1]); SBAR(); \
        wl[F ^ 1][1] = lds_tr(wp_ + 8192); wh[F ^ 1][1] = lds_tr(wp_ + 8192 + 512); SBAR(); \
        acc[0][2] = MFMA32(xf[F][0], K1_WF(F, 2), acc[0][2]); SBAR(); \
        wl[F ^ 1][2] = lds_tr(wp_ + 16384); wh[F ^ 1][2] = lds_tr(wp_ + 16384 + 512); xf[F ^ 1][1] = lds_rd128(xp_ + 4096); SBAR(); \
        acc[1][0] = MFMA32(K1_WF(F, 0), xf[F][1], acc[1][0]); SBAR(); \
        acc[1][1] = MFMA32(K1_WF(F, 1), xf[F][1], acc[1][1]); \
        acc[1][2] = MFMA32(xf[F][1], K1_WF(F, 2), acc[1][2]); SBAR(); \
    } while (0)

    WAIT_BAR(6);
    DG(3);
    {
        const lds_ptr xp_ = lds + xoff0; const lds_ptr wp_ = lds + woff0;
        xf[0][0] = lds_rd128(xp_); xf[0][1] = lds_rd128(xp_ + 4096);
#pragma unroll
        for (int sec = 0; sec < 3; ++sec) { wl[0][sec] = lds_tr(wp_ + sec * 8192); wh[0][sec] = lds_tr(wp_ + sec * 8192 + 512); }
    }
    SBAR();
    constexpr int NJ = 3, JOB[NJ] = {15, 31, 47};
    constexpr int KT0 = 16 - NJ / 3;
    bf16x8 jf[3][3];
    auto jrd = [&](auto gc, int part) __attribute__((always_inline)) {
        constexpr int g = decltype(gc)::value;
        if constexpr (g < 4 * NJ) {
            constexpr int pr = JOB[g >> 2] >> 4, tl = JOB[g >> 2] & 15, s = g & 3;
            const lds_ptr st = lds + (tl & 3) * K1_STAGE;
            const lds_ptr xp_ = st + (xoff0 ^ (s << 5)); const lds_ptr wp_ = st + woff0 + s * 2048;
            if (part == 0) {
                if constexpr (s == 0 && tl > KT0) {
                    bool seen = false;
                    for (int j = 0; j < (g >> 2); ++j) seen = seen || (JOB[j] & 15) == tl;
                    if (!seen) { RAW_BAR(); SBAR(); }
                }
                jf[g % 3][0] = lds_rd128(xp_ + (pr == 1 ? 4096 : 0));
                if constexpr (pr < 2) jf[g % 3][1] = cat8(lds_tr(wp_ + 8192), lds_tr(wp_ + 8192 + 512));
                else jf[g % 3][2] = cat8(lds_tr(wp_), lds_tr(wp_ + 512));
            } else {
                if constexpr (pr < 2) jf[g % 3][2] = cat8(lds_tr(wp_ + 16384), lds_tr(wp_ + 16384 + 512));
                else jf[g % 3][1] = lds_rd128(xp_ + 4096);
            }
            SBAR();
        }
    };
#define IC(x) std::integral_constant<int, (x)>{}
#pragma unroll
    for (int kt = 0; kt < KT0; ++kt) {
        const lds_ptr st = lds + (kt & 3) * K1_STAGE;
        const lds_ptr sn = lds + ((kt + 1) & 3) * K1_STAGE;
        K1_STEP(0, st, 1, false);
        K1_STEP(1, st, 2, false);
        K1_STEP(0, st, 3, false);
        if (kt < KT0 - 1) K1_STEP(1, sn, 0, kt + 1);
    }
    acc[0][0] = MFMA32(K1_WF(1, 0), xf[1][0], acc[0][0]); SBAR();
    WAIT_BAR(0); SBAR();
    asm volatile("" :: "v"(cpf));
    jrd(IC(0), 0);
    acc[0][1] = MFMA32(K1_WF(1, 1), xf[1][0], acc[0][1]); SBAR();
    jrd(IC(0), 1);
    acc[0][2] = MFMA32(xf[1][0], K1_WF(1, 2), acc[0][2]); SBAR();
    jrd(IC(1), 0);
    acc[1][0] = MFMA32(K1_WF(1, 0), xf[1][1], acc[1][0]); SBAR();
    jrd(IC(1), 1);
    acc[1][1] = MFMA32(K1_WF(1, 1), xf[1][1], acc[1][1]);
    acc[1][2] = MFMA32(xf[1][1], K1_WF(1, 2), acc[1][2]); SBAR();
    const int h = 2 * hp + wn;
    const float qscale = 0.17677669529663687f * 1.4426950408889634f;
    const __amdgpu_buffer_rsrc_t rs = __builtin_amdgcn_make_buffer_rsrc(ws, 0, 32 << 20, 0x00020000);
    const int T0 = tb * 4 + wm * 2;
    unsigned dq = (unsigned)WS_Q + QT(h, T0) * 2048 + lane * 16;
    unsigned dk = (unsigned)WS_KV + (h * 64 + T0) * 4096 + lane * 16;
    unsigned dkk = (unsigned)WS_KV + (h * 64 + T0) * 4096 + ((lane & 51) | ((lane & 4) << 1) | ((lane & 8) >> 1)) * 16;
    asm volatile("" : "+v"(dq), "+v"(dk), "+v"(dkk));
    static_assert(KQ == 1 && SPM != 0, "");
    auto jst = [&](int pr, int i) __attribute__((always_inline)) {
        if (pr < 2) {
            if (i < 2) __builtin_amdgcn_raw_buffer_store_b128(pack8n(acc[pr][1], i), rs, (int)dkk + i * 1024, pr * 4096, K1_SAUX);
            else __builtin_amdgcn_raw_buffer_store_b128(pack8n(acc[pr][2], i - 2), rs, (int)dk + i * 1024, pr * 4096, K1_SAUX);
        } else K1_ST(pack8(acc[i >> 1][0], i & 1, qscale), rs, dq + i * 1024);
        SBAR();
    };
    sfor(std::make_integer_sequence<int, 4 * NJ>{}, [&](auto gc) __attribute__((always_inline)) {
        constexpr int g = decltype(gc)::value, pr = JOB[g >> 2] >> 4;
        constexpr int dn = g >= 4 && (JOB[(g >> 2) - (g >= 4)] & 15) == 15 ? JOB[(g >> 2) - (g >= 4)] >> 4 : -1;
        if constexpr (pr < 2) acc[pr][1] = MFMA32(jf[g % 3][1], jf[g % 3][0], acc[pr][1]);
        else acc[0][0] = MFMA32(jf[g % 3][2], jf[g % 3][0], acc[0][0]);
        SBAR();
        jrd(IC(g + 2), 0);
        if constexpr (dn >= 0 && (g & 3) == 1) jst(dn, 1);
        if constexpr (dn >= 0 && (g & 3) == 2) jst(dn, 3);
        if constexpr (pr < 2) acc[pr][2] = MFMA32(jf[g % 3][0], jf[g % 3][2], acc[pr][2]);
        else acc[1][0] = MFMA32(jf[g % 3][2], jf[g % 3][1], acc[1][0]);
        SBAR();
        jrd(IC(g + 2), 1);
        if constexpr (dn >= 0 && (g & 3) == 0) { jst(dn, 0); if (dn == 0) DG(6); }
        if constexpr (dn >= 0 && (g & 3) == 1) jst(dn, 2);
    });
    DG(4);
    asm volatile(".Lcp_k1z:");
#pragma unroll
    for (int i = 0; i < 4; ++i) jst(2, i);
    DG_END(5);
}

constexpr int K2_SLOT = 32768;
constexpr int K2_SF = 65536;
constexpr int K2_CV = 98304;
constexpr int CV_HA = 0, CV_HB = 4160, CV_KV = 8320, CV_BYTES = 8320 + 480 + 4096;
constexpr int K2_LX = K2_CV + 4 * CV_BYTES;
constexpr int K2_FL = K2_LX + 2048;
constexpr int K2_SCR = K2_FL + 256;
constexpr int K2_PAD = K2_SCR + 4096;
constexpr int K2_LDS = K2_PAD + 1024;
#define NB8 0
#define WW 0
constexpr int F_X = 0, F_R = 32, F_W2 = 64, C_OST = 68, C_S1 = 76, C_S2 = 80, C_S3 = 128, C_ZST = 88, F_ZGO = 92,
              C_KST = 96, C_U0 = 100, C_LD = 108, F_KGO = 116, C_END = 120, C_OL = 72, C_HF = 84;
#define NG_IN 0
#define GATE_WORD C_END
#define GATE_VAL 0
#define X_NZ 4
#define X_NW3 8
#define ZP_GATE 4u
#define ZL_GATE 4
#define ZL_AT 31
#define Z_SLEEP0 0
#define O_SLEEP 0
#define Z_SLEEP 8
#define OWIN 16
#define OW0 8
#define OWR 8
#define WD 2
#define W3_AT 10
#define X_NOLDW 0
#define X_NOLDO 0
#define X_NOCHK 0
#define KROT (nt * 2)
#define NG_LAST 0
#define PACE_IN "s_nop 7"
#define PACE_OUT "s_nop 1"
#define P3_PRIO 1
#define TOP_SLEEP 2
#define A_AUX 16
#define B_AUX 16
#define B_LDS 0
#define TL 8
#define XR_AT 62
#define W3_AUX 0
#define LORD 0
#define AORD 0
#define S2M 17
#define W3M (LORD ? 23 : 13)
#define A16 1
#define SEL4 2
#define KV_DOT 1
#define ZE_PRIO 3
#define P3_T1 99
#define P3_T2 99
#define AUX_PRIO 2

__global__ __launch_bounds__(768, 3) void k2_attn(char* __restrict__ ws, const char* __restrict__ Aq,
                                                  const char* __restrict__ Ak, const char* __restrict__ Av,
                                                  const char* __restrict__ shiftk, const char* __restrict__ ssmk,
                                                  const unsigned* __restrict__ ssmD, const char* __restrict__ Wo,
                                                  char* __restrict__ out) {
    __shared__ __attribute__((aligned(16))) char lds_raw[K2_LDS];
    const lds_ptr lds = (lds_ptr)lds_raw;
    const lds_ptr fl = lds + K2_FL;
    const unsigned lds0 = (unsigned)(uintptr_t)lds;
    const int tid = threadIdx.x, lane = tid & 63;
    const int w = __builtin_amdgcn_readfirstlane(tid >> 6);
    const int b = blockIdx.x, xcd = b & 7, idx = b >> 3;
    const int h = xcd * 4 + (idx >> 3), qb = idx & 7;
    const int tt = xcd * 2 + (idx >> 4), nt = idx & 15;
    const char* kvg = ws + WS_KV + (size_t)h * 64 * 4096;
    const __amdgpu_buffer_rsrc_t rs = __builtin_amdgcn_make_buffer_rsrc(ws, 0, 32 << 20, 0x00020000);
    const __amdgpu_buffer_rsrc_t ro = __builtin_amdgcn_make_buffer_rsrc(out, 0, 8 << 20, 0x00020000);
    gu32* const cnt = (gu32*)(ws + WS_CNT);
    auto signal = [&]() {
        if (!BK1 || lane < 32) (void)__hip_atomic_fetch_add(cnt + ((lane & 31) * 8 + (lane < 32 ? xcd : qb)) * 64, 1u, RLX_AGENT);
    };
    const unsigned wvoff = (unsigned)(lane >> 3) * 2048u + nt * 128 + (((lane & 7) ^ (((lane >> 4) & 1) << 2)) << 4);
    const int krot = KROT;
    auto wsrc = [&](int hf, int lp) { return Wo + (size_t)(hf * 64 + ((lp + krot * 2) & 63)) * 16384; };
    DG_DECL(1)
    const unsigned voff = lane * 16;
    const int cw = w & 3;
    auto issue = [&](int st) {
        const char* src = kvg + (st * 4 + cw) * 4096;
        const unsigned dst = lds0 + (st & 1) * K2_SLOT + cw * 4096;
#pragma unroll
        for (int i = 0; i < 4; ++i) glds16(voff, src + i * 1024, dst + i * 1024);
#pragma unroll
        for (int i = 0; i < 4; ++i) glds16(voff, src + 131072 + i * 1024, dst + 16384 + i * 1024);
    };
    asm volatile(".Lcp_k2a:");
    if (w >= 8) {
        __builtin_amdgcn_s_sleep(TOP_SLEEP);
#pragma unroll
        for (int i = 2; i < 8; ++i)
            glds16(voff, kvg + (i & 1) * 131072 + (i >> 1) * 4096 + cw * 1024, lds0 + (i & 1) * 16384 + (i >> 1) * 4096 + cw * 1024);
        RAW_BAR();
        issue(1);
        WAIT_BAR(8);
    } else {
        const int tq = cw * 2 + (w >> 2);
        const char* qg = ws + WS_Q + (size_t)QT(h, qb * 8 + tq) * 2048;
        glds16(voff, kvg + (w >> 2) * 131072 + cw * 1024, lds0 + (w >> 2) * 16384 + cw * 1024);
        glds16(voff, qg, lds0 + K2_SF + tq * 2048);
        glds16(voff, qg + 1024, lds0 + K2_SF + tq * 2048 + 1024);
    }
    int w2 = w;
    asm volatile("" : "+s"(w2));

    if (w2 >= 8) {
        __builtin_amdgcn_s_setprio(3);
        const int ch = xcd * 128 + idx * 4 + cw;
        const lds_ptr cv = lds + K2_CV + cw * CV_BYTES;
        {
            const u32x4 z4 = {0u, 0u, 0u, 0u};
            if (lane < 2) *(LAS u32x4*)(cv + CV_HA + lane * 16) = z4;
            if (lane < 30) *(LAS u32x4*)(cv + CV_KV + lane * 16) = z4;
            if (cw == 0 && lane < 16) *(LAS u32x4*)(fl + lane * 16) = z4;
        }
        const int c0 = xcd * 128 + cw * 32, cq = lane & 7, rg = lane >> 3;
        const int row0 = idx * 64 + rg * 8;
        const unsigned roff = (unsigned)row0 * 2048u + (c0 + cq * 4) * 2;
        const unsigned toffs = (unsigned)(c0 + (lane >> 3)) * 4096u + idx * 128;
        u32x4 sk[2];
        sk[0] = gld128((unsigned)(c0 + cq * 4) * 8u, shiftk); sk[1] = gld128((unsigned)(c0 + cq * 4) * 8u, shiftk + 16);
        unsigned dd = gld32((unsigned)(ch >> 1) * 4u, ssmD);
        auto stage = [&](const char* src, int dst) {
            const unsigned sbase = (unsigned)(idx * 64 + cw * 16 + (lane >> 4)) * 2048u + xcd * 256;
#pragma unroll
            for (int i = 0; i < 4; ++i)
                glds16p<K2_SNT>(sbase + (((lane & 15) ^ (((2 * cw + (i >> 1)) & 3) << 2)) << 4), src + i * 8192, lds0 + dst + cw * 4096 + i * 1024);
        };
        auto blank = [&](unsigned off) {
            const u32x4 e4 = {EMPTY, EMPTY, EMPTY, EMPTY};
            CFENCE();
            if (!BK1) { st_sc1(e4, rs, off + voff); st_sc1(e4, rs, off + 1024 + voff); }
            CFENCE();
        };
        WAIT_BAR(3);
        DG(12);
        issue(2);
        stage(Ak, K2_SF);
        glds16((unsigned)(idx * 64 - (idx ? 4 : 0) + (lane >> 4)) * 2048u + xcd * 256 + (((lane & 15) ^ 12) << 4), Ak, lds0 + K2_SCR + cw * 1024);
        blank((unsigned)WS_O + (unsigned)((qb * 8 + cw * 2) * 32 + h) * 2048u);
        auto warm = [&](int g) {
#pragma unroll
            for (int i = 0; i < 4; ++i) glds16(wvoff, wsrc(idx >> 4, 16 + g * 16 + cw * 4 + i), lds0 + K2_PAD);
        };
        WAITN_BAR(BK1 ? 5 : 7);
        DG(9);
        issue(3);
        stage(Av, K2_SF + 16384);
        blank((unsigned)WS_O + (unsigned)((qb * 8 + cw * 2 + 1) * 32 + h) * 2048u);
        WAITN_BAR(BK1 ? 4 : 6);
        DG(3);
        issue(4);
#pragma unroll
        for (int i = 0; i < 4; ++i) glds16p<K2_SNT>(voff, ssmk + (size_t)ch * 4096 + i * 1024, lds0 + K2_CV + cw * CV_BYTES + CV_HA + 32 + i * 1024);
        blank((unsigned)WS_ZT + ch * 4096); blank((unsigned)WS_ZT + ch * 4096 + 2048);
        WAITN_BAR(BK1 ? 4 : 8);
        DG(5);
        issue(5);
        SBAR();
        asm volatile("" : "+v"(sk[0]), "+v"(sk[1]), "+v"(dd));
        dd = __builtin_amdgcn_readfirstlane(dd);
        {
            u32x2 kr[11], vr[8];
            lds_ptr kb = lds + K2_SF + rg * 2048 + ((cw ^ (rg & 3)) << 6) + cq * 8;
            asm volatile("" : "+v"(kb));
            const lds_ptr hb = lds + (rg ? K2_SF + rg * 2048 - 768 : K2_SCR + cw * 1024 + 256) + ((cw ^ ((rg + 3) & 3)) << 6) + cq * 8;
#pragma unroll
            for (int i = 0; i < 3; ++i) kr[i] = *(const LAS u32x2*)(hb + i * 256);
#pragma unroll
            for (int i = 0; i < 8; ++i) kr[3 + i] = *(const LAS u32x2*)(kb + i * 256);
#pragma unroll
            for (int i = 0; i < 8; ++i) vr[i] = *(const LAS u32x2*)(kb + 16384 + i * 256);
            if (row0 == 0) { kr[0] = u32x2{0u, 0u}; kr[1] = u32x2{0u, 0u}; kr[2] = u32x2{0u, 0u}; }
            const lds_ptr tb = cv + CV_KV + 480;
#pragma unroll
            for (int c = 0; c < 4; ++c) {
                const unsigned s01 = sk[c >> 1][(c & 1) * 2], s23 = sk[c >> 1][(c & 1) * 2 + 1];
                unsigned kp[10];
                float kv[8], vf[8];
#pragma unroll
                for (int j = 0; j < 10; ++j) kp[j] = __builtin_amdgcn_perm(kr[j][c >> 1], kr[j + 1][c >> 1], (c & 1) ? 0x07060302u : 0x05040100u);
                SBAR();
#pragma unroll
                for (int i = 0; i < 8; ++i) asm("v_dot2_f32_bf16 %0, %1, %2, 0" : "=v"(kv[i]) : "v"(kp[i]), "v"(s23));
                SBAR();
#pragma unroll
                for (int i = 0; i < 8; ++i) asm("v_dot2_f32_bf16 %0, %1, %2, %0" : "+v"(kv[i]) : "v"(kp[i + 2]), "v"(s01));
                SBAR();
#pragma unroll
                for (int i = 0; i < 8; ++i) vf[i] = (c & 1) ? bfhi(vr[i][c >> 1]) : bflo(vr[i][c >> 1]);
                SBAR();
#pragma unroll
                for (int i = 0; i < 8; ++i) kv[i] = mul_vv(kv[i], vf[i]);
                u32x4 o4;
#pragma unroll
                for (int i = 0; i < 4; ++i) o4[i] = cvtpk(kv[7 - 2 * i], kv[6 - 2 * i]);
                *(LAS u32x4*)(tb + (cq * 4 + c) * 128 + ((rg ^ cq) << 4)) = o4;
            }
#pragma unroll
            for (int i = 0; i < 4; ++i) {
                const u32x4 t = *(const LAS u32x4*)(tb + i * 1024 + lane * 16);
                st_sc1(t, rs, (unsigned)WS_Q + (xcd * 32 + idx) * 16384 + cw * 4096 + i * 1024 + (lane >> 3) * 128 + (((lane & 7) ^ (2 * i + (lane >> 5))) << 4));
            }
        }
        CFENCE();
        DG(7);
        WAIT_BAR(4);
        DG(6);
        issue(6);
        asm volatile("s_waitcnt vmcnt(8)" ::: "memory");
        if (lds_add_rtn(fl + C_KST, lane) == 3u) signal();
#pragma unroll
        for (int i = 0; i < 8; ++i)
            glds16p<K2_WNT>(wvoff, wsrc(i >> 2, cw * 4 + (i & 3)), lds0 + K2_SF + (i >> 2) * 16384 + (cw * 4 + (i & 3)) * 1024);
        {
            const u32x4 z4 = {0u, 0u, 0u, 0u};
            if (lane < 2) *(LAS u32x4*)(cv + CV_HB + lane * 16) = z4;
#pragma unroll
            for (int i = 0; i < 4; ++i) {
                const int off = 32 + i * 1024 + lane * 16;
                const u32x4 hv = *(const LAS u32x4*)(cv + CV_HA + off);
                const unsigned pv = *(const LAS unsigned*)(cv + CV_HA + off - 4);
                u32x4 sh;
                sh[0] = __builtin_amdgcn_alignbit(hv[0], pv, 16);
                sh[1] = __builtin_amdgcn_alignbit(hv[1], hv[0], 16);
                sh[2] = __builtin_amdgcn_alignbit(hv[2], hv[1], 16);
                sh[3] = __builtin_amdgcn_alignbit(hv[3], hv[2], 16);
                *(LAS u32x4*)(cv + CV_HB + off) = sh;
            }
        }
        WAIT_BAR(8);
        DG(8);
        issue(7);
        auto warm23 = [&]() {
#pragma unroll
            for (int i = 0; i < 8; ++i)
                glds16p<K2_WNT>(wvoff, wsrc(idx >> 4, 32 + cw * 8 + i), lds0 + K2_SCR + cw * 1024);
        };
        auto chunk1 = [&]() {
#pragma unroll
            for (int i = 0; i < 8; ++i)
                glds16p<K2_WNT>(wvoff, wsrc(i >> 2, 16 + cw * 4 + (i & 3)), lds0 + (i >> 2) * 16384 + (cw * 4 + (i & 3)) * 1024);
        };
        warm23();
        WAIT_BAR(8);
        DG(11);
        if (cw == 0) { poll_ge(cnt + b * 64, BK1 ? 32u : 64u); lds_add(fl + F_KGO, lane); }
        else lds_wait_ge(fl + F_KGO, 1u);
        {
            u32x4 t[4];
#pragma unroll
            for (int i = 0; i < 4; ++i) t[i] = ld_sc1s(rs, (unsigned)WS_Q + (xcd * 32 + (lane >> 3)) * 16384 + (idx * 4 + cw) * 128 + (lane & 7) * 16, i * 131072);
#pragma unroll
            for (int i = 0; i < 4; ++i) *(LAS u32x4*)(cv + CV_KV + 480 + i * 1024 + lane * 16) = t[i];
        }
        const int r1 = lane & 15, q = lane >> 4;
        const int toff = (r1 & 1) ? CV_HA + 2 * (16 + r1 + 9 - 8 * q) : CV_HB + 2 * (16 + r1 + 10 - 8 * q);
        const int boff = CV_KV + 448 + 32 * r1 + 16 * q;
        f32x4 y[8];
        u32x4 tf[8];
        bf16x8 bf[8];
        const lds_ptr bp = cv + boff - 448;
        lds_ptr tp[4];
#pragma unroll
        for (int i = 0; i < 4; ++i) { tp[i] = cv + toff + 1024 * i; asm volatile("" : "+v"(tp[i])); }
#define LDT(bb, a, hf) do { const LAS unsigned* t_ = (const LAS unsigned*)(tp[(a) >> 1] + 64 * (bb) + 512 * ((a) & 1) + 8 * (hf)); \
            tf[a][2 * (hf)] = t_[0]; tf[a][2 * (hf) + 1] = t_[1]; SBAR(); } while (0)
#define LDB(bb, a) do { bf[a] = lds_rd128(bp + 448 - 64 * (bb) + 512 * (a)); SBAR(); } while (0)
#define CM(a, ww) do { asm volatile("v_mfma_f32_16x16x32_bf16 %0, %1, %2, %0" : "+v"(y[(a) + (ww)]) : "v"(tf[a]), "v"(bf[ww])); SBAR(); } while (0)
#define CM0(a, ww) do { asm volatile("v_mfma_f32_16x16x32_bf16 %0, %1, %2, 0" : "=&v"(y[(a) + (ww)]) : "v"(tf[a]), "v"(bf[ww])); SBAR(); } while (0)
#define PC() do { asm volatile(PACE_OUT); SBAR(); } while (0)
#pragma unroll
        for (int a = 0; a < 8; ++a) { if (a < 5) { LDT(0, a, 0); LDT(0, a, 1); } LDB(0, 7 - a); }
        chunk1();
#pragma unroll
        for (int i = 0; i < 4; ++i)
            glds16p<K2_QNT>((unsigned)(idx * 64 + cw * 16 + (lane >> 4)) * 2048u + xcd * 256 + (lane & 15) * 16, Aq + i * 8192, lds0 + K2_SCR + cw * 1024);
        RAW_BAR();
        asm volatile("s_waitcnt vmcnt(%0)" :: "n"(AORD == 2 ? 12 : 4) : "memory");
        lds_add(fl + C_S1, lane);
#define ZFL() PC()
        __builtin_amdgcn_s_setprio(AUX_PRIO);
        const float dch = (ch & 1) ? bfhi(dd) : bflo(dd);
        const lds_ptr kdp = cv + CV_KV + 480 + 32 * r1 + 16 * (q >> 1) + ((q & 1) ? 0 : 8);
        float kd[8][4];
        u32x2 kk;
        unsigned kc0, kc1, zo[8][2];
#define KD_RD(t) do { kk = *(const LAS u32x2*)(kdp + 512 * (t)); SBAR(); } while (0)
#define KD_UP0(t) do { kd[t][0] = bfhi(kk[1]); kd[t][1] = bflo(kk[1]); SBAR(); } while (0)
#define KD_UP1(t) do { kd[t][2] = bfhi(kk[0]); kd[t][3] = bflo(kk[0]); SBAR(); } while (0)
#define KD_MU0(t) do { kd[t][0] = mul_sv(dch, kd[t][0]); kd[t][1] = mul_sv(dch, kd[t][1]); SBAR(); } while (0)
#define KD_MU1(t) do { kd[t][2] = mul_sv(dch, kd[t][2]); kd[t][3] = mul_sv(dch, kd[t][3]); SBAR(); } while (0)
#define KD_CV(t) do { kc0 = cvtpk(kd[t][0], kd[t][1]); kc1 = cvtpk(kd[t][2], kd[t][3]); SBAR(); } while (0)
#define KD_X0(t) do { kd[t][0] = bflo(kc0); kd[t][1] = bfhi(kc0); SBAR(); } while (0)
#define KD_X1(t) do { kd[t][2] = bflo(kc1); kd[t][3] = bfhi(kc1); SBAR(); } while (0)
#define ZF0(t) do { kc0 = cvtpk(y[t][0], y[t][1]); kc1 = cvtpk(y[t][2], y[t][3]); SBAR(); } while (0)
#define ZF1(t) do { y[t][0] = bflo(kc0); y[t][1] = bfhi(kc0); SBAR(); } while (0)
#define ZF2(t) do { y[t][2] = bflo(kc1); y[t][3] = bfhi(kc1); SBAR(); } while (0)
#define ZF3(t) do { y[t][0] = add_vv(y[t][0], kd[t][0]); y[t][1] = add_vv(y[t][1], kd[t][1]); SBAR(); } while (0)
#define ZF4(t) do { y[t][2] = add_vv(y[t][2], kd[t][2]); y[t][3] = add_vv(y[t][3], kd[t][3]); SBAR(); } while (0)
#define ZF5(t) do { zo[t][0] = nz(cvtpk(y[t][0], y[t][1])); zo[t][1] = nz(cvtpk(y[t][2], y[t][3])); SBAR(); } while (0)
#define ZST(pr) do { auto s0 = __builtin_amdgcn_permlane16_swap(zo[2 * (pr)][0], zo[2 * (pr) + 1][0], false, false); \
            auto s1 = __builtin_amdgcn_permlane16_swap(zo[2 * (pr)][1], zo[2 * (pr) + 1][1], false, false); \
            st_sc1(u32x4{s0[0], s1[0], s0[1], s1[1]}, rs, (unsigned)WS_ZT + ch * 4096 + (pr) * 1024 + voff); SBAR(); } while (0)
#define W3(i) do { if ((i) == 0) PC(); } while (0)
#define HF() do { lds_add(fl + C_HF, lane); SBAR(); } while (0)
        CM0(0, 7); LDB(1, 7); CM0(0, 6); LDT(0, 5, 0); CM0(0, 5); LDT(0, 5, 1); CM0(0, 4); LDT(0, 6, 0); CM0(0, 3); LDT(0, 6, 1); CM0(0, 2); LDT(0, 7, 0); CM0(0, 1); LDT(0, 7, 1); CM0(0, 0); KD_RD(0);
        CM(1, 6); LDB(1, 6); CM(1, 5); LDT(1, 0, 0); CM(1, 4); LDT(1, 0, 1); CM(1, 3); PC(); CM(1, 2); PC(); CM(1, 1); KD_UP0(0); CM(1, 0); KD_UP1(0);
        CM(2, 5); LDB(1, 5); CM(2, 4); LDT(1, 1, 0); CM(2, 3); LDT(1, 1, 1); CM(2, 2); KD_MU0(0); CM(2, 1); KD_MU1(0); CM(2, 0); KD_CV(0);
        CM(3, 4); LDB(1, 4); CM(3, 3); LDT(1, 2, 0); CM(3, 2); LDT(1, 2, 1); CM(3, 1); KD_X0(0); CM(3, 0); KD_X1(0);
        CM(4, 3); LDB(1, 3); CM(4, 2); LDT(1, 3, 0); CM(4, 1); LDT(1, 3, 1); CM(4, 0); KD_RD(1);
        CM(5, 2); LDB(1, 2); CM(5, 1); LDT(1, 4, 0); CM(5, 0); LDT(1, 4, 1);
        CM(6, 1); LDB(1, 1); CM(6, 0); PC();
        CM(7, 0); LDB(1, 0);
        DGV(6);
        CM(0, 7); LDB(2, 7); CM(0, 6); LDT(1, 5, 0); CM(0, 5); LDT(1, 5, 1); CM(0, 4); LDT(1, 6, 0); CM(0, 3); LDT(1, 6, 1); CM(0, 2); LDT(1, 7, 0); CM(0, 1); LDT(1, 7, 1); CM(0, 0); PC();
        CM(1, 6); LDB(2, 6); CM(1, 5); LDT(2, 0, 0); CM(1, 4); LDT(2, 0, 1); CM(1, 3); KD_UP0(1); CM(1, 2); KD_UP1(1); CM(1, 1); KD_MU0(1); CM(1, 0); KD_MU1(1);
        CM(2, 5); LDB(2, 5); CM(2, 4); LDT(2, 1, 0); CM(2, 3); LDT(2, 1, 1); CM(2, 2); KD_CV(1); CM(2, 1); KD_X0(1); CM(2, 0); KD_X1(1);
        CM(3, 4); LDB(2, 4); CM(3, 3); LDT(2, 2, 0); CM(3, 2); LDT(2, 2, 1); CM(3, 1); KD_RD(2); CM(3, 0); PC();
        CM(4, 3); LDB(2, 3); CM(4, 2); LDT(2, 3, 0); CM(4, 1); LDT(2, 3, 1); CM(4, 0); PC();
        CM(5, 2); LDB(2, 2); CM(5, 1); LDT(2, 4, 0); CM(5, 0); LDT(2, 4, 1);
        CM(6, 1); LDB(2, 1); CM(6, 0); KD_UP0(2);
        CM(7, 0); LDB(2, 0);
        DGV(8);
        CM(0, 7); LDB(3, 7); CM(0, 6); LDT(2, 5, 0); CM(0, 5); LDT(2, 5, 1); CM(0, 4); LDT(2, 6, 0); CM(0, 3); LDT(2, 6, 1); CM(0, 2); LDT(2, 7, 0); CM(0, 1); LDT(2, 7, 1); CM(0, 0); KD_UP1(2);
        CM(1, 6); LDB(3, 6); CM(1, 5); LDT(3, 0, 0); CM(1, 4); LDT(3, 0, 1); CM(1, 3); KD_MU0(2); CM(1, 2); KD_MU1(2); CM(1, 1); KD_CV(2); CM(1, 0); KD_X0(2);
        CM(2, 5); LDB(3, 5); CM(2, 4); LDT(3, 1, 0); CM(2, 3); LDT(3, 1, 1); CM(2, 2); KD_X1(2); CM(2, 1); KD_RD(3); CM(2, 0); PC();
        CM(3, 4); LDB(3, 4); CM(3, 3); LDT(3, 2, 0); CM(3, 2); LDT(3, 2, 1); CM(3, 1); PC(); CM(3, 0); KD_UP0(3);
        CM(4, 3); LDB(3, 3); CM(4, 2); LDT(3, 3, 0); CM(4, 1); LDT(3, 3, 1); CM(4, 0); KD_UP1(3);
        CM(5, 2); LDB(3, 2); CM(5, 1); LDT(3, 4, 0); CM(5, 0); LDT(3, 4, 1);
        CM(6, 1); LDB(3, 1); CM(6, 0); KD_MU0(3);
        CM(7, 0); LDB(3, 0);
        DGV(12);
        CM(0, 7); LDB(4, 7); CM(0, 6); LDT(3, 5, 0); CM(0, 5); LDT(3, 5, 1); CM(0, 4); LDT(3, 6, 0); CM(0, 3); LDT(3, 6, 1); CM(0, 2); LDT(3, 7, 0); CM(0, 1); LDT(3, 7, 1); CM(0, 0); KD_MU1(3);
        CM(1, 6); LDB(4, 6); CM(1, 5); LDT(4, 0, 0); CM(1, 4); LDT(4, 0, 1); CM(1, 3); KD_CV(3); CM(1, 2); KD_X0(3); CM(1, 1); KD_X1(3); CM(1, 0); KD_RD(4);
        CM(2, 5); LDB(4, 5); CM(2, 4); LDT(4, 1, 0); CM(2, 3); LDT(4, 1, 1); CM(2, 2); PC(); CM(2, 1); PC(); CM(2, 0); KD_UP0(4);
        CM(3, 4); LDB(4, 4); CM(3, 3); LDT(4, 2, 0); CM(3, 2); LDT(4, 2, 1); CM(3, 1); KD_UP1(4); CM(3, 0); KD_MU0(4);
        CM(4, 3); LDB(4, 3); CM(4, 2); LDT(4, 3, 0); CM(4, 1); LDT(4, 3, 1); CM(4, 0); KD_MU1(4);
        CM(5, 2); LDB(4, 2); CM(5, 1); LDT(4, 4, 0); CM(5, 0); LDT(4, 4, 1);
        CM(6, 1); LDB(4, 1); CM(6, 0); KD_CV(4);
        CM(7, 0); LDB(4, 0);
        DGV(9);
        CM(0, 7); LDB(5, 7); CM(0, 6); LDT(4, 5, 0); CM(0, 5); LDT(4, 5, 1); CM(0, 4); LDT(4, 6, 0); CM(0, 3); LDT(4, 6, 1); CM(0, 2); LDT(4, 7, 0); CM(0, 1); LDT(4, 7, 1); CM(0, 0); KD_X0(4);
        CM(1, 6); LDB(5, 6); CM(1, 5); LDT(5, 0, 0); CM(1, 4); LDT(5, 0, 1); CM(1, 3); KD_X1(4); CM(1, 2); KD_RD(5); CM(1, 1); PC(); CM(1, 0); PC();
        CM(2, 5); LDB(5, 5); CM(2, 4); LDT(5, 1, 0); CM(2, 3); LDT(5, 1, 1); CM(2, 2); KD_UP0(5); CM(2, 1); KD_UP1(5); CM(2, 0); KD_MU0(5);
        CM(3, 4); LDB(5, 4); CM(3, 3); LDT(5, 2, 0); CM(3, 2); LDT(5, 2, 1); CM(3, 1); KD_MU1(5); CM(3, 0); KD_CV(5);
        CM(4, 3); LDB(5, 3); CM(4, 2); LDT(5, 3, 0); CM(4, 1); LDT(5, 3, 1); CM(4, 0); KD_X0(5);
        CM(5, 2); LDB(5, 2); CM(5, 1); LDT(5, 4, 0); CM(5, 0); LDT(5, 4, 1);
        CM(6, 1); LDB(5, 1); CM(6, 0); KD_X1(5);
        CM(7, 0); LDB(5, 0);
        DGV(11);
        CM(0, 7); LDB(6, 7); CM(0, 6); LDT(5, 5, 0); CM(0, 5); LDT(5, 5, 1); CM(0, 4); LDT(5, 6, 0); CM(0, 3); LDT(5, 6, 1); CM(0, 2); LDT(5, 7, 0); CM(0, 1); LDT(5, 7, 1); CM(0, 0); KD_RD(6);
        CM(1, 6); LDB(6, 6); CM(1, 5); LDT(6, 0, 0); CM(1, 4); LDT(6, 0, 1); CM(1, 3); PC(); CM(1, 2); PC(); CM(1, 1); KD_UP0(6); CM(1, 0); KD_UP1(6);
        CM(2, 5); LDB(6, 5); CM(2, 4); LDT(6, 1, 0); CM(2, 3); LDT(6, 1, 1); CM(2, 2); KD_MU0(6); CM(2, 1); KD_MU1(6); CM(2, 0); KD_CV(6);
        CM(3, 4); LDB(6, 4); CM(3, 3); LDT(6, 2, 0); CM(3, 2); LDT(6, 2, 1); CM(3, 1); KD_X0(6); CM(3, 0); KD_X1(6);
        CM(4, 3); LDB(6, 3); CM(4, 2); LDT(6, 3, 0); CM(4, 1); LDT(6, 3, 1); CM(4, 0); KD_RD(7);
        CM(5, 2); LDB(6, 2); CM(5, 1); LDT(6, 4, 0); CM(5, 0); LDT(6, 4, 1);
        CM(6, 1); LDB(6, 1); CM(6, 0); PC();
        CM(7, 0); LDB(6, 0);
        DGV(7);
        CM(0, 7); LDB(7, 7); CM(0, 6); LDT(6, 5, 0); CM(0, 5); LDT(6, 5, 1); CM(0, 4); LDT(6, 6, 0); CM(0, 3); LDT(6, 6, 1); CM(0, 2); LDT(6, 7, 0); CM(0, 1); LDT(6, 7, 1); CM(0, 0); PC();
        CM(1, 6); LDB(7, 6); CM(1, 5); LDT(7, 0, 0); CM(1, 4); LDT(7, 0, 1); CM(1, 3); KD_UP0(7); CM(1, 2); KD_UP1(7); CM(1, 1); KD_MU0(7); CM(1, 0); KD_MU1(7);
        CM(2, 5); LDB(7, 5); CM(2, 4); LDT(7, 1, 0); CM(2, 3); LDT(7, 1, 1); CM(2, 2); KD_CV(7); CM(2, 1); KD_X0(7); CM(2, 0); KD_X1(7);
        CM(3, 4); LDB(7, 4); CM(3, 3); LDT(7, 2, 0); CM(3, 2); LDT(7, 2, 1); CM(3, 1); PC(); CM(3, 0); PC();
        CM(4, 3); LDB(7, 3); CM(4, 2); LDT(7, 3, 0); CM(4, 1); LDT(7, 3, 1); CM(4, 0); PC();
        CM(5, 2); LDB(7, 2); CM(5, 1); LDT(7, 4, 0); CM(5, 0); LDT(7, 4, 1);
        CM(6, 1); LDB(7, 1); CM(6, 0); PC();
        CM(7, 0); LDB(7, 0);
        DGV(3);
        CM(0, 3); LDT(7, 5, 0); CM(0, 2); LDT(7, 5, 1); CM(1, 2); LDT(7, 6, 0); CM(1, 1); LDT(7, 6, 1); CM(2, 1); LDT(7, 7, 0); CM(2, 0); LDT(7, 7, 1);
        CM(3, 0); HF(); CM(0, 1); ZFL(); CM(0, 0); W3(0); CM(1, 0); ZF0(2); W3(1); CM(0, 5); ZF1(2); W3(2); CM(0, 4); ZF2(2); W3(3);
        CM(1, 4); ZF3(2); W3(4); CM(1, 3); ZF4(2); W3(5); CM(2, 3); ZF5(2); W3(6); CM(2, 2); ZF0(3); W3(7); CM(3, 2); ZF1(3);  CM(3, 1); ZF2(3);
        CM(4, 1); ZF3(3);  CM(4, 0); ZF4(3);  CM(5, 0); ZF5(3);  CM(0, 7); ZST(1);  CM(0, 6); ZF0(0);  CM(1, 6); ZF1(0);
        CM(1, 5); ZF2(0);  CM(2, 5); ZF3(0);  CM(2, 4); ZF4(0);  CM(3, 4); ZF5(0);  CM(3, 3); ZF0(1);  CM(4, 3); ZF1(1);
        CM(4, 2); ZF2(1);  CM(5, 2); ZF3(1);  CM(5, 1); ZF4(1);  CM(6, 1); ZF5(1);  CM(6, 0); ZST(0);  CM(7, 0); ZF0(4);
        DG(4);
        ZF1(4); ZF2(4); ZF3(4); ZF4(4); ZF5(4); ZF0(5); ZF1(5); ZF2(5); ZF3(5); ZF4(5); ZF5(5); ZST(2); ZF0(6); ZF1(6); ZF2(6); ZF3(6); ZF4(6); ZF5(6); ZF0(7); ZF1(7); ZF2(7); ZF3(7); ZF4(7); ZF5(7); ZST(3);
        DG(10);
        __builtin_amdgcn_s_setprio(3);
        while ((unsigned)__builtin_amdgcn_readfirstlane(*(volatile LAS unsigned*)(fl + C_OL)) < ZL_GATE) __builtin_amdgcn_s_sleep(4);
        CFENCE();
        const int arow = idx * 64 + (cw >> 1) * 32 + rg * 4, acol = xcd * 256 + (cw & 1) * 128 + cq * 16;
        u32x4 qr[4];
#pragma unroll
        for (int i = 0; i < 4; ++i) qr[i] = gld128((unsigned)(arow * 2048 + acol), Aq + i * 2048);
        {
            const lds_ptr tr = cv + CV_KV + 480;
            u32x4 t[4];
            const int ck = (cw >> 1) * 4 + ((lane ^ ((lane >> 5) * 2)) & 3);
            const unsigned zb = (unsigned)WS_ZT + (unsigned)(xcd * 128 + (cw & 1) * 64 + (lane >> 4) * 4) * 4096u + (idx >> 3) * 1024 +
                                ((idx & 3) * 4 + ((idx >> 2) & 1) * 16 + (ck >> 1) + (ck & 1) * 32) * 16;
#pragma unroll 1
            for (unsigned spins = 0; spins < (1u << 20); ++spins) {
#pragma unroll
                for (int i = 0; i < 4; ++i) t[i] = ld_sc1s(rs, zb + ((((lane >> 2) & 3) ^ i) << 12), i * 65536);
                if (!missing(mx4(mx4(mx8(t[0], t[1]), t[2]), t[3]))) break;
                __builtin_amdgcn_s_sleep(Z_SLEEP);
            }
            DG(13);
            __builtin_amdgcn_s_setprio(ZE_PRIO);
#pragma unroll
            for (int i = 0; i < 4; ++i) *(LAS u32x4*)(tr + i * 1024 + lane * 16) = t[i];
#pragma unroll
            for (int i = 0; i < 4; ++i) asm volatile("" : "+v"(qr[i]));
            u32x2 zc[8];
#pragma unroll
            for (int c = 0; c < 8; ++c)
                zc[c] = *(const LAS u32x2*)(tr + cq * 512 + (c >> 2) * 256 + ((((c & 3) * 4 + (rg >> 1)) ^ (cq * 2)) << 4) + (rg & 1) * 8);
#pragma unroll
            for (int i = 0; i < 4; ++i) {
                u32x4 o;
#pragma unroll
                for (int c = 0; c < 4; ++c) {
                    const unsigned z0 = zc[2 * c][i >> 1], z1 = zc[2 * c + 1][i >> 1];
                    o[c] = cvtpk(mul_vv(bflo(qr[i][c]), (i & 1) ? bfhi(z0) : bflo(z0)), mul_vv(bfhi(qr[i][c]), (i & 1) ? bfhi(z1) : bflo(z1)));
                }
                __builtin_amdgcn_raw_buffer_store_b128(o, ro, arow * 4096 + acol, i * 4096, OUT_AUX);
            }
        }
        asm volatile(".Lcp_k2z:");
        DG_END(14);
        return;
    }

    const int half = w >> 2;
    const int TA = qb * 8 + (w & 3) * 2 + half, TB = TA ^ 1;
    bf16x8 sel;
    {
        const short one = (((lane >> 2) & 1) == ((lane >> 4) & 1) && (lane & 11) == 0) ? (short)0x3f80 : (short)0;
#pragma unroll
        for (int i = 0; i < 8; ++i) sel[i] = SPM ? ((lane & 15) == (i & 4) ? (short)0x3f80 : (short)0) : one;
    }
    int sidx = (int)0xdddddddd;
#define LO8(x) __builtin_bit_cast(bfx16_t, __builtin_shufflevector(x, x, 0, 1, 2, 3, 4, 5, 6, 7))
#define HI8(x) __builtin_bit_cast(bfx16_t, __builtin_shufflevector(x, x, 8, 9, 10, 11, 12, 13, 14, 15))
#define SPV(v, p, o) __builtin_amdgcn_smfmac_f32_32x32x32_bf16(__builtin_bit_cast(bfx8_t, v), p, o, sidx, 0, 0)
#define SSUM(p, l) __builtin_amdgcn_smfmac_f32_16x16x64_bf16(__builtin_bit_cast(bfx8_t, sel), p, l, sidx, 0, 0)
    f32x16 oA = f32x16{}, oB = f32x16{};
    f32x4 lA = f32x4{}, lB = f32x4{};
    f32x16 negm, sA, sB;
    lds_ptr lp = lds + half * 16384 + lane * 16;
    bf16x8 k0[2], k1[2], v0[2], v1[2];
    u32x4 p0, p1 = {0u, 0u, 0u, 0u};
#define EX(X, i) X[i] = __builtin_amdgcn_exp2f(X[i])
    lds_ptr qpa = lds + K2_SF + (TA & 7) * 2048 + lane * 16, qpb = lds + K2_SF + (TB & 7) * 2048 + lane * 16;
    asm volatile("" : "+v"(qpa), "+v"(qpb), "+v"(lp), "+v"(sel), "+v"(oA), "+v"(oB), "+v"(lA), "+v"(lB), "+v"(sidx));
    WAIT_BAR(0);
    DG(3);
    const bf16x8 qa0 = lds_rd128(qpa), qb0 = lds_rd128(qpb);
    const bf16x8 x0 = lds_rd128(lds + lane * 16), x1 = lds_rd128(lds + lane * 16 + 1024);
    const bf16x8 qa1 = lds_rd128(qpa + 1024), qb1 = lds_rd128(qpb + 1024);
    {
        k0[0] = lds_rd128(lp); k1[0] = lds_rd128(lp + 1024);
        v0[0] = lds_rd128(lp + 2048); v1[0] = lds_rd128(lp + 3072);
        sA = MFMA32(x0, qa0, f32x16{});
        sB = MFMA32(x0, qb0, f32x16{});
        sA = MFMA32(x1, qa1, sA);
        sB = MFMA32(x1, qb1, sB);
        float m = fmx(sA[0], sA[1]), m2 = fmx(sB[0], sB[1]);
#pragma unroll
        for (int i = 2; i < 16; i += 2) m = fmx(fmx(m, sA[i]), sA[i + 1]);
#pragma unroll
        for (int i = 2; i < 16; i += 2) m2 = fmx(fmx(m2, sB[i]), sB[i + 1]);
        m = fmx(m, m2);
        auto rr = __builtin_amdgcn_permlane32_swap(__float_as_uint(m), __float_as_uint(m), false, false);
        m = fmx(__uint_as_float(rr[0]), __uint_as_float(rr[1]));
#pragma unroll
        for (int i = 0; i < 16; ++i) negm[i] = -m;
        sA = MFMA32(k0[0], qa0, negm);
        sA = MFMA32(k1[0], qa1, sA);
        v1[1] = v1[0];
#pragma unroll
        for (int i = 8; i < 16; ++i) sB[i] = 0.f;
        asm volatile("" : "+v"(sB));
        SBAR();
        RAW_BAR();
        SBAR();
        EX(sA, 0); EX(sA, 1);
    }
    DG(10);
#define SUM16(l, p) asm volatile("v_mfma_f32_16x16x32_bf16 %0, %1, %2, %0" : "+v"(l) : "v"(sel), "v"(p))
#define HALF(C, oC, lC, N, qx, qy, ka, kb, oP, lP, vprev, vcur0, PRE) do { \
        oP = SPV(vprev, HI8(N), oP); SBAR(); \
        PRE; EX(C, 2); EX(C, 3); EX(C, 4); SBAR(); \
        lP = SSUM(HI8(N), lP); SBAR(); \
        EX(C, 5); SBAR(); \
        N = MFMA32(ka, qx, negm); SBAR(); \
        EX(C, 6); EX(C, 7); EX(C, 8); SBAR(); \
        N = MFMA32(kb, qy, N); SBAR(); \
        EX(C, 9); EX(C, 10); EX(C, 11); SBAR(); \
        oC = SPV(vcur0, LO8(C), oC); SBAR(); \
        EX(C, 12); EX(C, 13); EX(C, 14); SBAR(); \
        lC = SSUM(LO8(C), lC); SBAR(); \
        EX(C, 15); EX(N, 0); EX(N, 1); SBAR(); \
    } while (0)
#define RD4(Y, nx) do { v1[Y] = lds_rd128((nx) + 3072); v0[Y] = lds_rd128((nx) + 2048); \
        k1[Y] = lds_rd128((nx) + 1024); k0[Y] = lds_rd128(nx); } while (0)
#define KT(X, nx, PRE0) do { \
        HALF(sA, oA, lA, sB, qb0, qb1, k0[X], k1[X], oB, lB, v1[X ^ 1], v0[X], PRE0; RD4(X ^ 1, nx)); \
        HALF(sB, oB, lB, sA, qa0, qa1, k0[X ^ 1], k1[X ^ 1], oA, lA, v1[X], v0[X], (void)0); \
    } while (0)
#define RING_BAR() do { unsigned long long sv_; \
        asm volatile("s_cmp_eq_u32 %1, 7\n\ts_cbranch_scc1 1f\n\ts_barrier\n1:\n\ts_mov_b64 %0, exec\n\ts_mov_b64 exec, 1\n\tds_add_u32 %2, %3\n\ts_mov_b64 exec, %0" \
                     : "=&s"(sv_) : "s"(st), "v"(fl + C_LD), "v"(1u) : "memory", "scc"); } while (0)
    SBAR();
#pragma unroll 1
    for (int st = 0; st < 8; ++st) {
        const lds_ptr cur = lp + (st & 1) * K2_SLOT;
        const lds_ptr nxt = lp + ((st + 1) & 1) * K2_SLOT;
        PRIO(2);
        KT(0, cur + 4096, (void)0);
        PRIO(1);
        KT(1, cur + 8192, (void)0);
        PRIO(0);
        KT(0, cur + 12288, (void)0);
        PRIO(3);
        KT(1, nxt, RAW_BAR(); SBAR());
    }
    oB = SPV(v1[1], HI8(sB), oB);
    SBAR();
    lB = SSUM(HI8(sB), lB);
    SBAR();
    DG(4);
    {
        const lds_ptr xo = lds + K2_SLOT + w * 4096 + lane * 16;
        *(LAS float*)(lds + K2_LX + w * 256 + lane * 4) = lB[0];
#pragma unroll
        for (int i = 0; i < 4; ++i) *(LAS f32x4*)(xo + i * 1024) = f32x4{oB[4 * i], oB[4 * i + 1], oB[4 * i + 2], oB[4 * i + 3]};
        lds_add(fl + F_X + w * 4, lane);
        lds_add(fl + C_END + half * 4, lane);
        lds_wait_ge(fl + F_X + (w ^ 4) * 4, 1u);
        const lds_ptr xi = lds + K2_SLOT + (w ^ 4) * 4096 + lane * 16;
        f32x4 t[4];
        const float lx = *(const LAS float*)(lds + K2_LX + (w ^ 4) * 256 + (lane & 31) * 4);
        { auto rr = __builtin_amdgcn_permlane32_swap(__float_as_uint(lA[0]), __float_as_uint(lA[0]), false, false); lA[0] = __uint_as_float(rr[0]); }
#pragma unroll
        for (int i = 0; i < 4; ++i) t[i] = *(const LAS f32x4*)(xi + i * 1024);
        SBAR();
        lA[0] += lx;
#pragma unroll
        for (int i = 0; i < 16; ++i) { float o = oA[i]; acc_vv(o, t[i >> 2][i & 3]); oA[i] = o; }
    }
    DG(9);
    const float l = lA[0];
    u32x4 on0, on1;
    auto finish = [&](float ls) {
        float rl = __builtin_amdgcn_rcpf(ls);
        asm volatile("s_nop 1" : "+v"(rl));
        f32x16 o;
#pragma unroll
        for (int i = 0; i < 16; ++i) o[i] = mul_vv(oA[i], rl);
        on0 = nz4(pack8n(o, 0)); on1 = nz4(pack8n(o, 1));
    };
    finish(l);
    {
        float t0, t1;
        asm("v_mul_f32_e32 %0, 0, %2\n\tv_mul_f32_e32 %1, 0, %3\n\tv_fmac_f32_e32 %0, 0, %4\n\tv_fmac_f32_e32 %1, 0, %5\n\tv_fmac_f32_e32 %0, 0, %6\n\tv_fmac_f32_e32 %1, 0, %7\n\tv_fmac_f32_e32 %0, 0, %8\n\tv_fmac_f32_e32 %1, 0, %9\n\t"
            "v_fmac_f32_e32 %0, 0, %10\n\tv_fmac_f32_e32 %1, 0, %11\n\tv_fmac_f32_e32 %0, 0, %12\n\tv_fmac_f32_e32 %1, 0, %13\n\tv_fmac_f32_e32 %0, 0, %14\n\tv_fmac_f32_e32 %1, 0, %15\n\tv_fmac_f32_e32 %0, 0, %16\n\tv_fmac_f32_e32 %1, 0, %17"
            : "=&v"(t0), "=&v"(t1) : "v"(oA[0]), "v"(oA[1]), "v"(oA[2]), "v"(oA[3]), "v"(oA[4]), "v"(oA[5]), "v"(oA[6]), "v"(oA[7]), "v"(oA[8]), "v"(oA[9]), "v"(oA[10]), "v"(oA[11]), "v"(oA[12]), "v"(oA[13]), "v"(oA[14]), "v"(oA[15]));
        const bool ok = (l <= 1.0e30f) && (l >= 1.0e-18f) && (t0 + t1 == 0.f);
        if (__builtin_expect(__any(!ok), 0)) {
            float m = -INFINITY, ls = 0.f;
            oA = f32x16{};
#pragma unroll 1
            for (int t = 0; t < 64; ++t) {
                const char* g = kvg + t * 4096 + lane * 16;
                const bf16x8 a0 = *(const bf16x8*)g, a1 = *(const bf16x8*)(g + 1024);
                const bf16x8 b0 = *(const bf16x8*)(g + 2048), b1 = *(const bf16x8*)(g + 3072);
                f32x16 s = MFMA32(a0, qa0, f32x16{});
                s = MFMA32(a1, qa1, s);
                float tm = s[0];
#pragma unroll
                for (int i = 1; i < 16; ++i) tm = __builtin_fmaxf(tm, s[i]);
                auto rr = __builtin_amdgcn_permlane32_swap(__float_as_uint(tm), __float_as_uint(tm), false, false);
                tm = __builtin_fmaxf(__uint_as_float(rr[0]), __uint_as_float(rr[1]));
                const float mn = __builtin_fmaxf(m, tm);
                const float alpha = __builtin_amdgcn_exp2f(m - mn);
                m = mn;
                float ps = 0.f;
#pragma unroll
                for (int i = 0; i < 16; ++i) { s[i] = bfhi(__float_as_uint(__builtin_amdgcn_exp2f(s[i] - mn))); ps += s[i]; oA[i] *= alpha; }
                ls = ls * alpha + ps;
                oA = SPV(b0, LO8(s), oA);
                oA = SPV(b1, HI8(s), oA);
            }
            auto rr = __builtin_amdgcn_permlane32_swap(__float_as_uint(ls), __float_as_uint(ls), false, false);
            asm volatile("s_nop 7\n\ts_nop 7" : "+v"(oA));
            finish(__uint_as_float(rr[0]) + __uint_as_float(rr[1]));
        }
    }
    u32x4 w3[4];
    const char* s2 = wsrc(half ^ 1, 32 + (w & 3) * 4);
    const char* s3 = wsrc(half, 48 + (w & 3) * 4);
    auto ldw2 = [&]() {
#pragma unroll
        for (int i = 0; i < 4; ++i)
            glds16p<K2_WNT>(wvoff, s2 + i * 16384, lds0 + K2_SLOT + ((w ^ 4) * 4 + i) * 1024);
    };
    auto ldw3 = [&]() {
#pragma unroll
        for (int i = 0; i < 4; ++i) w3[i] = gld128(wvoff, s3 + i * 16384);
    };
    {
        const unsigned og = (unsigned)WS_O + (unsigned)(TA * 32 + h) * 2048u + lane * 16;
        st_sc1(on0, rs, og);
        st_sc1(on1, rs, og + 1024);
        lds_add(fl + C_S1, lane);
        lds_wait_ge(fl + C_S1, 12u);
        DG(5);
        ldw2();
        ldw3();
    }
    {
        const int half = w & 1;
        const int T3 = tt * 4 + (w >> 1);
        const unsigned og = (unsigned)WS_O + (unsigned)T3 * 65536u + half * 32768 + lane * 16;
        const int r = lane & 31, hh = lane >> 5;
        const int g = lane >> 4, q = (lane & 15) >> 2, p = lane & 3;
        int woff[2];
#pragma unroll
        for (int n = 0; n < 2; ++n) {
            const int c = 4 * (n ^ half) + 2 * (p & 1) + (g & 1);
            woff[n] = (4 * hh + q) * 128 + ((c ^ ((q >> 1) << 2)) << 4) + (p >> 1) * 8;
        }
        f32x16 acc[2] = {f32x16{}, f32x16{}};
        lds_ptr wl[3][2] = {{lds + half * 16384 + woff[0], lds + half * 16384 + woff[1]},
                            {lds + K2_SF + half * 16384 + woff[0], lds + K2_SF + half * 16384 + woff[1]},
                            {lds + K2_CV + half * 2 * CV_BYTES + woff[0], lds + K2_CV + half * 2 * CV_BYTES + woff[1]}};
        asm volatile("" : "+v"(wl[0][0]), "+v"(wl[0][1]), "+v"(wl[1][0]), "+v"(wl[1][1]), "+v"(wl[2][0]), "+v"(wl[2][1]));
#define MS(m) ((m) < 64 - 2 * TL ? (m) >> 1 : 32 - TL + ((m) - (64 - 2 * TL)) % TL)
#define MN(m) ((m) < 64 - 2 * TL ? (m) & 1 : (m) < 64 - TL)
        constexpr int WR = 2 * WD + 2;
        s16x4 wa[WR], wb[WR];
#define LDW(m) do { const int s_ = MS(m), f_ = s_ < 8 ? 1 : s_ < 24 ? 0 : 2; \
            const int o_ = f_ == 2 ? ((s_ >> 2) & 1) * CV_BYTES + (s_ & 3) * 2048 : (f_ ? 0 : ((s_ >> 3) - 1) * K2_SLOT) + (s_ & 7) * 2048; \
            wa[(m) % WR] = lds_tr(wl[f_][MN(m)] + o_); wb[(m) % WR] = lds_tr(wl[f_][MN(m)] + o_ + 1024); SBAR(); } while (0)
#pragma unroll
        for (int m = 0; m < 2 * WD; ++m) LDW(m);
        u32x4 of[OWIN];
#define LDO(i) do { of[(i) % OWIN] = ld_sc1s(rs, og, (((i) + krot) & 31) * 1024); SBAR(); } while (0)
        for (unsigned spins = 0; spins < (1u << 20); ++spins) {
#pragma unroll
            for (int i = 0; i < OW0; ++i) LDO(i);
            unsigned m = mx8(of[0], of[1]);
#pragma unroll
            for (int i = 2; i < OW0; ++i) m = mx4(m, of[i]);
            if (!missing(m)) break;
            DGC(12);
        }
        lds_add(fl + C_S2, lane);
#pragma unroll
        for (int i = OW0; i < OWIN; ++i) LDO(i);
        DG(6);
        __builtin_amdgcn_s_setprio(P3_PRIO);
        SBAR();
        unsigned fv = 0;
#define FLAG_RD(a) do { fv = *(volatile LAS unsigned*)(a); SBAR(); } while (0)
#define FLAG_CHK(a, n) do { if (__builtin_expect((unsigned)__builtin_amdgcn_readfirstlane(fv) < (n), 0)) lds_wait_ge(a, n); CFENCE(); SBAR(); } while (0)
        lds_ptr xo = lds + K2_SF + w * 4096 + lane * 16, xi = lds + K2_SF + (w ^ 1) * 4096 + lane * 16;
        int bo = (T3 * 32 + (lane & 15)) * 4096 + 2048 + nt * 128 + half * 64 + g * 16;
        lds_ptr w3d = lds + K2_CV + (w >> 1) * CV_BYTES + (w & 1) * 4096 + lane * 16;
        asm volatile("" : "+v"(xo), "+v"(xi), "+v"(bo), "+v"(w3d));
        f32x4 xt[4];
        constexpr int M3 = TL > 8 ? 64 - 2 * TL + TL - 8 - 2 * WD : 48 - 2 * WD;
        sfor(std::make_integer_sequence<int, 64>{}, [&](auto mc) __attribute__((always_inline)) {
            constexpr int m = decltype(mc)::value, s = MS(m), n = MN(m);
            constexpr bool first = m < 64 - 2 * TL ? n == 0 : n == 1;
            constexpr bool last = m < 64 - 2 * TL ? n == 1 : n == 0;
            acc[n] = MFMA32(cat8(wa[m % WR], wb[m % WR]), PB(of[s % OWIN]), acc[n]);
            SBAR();
            if constexpr (m == 32 - 2 * WD) FLAG_CHK(fl + C_S2, 8u);
            if constexpr (m == M3) FLAG_CHK(fl + C_S3, 8u);
            if constexpr (m + 2 * WD < 64) LDW(m + 2 * WD);
            if constexpr (first && (s & 1) && s + 1 >= OW0 && s + 1 < 32) {
                if (__builtin_expect(missing(mx8(of[(s + 1) % OWIN], of[(s + 2) % OWIN])), 0)) {
#pragma unroll 1
                    for (unsigned spins = 0; spins < (1u << 20); ++spins) {
                        __builtin_amdgcn_s_sleep(2);
                        LDO(s + 1); LDO(s + 2);
                        DGC(13);
                        if (!missing(mx8(of[(s + 1) % OWIN], of[(s + 2) % OWIN]))) break;
                    }
                }
            }
            if constexpr (last && s + OWIN < 32) LDO(s + OWIN);
            if constexpr (LORD && m == S2M) { asm volatile("s_waitcnt vmcnt(%0)" :: "n"(4 + (m + 1) / 2) : "memory"); lds_add(fl + C_S2, lane); }
            if constexpr (LORD && m == W3M) asm volatile("s_waitcnt vmcnt(%0)" :: "n"((m + 1) / 2 < 16 ? (m + 1) / 2 : 16) : "memory");
            if constexpr (m == 15 - 2 * WD) lds_add(fl + C_U0, lane);
            if constexpr (m == 29 - 2 * WD) FLAG_RD(fl + C_S2);
            if constexpr (m == W3M - 3) FLAG_RD(fl + C_HF);
            if constexpr (m == W3M) FLAG_CHK(fl + C_HF, 4u);
            if constexpr (m >= W3M && m < W3M + 8 && ((m - W3M) & 1) == 0) {
                asm volatile("" : "+v"(w3[((m - W3M) >> 1) & 3]));
                *(LAS u32x4*)(w3d + (((m - W3M) >> 1) & 3) * 1024) = w3[((m - W3M) >> 1) & 3];
            }
            if constexpr (m == W3M + 6) lds_add(fl + C_S3, lane);
            if constexpr (m == M3 - 3) FLAG_RD(fl + C_S3);
            if constexpr (m == 64 - TL - 3) FLAG_RD(fl + C_U0);
            if constexpr (m == 64 - TL) FLAG_CHK(fl + C_U0, 8u);
            if constexpr (m == 64 - TL + 1 || m == 64 - TL + 2) {
                constexpr int i = 2 * (m - (64 - TL + 1));
                *(LAS f32x4*)(xo + i * 1024) = f32x4{acc[1][4 * i], acc[1][4 * i + 1], acc[1][4 * i + 2], acc[1][4 * i + 3]};
                *(LAS f32x4*)(xo + i * 1024 + 1024) = f32x4{acc[1][4 * i + 4], acc[1][4 * i + 5], acc[1][4 * i + 6], acc[1][4 * i + 7]};
            }
            if constexpr (m == 64 - TL + 2) lds_add(fl + F_R + w * 4, lane);
            if constexpr (m == XR_AT - 2) FLAG_RD(fl + F_R + (w ^ 1) * 4);
            if constexpr (m == XR_AT) {
                FLAG_CHK(fl + F_R + (w ^ 1) * 4, 1u);
#pragma unroll
                for (int i = 0; i < 4; ++i) xt[i] = *(const LAS f32x4*)(xi + i * 1024);
            }
            if constexpr (ZL_GATE && last && s + OWIN == ZL_AT) lds_add(fl + C_OL, lane);
            if constexpr (m == 2 * P3_T1 + 1) __builtin_amdgcn_s_setprio(P3_PRIO - 1);
            SBAR();
            if constexpr (m == 31) DG(11);
            if constexpr (m == 47) DG(14);
        });
        asm volatile("s_nop 11" : "+v"(acc[0]));
        DG(7);
#pragma unroll
        for (int i = 0; i < 16; ++i) { float o = xt[i >> 2][i & 3]; acc_vv(o, acc[0][i]); acc[1][i] = o; }
        u32x4 pa = pack8n(acc[1], 0), pb = pack8n(acc[1], 1);
#pragma unroll
        for (int i = 0; i < 4; ++i) {
            auto sw = __builtin_amdgcn_permlane16_swap(pa[i], pb[i], false, false);
            pa[i] = sw[0]; pb[i] = sw[1];
        }
        asm volatile("" :: "v"(acc[0]));
        __builtin_amdgcn_raw_buffer_store_b128(pa, ro, bo, 0, OUT_AUX);
        __builtin_amdgcn_raw_buffer_store_b128(pb, ro, bo, 65536, OUT_AUX);
    }
    DG_END(8);
}

extern "C" void kernel_launch(void* const* d_in, const int* in_sizes, int n_in,
                              void* d_out, int out_size, void* d_ws, size_t ws_size,
                              hipStream_t stream) {
    const void* A_q = d_in[0];
    const void* A_k = d_in[1];
    const void* A_v = d_in[2];
    const void* A_shift = d_in[3];
    const void* A_ssmk = d_in[4];
    const void* A_D = d_in[5];
    const void* B_x = d_in[6];
    const void* B_wqkv = d_in[7];
    const void* B_wo = d_in[8];
    char* ws = (char*)d_ws;
    k1_qkv<<<256, 512, 0, stream>>>((const char*)B_x, (const char*)B_wqkv, ws, (const char*)B_wo, (const char*)A_q,
                                    (const char*)A_k, (const char*)A_v, (const char*)A_ssmk);
    k2_attn<<<256, 768, 0, stream>>>(ws, (const char*)A_q, (const char*)A_k, (const char*)A_v, (const char*)A_shift,
                                     (const char*)A_ssmk, (const unsigned*)A_D, (const char*)B_wo, (char*)d_out);
}
```

```cpp
#include <hip/hip_runtime.h>
#include <hip/hip_bf16.h>
#include <stdint.h>
#include <utility>

typedef short bf16x8 __attribute__((ext_vector_type(8)));
typedef short s16x4 __attribute__((ext_vector_type(4)));
typedef float f32x16 __attribute__((ext_vector_type(16)));
typedef float f32x4 __attribute__((ext_vector_type(4)));
typedef float f32x2 __attribute__((ext_vector_type(2)));
typedef unsigned u32x4 __attribute__((ext_vector_type(4)));
typedef unsigned u32x2 __attribute__((ext_vector_type(2)));
typedef __bf16 bf16x2_t __attribute__((ext_vector_type(2)));

#define LAS __attribute__((address_space(3)))
typedef LAS char* lds_ptr;
#define DI __device__ __forceinline__
#define SBAR() __builtin_amdgcn_sched_barrier(0)
#define MFMA32(a, b, c) __builtin_amdgcn_mfma_f32_32x32x16_bf16((a), (b), (c), 0, 0, 0)
#define MFMA16(a, b, c) __builtin_amdgcn_mfma_f32_16x16x32_bf16((a), (b), (c), 0, 0, 0)
typedef __bf16 bfx8_t __attribute__((ext_vector_type(8)));
typedef __bf16 bfx16_t __attribute__((ext_vector_type(16)));
typedef float f32x8 __attribute__((ext_vector_type(8)));
#define SPM 1
#define WAIT_BAR(N) asm volatile("s_waitcnt vmcnt(" #N ") lgkmcnt(0)\n\ts_barrier" ::: "memory")
#define LGKM_BAR() asm volatile("s_waitcnt lgkmcnt(0)\n\ts_barrier" ::: "memory")

#define DG_DECL(K)
#define DG(i)
#define DG_END(i)
#define DGC(i)

#define DGV(i)
constexpr int S_LEN = 2048, DM = 1024, NH = 32;

#define KQ 1
#define OQ 0
#define QT(h, T) (KQ ? (h) * 64 + (T) : (T) * 32 + (h))
constexpr size_t WS_Q = 0;
constexpr size_t WS_KV = 4u << 20;
constexpr size_t WS_O = OQ ? WS_Q : 12u << 20;
constexpr size_t WS_KVR = 16u << 20;
#define ZK 0
constexpr size_t WS_ZT = ZK ? WS_KVR : 20u << 20;
constexpr size_t WS_CNT = 24u << 20;

template <int... I, class F> DI void sfor(std::integer_sequence<int, I...>, F&& f) { (f(std::integral_constant<int, I>{}), ...); }
DI unsigned cvtpk(float lo, float hi) {
    f32x2 v = {lo, hi};
    return __builtin_bit_cast(unsigned, __builtin_convertvector(v, bf16x2_t));
}
DI float bflo(unsigned u) { return __uint_as_float(u << 16); }
DI float bfhi(unsigned u) { return __uint_as_float(u & 0xffff0000u); }

DI void glds16(unsigned voff, const void* sbase, unsigned lds_dst) {
    unsigned sv;
    asm volatile("s_mov_b32 %0, m0\n\ts_mov_b32 m0, %3\n\ts_nop 0\n\tglobal_load_lds_dwordx4 %1, %2\n\ts_mov_b32 m0, %0"
                 : "=&s"(sv) : "v"(voff), "s"(sbase), "s"(lds_dst) : "memory");
}
template <int NT> DI void glds16p(unsigned voff, const void* sbase, unsigned lds_dst) {
    if constexpr (NT) {
        unsigned sv;
        asm volatile("s_mov_b32 %0, m0\n\ts_mov_b32 m0, %3\n\ts_nop 0\n\tglobal_load_lds_dwordx4 %1, %2 nt\n\ts_mov_b32 m0, %0"
                     : "=&s"(sv) : "v"(voff), "s"(sbase), "s"(lds_dst) : "memory");
    } else glds16(voff, sbase, lds_dst);
}
#define K1_XNT 0
#define K1_WNT 0
#define K2_SNT 1
#define K2_QNT 0
#define K2_WNT 0
#define OUT_AUX 16
#define K1_SAUX 16
DI bf16x8 lds_rd128(lds_ptr p) { return *(const LAS bf16x8*)p; }
DI s16x4 lds_tr(lds_ptr p) {
    return __builtin_bit_cast(s16x4, __builtin_amdgcn_ds_read_tr16_b64_v4i16((LAS s16x4*)p));
}
DI bf16x8 cat8(s16x4 a, s16x4 b) { return __builtin_shufflevector(a, b, 0, 1, 2, 3, 4, 5, 6, 7); }
DI u32x4 pack8(const f32x16& x, int s, float sc) {
    u32x4 p;
    p[0] = cvtpk(x[8 * s + 0] * sc, x[8 * s + 1] * sc);
    p[1] = cvtpk(x[8 * s + 2] * sc, x[8 * s + 3] * sc);
    p[2] = cvtpk(x[8 * s + 4] * sc, x[8 * s + 5] * sc);
    p[3] = cvtpk(x[8 * s + 6] * sc, x[8 * s + 7] * sc);
    return p;
}
DI u32x4 pack8n(const f32x16& x, int s) {
    u32x4 p;
    p[0] = cvtpk(x[8 * s + 0], x[8 * s + 1]);
    p[1] = cvtpk(x[8 * s + 2], x[8 * s + 3]);
    p[2] = cvtpk(x[8 * s + 4], x[8 * s + 5]);
    p[3] = cvtpk(x[8 * s + 6], x[8 * s + 7]);
    return p;
}
DI void rnd2(float& a, float& b) { unsigned u = cvtpk(a, b); a = bflo(u); b = bfhi(u); }

#define CONV_PACE()
#define RAW_BAR() asm volatile("s_barrier" ::: "memory")
DI unsigned gld32(unsigned voff, const void* sbase) {
    unsigned r;
    asm volatile("global_load_dword %0, %1, %2" : "=v"(r) : "v"(voff), "s"(sbase) : "memory");
    return r;
}
DI u32x2 gld64(unsigned voff, const void* sbase) {
    u32x2 r;
    asm volatile("global_load_dwordx2 %0, %1, %2" : "=v"(r) : "v"(voff), "s"(sbase) : "memory");
    return r;
}
DI float mul_sv(float s, float v) { float r; asm("v_mul_f32_e32 %0, %1, %2" : "=v"(r) : "s"(s), "v"(v)); return r; }
DI float mul_vv(float a, float b) { float r; asm("v_mul_f32_e32 %0, %1, %2" : "=v"(r) : "v"(a), "v"(b)); return r; }
DI float add_vv(float a, float b) { float r; asm("v_add_f32_e32 %0, %1, %2" : "=v"(r) : "v"(a), "v"(b)); return r; }
DI void acc_vv(float& a, float b) { asm("v_add_f32_e32 %0, %0, %1" : "+v"(a) : "v"(b)); }
DI void fmac_vv(float& acc, float a, float b) { asm("v_fmac_f32_e32 %0, %1, %2" : "+v"(acc) : "v"(a), "v"(b)); }
DI void fmac_sv(float& acc, float s, float v) { asm("v_fmac_f32_e32 %0, %1, %2" : "+v"(acc) : "s"(s), "v"(v)); }
typedef __attribute__((address_space(1))) unsigned gu32;
#define RLX_AGENT __ATOMIC_RELAXED, __HIP_MEMORY_SCOPE_AGENT
#define CFENCE() asm volatile("" ::: "memory")
#define DRAIN() asm volatile("s_waitcnt vmcnt(0)" ::: "memory")
constexpr int AUX_SC1 = 16;
DI u32x4 ld_sc1(__amdgpu_buffer_rsrc_t rs, unsigned off) { return __builtin_amdgcn_raw_buffer_load_b128(rs, (int)off, 0, AUX_SC1); }
DI u32x4 ld_sc1s(__amdgpu_buffer_rsrc_t rs, unsigned off, int soff) { return __builtin_amdgcn_raw_buffer_load_b128(rs, (int)off, soff, AUX_SC1); }
DI void st_sc1(u32x4 v, __amdgpu_buffer_rsrc_t rs, unsigned off) { __builtin_amdgcn_raw_buffer_store_b128(v, rs, (int)off, 0, AUX_SC1); }
constexpr unsigned EMPTY = 0xffffffffu;
DI unsigned nz(unsigned x) { return x < EMPTY - 1u ? x : EMPTY - 1u; }
DI u32x4 nz4(u32x4 v) { return u32x4{nz(v[0]), nz(v[1]), nz(v[2]), nz(v[3])}; }
DI float fmx(float a, float b) { return __builtin_elementwise_maximum(a, b); }
DI unsigned umax(unsigned a, unsigned b) { return a > b ? a : b; }
DI unsigned umax3(unsigned a, unsigned b, unsigned c) { return umax(umax(a, b), c); }
DI unsigned mx4(unsigned m, u32x4 v) { return umax3(umax3(m, v[0], v[1]), v[2], v[3]); }
DI unsigned mx8(u32x4 a, u32x4 b) { return umax(umax3(umax3(umax3(a[0], a[1], a[2]), a[3], b[0]), b[1], b[2]), b[3]); }
DI bool missing(unsigned m) { return __builtin_amdgcn_ballot_w64(m == EMPTY) != 0; }
DI unsigned lds_add_rtn(lds_ptr p, int lane) {
    unsigned old = 0;
    CFENCE();
    if (lane == 0) old = __hip_atomic_fetch_add((LAS unsigned*)p, 1u, __ATOMIC_RELAXED, __HIP_MEMORY_SCOPE_WORKGROUP);
    old = __builtin_amdgcn_readfirstlane(old);
    CFENCE();
    return old;
}
DI void lds_add(lds_ptr p, int lane) {
    unsigned long long sv;
    asm volatile("s_mov_b64 %0, exec\n\ts_mov_b64 exec, 1\n\tds_add_u32 %1, %2\n\ts_mov_b64 exec, %0"
                 : "=&s"(sv) : "v"(p), "v"(1u) : "memory");
}
DI void lds_wait_ge(lds_ptr p, unsigned v) {
    CFENCE();
    while ((unsigned)__builtin_amdgcn_readfirstlane(*(volatile LAS unsigned*)p) < v) __builtin_amdgcn_s_sleep(1);
    CFENCE();
}
DI void poll_ge(gu32* word, unsigned want) {
    CFENCE();
    for (unsigned spins = 0; spins < (1u << 22); ++spins) {
        if ((unsigned)__builtin_amdgcn_readfirstlane(__hip_atomic_load(word, RLX_AGENT)) >= want) break;
        __builtin_amdgcn_s_sleep(1);
    }
    __builtin_amdgcn_fence(__ATOMIC_ACQUIRE, "wavefront");
    CFENCE();
}
#define PB(x) __builtin_bit_cast(bf16x8, x)
#define PRIO(x) do { __builtin_amdgcn_s_setprio(x); SBAR(); } while (0)
DI u32x4 gld128(unsigned voff, const void* sbase) {
    u32x4 r;
    asm volatile("global_load_dwordx4 %0, %1, %2" : "=v"(r) : "v"(voff), "s"(sbase) : "memory");
    return r;
}

#define K1_UNR 15
#define K1_LA 5
#define K1_ROT ((tb & 3) * 4)
#define K1_PF 0
#define K1_PG 0
#define K1_TW 0
#define CPF 3
#define SKD 10
#define PK2 0
#define BK1 0
#define PKC 0
#define PKC_AT 6
#define PK2_AT 13
#define CPF_POS 1
#define PG1_AT 0
#define PG2_AT 2
#define PG3_AT 6
#define K1_WB(n) WAIT_BAR(1)
#define PF1_AT -1
#define PF1_LO 4
#define PF1_N 4
#define PF2_AT 1
#define PF2_LO 8
#define PF2_N 4
#define PF3_AT 5
#define PF3_LO 12
#define PF3_N 4
#define K1_KR ((kt + rot) & 15)
#define K1_ST(v, rs, off) __builtin_amdgcn_raw_buffer_store_b128(v, rs, (int)(off), 0, K1_SAUX)
constexpr int K1_STAGE = 40960, K1U = K1_UNR;
__global__ __launch_bounds__(512, 2) void k1_qkv(const char* __restrict__ X, const char* __restrict__ W,
                                                 char* __restrict__ ws, const char* __restrict__ Wo,
                                                 const char* __restrict__ Aq, const char* __restrict__ Ak,
                                                 const char* __restrict__ Av, const char* __restrict__ ssmk) {
    __shared__ __attribute__((aligned(16))) char lds_raw[4 * K1_STAGE];
    const lds_ptr lds = (lds_ptr)lds_raw;
    const unsigned lds0 = (unsigned)(uintptr_t)lds;
    const int tid = threadIdx.x, lane = tid & 63;
    const int w = __builtin_amdgcn_readfirstlane(tid >> 6);
    const int b = blockIdx.x, xcd = b & 7, idx = b >> 3;
    const int hp = (xcd >> 1) * 4 + (idx & 3), tb = (xcd & 1) * 8 + (idx >> 2);
    DG_DECL(0)

    const int lw = w & 3;
    const int rot = K1_ROT;
#define XROW(pp) (SKD >= 8 ? 64 * (lw >> 1) + 16 * (lw & 1) + 8 * ((pp) & 1) + 32 * ((pp) >> 1) : 32 * lw + 8 * (pp))
    unsigned voffX[4], voffW[2];
#pragma unroll
    for (int pp = 0; pp < 4; ++pp) {
        const int row = XROW(pp) + (lane >> 3);
        const int ch = (lane & 7) ^ ((row >> 1) & 7);
        voffX[pp] = (unsigned)(tb * 128 + row) * 2048u + ch * 16;
    }
#pragma unroll
    for (int pp = 0; pp < 2; ++pp) {
        const int krow = 16 * lw + 8 * pp + (lane >> 3);
        const int ch = (lane & 7) ^ (((lane >> 4) & 1) << 2);
        voffW[pp] = (unsigned)krow * 6144u + ch * 16;
    }
    auto issuem = [&](int kt, int mask) {
        const int kr = K1_KR;
        const char* xb = X + kr * 128;
        const char* wb = W + (size_t)kr * (64 * 6144) + hp * 128;
        const unsigned st = lds0 + (kt & 3) * K1_STAGE;
#pragma unroll
        for (int pp = 0; pp < 4; ++pp)
            if (mask >> pp & 1) glds16p<K1_XNT>(voffX[pp], xb, st + XROW(pp) * 128);
#pragma unroll
        for (int sec = 0; sec < 3; ++sec)
#pragma unroll
            for (int pp = 0; pp < 2; ++pp)
                if (mask >> (4 + 2 * sec + pp) & 1)
                    glds16p<K1_WNT>(voffW[pp], wb + sec * 2048, st + 16384 + sec * 8192 + (2 * lw + pp) * 1024);
    };
    auto issue = [&](int kt, int lo, int hi) { issuem(kt, (1 << hi) - (1 << lo)); };
    DG(13);
    unsigned cpf;
    auto codepf = [&]() {
        unsigned long long pc; unsigned lo, hi;
        asm volatile("s_getpc_b64 %0\n.Lcp_k1a:\n\ts_mov_b32 %1, %3\n\ts_mov_b32 %2, %4" : "=s"(pc), "=s"(lo), "=s"(hi) : "n"(0), "n"(0));
        if (CPF & 1) lo = 0; else asm volatile("s_mov_b32 %0, .Lcp_k2a-.Lcp_k1a" : "=s"(lo));
        if (CPF & 2) asm volatile("s_mov_b32 %0, .Lcp_k2z-.Lcp_k1a" : "=s"(hi)); else asm volatile("s_mov_b32 %0, .Lcp_k1z-.Lcp_k1a" : "=s"(hi));
        unsigned off = lo + (unsigned)(idx + 32 * (lane & 7) + 256 * w) * 64u;
        off = off < hi ? off : hi;
        asm volatile("global_load_dword %0, %1, %2" : "=v"(cpf) : "v"(off), "s"(pc) : "memory");
    };
    if (w < 4) { issue(0, 0, 5); issue(1, 0, 5); codepf(); }
    else { issue(0, 5, 10); issue(1, 5, 10); }
    DG(14);
    int w2 = w;
    asm volatile("" : "+s"(w2));

    if (w2 >= 4) {
#define WAITN_BAR(N) asm volatile("s_waitcnt vmcnt(%0) lgkmcnt(0)\n\ts_barrier" :: "n"(N) : "memory")
        issue(2, 0, K1_LA);
        asm volatile("s_waitcnt vmcnt(%0)" :: "n"(5 + K1_LA) : "memory");
#pragma unroll 1
        for (int kt = 0; kt < (SKD == 8 ? 9 : SKD == 9 ? 12 : SKD == 10 ? 11 : 13); ++kt) {
            WAITN_BAR(10 + K1_LA);
            issue(kt + 2, K1_LA, 10);
            issue(kt + 3, 0, K1_LA);
        }
        {
            constexpr int E = 0x3c3, EA = 0x1c3, XL = 0x00c, WL = 0x030;
            WAIT_BAR(15);
            issue(13, 5, 10); issuem(14, EA);
            WAIT_BAR(15);
            DG(12);
            issuem(14, E - EA); issuem(15, E); issuem(14, XL); issuem(15, 4);
            WAIT_BAR(15);
            issuem(15, 8); issuem(14, WL); issuem(15, WL);
            WAIT_BAR(14);
            WAIT_BAR(8);
            DG(9);
            WAIT_BAR(6);
            WAIT_BAR(4);
            WAIT_BAR(0);
        }
        if (lw == 0 && lane < 2) *(unsigned*)(ws + WS_CNT + b * 256 + lane * 128) = 0u;
        DG_END(5);
        return;
    }

    const int wm = w >> 1, wn = w & 1;
    const int r = lane & 31, hh = lane >> 5;
    const int g = lane >> 4, q = (lane & 15) >> 2, p = lane & 3;
    const int xoff0 = (64 * wm + r) * 128 + ((hh ^ ((r >> 1) & 7)) << 4);
    const int woff0 = 16384 + (8 * hh + q) * 128 + (((4 * wn + 2 * (g & 1) + (p >> 1)) ^ ((q >> 1) << 2)) << 4) + (p & 1) * 8;

    f32x16 acc[2][3];
#pragma unroll
    for (int m = 0; m < 2; ++m)
#pragma unroll
        for (int s = 0; s < 3; ++s) acc[m][s] = f32x16{};
    bf16x8 xf[2][2];
    s16x4 wl[2][3], wh[2][3];
#define K1_WF(f, sec) cat8(wl[f][sec], wh[f][sec])
#define K1_STEP(F, st, t, BAR_HERE) do { \
        const lds_ptr xp_ = (st) + (xoff0 ^ ((t) << 5)); const lds_ptr wp_ = (st) + woff0 + (t) * 2048; \
        acc[0][0] = MFMA32(K1_WF(F, 0), xf[F][0], acc[0][0]); SBAR(); \
        if (BAR_HERE) { K1_WB(BAR_HERE); SBAR(); } \
        xf[F ^ 1][0] = lds_rd128(xp_); wl[F ^ 1][0] = lds_tr(wp_); wh[F ^ 1][0] = lds_tr(wp_ + 512); SBAR(); \
        acc[0][1] = MFMA32(K1_WF(F, 1), xf[F][0], acc[0][1]); SBAR(); \
        wl[F ^ 1][1] = lds_tr(wp_ + 8192); wh[F ^ 1][1] = lds_tr(wp_ + 8192 + 512); SBAR(); \
        acc[0][2] = MFMA32(xf[F][0], K1_WF(F, 2), acc[0][2]); SBAR(); \
        wl[F ^ 1][2] = lds_tr(wp_ + 16384); wh[F ^ 1][2] = lds_tr(wp_ + 16384 + 512); xf[F ^ 1][1] = lds_rd128(xp_ + 4096); SBAR(); \
        acc[1][0] = MFMA32(K1_WF(F, 0), xf[F][1], acc[1][0]); SBAR(); \
        acc[1][1] = MFMA32(K1_WF(F, 1), xf[F][1], acc[1][1]); \
        acc[1][2] = MFMA32(xf[F][1], K1_WF(F, 2), acc[1][2]); SBAR(); \
    } while (0)

    WAIT_BAR(6);
    DG(3);
    {
        const lds_ptr xp_ = lds + xoff0; const lds_ptr wp_ = lds + woff0;
        xf[0][0] = lds_rd128(xp_); xf[0][1] = lds_rd128(xp_ + 4096);
#pragma unroll
        for (int sec = 0; sec < 3; ++sec) { wl[0][sec] = lds_tr(wp_ + sec * 8192); wh[0][sec] = lds_tr(wp_ + sec * 8192 + 512); }
    }
    SBAR();
    constexpr int NJ = 9, JOB[NJ] = {13, 29, 14, 15, 45, 30, 31, 46, 47}, JBAR = 0xec;
    constexpr int KT0 = 16 - NJ / 3;
    bf16x8 jf[3][3];
    auto jrd = [&](auto gc, int part) __attribute__((always_inline)) {
        constexpr int g = decltype(gc)::value;
        if constexpr (g < 4 * NJ) {
            constexpr int pr = JOB[g >> 2] >> 4, tl = JOB[g >> 2] & 15, s = g & 3;
            const lds_ptr st = lds + (tl & 3) * K1_STAGE;
            const lds_ptr xp_ = st + (xoff0 ^ (s << 5)); const lds_ptr wp_ = st + woff0 + s * 2048;
            if (part == 0) {
                if constexpr (s == 0 && (JBAR >> (g >> 2) & 1)) { RAW_BAR(); SBAR(); }
                if constexpr (false) {
                    bool seen = false;
                    for (int j = 0; j < (g >> 2); ++j) seen = seen || (JOB[j] & 15) == tl;
                    if (!seen) { RAW_BAR(); SBAR(); }
                }
                jf[g % 3][0] = lds_rd128(xp_ + (pr == 1 ? 4096 : 0));
                if constexpr (pr < 2) jf[g % 3][1] = cat8(lds_tr(wp_ + 8192), lds_tr(wp_ + 8192 + 512));
                else jf[g % 3][2] = cat8(lds_tr(wp_), lds_tr(wp_ + 512));
            } else {
                if constexpr (pr < 2) jf[g % 3][2] = cat8(lds_tr(wp_ + 16384), lds_tr(wp_ + 16384 + 512));
                else jf[g % 3][1] = lds_rd128(xp_ + 4096);
            }
            SBAR();
        }
    };
#define IC(x) std::integral_constant<int, (x)>{}
#pragma unroll
    for (int kt = 0; kt < KT0; ++kt) {
        const lds_ptr st = lds + (kt & 3) * K1_STAGE;
        const lds_ptr sn = lds + ((kt + 1) & 3) * K1_STAGE;
        K1_STEP(0, st, 1, false);
        K1_STEP(1, st, 2, false);
        K1_STEP(0, st, 3, false);
        if (kt < KT0 - 1) K1_STEP(1, sn, 0, kt + 1);
    }
    acc[0][0] = MFMA32(K1_WF(1, 0), xf[1][0], acc[0][0]); SBAR();
    WAIT_BAR(0); SBAR();
    asm volatile("" :: "v"(cpf));
    jrd(IC(0), 0);
    acc[0][1] = MFMA32(K1_WF(1, 1), xf[1][0], acc[0][1]); SBAR();
    jrd(IC(0), 1);
    acc[0][2] = MFMA32(xf[1][0], K1_WF(1, 2), acc[0][2]); SBAR();
    jrd(IC(1), 0);
    acc[1][0] = MFMA32(K1_WF(1, 0), xf[1][1], acc[1][0]); SBAR();
    jrd(IC(1), 1);
    acc[1][1] = MFMA32(K1_WF(1, 1), xf[1][1], acc[1][1]);
    acc[1][2] = MFMA32(xf[1][1], K1_WF(1, 2), acc[1][2]); SBAR();
    const int h = 2 * hp + wn;
    const float qscale = 0.17677669529663687f * 1.4426950408889634f;
    const __amdgpu_buffer_rsrc_t rs = __builtin_amdgcn_make_buffer_rsrc(ws, 0, 32 << 20, 0x00020000);
    const int T0 = tb * 4 + wm * 2;
    unsigned dq = (unsigned)WS_Q + QT(h, T0) * 2048 + lane * 16;
    unsigned dk = (unsigned)WS_KV + (h * 64 + T0) * 4096 + lane * 16;
    unsigned dkk = (unsigned)WS_KV + (h * 64 + T0) * 4096 + ((lane & 51) | ((lane & 4) << 1) | ((lane & 8) >> 1)) * 16;
    asm volatile("" : "+v"(dq), "+v"(dk), "+v"(dkk));
    static_assert(KQ == 1 && SPM != 0, "");
    auto jst = [&](int pr, int i) __attribute__((always_inline)) {
        if (pr < 2) {
            if (i < 2) __builtin_amdgcn_raw_buffer_store_b128(pack8n(acc[pr][1], i), rs, (int)dkk + i * 1024, pr * 4096, K1_SAUX);
            else __builtin_amdgcn_raw_buffer_store_b128(pack8n(acc[pr][2], i - 2), rs, (int)dk + i * 1024, pr * 4096, K1_SAUX);
        } else K1_ST(pack8(acc[i >> 1][0], i & 1, qscale), rs, dq + i * 1024);
        SBAR();
    };
    sfor(std::make_integer_sequence<int, 4 * NJ>{}, [&](auto gc) __attribute__((always_inline)) {
        constexpr int g = decltype(gc)::value, pr = JOB[g >> 2] >> 4;
        constexpr int dn = g >= 4 && (JOB[(g >> 2) - (g >= 4)] & 15) == 15 ? JOB[(g >> 2) - (g >= 4)] >> 4 : -1;
        if constexpr (pr < 2) acc[pr][1] = MFMA32(jf[g % 3][1], jf[g % 3][0], acc[pr][1]);
        else acc[0][0] = MFMA32(jf[g % 3][2], jf[g % 3][0], acc[0][0]);
        SBAR();
        jrd(IC(g + 2), 0);
        if constexpr (dn >= 0 && (g & 3) == 1) jst(dn, 1);
        if constexpr (dn >= 0 && (g & 3) == 2) jst(dn, 3);
        if constexpr (pr < 2) acc[pr][2] = MFMA32(jf[g % 3][0], jf[g % 3][2], acc[pr][2]);
        else acc[1][0] = MFMA32(jf[g % 3][2], jf[g % 3][1], acc[1][0]);
        SBAR();
        jrd(IC(g + 2), 1);
        if constexpr (dn >= 0 && (g & 3) == 0) { jst(dn, 0); if (dn == 0) DG(6); }
        if constexpr (dn >= 0 && (g & 3) == 1) jst(dn, 2);
    });
    DG(4);
    asm volatile(".Lcp_k1z:");
#pragma unroll
    for (int i = 0; i < 4; ++i) jst(2, i);
    DG_END(5);
}

constexpr int K2_SLOT = 32768;
constexpr int K2_SF = 65536;
constexpr int K2_CV = 98304;
constexpr int CV_HA = 0, CV_HB = 4160, CV_KV = 8320, CV_BYTES = 8320 + 480 + 4096;
constexpr int K2_LX = K2_CV + 4 * CV_BYTES;
constexpr int K2_FL = K2_LX + 2048;
constexpr int K2_SCR = K2_FL + 256;
constexpr int K2_PAD = K2_SCR + 4096;
constexpr int K2_LDS = K2_PAD + 1024;
#define NB8 0
#define WW 0
constexpr int F_X = 0, F_R = 32, F_W2 = 64, C_OST = 68, C_S1 = 76, C_S2 = 80, C_S3 = 128, C_ZST = 88, F_ZGO = 92,
              C_KST = 96, C_U0 = 100, C_LD = 108, F_KGO = 116, C_END = 120, C_OL = 72, C_HF = 84;
#define NG_IN 0
#define GATE_WORD C_END
#define GATE_VAL 0
#define X_NZ 4
#define X_NW3 8
#define ZP_GATE 4u
#define ZL_GATE 4
#define ZL_AT 31
#define Z_SLEEP0 0
#define O_SLEEP 0
#define Z_SLEEP 8
#define OWIN 16
#define OW0 8
#define OWR 8
#define WD 2
#define W3_AT 10
#define X_NOLDW 0
#define X_NOLDO 0
#define X_NOCHK 0
#define KROT (nt * 2)
#define NG_LAST 0
#define PACE_IN "s_nop 7"
#define PACE_OUT "s_nop 1"
#define P3_PRIO 1
#define TOP_SLEEP 2
#define A_AUX 16
#define B_AUX 16
#define B_LDS 0
#define TL 8
#define XR_AT 62
#define W3_AUX 0
#define LORD 0
#define AORD 0
#define S2M 17
#define W3M (LORD ? 23 : 13)
#define A16 1
#define SEL4 2
#define KV_DOT 1
#define ZE_PRIO 3
#define P3_T1 99
#define P3_T2 99
#define AUX_PRIO 2

__global__ __launch_bounds__(768, 3) void k2_attn(char* __restrict__ ws, const char* __restrict__ Aq,
                                                  const char* __restrict__ Ak, const char* __restrict__ Av,
                                                  const char* __restrict__ shiftk, const char* __restrict__ ssmk,
                                                  const unsigned* __restrict__ ssmD, const char* __restrict__ Wo,
                                                  char* __restrict__ out) {
    __shared__ __attribute__((aligned(16))) char lds_raw[K2_LDS];
    const lds_ptr lds = (lds_ptr)lds_raw;
    const lds_ptr fl = lds + K2_FL;
    const unsigned lds0 = (unsigned)(uintptr_t)lds;
    const int tid = threadIdx.x, lane = tid & 63;
    const int w = __builtin_amdgcn_readfirstlane(tid >> 6);
    const int b = blockIdx.x, xcd = b & 7, idx = b >> 3;
    const int h = xcd * 4 + (idx >> 3), qb = idx & 7;
    const int tt = xcd * 2 + (idx >> 4), nt = idx & 15;
    const char* kvg = ws + WS_KV + (size_t)h * 64 * 4096;
    const __amdgpu_buffer_rsrc_t rs = __builtin_amdgcn_make_buffer_rsrc(ws, 0, 32 << 20, 0x00020000);
    const __amdgpu_buffer_rsrc_t ro = __builtin_amdgcn_make_buffer_rsrc(out, 0, 8 << 20, 0x00020000);
    gu32* const cnt = (gu32*)(ws + WS_CNT);
    auto signal = [&]() {
        if (!BK1 || lane < 32) (void)__hip_atomic_fetch_add(cnt + ((lane & 31) * 8 + (lane < 32 ? xcd : qb)) * 64, 1u, RLX_AGENT);
    };
    const unsigned wvoff = (unsigned)(lane >> 3) * 2048u + nt * 128 + (((lane & 7) ^ (((lane >> 4) & 1) << 2)) << 4);
    const int krot = KROT;
    auto wsrc = [&](int hf, int lp) { return Wo + (size_t)(hf * 64 + ((lp + krot * 2) & 63)) * 16384; };
    DG_DECL(1)
    const unsigned voff = lane * 16;
    const int cw = w & 3;
    auto issue = [&](int st) {
        const char* src = kvg + (st * 4 + cw) * 4096;
        const unsigned dst = lds0 + (st & 1) * K2_SLOT + cw * 4096;
#pragma unroll
        for (int i = 0; i < 4; ++i) glds16(voff, src + i * 1024, dst + i * 1024);
#pragma unroll
        for (int i = 0; i < 4; ++i) glds16(voff, src + 131072 + i * 1024, dst + 16384 + i * 1024);
    };
    asm volatile(".Lcp_k2a:");
    if (w >= 8) {
        __builtin_amdgcn_s_sleep(TOP_SLEEP);
#pragma unroll
        for (int i = 2; i < 8; ++i)
            glds16(voff, kvg + (i & 1) * 131072 + (i >> 1) * 4096 + cw * 1024, lds0 + (i & 1) * 16384 + (i >> 1) * 4096 + cw * 1024);
        RAW_BAR();
        issue(1);
        WAIT_BAR(8);
    } else {
        const int tq = cw * 2 + (w >> 2);
        const char* qg = ws + WS_Q + (size_t)QT(h, qb * 8 + tq) * 2048;
        glds16(voff, kvg + (w >> 2) * 131072 + cw * 1024, lds0 + (w >> 2) * 16384 + cw * 1024);
        glds16(voff, qg, lds0 + K2_SF + tq * 2048);
        glds16(voff, qg + 1024, lds0 + K2_SF + tq * 2048 + 1024);
    }
    int w2 = w;
    asm volatile("" : "+s"(w2));

    if (w2 >= 8) {
        __builtin_amdgcn_s_setprio(3);
        const int ch = xcd * 128 + idx * 4 + cw;
        const lds_ptr cv = lds + K2_CV + cw * CV_BYTES;
        {
            const u32x4 z4 = {0u, 0u, 0u, 0u};
            if (lane < 2) *(LAS u32x4*)(cv + CV_HA + lane * 16) = z4;
            if (lane < 30) *(LAS u32x4*)(cv + CV_KV + lane * 16) = z4;
            if (cw == 0 && lane < 16) *(LAS u32x4*)(fl + lane * 16) = z4;
        }
        const int c0 = xcd * 128 + cw * 32, cq = lane & 7, rg = lane >> 3;
        const int row0 = idx * 64 + rg * 8;
        const unsigned roff = (unsigned)row0 * 2048u + (c0 + cq * 4) * 2;
        const unsigned toffs = (unsigned)(c0 + (lane >> 3)) * 4096u + idx * 128;
        u32x4 sk[2];
        sk[0] = gld128((unsigned)(c0 + cq * 4) * 8u, shiftk); sk[1] = gld128((unsigned)(c0 + cq * 4) * 8u, shiftk + 16);
        unsigned dd = gld32((unsigned)(ch >> 1) * 4u, ssmD);
        auto stage = [&](const char* src, int dst) {
            const unsigned sbase = (unsigned)(idx * 64 + cw * 16 + (lane >> 4)) * 2048u + xcd * 256;
#pragma unroll
            for (int i = 0; i < 4; ++i)
                glds16p<K2_SNT>(sbase + (((lane & 15) ^ (((2 * cw + (i >> 1)) & 3) << 2)) << 4), src + i * 8192, lds0 + dst + cw * 4096 + i * 1024);
        };
        auto blank = [&](unsigned off) {
            const u32x4 e4 = {EMPTY, EMPTY, EMPTY, EMPTY};
            CFENCE();
            if (!BK1) { st_sc1(e4, rs, off + voff); st_sc1(e4, rs, off + 1024 + voff); }
            CFENCE();
        };
        WAIT_BAR(3);
        DG(12);
        issue(2);
        stage(Ak, K2_SF);
        glds16((unsigned)(idx * 64 - (idx ? 4 : 0) + (lane >> 4)) * 2048u + xcd * 256 + (((lane & 15) ^ 12) << 4), Ak, lds0 + K2_SCR + cw * 1024);
        blank((unsigned)WS_O + (unsigned)((qb * 8 + cw * 2) * 32 + h) * 2048u);
        auto warm = [&](int g) {
#pragma unroll
            for (int i = 0; i < 4; ++i) glds16(wvoff, wsrc(idx >> 4, 16 + g * 16 + cw * 4 + i), lds0 + K2_PAD);
        };
        WAITN_BAR(BK1 ? 5 : 7);
        DG(9);
        issue(3);
        stage(Av, K2_SF + 16384);
        blank((unsigned)WS_O + (unsigned)((qb * 8 + cw * 2 + 1) * 32 + h) * 2048u);
        WAITN_BAR(BK1 ? 4 : 6);
        DG(3);
        issue(4);
#pragma unroll
        for (int i = 0; i < 4; ++i) glds16p<K2_SNT>(voff, ssmk + (size_t)ch * 4096 + i * 1024, lds0 + K2_CV + cw * CV_BYTES + CV_HA + 32 + i * 1024);
        blank((unsigned)WS_ZT + ch * 4096); blank((unsigned)WS_ZT + ch * 4096 + 2048);
        WAITN_BAR(BK1 ? 4 : 8);
        DG(5);
        issue(5);
        SBAR();
        asm volatile("" : "+v"(sk[0]), "+v"(sk[1]), "+v"(dd));
        dd = __builtin_amdgcn_readfirstlane(dd);
        {
            u32x2 kr[11], vr[8];
            lds_ptr kb = lds + K2_SF + rg * 2048 + ((cw ^ (rg & 3)) << 6) + cq * 8;
            asm volatile("" : "+v"(kb));
            const lds_ptr hb = lds + (rg ? K2_SF + rg * 2048 - 768 : K2_SCR + cw * 1024 + 256) + ((cw ^ ((rg + 3) & 3)) << 6) + cq * 8;
#pragma unroll
            for (int i = 0; i < 3; ++i) kr[i] = *(const LAS u32x2*)(hb + i * 256);
#pragma unroll
            for (int i = 0; i < 8; ++i) kr[3 + i] = *(const LAS u32x2*)(kb + i * 256);
#pragma unroll
            for (int i = 0; i < 8; ++i) vr[i] = *(const LAS u32x2*)(kb + 16384 + i * 256);
            if (row0 == 0) { kr[0] = u32x2{0u, 0u}; kr[1] = u32x2{0u, 0u}; kr[2] = u32x2{0u, 0u}; }
            const lds_ptr tb = cv + CV_KV + 480;
#pragma unroll
            for (int c = 0; c < 4; ++c) {
                const unsigned s01 = sk[c >> 1][(c & 1) * 2], s23 = sk[c >> 1][(c & 1) * 2 + 1];
                unsigned kp[10];
                float kv[8], vf[8];
#pragma unroll
                for (int j = 0; j < 10; ++j) kp[j] = __builtin_amdgcn_perm(kr[j][c >> 1], kr[j + 1][c >> 1], (c & 1) ? 0x07060302u : 0x05040100u);
                SBAR();
#pragma unroll
                for (int i = 0; i < 8; ++i) asm("v_dot2_f32_bf16 %0, %1, %2, 0" : "=v"(kv[i]) : "v"(kp[i]), "v"(s23));
                SBAR();
#pragma unroll
                for (int i = 0; i < 8; ++i) asm("v_dot2_f32_bf16 %0, %1, %2, %0" : "+v"(kv[i]) : "v"(kp[i + 2]), "v"(s01));
                SBAR();
#pragma unroll
                for (int i = 0; i < 8; ++i) vf[i] = (c & 1) ? bfhi(vr[i][c >> 1]) : bflo(vr[i][c >> 1]);
                SBAR();
#pragma unroll
                for (int i = 0; i < 8; ++i) kv[i] = mul_vv(kv[i], vf[i]);
                u32x4 o4;
#pragma unroll
                for (int i = 0; i < 4; ++i) o4[i] = cvtpk(kv[7 - 2 * i], kv[6 - 2 * i]);
                *(LAS u32x4*)(tb + (cq * 4 + c) * 128 + ((rg ^ cq) << 4)) = o4;
            }
#pragma unroll
            for (int i = 0; i < 4; ++i) {
                const u32x4 t = *(const LAS u32x4*)(tb + i * 1024 + lane * 16);
                st_sc1(t, rs, (unsigned)WS_Q + (xcd * 32 + idx) * 16384 + cw * 4096 + i * 1024 + (lane >> 3) * 128 + (((lane & 7) ^ (2 * i + (lane >> 5))) << 4));
            }
        }
        CFENCE();
        DG(7);
        WAIT_BAR(4);
        DG(6);
        issue(6);
        asm volatile("s_waitcnt vmcnt(8)" ::: "memory");
        if (lds_add_rtn(fl + C_KST, lane) == 3u) signal();
#pragma unroll
        for (int i = 0; i < 8; ++i)
            glds16p<K2_WNT>(wvoff, wsrc(i >> 2, cw * 4 + (i & 3)), lds0 + K2_SF + (i >> 2) * 16384 + (cw * 4 + (i & 3)) * 1024);
        {
            const u32x4 z4 = {0u, 0u, 0u, 0u};
            if (lane < 2) *(LAS u32x4*)(cv + CV_HB + lane * 16) = z4;
#pragma unroll
            for (int i = 0; i < 4; ++i) {
                const int off = 32 + i * 1024 + lane * 16;
                const u32x4 hv = *(const LAS u32x4*)(cv + CV_HA + off);
                const unsigned pv = *(const LAS unsigned*)(cv + CV_HA + off - 4);
                u32x4 sh;
                sh[0] = __builtin_amdgcn_alignbit(hv[0], pv, 16);
                sh[1] = __builtin_amdgcn_alignbit(hv[1], hv[0], 16);
                sh[2] = __builtin_amdgcn_alignbit(hv[2], hv[1], 16);
                sh[3] = __builtin_amdgcn_alignbit(hv[3], hv[2], 16);
                *(LAS u32x4*)(cv + CV_HB + off) = sh;
            }
        }
        WAIT_BAR(8);
        DG(8);
        issue(7);
        auto warm23 = [&]() {
#pragma unroll
            for (int i = 0; i < 8; ++i)
                glds16p<K2_WNT>(wvoff, wsrc(idx >> 4, 32 + cw * 8 + i), lds0 + K2_SCR + cw * 1024);
        };
        auto chunk1 = [&]() {
#pragma unroll
            for (int i = 0; i < 8; ++i)
                glds16p<K2_WNT>(wvoff, wsrc(i >> 2, 16 + cw * 4 + (i & 3)), lds0 + (i >> 2) * 16384 + (cw * 4 + (i & 3)) * 1024);
        };
        warm23();
        WAIT_BAR(8);
        DG(11);
        if (cw == 0) { poll_ge(cnt + b * 64, BK1 ? 32u : 64u); lds_add(fl + F_KGO, lane); }
        else lds_wait_ge(fl + F_KGO, 1u);
        {
            u32x4 t[4];
#pragma unroll
            for (int i = 0; i < 4; ++i) t[i] = ld_sc1s(rs, (unsigned)WS_Q + (xcd * 32 + (lane >> 3)) * 16384 + (idx * 4 + cw) * 128 + (lane & 7) * 16, i * 131072);
#pragma unroll
            for (int i = 0; i < 4; ++i) *(LAS u32x4*)(cv + CV_KV + 480 + i * 1024 + lane * 16) = t[i];
        }
        const int r1 = lane & 15, q = lane >> 4;
        const int toff = (r1 & 1) ? CV_HA + 2 * (16 + r1 + 9 - 8 * q) : CV_HB + 2 * (16 + r1 + 10 - 8 * q);
        const int boff = CV_KV + 448 + 32 * r1 + 16 * q;
        f32x4 y[8];
        u32x4 tf[8];
        bf16x8 bf[8];
        const lds_ptr bp = cv + boff - 448;
        lds_ptr tp[4];
#pragma unroll
        for (int i = 0; i < 4; ++i) { tp[i] = cv + toff + 1024 * i; asm volatile("" : "+v"(tp[i])); }
#define LDT(bb, a, hf) do { const LAS unsigned* t_ = (const LAS unsigned*)(tp[(a) >> 1] + 64 * (bb) + 512 * ((a) & 1) + 8 * (hf)); \
            tf[a][2 * (hf)] = t_[0]; tf[a][2 * (hf) + 1] = t_[1]; SBAR(); } while (0)
#define LDB(bb, a) do { bf[a] = lds_rd128(bp + 448 - 64 * (bb) + 512 * (a)); SBAR(); } while (0)
#define CM(a, ww) do { asm volatile("v_mfma_f32_16x16x32_bf16 %0, %1, %2, %0" : "+v"(y[(a) + (ww)]) : "v"(tf[a]), "v"(bf[ww])); SBAR(); } while (0)
#define CM0(a, ww) do { asm volatile("v_mfma_f32_16x16x32_bf16 %0, %1, %2, 0" : "=&v"(y[(a) + (ww)]) : "v"(tf[a]), "v"(bf[ww])); SBAR(); } while (0)
#define PC() do { asm volatile(PACE_OUT); SBAR(); } while (0)
#pragma unroll
        for (int a = 0; a < 8; ++a) { if (a < 5) { LDT(0, a, 0); LDT(0, a, 1); } LDB(0, 7 - a); }
        chunk1();
#pragma unroll
        for (int i = 0; i < 4; ++i)
            glds16p<K2_QNT>((unsigned)(idx * 64 + cw * 16 + (lane >> 4)) * 2048u + xcd * 256 + (lane & 15) * 16, Aq + i * 8192, lds0 + K2_SCR + cw * 1024);
        RAW_BAR();
        asm volatile("s_waitcnt vmcnt(%0)" :: "n"(AORD == 2 ? 12 : 4) : "memory");
        lds_add(fl + C_S1, lane);
#define ZFL() PC()
        __builtin_amdgcn_s_setprio(AUX_PRIO);
        const float dch = (ch & 1) ? bfhi(dd) : bflo(dd);
        const lds_ptr kdp = cv + CV_KV + 480 + 32 * r1 + 16 * (q >> 1) + ((q & 1) ? 0 : 8);
        float kd[8][4];
        u32x2 kk;
        unsigned kc0, kc1, zo[8][2];
#define KD_RD(t) do { kk = *(const LAS u32x2*)(kdp + 512 * (t)); SBAR(); } while (0)
#define KD_UP0(t) do { kd[t][0] = bfhi(kk[1]); kd[t][1] = bflo(kk[1]); SBAR(); } while (0)
#define KD_UP1(t) do { kd[t][2] = bfhi(kk[0]); kd[t][3] = bflo(kk[0]); SBAR(); } while (0)
#define KD_MU0(t) do { kd[t][0] = mul_sv(dch, kd[t][0]); kd[t][1] = mul_sv(dch, kd[t][1]); SBAR(); } while (0)
#define KD_MU1(t) do { kd[t][2] = mul_sv(dch, kd[t][2]); kd[t][3] = mul_sv(dch, kd[t][3]); SBAR(); } while (0)
#define KD_CV(t) do { kc0 = cvtpk(kd[t][0], kd[t][1]); kc1 = cvtpk(kd[t][2], kd[t][3]); SBAR(); } while (0)
#define KD_X0(t) do { kd[t][0] = bflo(kc0); kd[t][1] = bfhi(kc0); SBAR(); } while (0)
#define KD_X1(t) do { kd[t][2] = bflo(kc1); kd[t][3] = bfhi(kc1); SBAR(); } while (0)
#define ZF0(t) do { kc0 = cvtpk(y[t][0], y[t][1]); kc1 = cvtpk(y[t][2], y[t][3]); SBAR(); } while (0)
#define ZF1(t) do { y[t][0] = bflo(kc0); y[t][1] = bfhi(kc0); SBAR(); } while (0)
#define ZF2(t) do { y[t][2] = bflo(kc1); y[t][3] = bfhi(kc1); SBAR(); } while (0)
#define ZF3(t) do { y[t][0] = add_vv(y[t][0], kd[t][0]); y[t][1] = add_vv(y[t][1], kd[t][1]); SBAR(); } while (0)
#define ZF4(t) do { y[t][2] = add_vv(y[t][2], kd[t][2]); y[t][3] = add_vv(y[t][3], kd[t][3]); SBAR(); } while (0)
#define ZF5(t) do { zo[t][0] = nz(cvtpk(y[t][0], y[t][1])); zo[t][1] = nz(cvtpk(y[t][2], y[t][3])); SBAR(); } while (0)
#define ZST(pr) do { auto s0 = __builtin_amdgcn_permlane16_swap(zo[2 * (pr)][0], zo[2 * (pr) + 1][0], false, false); \
            auto s1 = __builtin_amdgcn_permlane16_swap(zo[2 * (pr)][1], zo[2 * (pr) + 1][1], false, false); \
            st_sc1(u32x4{s0[0], s1[0], s0[1], s1[1]}, rs, (unsigned)WS_ZT + ch * 4096 + (pr) * 1024 + voff); SBAR(); } while (0)
#define W3(i) do { if ((i) == 0) PC(); } while (0)
#define HF() do { lds_add(fl + C_HF, lane); SBAR(); } while (0)
        CM0(0, 7); LDB(1, 7); CM0(0, 6); LDT(0, 5, 0); CM0(0, 5); LDT(0, 5, 1); CM0(0, 4); LDT(0, 6, 0); CM0(0, 3); LDT(0, 6, 1); CM0(0, 2); LDT(0, 7, 0); CM0(0, 1); LDT(0, 7, 1); CM0(0, 0); KD_RD(0);
        CM(1, 6); LDB(1, 6); CM(1, 5); LDT(1, 0, 0); CM(1, 4); LDT(1, 0, 1); CM(1, 3); PC(); CM(1, 2); PC(); CM(1, 1); KD_UP0(0); CM(1, 0); KD_UP1(0);
        CM(2, 5); LDB(1, 5); CM(2, 4); LDT(1, 1, 0); CM(2, 3); LDT(1, 1, 1); CM(2, 2); KD_MU0(0); CM(2, 1); KD_MU1(0); CM(2, 0); KD_CV(0);
        CM(3, 4); LDB(1, 4); CM(3, 3); LDT(1, 2, 0); CM(3, 2); LDT(1, 2, 1); CM(3, 1); KD_X0(0); CM(3, 0); KD_X1(0);
        CM(4, 3); LDB(1, 3); CM(4, 2); LDT(1, 3, 0); CM(4, 1); LDT(1, 3, 1); CM(4, 0); KD_RD(1);
        CM(5, 2); LDB(1, 2); CM(5, 1); LDT(1, 4, 0); CM(5, 0); LDT(1, 4, 1);
        CM(6, 1); LDB(1, 1); CM(6, 0); PC();
        CM(7, 0); LDB(1, 0);
        DGV(6);
        CM(0, 7); LDB(2, 7); CM(0, 6); LDT(1, 5, 0); CM(0, 5); LDT(1, 5, 1); CM(0, 4); LDT(1, 6, 0); CM(0, 3); LDT(1, 6, 1); CM(0, 2); LDT(1, 7, 0); CM(0, 1); LDT(1, 7, 1); CM(0, 0); PC();
        CM(1, 6); LDB(2, 6); CM(1, 5); LDT(2, 0, 0); CM(1, 4); LDT(2, 0, 1); CM(1, 3); KD_UP0(1); CM(1, 2); KD_UP1(1); CM(1, 1); KD_MU0(1); CM(1, 0); KD_MU1(1);
        CM(2, 5); LDB(2, 5); CM(2, 4); LDT(2, 1, 0); CM(2, 3); LDT(2, 1, 1); CM(2, 2); KD_CV(1); CM(2, 1); KD_X0(1); CM(2, 0); KD_X1(1);
        CM(3, 4); LDB(2, 4); CM(3, 3); LDT(2, 2, 0); CM(3, 2); LDT(2, 2, 1); CM(3, 1); KD_RD(2); CM(3, 0); PC();
        CM(4, 3); LDB(2, 3); CM(4, 2); LDT(2, 3, 0); CM(4, 1); LDT(2, 3, 1); CM(4, 0); PC();
        CM(5, 2); LDB(2, 2); CM(5, 1); LDT(2, 4, 0); CM(5, 0); LDT(2, 4, 1);
        CM(6, 1); LDB(2, 1); CM(6, 0); KD_UP0(2);
        CM(7, 0); LDB(2, 0);
        DGV(8);
        CM(0, 7); LDB(3, 7); CM(0, 6); LDT(2, 5, 0); CM(0, 5); LDT(2, 5, 1); CM(0, 4); LDT(2, 6, 0); CM(0, 3); LDT(2, 6, 1); CM(0, 2); LDT(2, 7, 0); CM(0, 1); LDT(2, 7, 1); CM(0, 0); KD_UP1(2);
        CM(1, 6); LDB(3, 6); CM(1, 5); LDT(3, 0, 0); CM(1, 4); LDT(3, 0, 1); CM(1, 3); KD_MU0(2); CM(1, 2); KD_MU1(2); CM(1, 1); KD_CV(2); CM(1, 0); KD_X0(2);
        CM(2, 5); LDB(3, 5); CM(2, 4); LDT(3, 1, 0); CM(2, 3); LDT(3, 1, 1); CM(2, 2); KD_X1(2); CM(2, 1); KD_RD(3); CM(2, 0); PC();
        CM(3, 4); LDB(3, 4); CM(3, 3); LDT(3, 2, 0); CM(3, 2); LDT(3, 2, 1); CM(3, 1); PC(); CM(3, 0); KD_UP0(3);
        CM(4, 3); LDB(3, 3); CM(4, 2); LDT(3, 3, 0); CM(4, 1); LDT(3, 3, 1); CM(4, 0); KD_UP1(3);
        CM(5, 2); LDB(3, 2); CM(5, 1); LDT(3, 4, 0); CM(5, 0); LDT(3, 4, 1);
        CM(6, 1); LDB(3, 1); CM(6, 0); KD_MU0(3);
        CM(7, 0); LDB(3, 0);
        DGV(12);
        CM(0, 7); LDB(4, 7); CM(0, 6); LDT(3, 5, 0); CM(0, 5); LDT(3, 5, 1); CM(0, 4); LDT(3, 6, 0); CM(0, 3); LDT(3, 6, 1); CM(0, 2); LDT(3, 7, 0); CM(0, 1); LDT(3, 7, 1); CM(0, 0); KD_MU1(3);
        CM(1, 6); LDB(4, 6); CM(1, 5); LDT(4, 0, 0); CM(1, 4); LDT(4, 0, 1); CM(1, 3); KD_CV(3); CM(1, 2); KD_X0(3); CM(1, 1); KD_X1(3); CM(1, 0); KD_RD(4);
        CM(2, 5); LDB(4, 5); CM(2, 4); LDT(4, 1, 0); CM(2, 3); LDT(4, 1, 1); CM(2, 2); PC(); CM(2, 1); PC(); CM(2, 0); KD_UP0(4);
        CM(3, 4); LDB(4, 4); CM(3, 3); LDT(4, 2, 0); CM(3, 2); LDT(4, 2, 1); CM(3, 1); KD_UP1(4); CM(3, 0); KD_MU0(4);
        CM(4, 3); LDB(4, 3); CM(4, 2); LDT(4, 3, 0); CM(4, 1); LDT(4, 3, 1); CM(4, 0); KD_MU1(4);
        CM(5, 2); LDB(4, 2); CM(5, 1); LDT(4, 4, 0); CM(5, 0); LDT(4, 4, 1);
        CM(6, 1); LDB(4, 1); CM(6, 0); KD_CV(4);
        CM(7, 0); LDB(4, 0);
        DGV(9);
        CM(0, 7); LDB(5, 7); CM(0, 6); LDT(4, 5, 0); CM(0, 5); LDT(4, 5, 1); CM(0, 4); LDT(4, 6, 0); CM(0, 3); LDT(4, 6, 1); CM(0, 2); LDT(4, 7, 0); CM(0, 1); LDT(4, 7, 1); CM(0, 0); KD_X0(4);
        CM(1, 6); LDB(5, 6); CM(1, 5); LDT(5, 0, 0); CM(1, 4); LDT(5, 0, 1); CM(1, 3); KD_X1(4); CM(1, 2); KD_RD(5); CM(1, 1); PC(); CM(1, 0); PC();
        CM(2, 5); LDB(5, 5); CM(2, 4); LDT(5, 1, 0); CM(2, 3); LDT(5, 1, 1); CM(2, 2); KD_UP0(5); CM(2, 1); KD_UP1(5); CM(2, 0); KD_MU0(5);
        CM(3, 4); LDB(5, 4); CM(3, 3); LDT(5, 2, 0); CM(3, 2); LDT(5, 2, 1); CM(3, 1); KD_MU1(5); CM(3, 0); KD_CV(5);
        CM(4, 3); LDB(5, 3); CM(4, 2); LDT(5, 3, 0); CM(4, 1); LDT(5, 3, 1); CM(4, 0); KD_X0(5);
        CM(5, 2); LDB(5, 2); CM(5, 1); LDT(5, 4, 0); CM(5, 0); LDT(5, 4, 1);
        CM(6, 1); LDB(5, 1); CM(6, 0); KD_X1(5);
        CM(7, 0); LDB(5, 0);
        DGV(11);
        CM(0, 7); LDB(6, 7); CM(0, 6); LDT(5, 5, 0); CM(0, 5); LDT(5, 5, 1); CM(0, 4); LDT(5, 6, 0); CM(0, 3); LDT(5, 6, 1); CM(0, 2); LDT(5, 7, 0); CM(0, 1); LDT(5, 7, 1); CM(0, 0); KD_RD(6);
        CM(1, 6); LDB(6, 6); CM(1, 5); LDT(6, 0, 0); CM(1, 4); LDT(6, 0, 1); CM(1, 3); PC(); CM(1, 2); PC(); CM(1, 1); KD_UP0(6); CM(1, 0); KD_UP1(6);
        CM(2, 5); LDB(6, 5); CM(2, 4); LDT(6, 1, 0); CM(2, 3); LDT(6, 1, 1); CM(2, 2); KD_MU0(6); CM(2, 1); KD_MU1(6); CM(2, 0); KD_CV(6);
        CM(3, 4); LDB(6, 4); CM(3, 3); LDT(6, 2, 0); CM(3, 2); LDT(6, 2, 1); CM(3, 1); KD_X0(6); CM(3, 0); KD_X1(6);
        CM(4, 3); LDB(6, 3); CM(4, 2); LDT(6, 3, 0); CM(4, 1); LDT(6, 3, 1); CM(4, 0); KD_RD(7);
        CM(5, 2); LDB(6, 2); CM(5, 1); LDT(6, 4, 0); CM(5, 0); LDT(6, 4, 1);
        CM(6, 1); LDB(6, 1); CM(6, 0); PC();
        CM(7, 0); LDB(6, 0);
        DGV(7);
        CM(0, 7); LDB(7, 7); CM(0, 6); LDT(6, 5, 0); CM(0, 5); LDT(6, 5, 1); CM(0, 4); LDT(6, 6, 0); CM(0, 3); LDT(6, 6, 1); CM(0, 2); LDT(6, 7, 0); CM(0, 1); LDT(6, 7, 1); CM(0, 0); PC();
        CM(1, 6); LDB(7, 6); CM(1, 5); LDT(7, 0, 0); CM(1, 4); LDT(7, 0, 1); CM(1, 3); KD_UP0(7); CM(1, 2); KD_UP1(7); CM(1, 1); KD_MU0(7); CM(1, 0); KD_MU1(7);
        CM(2, 5); LDB(7, 5); CM(2, 4); LDT(7, 1, 0); CM(2, 3); LDT(7, 1, 1); CM(2, 2); KD_CV(7); CM(2, 1); KD_X0(7); CM(2, 0); KD_X1(7);
        CM(3, 4); LDB(7, 4); CM(3, 3); LDT(7, 2, 0); CM(3, 2); LDT(7, 2, 1); CM(3, 1); PC(); CM(3, 0); PC();
        CM(4, 3); LDB(7, 3); CM(4, 2); LDT(7, 3, 0); CM(4, 1); LDT(7, 3, 1); CM(4, 0); PC();
        CM(5, 2); LDB(7, 2); CM(5, 1); LDT(7, 4, 0); CM(5, 0); LDT(7, 4, 1);
        CM(6, 1); LDB(7, 1); CM(6, 0); PC();
        CM(7, 0); LDB(7, 0);
        DGV(3);
        CM(0, 3); LDT(7, 5, 0); CM(0, 2); LDT(7, 5, 1); CM(1, 2); LDT(7, 6, 0); CM(1, 1); LDT(7, 6, 1); CM(2, 1); LDT(7, 7, 0); CM(2, 0); LDT(7, 7, 1);
        CM(3, 0); HF(); CM(0, 1); ZFL(); CM(0, 0); W3(0); CM(1, 0); ZF0(2); W3(1); CM(0, 5); ZF1(2); W3(2); CM(0, 4); ZF2(2); W3(3);
        CM(1, 4); ZF3(2); W3(4); CM(1, 3); ZF4(2); W3(5); CM(2, 3); ZF5(2); W3(6); CM(2, 2); ZF0(3); W3(7); CM(3, 2); ZF1(3);  CM(3, 1); ZF2(3);
        CM(4, 1); ZF3(3);  CM(4, 0); ZF4(3);  CM(5, 0); ZF5(3);  CM(0, 7); ZST(1);  CM(0, 6); ZF0(0);  CM(1, 6); ZF1(0);
        CM(1, 5); ZF2(0);  CM(2, 5); ZF3(0);  CM(2, 4); ZF4(0);  CM(3, 4); ZF5(0);  CM(3, 3); ZF0(1);  CM(4, 3); ZF1(1);
        CM(4, 2); ZF2(1);  CM(5, 2); ZF3(1);  CM(5, 1); ZF4(1);  CM(6, 1); ZF5(1);  CM(6, 0); ZST(0);  CM(7, 0); ZF0(4);
        DG(4);
        ZF1(4); ZF2(4); ZF3(4); ZF4(4); ZF5(4); ZF0(5); ZF1(5); ZF2(5); ZF3(5); ZF4(5); ZF5(5); ZST(2); ZF0(6); ZF1(6); ZF2(6); ZF3(6); ZF4(6); ZF5(6); ZF0(7); ZF1(7); ZF2(7); ZF3(7); ZF4(7); ZF5(7); ZST(3);
        DG(10);
        __builtin_amdgcn_s_setprio(3);
        while ((unsigned)__builtin_amdgcn_readfirstlane(*(volatile LAS unsigned*)(fl + C_OL)) < ZL_GATE) __builtin_amdgcn_s_sleep(4);
        CFENCE();
        const int arow = idx * 64 + (cw >> 1) * 32 + rg * 4, acol = xcd * 256 + (cw & 1) * 128 + cq * 16;
        u32x4 qr[4];
#pragma unroll
        for (int i = 0; i < 4; ++i) qr[i] = gld128((unsigned)(arow * 2048 + acol), Aq + i * 2048);
        {
            const lds_ptr tr = cv + CV_KV + 480;
            u32x4 t[4];
            const int ck = (cw >> 1) * 4 + ((lane ^ ((lane >> 5) * 2)) & 3);
            const unsigned zb = (unsigned)WS_ZT + (unsigned)(xcd * 128 + (cw & 1) * 64 + (lane >> 4) * 4) * 4096u + (idx >> 3) * 1024 +
                                ((idx & 3) * 4 + ((idx >> 2) & 1) * 16 + (ck >> 1) + (ck & 1) * 32) * 16;
#pragma unroll 1
            for (unsigned spins = 0; spins < (1u << 20); ++spins) {
#pragma unroll
                for (int i = 0; i < 4; ++i) t[i] = ld_sc1s(rs, zb + ((((lane >> 2) & 3) ^ i) << 12), i * 65536);
                if (!missing(mx4(mx4(mx8(t[0], t[1]), t[2]), t[3]))) break;
                __builtin_amdgcn_s_sleep(Z_SLEEP);
            }
            DG(13);
            __builtin_amdgcn_s_setprio(ZE_PRIO);
#pragma unroll
            for (int i = 0; i < 4; ++i) *(LAS u32x4*)(tr + i * 1024 + lane * 16) = t[i];
#pragma unroll
            for (int i = 0; i < 4; ++i) asm volatile("" : "+v"(qr[i]));
            u32x2 zc[8];
#pragma unroll
            for (int c = 0; c < 8; ++c)
                zc[c] = *(const LAS u32x2*)(tr + cq * 512 + (c >> 2) * 256 + ((((c & 3) * 4 + (rg >> 1)) ^ (cq * 2)) << 4) + (rg & 1) * 8);
#pragma unroll
            for (int i = 0; i < 4; ++i) {
                u32x4 o;
#pragma unroll
                for (int c = 0; c < 4; ++c) {
                    const unsigned z0 = zc[2 * c][i >> 1], z1 = zc[2 * c + 1][i >> 1];
                    o[c] = cvtpk(mul_vv(bflo(qr[i][c]), (i & 1) ? bfhi(z0) : bflo(z0)), mul_vv(bfhi(qr[i][c]), (i & 1) ? bfhi(z1) : bflo(z1)));
                }
                __builtin_amdgcn_raw_buffer_store_b128(o, ro, arow * 4096 + acol, i * 4096, OUT_AUX);
            }
        }
        asm volatile(".Lcp_k2z:");
        DG_END(14);
        return;
    }

    const int half = w >> 2;
    const int TA = qb * 8 + (w & 3) * 2 + half, TB = TA ^ 1;
    bf16x8 sel;
    {
        const short one = (((lane >> 2) & 1) == ((lane >> 4) & 1) && (lane & 11) == 0) ? (short)0x3f80 : (short)0;
#pragma unroll
        for (int i = 0; i < 8; ++i) sel[i] = SPM ? ((lane & 15) == (i & 4) ? (short)0x3f80 : (short)0) : one;
    }
    int sidx = (int)0xdddddddd;
#define LO8(x) __builtin_bit_cast(bfx16_t, __builtin_shufflevector(x, x, 0, 1, 2, 3, 4, 5, 6, 7))
#define HI8(x) __builtin_bit_cast(bfx16_t, __builtin_shufflevector(x, x, 8, 9, 10, 11, 12, 13, 14, 15))
#define SPV(v, p, o) __builtin_amdgcn_smfmac_f32_32x32x32_bf16(__builtin_bit_cast(bfx8_t, v), p, o, sidx, 0, 0)
#define SSUM(p, l) __builtin_amdgcn_smfmac_f32_16x16x64_bf16(__builtin_bit_cast(bfx8_t, sel), p, l, sidx, 0, 0)
    f32x16 oA = f32x16{}, oB = f32x16{};
    f32x4 lA = f32x4{}, lB = f32x4{};
    f32x16 negm, sA, sB;
    lds_ptr lp = lds + half * 16384 + lane * 16;
    bf16x8 k0[2], k1[2], v0[2], v1[2];
    u32x4 p0, p1 = {0u, 0u, 0u, 0u};
#define EX(X, i) X[i] = __builtin_amdgcn_exp2f(X[i])
    lds_ptr qpa = lds + K2_SF + (TA & 7) * 2048 + lane * 16, qpb = lds + K2_SF + (TB & 7) * 2048 + lane * 16;
    asm volatile("" : "+v"(qpa), "+v"(qpb), "+v"(lp), "+v"(sel), "+v"(oA), "+v"(oB), "+v"(lA), "+v"(lB), "+v"(sidx));
    WAIT_BAR(0);
    DG(3);
    const bf16x8 qa0 = lds_rd128(qpa), qb0 = lds_rd128(qpb);
    const bf16x8 x0 = lds_rd128(lds + lane * 16), x1 = lds_rd128(lds + lane * 16 + 1024);
    const bf16x8 qa1 = lds_rd128(qpa + 1024), qb1 = lds_rd128(qpb + 1024);
    {
        k0[0] = lds_rd128(lp); k1[0] = lds_rd128(lp + 1024);
        v0[0] = lds_rd128(lp + 2048); v1[0] = lds_rd128(lp + 3072);
        sA = MFMA32(x0, qa0, f32x16{});
        sB = MFMA32(x0, qb0, f32x16{});
        sA = MFMA32(x1, qa1, sA);
        sB = MFMA32(x1, qb1, sB);
        float m = fmx(sA[0], sA[1]), m2 = fmx(sB[0], sB[1]);
#pragma unroll
        for (int i = 2; i < 16; i += 2) m = fmx(fmx(m, sA[i]), sA[i + 1]);
#pragma unroll
        for (int i = 2; i < 16; i += 2) m2 = fmx(fmx(m2, sB[i]), sB[i + 1]);
        m = fmx(m, m2);
        auto rr = __builtin_amdgcn_permlane32_swap(__float_as_uint(m), __float_as_uint(m), false, false);
        m = fmx(__uint_as_float(rr[0]), __uint_as_float(rr[1]));
#pragma unroll
        for (int i = 0; i < 16; ++i) negm[i] = -m;
        sA = MFMA32(k0[0], qa0, negm);
        sA = MFMA32(k1[0], qa1, sA);
        v1[1] = v1[0];
#pragma unroll
        for (int i = 8; i < 16; ++i) sB[i] = 0.f;
        asm volatile("" : "+v"(sB));
        SBAR();
        RAW_BAR();
        SBAR();
        EX(sA, 0); EX(sA, 1);
    }
    DG(10);
#define SUM16(l, p) asm volatile("v_mfma_f32_16x16x32_bf16 %0, %1, %2, %0" : "+v"(l) : "v"(sel), "v"(p))
#define HALF(C, oC, lC, N, qx, qy, ka, kb, oP, lP, vprev, vcur0, PRE) do { \
        oP = SPV(vprev, HI8(N), oP); SBAR(); \
        PRE; EX(C, 2); EX(C, 3); EX(C, 4); SBAR(); \
        lP = SSUM(HI8(N), lP); SBAR(); \
        EX(C, 5); SBAR(); \
        N = MFMA32(ka, qx, negm); SBAR(); \
        EX(C, 6); EX(C, 7); EX(C, 8); SBAR(); \
        N = MFMA32(kb, qy, N); SBAR(); \
        EX(C, 9); EX(C, 10); EX(C, 11); SBAR(); \
        oC = SPV(vcur0, LO8(C), oC); SBAR(); \
        EX(C, 12); EX(C, 13); EX(C, 14); SBAR(); \
        lC = SSUM(LO8(C), lC); SBAR(); \
        EX(C, 15); EX(N, 0); EX(N, 1); SBAR(); \
    } while (0)
#define RD4(Y, nx) do { v1[Y] = lds_rd128((nx) + 3072); v0[Y] = lds_rd128((nx) + 2048); \
        k1[Y] = lds_rd128((nx) + 1024); k0[Y] = lds_rd128(nx); } while (0)
#define KT(X, nx, PRE0) do { \
        HALF(sA, oA, lA, sB, qb0, qb1, k0[X], k1[X], oB, lB, v1[X ^ 1], v0[X], PRE0; RD4(X ^ 1, nx)); \
        HALF(sB, oB, lB, sA, qa0, qa1, k0[X ^ 1], k1[X ^ 1], oA, lA, v1[X], v0[X], (void)0); \
    } while (0)
#define RING_BAR() do { unsigned long long sv_; \
        asm volatile("s_cmp_eq_u32 %1, 7\n\ts_cbranch_scc1 1f\n\ts_barrier\n1:\n\ts_mov_b64 %0, exec\n\ts_mov_b64 exec, 1\n\tds_add_u32 %2, %3\n\ts_mov_b64 exec, %0" \
                     : "=&s"(sv_) : "s"(st), "v"(fl + C_LD), "v"(1u) : "memory", "scc"); } while (0)
    SBAR();
#pragma unroll 1
    for (int st = 0; st < 8; ++st) {
        const lds_ptr cur = lp + (st & 1) * K2_SLOT;
        const lds_ptr nxt = lp + ((st + 1) & 1) * K2_SLOT;
        PRIO(2);
        KT(0, cur + 4096, (void)0);
        PRIO(1);
        KT(1, cur + 8192, (void)0);
        PRIO(0);
        KT(0, cur + 12288, (void)0);
        PRIO(3);
        KT(1, nxt, RAW_BAR(); SBAR());
    }
    oB = SPV(v1[1], HI8(sB), oB);
    SBAR();
    lB = SSUM(HI8(sB), lB);
    SBAR();
    DG(4);
    {
        const lds_ptr xo = lds + K2_SLOT + w * 4096 + lane * 16;
        *(LAS float*)(lds + K2_LX + w * 256 + lane * 4) = lB[0];
#pragma unroll
        for (int i = 0; i < 4; ++i) *(LAS f32x4*)(xo + i * 1024) = f32x4{oB[4 * i], oB[4 * i + 1], oB[4 * i + 2], oB[4 * i + 3]};
        lds_add(fl + F_X + w * 4, lane);
        lds_add(fl + C_END + half * 4, lane);
        lds_wait_ge(fl + F_X + (w ^ 4) * 4, 1u);
        const lds_ptr xi = lds + K2_SLOT + (w ^ 4) * 4096 + lane * 16;
        f32x4 t[4];
        const float lx = *(const LAS float*)(lds + K2_LX + (w ^ 4) * 256 + (lane & 31) * 4);
        { auto rr = __builtin_amdgcn_permlane32_swap(__float_as_uint(lA[0]), __float_as_uint(lA[0]), false, false); lA[0] = __uint_as_float(rr[0]); }
#pragma unroll
        for (int i = 0; i < 4; ++i) t[i] = *(const LAS f32x4*)(xi + i * 1024);
        SBAR();
        lA[0] += lx;
#pragma unroll
        for (int i = 0; i < 16; ++i) { float o = oA[i]; acc_vv(o, t[i >> 2][i & 3]); oA[i] = o; }
    }
    DG(9);
    const float l = lA[0];
    u32x4 on0, on1;
    auto finish = [&](float ls) {
        float rl = __builtin_amdgcn_rcpf(ls);
        asm volatile("s_nop 1" : "+v"(rl));
        f32x16 o;
#pragma unroll
        for (int i = 0; i < 16; ++i) o[i] = mul_vv(oA[i], rl);
        on0 = nz4(pack8n(o, 0)); on1 = nz4(pack8n(o, 1));
    };
    finish(l);
    {
        float t0, t1;
        asm("v_mul_f32_e32 %0, 0, %2\n\tv_mul_f32_e32 %1, 0, %3\n\tv_fmac_f32_e32 %0, 0, %4\n\tv_fmac_f32_e32 %1, 0, %5\n\tv_fmac_f32_e32 %0, 0, %6\n\tv_fmac_f32_e32 %1, 0, %7\n\tv_fmac_f32_e32 %0, 0, %8\n\tv_fmac_f32_e32 %1, 0, %9\n\t"
            "v_fmac_f32_e32 %0, 0, %10\n\tv_fmac_f32_e32 %1, 0, %11\n\tv_fmac_f32_e32 %0, 0, %12\n\tv_fmac_f32_e32 %1, 0, %13\n\tv_fmac_f32_e32 %0, 0, %14\n\tv_fmac_f32_e32 %1, 0, %15\n\tv_fmac_f32_e32 %0, 0, %16\n\tv_fmac_f32_e32 %1, 0, %17"
            : "=&v"(t0), "=&v"(t1) : "v"(oA[0]), "v"(oA[1]), "v"(oA[2]), "v"(oA[3]), "v"(oA[4]), "v"(oA[5]), "v"(oA[6]), "v"(oA[7]), "v"(oA[8]), "v"(oA[9]), "v"(oA[10]), "v"(oA[11]), "v"(oA[12]), "v"(oA[13]), "v"(oA[14]), "v"(oA[15]));
        const bool ok = (l <= 1.0e30f) && (l >= 1.0e-18f) && (t0 + t1 == 0.f);
        if (__builtin_expect(__any(!ok), 0)) {
            float m = -INFINITY, ls = 0.f;
            oA = f32x16{};
#pragma unroll 1
            for (int t = 0; t < 64; ++t) {
                const char* g = kvg + t * 4096 + lane * 16;
                const bf16x8 a0 = *(const bf16x8*)g, a1 = *(const bf16x8*)(g + 1024);
                const bf16x8 b0 = *(const bf16x8*)(g + 2048), b1 = *(const bf16x8*)(g + 3072);
                f32x16 s = MFMA32(a0, qa0, f32x16{});
                s = MFMA32(a1, qa1, s);
                float tm = s[0];
#pragma unroll
                for (int i = 1; i < 16; ++i) tm = __builtin_fmaxf(tm, s[i]);
                auto rr = __builtin_amdgcn_permlane32_swap(__float_as_uint(tm), __float_as_uint(tm), false, false);
                tm = __builtin_fmaxf(__uint_as_float(rr[0]), __uint_as_float(rr[1]));
                const float mn = __builtin_fmaxf(m, tm);
                const float alpha = __builtin_amdgcn_exp2f(m - mn);
                m = mn;
                float ps = 0.f;
#pragma unroll
                for (int i = 0; i < 16; ++i) { s[i] = bfhi(__float_as_uint(__builtin_amdgcn_exp2f(s[i] - mn))); ps += s[i]; oA[i] *= alpha; }
                ls = ls * alpha + ps;
                oA = SPV(b0, LO8(s), oA);
                oA = SPV(b1, HI8(s), oA);
            }
            auto rr = __builtin_amdgcn_permlane32_swap(__float_as_uint(ls), __float_as_uint(ls), false, false);
            asm volatile("s_nop 7\n\ts_nop 7" : "+v"(oA));
            finish(__uint_as_float(rr[0]) + __uint_as_float(rr[1]));
        }
    }
    u32x4 w3[4];
    const char* s2 = wsrc(half ^ 1, 32 + (w & 3) * 4);
    const char* s3 = wsrc(half, 48 + (w & 3) * 4);
    auto ldw2 = [&]() {
#pragma unroll
        for (int i = 0; i < 4; ++i)
            glds16p<K2_WNT>(wvoff, s2 + i * 16384, lds0 + K2_SLOT + ((w ^ 4) * 4 + i) * 1024);
    };
    auto ldw3 = [&]() {
#pragma unroll
        for (int i = 0; i < 4; ++i) w3[i] = gld128(wvoff, s3 + i * 16384);
    };
    {
        const unsigned og = (unsigned)WS_O + (unsigned)(TA * 32 + h) * 2048u + lane * 16;
        st_sc1(on0, rs, og);
        st_sc1(on1, rs, og + 1024);
        lds_add(fl + C_S1, lane);
        lds_wait_ge(fl + C_S1, 12u);
        DG(5);
        ldw2();
        ldw3();
    }
    {
        const int half = w & 1;
        const int T3 = tt * 4 + (w >> 1);
        const unsigned og = (unsigned)WS_O + (unsigned)T3 * 65536u + half * 32768 + lane * 16;
        const int r = lane & 31, hh = lane >> 5;
        const int g = lane >> 4, q = (lane & 15) >> 2, p = lane & 3;
        int woff[2];
#pragma unroll
        for (int n = 0; n < 2; ++n) {
            const int c = 4 * (n ^ half) + 2 * (p & 1) + (g & 1);
            woff[n] = (4 * hh + q) * 128 + ((c ^ ((q >> 1) << 2)) << 4) + (p >> 1) * 8;
        }
        f32x16 acc[2] = {f32x16{}, f32x16{}};
        lds_ptr wl[3][2] = {{lds + half * 16384 + woff[0], lds + half * 16384 + woff[1]},
                            {lds + K2_SF + half * 16384 + woff[0], lds + K2_SF + half * 16384 + woff[1]},
                            {lds + K2_CV + half * 2 * CV_BYTES + woff[0], lds + K2_CV + half * 2 * CV_BYTES + woff[1]}};
        asm volatile("" : "+v"(wl[0][0]), "+v"(wl[0][1]), "+v"(wl[1][0]), "+v"(wl[1][1]), "+v"(wl[2][0]), "+v"(wl[2][1]));
#define MS(m) ((m) < 64 - 2 * TL ? (m) >> 1 : 32 - TL + ((m) - (64 - 2 * TL)) % TL)
#define MN(m) ((m) < 64 - 2 * TL ? (m) & 1 : (m) < 64 - TL)
        constexpr int WR = 2 * WD + 2;
        s16x4 wa[WR], wb[WR];
#define LDW(m) do { const int s_ = MS(m), f_ = s_ < 8 ? 1 : s_ < 24 ? 0 : 2; \
            const int o_ = f_ == 2 ? ((s_ >> 2) & 1) * CV_BYTES + (s_ & 3) * 2048 : (f_ ? 0 : ((s_ >> 3) - 1) * K2_SLOT) + (s_ & 7) * 2048; \
            wa[(m) % WR] = lds_tr(wl[f_][MN(m)] + o_); wb[(m) % WR] = lds_tr(wl[f_][MN(m)] + o_ + 1024); SBAR(); } while (0)
#pragma unroll
        for (int m = 0; m < 2 * WD; ++m) LDW(m);
        u32x4 of[OWIN];
#define LDO(i) do { of[(i) % OWIN] = ld_sc1s(rs, og, (((i) + krot) & 31) * 1024); SBAR(); } while (0)
        for (unsigned spins = 0; spins < (1u << 20); ++spins) {
#pragma unroll
            for (int i = 0; i < OW0; ++i) LDO(i);
            unsigned m = mx8(of[0], of[1]);
#pragma unroll
            for (int i = 2; i < OW0; ++i) m = mx4(m, of[i]);
            if (!missing(m)) break;
            DGC(12);
        }
        lds_add(fl + C_S2, lane);
#pragma unroll
        for (int i = OW0; i < OWIN; ++i) LDO(i);
        DG(6);
        __builtin_amdgcn_s_setprio(P3_PRIO);
        SBAR();
        unsigned fv = 0;
#define FLAG_RD(a) do { fv = *(volatile LAS unsigned*)(a); SBAR(); } while (0)
#define FLAG_CHK(a, n) do { if (__builtin_expect((unsigned)__builtin_amdgcn_readfirstlane(fv) < (n), 0)) lds_wait_ge(a, n); CFENCE(); SBAR(); } while (0)
        lds_ptr xo = lds + K2_SF + w * 4096 + lane * 16, xi = lds + K2_SF + (w ^ 1) * 4096 + lane * 16;
        int bo = (T3 * 32 + (lane & 15)) * 4096 + 2048 + nt * 128 + half * 64 + g * 16;
        lds_ptr w3d = lds + K2_CV + (w >> 1) * CV_BYTES + (w & 1) * 4096 + lane * 16;
        asm volatile("" : "+v"(xo), "+v"(xi), "+v"(bo), "+v"(w3d));
        f32x4 xt[4];
        constexpr int M3 = TL > 8 ? 64 - 2 * TL + TL - 8 - 2 * WD : 48 - 2 * WD;
        sfor(std::make_integer_sequence<int, 64>{}, [&](auto mc) __attribute__((always_inline)) {
            constexpr int m = decltype(mc)::value, s = MS(m), n = MN(m);
            constexpr bool first = m < 64 - 2 * TL ? n == 0 : n == 1;
            constexpr bool last = m < 64 - 2 * TL ? n == 1 : n == 0;
            acc[n] = MFMA32(cat8(wa[m % WR], wb[m % WR]), PB(of[s % OWIN]), acc[n]);
            SBAR();
            if constexpr (m == 32 - 2 * WD) FLAG_CHK(fl + C_S2, 8u);
            if constexpr (m == M3) FLAG_CHK(fl + C_S3, 8u);
            if constexpr (m + 2 * WD < 64) LDW(m + 2 * WD);
            if constexpr (first && (s & 1) && s + 1 >= OW0 && s + 1 < 32) {
                if (__builtin_expect(missing(mx8(of[(s + 1) % OWIN], of[(s + 2) % OWIN])), 0)) {
#pragma unroll 1
                    for (unsigned spins = 0; spins < (1u << 20); ++spins) {
                        __builtin_amdgcn_s_sleep(2);
                        LDO(s + 1); LDO(s + 2);
                        DGC(13);
                        if (!missing(mx8(of[(s + 1) % OWIN], of[(s + 2) % OWIN]))) break;
                    }
                }
            }
            if constexpr (last && s + OWIN < 32) LDO(s + OWIN);
            if constexpr (LORD && m == S2M) { asm volatile("s_waitcnt vmcnt(%0)" :: "n"(4 + (m + 1) / 2) : "memory"); lds_add(fl + C_S2, lane); }
            if constexpr (LORD && m == W3M) asm volatile("s_waitcnt vmcnt(%0)" :: "n"((m + 1) / 2 < 16 ? (m + 1) / 2 : 16) : "memory");
            if constexpr (m == 15 - 2 * WD) lds_add(fl + C_U0, lane);
            if constexpr (m == 29 - 2 * WD) FLAG_RD(fl + C_S2);
            if constexpr (m == W3M - 3) FLAG_RD(fl + C_HF);
            if constexpr (m == W3M) FLAG_CHK(fl + C_HF, 4u);
            if constexpr (m >= W3M && m < W3M + 8 && ((m - W3M) & 1) == 0) {
                asm volatile("" : "+v"(w3[((m - W3M) >> 1) & 3]));
                *(LAS u32x4*)(w3d + (((m - W3M) >> 1) & 3) * 1024) = w3[((m - W3M) >> 1) & 3];
            }
            if constexpr (m == W3M + 6) lds_add(fl + C_S3, lane);
            if constexpr (m == M3 - 3) FLAG_RD(fl + C_S3);
            if constexpr (m == 64 - TL - 3) FLAG_RD(fl + C_U0);
            if constexpr (m == 64 - TL) FLAG_CHK(fl + C_U0, 8u);
            if constexpr (m == 64 - TL + 1 || m == 64 - TL + 2) {
                constexpr int i = 2 * (m - (64 - TL + 1));
                *(LAS f32x4*)(xo + i * 1024) = f32x4{acc[1][4 * i], acc[1][4 * i + 1], acc[1][4 * i + 2], acc[1][4 * i + 3]};
                *(LAS f32x4*)(xo + i * 1024 + 1024) = f32x4{acc[1][4 * i + 4], acc[1][4 * i + 5], acc[1][4 * i + 6], acc[1][4 * i + 7]};
            }
            if constexpr (m == 64 - TL + 2) lds_add(fl + F_R + w * 4, lane);
            if constexpr (m == XR_AT - 2) FLAG_RD(fl + F_R + (w ^ 1) * 4);
            if constexpr (m == XR_AT) {
                FLAG_CHK(fl + F_R + (w ^ 1) * 4, 1u);
#pragma unroll
                for (int i = 0; i < 4; ++i) xt[i] = *(const LAS f32x4*)(xi + i * 1024);
            }
            if constexpr (ZL_GATE && last && s + OWIN == ZL_AT) lds_add(fl + C_OL, lane);
            if constexpr (m == 2 * P3_T1 + 1) __builtin_amdgcn_s_setprio(P3_PRIO - 1);
            SBAR();
            if constexpr (m == 31) DG(11);
            if constexpr (m == 47) DG(14);
        });
        asm volatile("s_nop 11" : "+v"(acc[0]));
        DG(7);
#pragma unroll
        for (int i = 0; i < 16; ++i) { float o = xt[i >> 2][i & 3]; acc_vv(o, acc[0][i]); acc[1][i] = o; }
        u32x4 pa = pack8n(acc[1], 0), pb = pack8n(acc[1], 1);
#pragma unroll
        for (int i = 0; i < 4; ++i) {
            auto sw = __builtin_amdgcn_permlane16_swap(pa[i], pb[i], false, false);
            pa[i] = sw[0]; pb[i] = sw[1];
        }
        asm volatile("" :: "v"(acc[0]));
        __builtin_amdgcn_raw_buffer_store_b128(pa, ro, bo, 0, OUT_AUX);
        __builtin_amdgcn_raw_buffer_store_b128(pb, ro, bo, 65536, OUT_AUX);
    }
    DG_END(8);
}

extern "C" void kernel_launch(void* const* d_in, const int* in_sizes, int n_in,
                              void* d_out, int out_size, void* d_ws, size_t ws_size,
                              hipStream_t stream) {
    const void* A_q = d_in[0];
    const void* A_k = d_in[1];
    const void* A_v = d_in[2];
    const void* A_shift = d_in[3];
    const void* A_ssmk = d_in[4];
    const void* A_D = d_in[5];
    const void* B_x = d_in[6];
    const void* B_wqkv = d_in[7];
    const void* B_wo = d_in[8];
    char* ws = (char*)d_ws;
    k1_qkv<<<256, 512, 0, stream>>>((const char*)B_x, (const char*)B_wqkv, ws, (const char*)B_wo, (const char*)A_q,
                                    (const char*)A_k, (const char*)A_v, (const char*)A_ssmk);
    k2_attn<<<256, 768, 0, stream>>>(ws, (const char*)A_q, (const char*)A_k, (const char*)A_v, (const char*)A_shift,
                                     (const char*)A_ssmk, (const unsigned*)A_D, (const char*)B_wo, (char*)d_out);
}
```

```cpp
#include <hip/hip_runtime.h>
#include <hip/hip_bf16.h>
#include <stdint.h>
#include <utility>

typedef short bf16x8 __attribute__((ext_vector_type(8)));
typedef short s16x4 __attribute__((ext_vector_type(4)));
typedef float f32x16 __attribute__((ext_vector_type(16)));
typedef float f32x4 __attribute__((ext_vector_type(4)));
typedef float f32x2 __attribute__((ext_vector_type(2)));
typedef unsigned u32x4 __attribute__((ext_vector_type(4)));
typedef unsigned u32x2 __attribute__((ext_vector_type(2)));
typedef __bf16 bf16x2_t __attribute__((ext_vector_type(2)));

#define LAS __attribute__((address_space(3)))
typedef LAS char* lds_ptr;
#define DI __device__ __forceinline__
#define SBAR() __builtin_amdgcn_sched_barrier(0)
#define MFMA32(a, b, c) __builtin_amdgcn_mfma_f32_32x32x16_bf16((a), (b), (c), 0, 0, 0)
#define MFMA16(a, b, c) __builtin_amdgcn_mfma_f32_16x16x32_bf16((a), (b), (c), 0, 0, 0)
typedef __bf16 bfx8_t __attribute__((ext_vector_type(8)));
typedef __bf16 bfx16_t __attribute__((ext_vector_type(16)));
typedef float f32x8 __attribute__((ext_vector_type(8)));
#define SPM 1
#define WAIT_BAR(N) asm volatile("s_waitcnt vmcnt(" #N ") lgkmcnt(0)\n\ts_barrier" ::: "memory")
#define LGKM_BAR() asm volatile("s_waitcnt lgkmcnt(0)\n\ts_barrier" ::: "memory")

#define DG_DECL(K)
#define DG(i)
#define DG_END(i)
#define DGC(i)

#define DGV(i)
constexpr int S_LEN = 2048, DM = 1024, NH = 32;

#define KQ 1
#define OQ 0
#define QT(h, T) (KQ ? (h) * 64 + (T) : (T) * 32 + (h))
constexpr size_t WS_Q = 0;
constexpr size_t WS_KV = 4u << 20;
constexpr size_t WS_O = OQ ? WS_Q : 12u << 20;
constexpr size_t WS_KVR = 16u << 20;
#define ZK 0
constexpr size_t WS_ZT = ZK ? WS_KVR : 20u << 20;
constexpr size_t WS_CNT = 24u << 20;

template <int... I, class F> DI void sfor(std::integer_sequence<int, I...>, F&& f) { (f(std::integral_constant<int, I>{}), ...); }
DI unsigned cvtpk(float lo, float hi) {
    f32x2 v = {lo, hi};
    return __builtin_bit_cast(unsigned, __builtin_convertvector(v, bf16x2_t));
}
DI float bflo(unsigned u) { return __uint_as_float(u << 16); }
DI float bfhi(unsigned u) { return __uint_as_float(u & 0xffff0000u); }

DI void glds16(unsigned voff, const void* sbase, unsigned lds_dst) {
    unsigned sv;
    asm volatile("s_mov_b32 %0, m0\n\ts_mov_b32 m0, %3\n\ts_nop 0\n\tglobal_load_lds_dwordx4 %1, %2\n\ts_mov_b32 m0, %0"
                 : "=&s"(sv) : "v"(voff), "s"(sbase), "s"(lds_dst) : "memory");
}
template <int NT> DI void glds16p(unsigned voff, const void* sbase, unsigned lds_dst) {
    if constexpr (NT) {
        unsigned sv;
        asm volatile("s_mov_b32 %0, m0\n\ts_mov_b32 m0, %3\n\ts_nop 0\n\tglobal_load_lds_dwordx4 %1, %2 nt\n\ts_mov_b32 m0, %0"
                     : "=&s"(sv) : "v"(voff), "s"(sbase), "s"(lds_dst) : "memory");
    } else glds16(voff, sbase, lds_dst);
}
#define K1_XNT 0
#define K1_WNT 0
#define K2_SNT 1
#define K2_QNT 0
#define K2_WNT 0
#define OUT_AUX 16
#define K1_SAUX 16
DI bf16x8 lds_rd128(lds_ptr p) { return *(const LAS bf16x8*)p; }
DI s16x4 lds_tr(lds_ptr p) {
    return __builtin_bit_cast(s16x4, __builtin_amdgcn_ds_read_tr16_b64_v4i16((LAS s16x4*)p));
}
DI bf16x8 cat8(s16x4 a, s16x4 b) { return __builtin_shufflevector(a, b, 0, 1, 2, 3, 4, 5, 6, 7); }
DI u32x4 pack8(const f32x16& x, int s, float sc) {
    u32x4 p;
    p[0] = cvtpk(x[8 * s + 0] * sc, x[8 * s + 1] * sc);
    p[1] = cvtpk(x[8 * s + 2] * sc, x[8 * s + 3] * sc);
    p[2] = cvtpk(x[8 * s + 4] * sc, x[8 * s + 5] * sc);
    p[3] = cvtpk(x[8 * s + 6] * sc, x[8 * s + 7] * sc);
    return p;
}
DI u32x4 pack8n(const f32x16& x, int s) {
    u32x4 p;
    p[0] = cvtpk(x[8 * s + 0], x[8 * s + 1]);
    p[1] = cvtpk(x[8 * s + 2], x[8 * s + 3]);
    p[2] = cvtpk(x[8 * s + 4], x[8 * s + 5]);
    p[3] = cvtpk(x[8 * s + 6], x[8 * s + 7]);
    return p;
}
DI void rnd2(float& a, float& b) { unsigned u = cvtpk(a, b); a = bflo(u); b = bfhi(u); }

#define CONV_PACE()
#define RAW_BAR() asm volatile("s_barrier" ::: "memory")
DI unsigned gld32(unsigned voff, const void* sbase) {
    unsigned r;
    asm volatile("global_load_dword %0, %1, %2" : "=v"(r) : "v"(voff), "s"(sbase) : "memory");
    return r;
}
DI u32x2 gld64(unsigned voff, const void* sbase) {
    u32x2 r;
    asm volatile("global_load_dwordx2 %0, %1, %2" : "=v"(r) : "v"(voff), "s"(sbase) : "memory");
    return r;
}
DI float mul_sv(float s, float v) { float r; asm("v_mul_f32_e32 %0, %1, %2" : "=v"(r) : "s"(s), "v"(v)); return r; }
DI float mul_vv(float a, float b) { float r; asm("v_mul_f32_e32 %0, %1, %2" : "=v"(r) : "v"(a), "v"(b)); return r; }
DI float add_vv(float a, float b) { float r; asm("v_add_f32_e32 %0, %1, %2" : "=v"(r) : "v"(a), "v"(b)); return r; }
DI void acc_vv(float& a, float b) { asm("v_add_f32_e32 %0, %0, %1" : "+v"(a) : "v"(b)); }
DI void fmac_vv(float& acc, float a, float b) { asm("v_fmac_f32_e32 %0, %1, %2" : "+v"(acc) : "v"(a), "v"(b)); }
DI void fmac_sv(float& acc, float s, float v) { asm("v_fmac_f32_e32 %0, %1, %2" : "+v"(acc) : "s"(s), "v"(v)); }
typedef __attribute__((address_space(1))) unsigned gu32;
#define RLX_AGENT __ATOMIC_RELAXED, __HIP_MEMORY_SCOPE_AGENT
#define CFENCE() asm volatile("" ::: "memory")
#define DRAIN() asm volatile("s_waitcnt vmcnt(0)" ::: "memory")
constexpr int AUX_SC1 = 16;
DI u32x4 ld_sc1(__amdgpu_buffer_rsrc_t rs, unsigned off) { return __builtin_amdgcn_raw_buffer_load_b128(rs, (int)off, 0, AUX_SC1); }
DI u32x4 ld_sc1s(__amdgpu_buffer_rsrc_t rs, unsigned off, int soff) { return __builtin_amdgcn_raw_buffer_load_b128(rs, (int)off, soff, AUX_SC1); }
DI void st_sc1(u32x4 v, __amdgpu_buffer_rsrc_t rs, unsigned off) { __builtin_amdgcn_raw_buffer_store_b128(v, rs, (int)off, 0, AUX_SC1); }
constexpr unsigned EMPTY = 0xffffffffu;
DI unsigned nz(unsigned x) { return x < EMPTY - 1u ? x : EMPTY - 1u; }
DI u32x4 nz4(u32x4 v) { return u32x4{nz(v[0]), nz(v[1]), nz(v[2]), nz(v[3])}; }
DI float fmx(float a, float b) { return __builtin_elementwise_maximum(a, b); }
DI unsigned umax(unsigned a, unsigned b) { return a > b ? a : b; }
DI unsigned umax3(unsigned a, unsigned b, unsigned c) { return umax(umax(a, b), c); }
DI unsigned mx4(unsigned m, u32x4 v) { return umax3(umax3(m, v[0], v[1]), v[2], v[3]); }
DI unsigned mx8(u32x4 a, u32x4 b) { return umax(umax3(umax3(umax3(a[0], a[1], a[2]), a[3], b[0]), b[1], b[2]), b[3]); }
DI bool missing(unsigned m) { return __builtin_amdgcn_ballot_w64(m == EMPTY) != 0; }
DI unsigned lds_add_rtn(lds_ptr p, int lane) {
    unsigned old = 0;
    CFENCE();
    if (lane == 0) old = __hip_atomic_fetch_add((LAS unsigned*)p, 1u, __ATOMIC_RELAXED, __HIP_MEMORY_SCOPE_WORKGROUP);
    old = __builtin_amdgcn_readfirstlane(old);
    CFENCE();
    return old;
}
DI void lds_add(lds_ptr p, int lane) {
    unsigned long long sv;
    asm volatile("s_mov_b64 %0, exec\n\ts_mov_b64 exec, 1\n\tds_add_u32 %1, %2\n\ts_mov_b64 exec, %0"
                 : "=&s"(sv) : "v"(p), "v"(1u) : "memory");
}
DI void lds_wait_ge(lds_ptr p, unsigned v) {
    CFENCE();
    while ((unsigned)__builtin_amdgcn_readfirstlane(*(volatile LAS unsigned*)p) < v) __builtin_amdgcn_s_sleep(1);
    CFENCE();
}
DI void poll_ge(gu32* word, unsigned want) {
    CFENCE();
    for (unsigned spins = 0; spins < (1u << 22); ++spins) {
        if ((unsigned)__builtin_amdgcn_readfirstlane(__hip_atomic_load(word, RLX_AGENT)) >= want) break;
        __builtin_amdgcn_s_sleep(1);
    }
    __builtin_amdgcn_fence(__ATOMIC_ACQUIRE, "wavefront");
    CFENCE();
}
#define PB(x) __builtin_bit_cast(bf16x8, x)
#define PRIO(x) do { __builtin_amdgcn_s_setprio(x); SBAR(); } while (0)
DI u32x4 gld128(unsigned voff, const void* sbase) {
    u32x4 r;
    asm volatile("global_load_dwordx4 %0, %1, %2" : "=v"(r) : "v"(voff), "s"(sbase) : "memory");
    return r;
}

#define K1_UNR 15
#define K1_LA 5
#define K1_ROT ((tb & 3) * 4)
#define K1_PF 0
#define K1_PG 0
#define K1_TW 0
#define CPF 3
#define SKD 10
#define PK2 0
#define BK1 0
#define PKC 0
#define PKC_AT 6
#define PK2_AT 13
#define CPF_POS 1
#define PG1_AT 0
#define PG2_AT 2
#define PG3_AT 6
#define K1_WB(n) WAIT_BAR(1)
#define PF1_AT -1
#define PF1_LO 4
#define PF1_N 4
#define PF2_AT 1
#define PF2_LO 8
#define PF2_N 4
#define PF3_AT 5
#define PF3_LO 12
#define PF3_N 4
#define K1_KR ((kt + rot) & 15)
#define K1_ST(v, rs, off) __builtin_amdgcn_raw_buffer_store_b128(v, rs, (int)(off), 0, K1_SAUX)
#define GWS 0
constexpr int K1_STAGE = 40960, K1U = K1_UNR;
__global__ __launch_bounds__(512, 2) void k1_qkv(const char* __restrict__ X, const char* __restrict__ W,
                                                 char* __restrict__ wsa, const char* __restrict__ Wo,
                                                 const char* __restrict__ Aq, const char* __restrict__ Ak,
                                                 const char* __restrict__ Av, const char* __restrict__ ssmk) {
    __shared__ __attribute__((aligned(16))) char lds_raw[4 * K1_STAGE];
    char* const ws = wsa;
    const lds_ptr lds = (lds_ptr)lds_raw;
    const unsigned lds0 = (unsigned)(uintptr_t)lds;
    const int tid = threadIdx.x, lane = tid & 63;
    const int w = __builtin_amdgcn_readfirstlane(tid >> 6);
    const int b = blockIdx.x, xcd = b & 7, idx = b >> 3;
    const int hp = (xcd >> 1) * 4 + (idx & 3), tb = (xcd & 1) * 8 + (idx >> 2);
    DG_DECL(0)

    const int lw = w & 3;
    const int rot = K1_ROT;
#define XROW(pp) (SKD >= 8 ? 64 * (lw >> 1) + 16 * (lw & 1) + 8 * ((pp) & 1) + 32 * ((pp) >> 1) : 32 * lw + 8 * (pp))
    unsigned voffX[4], voffW[2];
#pragma unroll
    for (int pp = 0; pp < 4; ++pp) {
        const int row = XROW(pp) + (lane >> 3);
        const int ch = (lane & 7) ^ ((row >> 1) & 7);
        voffX[pp] = (unsigned)(tb * 128 + row) * 2048u + ch * 16;
    }
#pragma unroll
    for (int pp = 0; pp < 2; ++pp) {
        const int krow = 16 * lw + 8 * pp + (lane >> 3);
        const int ch = (lane & 7) ^ (((lane >> 4) & 1) << 2);
        voffW[pp] = (unsigned)krow * 6144u + ch * 16;
    }
    auto issuem = [&](int kt, int mask) {
        const int kr = K1_KR;
        const char* xb = X + kr * 128;
        const char* wb = W + (size_t)kr * (64 * 6144) + hp * 128;
        const unsigned st = lds0 + (kt & 3) * K1_STAGE;
#pragma unroll
        for (int pp = 0; pp < 4; ++pp)
            if (mask >> pp & 1) glds16p<K1_XNT>(voffX[pp], xb, st + XROW(pp) * 128);
#pragma unroll
        for (int sec = 0; sec < 3; ++sec)
#pragma unroll
            for (int pp = 0; pp < 2; ++pp)
                if (mask >> (4 + 2 * sec + pp) & 1)
                    glds16p<K1_WNT>(voffW[pp], wb + sec * 2048, st + 16384 + sec * 8192 + (2 * lw + pp) * 1024);
    };
    auto issue = [&](int kt, int lo, int hi) { issuem(kt, (1 << hi) - (1 << lo)); };
    DG(13);
    unsigned cpf;
    auto codepf = [&]() {
        unsigned long long pc; unsigned lo, hi;
        asm volatile("s_getpc_b64 %0\n.Lcp_k1a:\n\ts_mov_b32 %1, %3\n\ts_mov_b32 %2, %4" : "=s"(pc), "=s"(lo), "=s"(hi) : "n"(0), "n"(0));
        if (CPF & 1) lo = 0; else asm volatile("s_mov_b32 %0, .Lcp_k2a-.Lcp_k1a" : "=s"(lo));
        if (CPF & 2) asm volatile("s_mov_b32 %0, .Lcp_k2z-.Lcp_k1a" : "=s"(hi)); else asm volatile("s_mov_b32 %0, .Lcp_k1z-.Lcp_k1a" : "=s"(hi));
        unsigned off = lo + (unsigned)(idx + 32 * (lane & 7) + 256 * w) * 64u;
        off = off < hi ? off : hi;
        asm volatile("global_load_dword %0, %1, %2" : "=v"(cpf) : "v"(off), "s"(pc) : "memory");
    };
    if (w < 4) { issue(0, 0, 5); issue(1, 0, 5); codepf(); }
    else { issue(0, 5, 10); issue(1, 5, 10); }
    DG(14);
    int w2 = w;
    asm volatile("" : "+s"(w2));

    if (w2 >= 4) {
#define WAITN_BAR(N) asm volatile("s_waitcnt vmcnt(%0) lgkmcnt(0)\n\ts_barrier" :: "n"(N) : "memory")
        issue(2, 0, K1_LA);
        asm volatile("s_waitcnt vmcnt(%0)" :: "n"(5 + K1_LA) : "memory");
#pragma unroll 1
        for (int kt = 0; kt < (SKD == 8 ? 9 : SKD == 9 ? 12 : SKD == 10 ? 11 : 13); ++kt) {
            WAITN_BAR(10 + K1_LA);
            issue(kt + 2, K1_LA, 10);
            issue(kt + 3, 0, K1_LA);
        }
        {
            constexpr int E = 0x3c3, EA = 0x1c3, XL = 0x00c, WL = 0x030;
            WAIT_BAR(15);
            issue(13, 5, 10); issuem(14, EA);
            WAIT_BAR(15);
            DG(12);
            issuem(14, E - EA); issuem(15, E); issuem(14, XL); issuem(15, 4);
            WAIT_BAR(15);
            issuem(15, 8); issuem(14, WL); issuem(15, WL);
            WAIT_BAR(14);
            WAIT_BAR(8);
            DG(9);
            WAIT_BAR(6);
            WAIT_BAR(4);
            WAIT_BAR(0);
        }
        if (lw == 0 && lane < 2) *(unsigned*)(ws + WS_CNT + b * 256 + lane * 128) = 0u;
        DG_END(5);
        return;
    }

    const int wm = w >> 1, wn = w & 1;
    const int r = lane & 31, hh = lane >> 5;
    const int g = lane >> 4, q = (lane & 15) >> 2, p = lane & 3;
    const int xoff0 = (64 * wm + r) * 128 + ((hh ^ ((r >> 1) & 7)) << 4);
    const int woff0 = 16384 + (8 * hh + q) * 128 + (((4 * wn + 2 * (g & 1) + (p >> 1)) ^ ((q >> 1) << 2)) << 4) + (p & 1) * 8;

    f32x16 acc[2][3];
#pragma unroll
    for (int m = 0; m < 2; ++m)
#pragma unroll
        for (int s = 0; s < 3; ++s) acc[m][s] = f32x16{};
    bf16x8 xf[2][2];
    s16x4 wl[2][3], wh[2][3];
#define K1_WF(f, sec) cat8(wl[f][sec], wh[f][sec])
#define K1_STEP(F, st, t, BAR_HERE) do { \
        const lds_ptr xp_ = (st) + (xoff0 ^ ((t) << 5)); const lds_ptr wp_ = (st) + woff0 + (t) * 2048; \
        acc[0][0] = MFMA32(K1_WF(F, 0), xf[F][0], acc[0][0]); SBAR(); \
        if (BAR_HERE) { K1_WB(BAR_HERE); SBAR(); } \
        xf[F ^ 1][0] = lds_rd128(xp_); wl[F ^ 1][0] = lds_tr(wp_); wh[F ^ 1][0] = lds_tr(wp_ + 512); SBAR(); \
        acc[0][1] = MFMA32(K1_WF(F, 1), xf[F][0], acc[0][1]); SBAR(); \
        wl[F ^ 1][1] = lds_tr(wp_ + 8192); wh[F ^ 1][1] = lds_tr(wp_ + 8192 + 512); SBAR(); \
        acc[0][2] = MFMA32(xf[F][0], K1_WF(F, 2), acc[0][2]); SBAR(); \
        wl[F ^ 1][2] = lds_tr(wp_ + 16384); wh[F ^ 1][2] = lds_tr(wp_ + 16384 + 512); xf[F ^ 1][1] = lds_rd128(xp_ + 4096); SBAR(); \
        acc[1][0] = MFMA32(K1_WF(F, 0), xf[F][1], acc[1][0]); SBAR(); \
        acc[1][1] = MFMA32(K1_WF(F, 1), xf[F][1], acc[1][1]); \
        acc[1][2] = MFMA32(xf[F][1], K1_WF(F, 2), acc[1][2]); SBAR(); \
    } while (0)

    WAIT_BAR(6);
    DG(3);
    {
        const lds_ptr xp_ = lds + xoff0; const lds_ptr wp_ = lds + woff0;
        xf[0][0] = lds_rd128(xp_); xf[0][1] = lds_rd128(xp_ + 4096);
#pragma unroll
        for (int sec = 0; sec < 3; ++sec) { wl[0][sec] = lds_tr(wp_ + sec * 8192); wh[0][sec] = lds_tr(wp_ + sec * 8192 + 512); }
    }
    SBAR();
    constexpr int NJ = 9, JOB[NJ] = {13, 29, 14, 15, 45, 30, 31, 46, 47}, JBAR = 0xec;
    constexpr int KT0 = 16 - NJ / 3;
    bf16x8 jf[3][3];
    auto jrd = [&](auto gc, int part) __attribute__((always_inline)) {
        constexpr int g = decltype(gc)::value;
        if constexpr (g < 4 * NJ) {
            constexpr int pr = JOB[g >> 2] >> 4, tl = JOB[g >> 2] & 15, s = g & 3;
            const lds_ptr st = lds + (tl & 3) * K1_STAGE;
            const lds_ptr xp_ = st + (xoff0 ^ (s << 5)); const lds_ptr wp_ = st + woff0 + s * 2048;
            if (part == 0) {
                if constexpr (s == 0 && (JBAR >> (g >> 2) & 1)) { RAW_BAR(); SBAR(); }
                if constexpr (false) {
                    bool seen = false;
                    for (int j = 0; j < (g >> 2); ++j) seen = seen || (JOB[j] & 15) == tl;
                    if (!seen) { RAW_BAR(); SBAR(); }
                }
                jf[g % 3][0] = lds_rd128(xp_ + (pr == 1 ? 4096 : 0));
                if constexpr (pr < 2) jf[g % 3][1] = cat8(lds_tr(wp_ + 8192), lds_tr(wp_ + 8192 + 512));
                else jf[g % 3][2] = cat8(lds_tr(wp_), lds_tr(wp_ + 512));
            } else {
                if constexpr (pr < 2) jf[g % 3][2] = cat8(lds_tr(wp_ + 16384), lds_tr(wp_ + 16384 + 512));
                else jf[g % 3][1] = lds_rd128(xp_ + 4096);
            }
            SBAR();
        }
    };
#define IC(x) std::integral_constant<int, (x)>{}
#pragma unroll
    for (int kt = 0; kt < KT0; ++kt) {
        const lds_ptr st = lds + (kt & 3) * K1_STAGE;
        const lds_ptr sn = lds + ((kt + 1) & 3) * K1_STAGE;
        K1_STEP(0, st, 1, false);
        K1_STEP(1, st, 2, false);
        K1_STEP(0, st, 3, false);
        if (kt < KT0 - 1) K1_STEP(1, sn, 0, kt + 1);
    }
    acc[0][0] = MFMA32(K1_WF(1, 0), xf[1][0], acc[0][0]); SBAR();
    WAIT_BAR(0); SBAR();
    asm volatile("" :: "v"(cpf));
    jrd(IC(0), 0);
    acc[0][1] = MFMA32(K1_WF(1, 1), xf[1][0], acc[0][1]); SBAR();
    jrd(IC(0), 1);
    acc[0][2] = MFMA32(xf[1][0], K1_WF(1, 2), acc[0][2]); SBAR();
    jrd(IC(1), 0);
    acc[1][0] = MFMA32(K1_WF(1, 0), xf[1][1], acc[1][0]); SBAR();
    jrd(IC(1), 1);
    acc[1][1] = MFMA32(K1_WF(1, 1), xf[1][1], acc[1][1]);
    acc[1][2] = MFMA32(xf[1][1], K1_WF(1, 2), acc[1][2]); SBAR();
    const int h = 2 * hp + wn;
    const float qscale = 0.17677669529663687f * 1.4426950408889634f;
    const __amdgpu_buffer_rsrc_t rs = __builtin_amdgcn_make_buffer_rsrc(ws, 0, 25 << 20, 0x00020000);
    const int T0 = tb * 4 + wm * 2;
    unsigned dq = (unsigned)WS_Q + QT(h, T0) * 2048 + lane * 16;
    unsigned dk = (unsigned)WS_KV + (h * 64 + T0) * 4096 + lane * 16;
    unsigned dkk = (unsigned)WS_KV + (h * 64 + T0) * 4096 + ((lane & 51) | ((lane & 4) << 1) | ((lane & 8) >> 1)) * 16;
    asm volatile("" : "+v"(dq), "+v"(dk), "+v"(dkk));
    static_assert(KQ == 1 && SPM != 0, "");
    auto jst = [&](int pr, int i) __attribute__((always_inline)) {
        if (pr < 2) {
            if (i < 2) __builtin_amdgcn_raw_buffer_store_b128(pack8n(acc[pr][1], i), rs, (int)dkk + i * 1024, pr * 4096, K1_SAUX);
            else __builtin_amdgcn_raw_buffer_store_b128(pack8n(acc[pr][2], i - 2), rs, (int)dk + i * 1024, pr * 4096, K1_SAUX);
        } else K1_ST(pack8(acc[i >> 1][0], i & 1, qscale), rs, dq + i * 1024);
        SBAR();
    };
    sfor(std::make_integer_sequence<int, 4 * NJ>{}, [&](auto gc) __attribute__((always_inline)) {
        constexpr int g = decltype(gc)::value, pr = JOB[g >> 2] >> 4;
        constexpr int dn = g >= 4 && (JOB[(g >> 2) - (g >= 4)] & 15) == 15 ? JOB[(g >> 2) - (g >= 4)] >> 4 : -1;
        if constexpr (pr < 2) acc[pr][1] = MFMA32(jf[g % 3][1], jf[g % 3][0], acc[pr][1]);
        else acc[0][0] = MFMA32(jf[g % 3][2], jf[g % 3][0], acc[0][0]);
        SBAR();
        jrd(IC(g + 2), 0);
        if constexpr (dn >= 0 && (g & 3) == 1) jst(dn, 1);
        if constexpr (dn >= 0 && (g & 3) == 2) jst(dn, 3);
        if constexpr (pr < 2) acc[pr][2] = MFMA32(jf[g % 3][0], jf[g % 3][2], acc[pr][2]);
        else acc[1][0] = MFMA32(jf[g % 3][2], jf[g % 3][1], acc[1][0]);
        SBAR();
        jrd(IC(g + 2), 1);
        if constexpr (dn >= 0 && (g & 3) == 0) { jst(dn, 0); if (dn == 0) DG(6); }
        if constexpr (dn >= 0 && (g & 3) == 1) jst(dn, 2);
    });
    DG(4);
    asm volatile(".Lcp_k1z:");
#pragma unroll
    for (int i = 0; i < 4; ++i) jst(2, i);
    DG_END(5);
}

constexpr int K2_SLOT = 32768;
constexpr int K2_SF = 65536;
constexpr int K2_CV = 98304;
constexpr int CV_HA = 0, CV_HB = 4160, CV_KV = 8320, CV_BYTES = 8320 + 480 + 4096;
constexpr int K2_LX = K2_CV + 4 * CV_BYTES;
constexpr int K2_FL = K2_LX + 2048;
constexpr int K2_SCR = K2_FL + 256;
constexpr int K2_PAD = K2_SCR + 4096;
constexpr int K2_LDS = K2_PAD + 1024;
#define NB8 0
#define WW 0
constexpr int F_X = 0, F_R = 32, F_W2 = 64, C_OST = 68, C_S1 = 76, C_S2 = 80, C_S3 = 128, C_ZST = 88, F_ZGO = 92,
              C_KST = 96, C_U0 = 100, C_LD = 108, F_KGO = 116, C_END = 120, C_OL = 72, C_HF = 84;
#define NG_IN 0
#define GATE_WORD C_END
#define GATE_VAL 0
#define X_NZ 4
#define X_NW3 8
#define ZP_GATE 4u
#define ZL_GATE 4
#define ZL_AT 31
#define Z_SLEEP0 0
#define O_SLEEP 0
#define Z_SLEEP 8
#define OWIN 16
#define OW0 8
#define OWR 8
#define WD 2
#define W3_AT 10
#define X_NOLDW 0
#define X_NOLDO 0
#define X_NOCHK 0
#define KROT (nt * 2)
#define NG_LAST 0
#define PACE_IN "s_nop 7"
#define PACE_OUT "s_nop 1"
#define P3_PRIO 1
#define TOP_SLEEP 2
#define A_AUX 16
#define B_AUX 16
#define B_LDS 0
#define TL 8
#define XR_AT 62
#define W3_AUX 0
#define OPL 0
#define WE 0
#define WEP 0
#define KPF 0
#define NSC 0
#define OALL 0
#define ORL 0
#define XPRIO -1
#define LPRIO -1
#define ZN 0
#define WP1 0
#define LORD 0
#define AORD 0
#define S2M 17
#define W3M (LORD ? 23 : 13)
#define A16 1
#define SEL4 2
#define KV_DOT 1
#define ZE_PRIO 3
#define P3_T1 99
#define P3_T2 99
#define AUX_PRIO 2

__global__ __launch_bounds__(768, 3) void k2_attn(char* __restrict__ wsa, const char* __restrict__ Aq,
                                                  const char* __restrict__ Ak, const char* __restrict__ Av,
                                                  const char* __restrict__ shiftk, const char* __restrict__ ssmk,
                                                  const unsigned* __restrict__ ssmD, const char* __restrict__ Wo,
                                                  char* __restrict__ out) {
    __shared__ __attribute__((aligned(16))) char lds_raw[K2_LDS];
    char* const ws = wsa;
    const lds_ptr lds = (lds_ptr)lds_raw;
    const lds_ptr fl = lds + K2_FL;
    const unsigned lds0 = (unsigned)(uintptr_t)lds;
    const int tid = threadIdx.x, lane = tid & 63;
    const int w = __builtin_amdgcn_readfirstlane(tid >> 6);
    const int b = blockIdx.x, xcd = b & 7, idx = b >> 3;
    const int h = xcd * 4 + (idx >> 3), qb = idx & 7;
    const int tt = xcd * 2 + (idx >> 4), nt = idx & 15;
    const char* kvg = ws + WS_KV + (size_t)h * 64 * 4096;
    const __amdgpu_buffer_rsrc_t rs = __builtin_amdgcn_make_buffer_rsrc(ws, 0, 25 << 20, 0x00020000);
    const __amdgpu_buffer_rsrc_t ro = __builtin_amdgcn_make_buffer_rsrc(out, 0, 8 << 20, 0x00020000);
    gu32* const cnt = (gu32*)(ws + WS_CNT);
    auto signal = [&]() {
        if (!BK1 || lane < 32) (void)__hip_atomic_fetch_add(cnt + ((lane & 31) * 8 + (lane < 32 ? xcd : qb)) * 64, 1u, RLX_AGENT);
    };
    const unsigned wvoff = (unsigned)(lane >> 3) * 2048u + nt * 128 + (((lane & 7) ^ (((lane >> 4) & 1) << 2)) << 4);
    const int krot = KROT;
    auto wsrc = [&](int hf, int lp) { return Wo + (size_t)(hf * 64 + ((lp + krot * 2) & 63)) * 16384; };
    DG_DECL(1)
    const unsigned voff = lane * 16;
    const int cw = w & 3;
    auto issue = [&](int st) {
        const char* src = kvg + (st * 4 + cw) * 4096;
        const unsigned dst = lds0 + (st & 1) * K2_SLOT + cw * 4096;
#pragma unroll
        for (int i = 0; i < 4; ++i) glds16(voff, src + i * 1024, dst + i * 1024);
#pragma unroll
        for (int i = 0; i < 4; ++i) glds16(voff, src + 131072 + i * 1024, dst + 16384 + i * 1024);
    };
    asm volatile(".Lcp_k2a:");
    if (w >= 8) {
        __builtin_amdgcn_s_sleep(TOP_SLEEP);
#pragma unroll
        for (int i = 2; i < 8; ++i)
            glds16(voff, kvg + (i & 1) * 131072 + (i >> 1) * 4096 + cw * 1024, lds0 + (i & 1) * 16384 + (i >> 1) * 4096 + cw * 1024);
        RAW_BAR();
        issue(1);
        WAIT_BAR(8);
    } else {
        const int tq = cw * 2 + (w >> 2);
        const char* qg = ws + WS_Q + (size_t)QT(h, qb * 8 + tq) * 2048;
        glds16(voff, kvg + (w >> 2) * 131072 + cw * 1024, lds0 + (w >> 2) * 16384 + cw * 1024);
        glds16(voff, qg, lds0 + K2_SF + tq * 2048);
        glds16(voff, qg + 1024, lds0 + K2_SF + tq * 2048 + 1024);
    }
    int w2 = w;
    asm volatile("" : "+s"(w2));

    if (w2 >= 8) {
        __builtin_amdgcn_s_setprio(3);
        const int ch = xcd * 128 + idx * 4 + cw;
        const lds_ptr cv = lds + K2_CV + cw * CV_BYTES;
        {
            const u32x4 z4 = {0u, 0u, 0u, 0u};
            if (lane < 2) *(LAS u32x4*)(cv + CV_HA + lane * 16) = z4;
            if (lane < 30) *(LAS u32x4*)(cv + CV_KV + lane * 16) = z4;
            if (cw == 0 && lane < 16) *(LAS u32x4*)(fl + lane * 16) = z4;
        }
        const int c0 = xcd * 128 + cw * 32, cq = lane & 7, rg = lane >> 3;
        const int row0 = idx * 64 + rg * 8;
        const unsigned roff = (unsigned)row0 * 2048u + (c0 + cq * 4) * 2;
        const unsigned toffs = (unsigned)(c0 + (lane >> 3)) * 4096u + idx * 128;
        u32x4 sk[2];
        sk[0] = gld128((unsigned)(c0 + cq * 4) * 8u, shiftk); sk[1] = gld128((unsigned)(c0 + cq * 4) * 8u, shiftk + 16);
        unsigned dd = gld32((unsigned)(ch >> 1) * 4u, ssmD);
        auto stage = [&](const char* src, int dst) {
            const unsigned sbase = (unsigned)(idx * 64 + cw * 16 + (lane >> 4)) * 2048u + xcd * 256;
#pragma unroll
            for (int i = 0; i < 4; ++i)
                glds16p<K2_SNT>(sbase + (((lane & 15) ^ (((2 * cw + (i >> 1)) & 3) << 2)) << 4), src + i * 8192, lds0 + dst + cw * 4096 + i * 1024);
        };
        auto blank = [&](unsigned off) {
            const u32x4 e4 = {EMPTY, EMPTY, EMPTY, EMPTY};
            CFENCE();
            if (!BK1) { st_sc1(e4, rs, off + voff); st_sc1(e4, rs, off + 1024 + voff); }
            CFENCE();
        };
        unsigned pfd = 0;
#define PFL(off, base) asm volatile("global_load_dword %0, %1, %2" : "+v"(pfd) : "v"(off), "s"(base) : "memory")
        if (KPF) {
            const int tl = lane < 48 ? lane : 47;
            const unsigned ko = (unsigned)(tl + (tl < 24 ? 8 : 16)) * 4096u + (qb * 4 + cw) * 128;
            PFL(ko, kvg);
            if (KPF == 2) PFL(ko, kvg + 64);
        }
        const unsigned wpo = (unsigned)(lane >> 1) * 2048u + nt * 128 + (lane & 1) * 64;
        auto wpf = [&](int g) { PFL(wpo, wsrc(idx >> 4, g * 16 + cw * 4)); };
        if (WP1 == 1) PFL((unsigned)(b * 4 + cw) * 2048u + (lane & 31) * 64, Wo);
        WAITN_BAR(3 + KPF + (WP1 == 1));
        DG(12);
        issue(2);
        stage(Ak, K2_SF);
        glds16((unsigned)(idx * 64 - (idx ? 4 : 0) + (lane >> 4)) * 2048u + xcd * 256 + (((lane & 15) ^ 12) << 4), Ak, lds0 + K2_SCR + cw * 1024);
        blank((unsigned)WS_O + (unsigned)((qb * 8 + cw * 2) * 32 + h) * 2048u);
        auto warm = [&](int g) {
#pragma unroll
            for (int i = 0; i < 4; ++i) glds16(wvoff, wsrc(idx >> 4, 16 + g * 16 + cw * 4 + i), lds0 + K2_PAD);
        };
        if (NSC) wpf(0);
        WAITN_BAR(7 + (NSC ? 1 : 0));
        DG(9);
        issue(3);
        stage(Av, K2_SF + 16384);
        blank((unsigned)WS_O + (unsigned)((qb * 8 + cw * 2 + 1) * 32 + h) * 2048u);
        if (NSC) wpf(1);
        if (WP1 == 2) PFL((unsigned)(b * 4 + cw) * 2048u + (lane & 31) * 64, Wo);
        WAITN_BAR(6 + (NSC ? 1 : 0) + (WP1 == 2));
        DG(3);
        issue(4);
#pragma unroll
        for (int i = 0; i < 4; ++i) glds16p<K2_SNT>(voff, ssmk + (size_t)ch * 4096 + i * 1024, lds0 + K2_CV + cw * CV_BYTES + CV_HA + 32 + i * 1024);
        blank((unsigned)WS_ZT + ch * 4096); blank((unsigned)WS_ZT + ch * 4096 + 2048);
        WAITN_BAR(BK1 ? 4 : 8);
        DG(5);
        issue(5);
        if (NSC) wpf(2);
        SBAR();
        asm volatile("" : "+v"(sk[0]), "+v"(sk[1]), "+v"(dd));
        dd = __builtin_amdgcn_readfirstlane(dd);
        {
            u32x2 kr[11], vr[8];
            lds_ptr kb = lds + K2_SF + rg * 2048 + ((cw ^ (rg & 3)) << 6) + cq * 8;
            asm volatile("" : "+v"(kb));
            const lds_ptr hb = lds + (rg ? K2_SF + rg * 2048 - 768 : K2_SCR + cw * 1024 + 256) + ((cw ^ ((rg + 3) & 3)) << 6) + cq * 8;
#pragma unroll
            for (int i = 0; i < 3; ++i) kr[i] = *(const LAS u32x2*)(hb + i * 256);
#pragma unroll
            for (int i = 0; i < 8; ++i) kr[3 + i] = *(const LAS u32x2*)(kb + i * 256);
#pragma unroll
            for (int i = 0; i < 8; ++i) vr[i] = *(const LAS u32x2*)(kb + 16384 + i * 256);
            if (row0 == 0) { kr[0] = u32x2{0u, 0u}; kr[1] = u32x2{0u, 0u}; kr[2] = u32x2{0u, 0u}; }
            const lds_ptr tb = cv + CV_KV + 480;
#pragma unroll
            for (int c = 0; c < 4; ++c) {
                const unsigned s01 = sk[c >> 1][(c & 1) * 2], s23 = sk[c >> 1][(c & 1) * 2 + 1];
                unsigned kp[10];
                float kv[8], vf[8];
#pragma unroll
                for (int j = 0; j < 10; ++j) kp[j] = __builtin_amdgcn_perm(kr[j][c >> 1], kr[j + 1][c >> 1], (c & 1) ? 0x07060302u : 0x05040100u);
                SBAR();
#pragma unroll
                for (int i = 0; i < 8; ++i) asm("v_dot2_f32_bf16 %0, %1, %2, 0" : "=v"(kv[i]) : "v"(kp[i]), "v"(s23));
                SBAR();
#pragma unroll
                for (int i = 0; i < 8; ++i) asm("v_dot2_f32_bf16 %0, %1, %2, %0" : "+v"(kv[i]) : "v"(kp[i + 2]), "v"(s01));
                SBAR();
#pragma unroll
                for (int i = 0; i < 8; ++i) vf[i] = (c & 1) ? bfhi(vr[i][c >> 1]) : bflo(vr[i][c >> 1]);
                SBAR();
#pragma unroll
                for (int i = 0; i < 8; ++i) kv[i] = mul_vv(kv[i], vf[i]);
                u32x4 o4;
#pragma unroll
                for (int i = 0; i < 4; ++i) o4[i] = cvtpk(kv[7 - 2 * i], kv[6 - 2 * i]);
                *(LAS u32x4*)(tb + (cq * 4 + c) * 128 + ((rg ^ cq) << 4)) = o4;
            }
#pragma unroll
            for (int i = 0; i < 4; ++i) {
                const u32x4 t = *(const LAS u32x4*)(tb + i * 1024 + lane * 16);
                st_sc1(t, rs, (unsigned)WS_Q + (xcd * 32 + idx) * 16384 + cw * 4096 + i * 1024 + (lane >> 3) * 128 + (((lane & 7) ^ (2 * i + (lane >> 5))) << 4));
            }
        }
        CFENCE();
        DG(7);
        WAITN_BAR(4 + (NSC ? 1 : 0));
        DG(6);
        issue(6);
        asm volatile("s_waitcnt vmcnt(8)" ::: "memory");
        if (lds_add_rtn(fl + C_KST, lane) == 3u) signal();
#pragma unroll
        for (int i = 0; i < 8; ++i)
            glds16p<K2_WNT>(wvoff, wsrc(i >> 2, cw * 4 + (i & 3)), lds0 + K2_SF + (i >> 2) * 16384 + (cw * 4 + (i & 3)) * 1024);
        if (NSC) wpf(3);
        if (NSC == 1) PFL((unsigned)(idx * 64 + cw * 16 + (lane >> 2)) * 2048u + xcd * 256 + (lane & 3) * 64, Aq);
        {
            const u32x4 z4 = {0u, 0u, 0u, 0u};
            if (lane < 2) *(LAS u32x4*)(cv + CV_HB + lane * 16) = z4;
#pragma unroll
            for (int i = 0; i < 4; ++i) {
                const int off = 32 + i * 1024 + lane * 16;
                const u32x4 hv = *(const LAS u32x4*)(cv + CV_HA + off);
                const unsigned pv = *(const LAS unsigned*)(cv + CV_HA + off - 4);
                u32x4 sh;
                sh[0] = __builtin_amdgcn_alignbit(hv[0], pv, 16);
                sh[1] = __builtin_amdgcn_alignbit(hv[1], hv[0], 16);
                sh[2] = __builtin_amdgcn_alignbit(hv[2], hv[1], 16);
                sh[3] = __builtin_amdgcn_alignbit(hv[3], hv[2], 16);
                *(LAS u32x4*)(cv + CV_HB + off) = sh;
            }
        }
        WAITN_BAR(8 + (NSC == 1 ? 2 : NSC ? 1 : 0));
        DG(8);
        issue(7);
        auto warm23 = [&]() {
#pragma unroll
            for (int i = 0; i < 8; ++i)
                glds16p<K2_WNT>(wvoff, wsrc(idx >> 4, 32 + cw * 8 + i), lds0 + K2_SCR + cw * 1024);
        };
        auto chunk1 = [&]() {
#pragma unroll
            for (int i = 0; i < 8; ++i)
                glds16p<K2_WNT>(wvoff, wsrc(i >> 2, 16 + cw * 4 + (i & 3)), lds0 + (i >> 2) * 16384 + (cw * 4 + (i & 3)) * 1024);
        };
        warm23();
        WAIT_BAR(8);
        DG(11);
        if (cw == 0) { poll_ge(cnt + b * 64, BK1 ? 32u : 64u); lds_add(fl + F_KGO, lane); }
        else lds_wait_ge(fl + F_KGO, 1u);
        {
            u32x4 t[4];
#pragma unroll
            for (int i = 0; i < 4; ++i) t[i] = ld_sc1s(rs, (unsigned)WS_Q + (xcd * 32 + (lane >> 3)) * 16384 + (idx * 4 + cw) * 128 + (lane & 7) * 16, i * 131072);
#pragma unroll
            for (int i = 0; i < 4; ++i) *(LAS u32x4*)(cv + CV_KV + 480 + i * 1024 + lane * 16) = t[i];
        }
        const int r1 = lane & 15, q = lane >> 4;
        const int toff = (r1 & 1) ? CV_HA + 2 * (16 + r1 + 9 - 8 * q) : CV_HB + 2 * (16 + r1 + 10 - 8 * q);
        const int boff = CV_KV + 448 + 32 * r1 + 16 * q;
        f32x4 y[8];
        u32x4 tf[8];
        bf16x8 bf[8];
        const lds_ptr bp = cv + boff - 448;
        lds_ptr tp[4];
#pragma unroll
        for (int i = 0; i < 4; ++i) { tp[i] = cv + toff + 1024 * i; asm volatile("" : "+v"(tp[i])); }
#define LDT(bb, a, hf) do { const LAS unsigned* t_ = (const LAS unsigned*)(tp[(a) >> 1] + 64 * (bb) + 512 * ((a) & 1) + 8 * (hf)); \
            tf[a][2 * (hf)] = t_[0]; tf[a][2 * (hf) + 1] = t_[1]; SBAR(); } while (0)
#define LDB(bb, a) do { bf[a] = lds_rd128(bp + 448 - 64 * (bb) + 512 * (a)); SBAR(); } while (0)
#define CM(a, ww) do { asm volatile("v_mfma_f32_16x16x32_bf16 %0, %1, %2, %0" : "+v"(y[(a) + (ww)]) : "v"(tf[a]), "v"(bf[ww])); SBAR(); } while (0)
#define CM0(a, ww) do { asm volatile("v_mfma_f32_16x16x32_bf16 %0, %1, %2, 0" : "=&v"(y[(a) + (ww)]) : "v"(tf[a]), "v"(bf[ww])); SBAR(); } while (0)
#define PC() do { asm volatile(PACE_OUT); SBAR(); } while (0)
#pragma unroll
        for (int a = 0; a < 8; ++a) { if (a < 5) { LDT(0, a, 0); LDT(0, a, 1); } LDB(0, 7 - a); }
        asm volatile("" :: "v"(pfd));
        chunk1();
#pragma unroll
        for (int i = 0; i < (NSC ? 0 : 4); ++i)
            glds16p<K2_QNT>((unsigned)(idx * 64 + cw * 16 + (lane >> 4)) * 2048u + xcd * 256 + (lane & 15) * 16, Aq + i * 8192, lds0 + K2_SCR + cw * 1024);
        RAW_BAR();
        asm volatile("s_waitcnt vmcnt(%0)" :: "n"(NSC ? 0 : AORD == 2 ? 12 : 4) : "memory");
        lds_add(fl + C_S1, lane);
#define ZFL() PC()
        __builtin_amdgcn_s_setprio(AUX_PRIO);
        const float dch = (ch & 1) ? bfhi(dd) : bflo(dd);
        const lds_ptr kdp = cv + CV_KV + 480 + 32 * r1 + 16 * (q >> 1) + ((q & 1) ? 0 : 8);
        float kd[8][4];
        u32x2 kk;
        unsigned kc0, kc1, zo[8][2];
#define KD_RD(t) do { kk = *(const LAS u32x2*)(kdp + 512 * (t)); SBAR(); } while (0)
#define KD_UP0(t) do { kd[t][0] = bfhi(kk[1]); kd[t][1] = bflo(kk[1]); SBAR(); } while (0)
#define KD_UP1(t) do { kd[t][2] = bfhi(kk[0]); kd[t][3] = bflo(kk[0]); SBAR(); } while (0)
#define KD_MU0(t) do { kd[t][0] = mul_sv(dch, kd[t][0]); kd[t][1] = mul_sv(dch, kd[t][1]); SBAR(); } while (0)
#define KD_MU1(t) do { kd[t][2] = mul_sv(dch, kd[t][2]); kd[t][3] = mul_sv(dch, kd[t][3]); SBAR(); } while (0)
#define KD_CV(t) do { kc0 = cvtpk(kd[t][0], kd[t][1]); kc1 = cvtpk(kd[t][2], kd[t][3]); SBAR(); } while (0)
#define KD_X0(t) do { kd[t][0] = bflo(kc0); kd[t][1] = bfhi(kc0); SBAR(); } while (0)
#define KD_X1(t) do { kd[t][2] = bflo(kc1); kd[t][3] = bfhi(kc1); SBAR(); } while (0)
#define ZSTORE st_sc1
#define ZF0(t) do { kc0 = cvtpk(y[t][0], y[t][1]); kc1 = cvtpk(y[t][2], y[t][3]); SBAR(); } while (0)
#define ZF1(t) do { y[t][0] = bflo(kc0); y[t][1] = bfhi(kc0); SBAR(); } while (0)
#define ZF2(t) do { y[t][2] = bflo(kc1); y[t][3] = bfhi(kc1); SBAR(); } while (0)
#define ZF3(t) do { y[t][0] = add_vv(y[t][0], kd[t][0]); y[t][1] = add_vv(y[t][1], kd[t][1]); SBAR(); } while (0)
#define ZF4(t) do { y[t][2] = add_vv(y[t][2], kd[t][2]); y[t][3] = add_vv(y[t][3], kd[t][3]); SBAR(); } while (0)
#define ZF5(t) do { zo[t][0] = nz(cvtpk(y[t][0], y[t][1])); zo[t][1] = nz(cvtpk(y[t][2], y[t][3])); SBAR(); } while (0)
#define ZST(pr) do { auto s0 = __builtin_amdgcn_permlane16_swap(zo[2 * (pr)][0], zo[2 * (pr) + 1][0], false, false); \
            auto s1 = __builtin_amdgcn_permlane16_swap(zo[2 * (pr)][1], zo[2 * (pr) + 1][1], false, false); \
            ZSTORE(u32x4{s0[0], s1[0], s0[1], s1[1]}, rs, (unsigned)WS_ZT + ch * 4096 + (pr) * 1024 + voff); SBAR(); } while (0)
#define W3(i) do { if ((i) == 0) PC(); } while (0)
#define HF() do { lds_add(fl + C_HF, lane); SBAR(); } while (0)
        CM0(0, 7); LDB(1, 7); CM0(0, 6); LDT(0, 5, 0); CM0(0, 5); LDT(0, 5, 1); CM0(0, 4); LDT(0, 6, 0); CM0(0, 3); LDT(0, 6, 1); CM0(0, 2); LDT(0, 7, 0); CM0(0, 1); LDT(0, 7, 1); CM0(0, 0); KD_RD(0);
        CM(1, 6); LDB(1, 6); CM(1, 5); LDT(1, 0, 0); CM(1, 4); LDT(1, 0, 1); CM(1, 3); PC(); CM(1, 2); PC(); CM(1, 1); KD_UP0(0); CM(1, 0); KD_UP1(0);
        CM(2, 5); LDB(1, 5); CM(2, 4); LDT(1, 1, 0); CM(2, 3); LDT(1, 1, 1); CM(2, 2); KD_MU0(0); CM(2, 1); KD_MU1(0); CM(2, 0); KD_CV(0);
        CM(3, 4); LDB(1, 4); CM(3, 3); LDT(1, 2, 0); CM(3, 2); LDT(1, 2, 1); CM(3, 1); KD_X0(0); CM(3, 0); KD_X1(0);
        CM(4, 3); LDB(1, 3); CM(4, 2); LDT(1, 3, 0); CM(4, 1); LDT(1, 3, 1); CM(4, 0); KD_RD(1);
        CM(5, 2); LDB(1, 2); CM(5, 1); LDT(1, 4, 0); CM(5, 0); LDT(1, 4, 1);
        CM(6, 1); LDB(1, 1); CM(6, 0); PC();
        CM(7, 0); LDB(1, 0);
        DGV(6);
        CM(0, 7); LDB(2, 7); CM(0, 6); LDT(1, 5, 0); CM(0, 5); LDT(1, 5, 1); CM(0, 4); LDT(1, 6, 0); CM(0, 3); LDT(1, 6, 1); CM(0, 2); LDT(1, 7, 0); CM(0, 1); LDT(1, 7, 1); CM(0, 0); PC();
        CM(1, 6); LDB(2, 6); CM(1, 5); LDT(2, 0, 0); CM(1, 4); LDT(2, 0, 1); CM(1, 3); KD_UP0(1); CM(1, 2); KD_UP1(1); CM(1, 1); KD_MU0(1); CM(1, 0); KD_MU1(1);
        CM(2, 5); LDB(2, 5); CM(2, 4); LDT(2, 1, 0); CM(2, 3); LDT(2, 1, 1); CM(2, 2); KD_CV(1); CM(2, 1); KD_X0(1); CM(2, 0); KD_X1(1);
        CM(3, 4); LDB(2, 4); CM(3, 3); LDT(2, 2, 0); CM(3, 2); LDT(2, 2, 1); CM(3, 1); KD_RD(2); CM(3, 0); PC();
        CM(4, 3); LDB(2, 3); CM(4, 2); LDT(2, 3, 0); CM(4, 1); LDT(2, 3, 1); CM(4, 0); PC();
        CM(5, 2); LDB(2, 2); CM(5, 1); LDT(2, 4, 0); CM(5, 0); LDT(2, 4, 1);
        CM(6, 1); LDB(2, 1); CM(6, 0); KD_UP0(2);
        CM(7, 0); LDB(2, 0);
        DGV(8);
        CM(0, 7); LDB(3, 7); CM(0, 6); LDT(2, 5, 0); CM(0, 5); LDT(2, 5, 1); CM(0, 4); LDT(2, 6, 0); CM(0, 3); LDT(2, 6, 1); CM(0, 2); LDT(2, 7, 0); CM(0, 1); LDT(2, 7, 1); CM(0, 0); KD_UP1(2);
        CM(1, 6); LDB(3, 6); CM(1, 5); LDT(3, 0, 0); CM(1, 4); LDT(3, 0, 1); CM(1, 3); KD_MU0(2); CM(1, 2); KD_MU1(2); CM(1, 1); KD_CV(2); CM(1, 0); KD_X0(2);
        CM(2, 5); LDB(3, 5); CM(2, 4); LDT(3, 1, 0); CM(2, 3); LDT(3, 1, 1); CM(2, 2); KD_X1(2); CM(2, 1); KD_RD(3); CM(2, 0); PC();
        CM(3, 4); LDB(3, 4); CM(3, 3); LDT(3, 2, 0); CM(3, 2); LDT(3, 2, 1); CM(3, 1); PC(); CM(3, 0); KD_UP0(3);
        CM(4, 3); LDB(3, 3); CM(4, 2); LDT(3, 3, 0); CM(4, 1); LDT(3, 3, 1); CM(4, 0); KD_UP1(3);
        CM(5, 2); LDB(3, 2); CM(5, 1); LDT(3, 4, 0); CM(5, 0); LDT(3, 4, 1);
        CM(6, 1); LDB(3, 1); CM(6, 0); KD_MU0(3);
        CM(7, 0); LDB(3, 0);
        DGV(12);
        CM(0, 7); LDB(4, 7); CM(0, 6); LDT(3, 5, 0); CM(0, 5); LDT(3, 5, 1); CM(0, 4); LDT(3, 6, 0); CM(0, 3); LDT(3, 6, 1); CM(0, 2); LDT(3, 7, 0); CM(0, 1); LDT(3, 7, 1); CM(0, 0); KD_MU1(3);
        CM(1, 6); LDB(4, 6); CM(1, 5); LDT(4, 0, 0); CM(1, 4); LDT(4, 0, 1); CM(1, 3); KD_CV(3); CM(1, 2); KD_X0(3); CM(1, 1); KD_X1(3); CM(1, 0); KD_RD(4);
        CM(2, 5); LDB(4, 5); CM(2, 4); LDT(4, 1, 0); CM(2, 3); LDT(4, 1, 1); CM(2, 2); PC(); CM(2, 1); PC(); CM(2, 0); KD_UP0(4);
        CM(3, 4); LDB(4, 4); CM(3, 3); LDT(4, 2, 0); CM(3, 2); LDT(4, 2, 1); CM(3, 1); KD_UP1(4); CM(3, 0); KD_MU0(4);
        CM(4, 3); LDB(4, 3); CM(4, 2); LDT(4, 3, 0); CM(4, 1); LDT(4, 3, 1); CM(4, 0); KD_MU1(4);
        CM(5, 2); LDB(4, 2); CM(5, 1); LDT(4, 4, 0); CM(5, 0); LDT(4, 4, 1);
        CM(6, 1); LDB(4, 1); CM(6, 0); KD_CV(4);
        CM(7, 0); LDB(4, 0);
        DGV(9);
        CM(0, 7); LDB(5, 7); CM(0, 6); LDT(4, 5, 0); CM(0, 5); LDT(4, 5, 1); CM(0, 4); LDT(4, 6, 0); CM(0, 3); LDT(4, 6, 1); CM(0, 2); LDT(4, 7, 0); CM(0, 1); LDT(4, 7, 1); CM(0, 0); KD_X0(4);
        CM(1, 6); LDB(5, 6); CM(1, 5); LDT(5, 0, 0); CM(1, 4); LDT(5, 0, 1); CM(1, 3); KD_X1(4); CM(1, 2); KD_RD(5); CM(1, 1); PC(); CM(1, 0); PC();
        CM(2, 5); LDB(5, 5); CM(2, 4); LDT(5, 1, 0); CM(2, 3); LDT(5, 1, 1); CM(2, 2); KD_UP0(5); CM(2, 1); KD_UP1(5); CM(2, 0); KD_MU0(5);
        CM(3, 4); LDB(5, 4); CM(3, 3); LDT(5, 2, 0); CM(3, 2); LDT(5, 2, 1); CM(3, 1); KD_MU1(5); CM(3, 0); KD_CV(5);
        CM(4, 3); LDB(5, 3); CM(4, 2); LDT(5, 3, 0); CM(4, 1); LDT(5, 3, 1); CM(4, 0); KD_X0(5);
        CM(5, 2); LDB(5, 2); CM(5, 1); LDT(5, 4, 0); CM(5, 0); LDT(5, 4, 1);
        CM(6, 1); LDB(5, 1); CM(6, 0); KD_X1(5);
        CM(7, 0); LDB(5, 0);
        DGV(11);
        CM(0, 7); LDB(6, 7); CM(0, 6); LDT(5, 5, 0); CM(0, 5); LDT(5, 5, 1); CM(0, 4); LDT(5, 6, 0); CM(0, 3); LDT(5, 6, 1); CM(0, 2); LDT(5, 7, 0); CM(0, 1); LDT(5, 7, 1); CM(0, 0); KD_RD(6);
        CM(1, 6); LDB(6, 6); CM(1, 5); LDT(6, 0, 0); CM(1, 4); LDT(6, 0, 1); CM(1, 3); PC(); CM(1, 2); PC(); CM(1, 1); KD_UP0(6); CM(1, 0); KD_UP1(6);
        CM(2, 5); LDB(6, 5); CM(2, 4); LDT(6, 1, 0); CM(2, 3); LDT(6, 1, 1); CM(2, 2); KD_MU0(6); CM(2, 1); KD_MU1(6); CM(2, 0); KD_CV(6);
        CM(3, 4); LDB(6, 4); CM(3, 3); LDT(6, 2, 0); CM(3, 2); LDT(6, 2, 1); CM(3, 1); KD_X0(6); CM(3, 0); KD_X1(6);
        CM(4, 3); LDB(6, 3); CM(4, 2); LDT(6, 3, 0); CM(4, 1); LDT(6, 3, 1); CM(4, 0); KD_RD(7);
        CM(5, 2); LDB(6, 2); CM(5, 1); LDT(6, 4, 0); CM(5, 0); LDT(6, 4, 1);
        CM(6, 1); LDB(6, 1); CM(6, 0); PC();
        CM(7, 0); LDB(6, 0);
        DGV(7);
        CM(0, 7); LDB(7, 7); CM(0, 6); LDT(6, 5, 0); CM(0, 5); LDT(6, 5, 1); CM(0, 4); LDT(6, 6, 0); CM(0, 3); LDT(6, 6, 1); CM(0, 2); LDT(6, 7, 0); CM(0, 1); LDT(6, 7, 1); CM(0, 0); PC();
        CM(1, 6); LDB(7, 6); CM(1, 5); LDT(7, 0, 0); CM(1, 4); LDT(7, 0, 1); CM(1, 3); KD_UP0(7); CM(1, 2); KD_UP1(7); CM(1, 1); KD_MU0(7); CM(1, 0); KD_MU1(7);
        CM(2, 5); LDB(7, 5); CM(2, 4); LDT(7, 1, 0); CM(2, 3); LDT(7, 1, 1); CM(2, 2); KD_CV(7); CM(2, 1); KD_X0(7); CM(2, 0); KD_X1(7);
        CM(3, 4); LDB(7, 4); CM(3, 3); LDT(7, 2, 0); CM(3, 2); LDT(7, 2, 1); CM(3, 1); PC(); CM(3, 0); PC();
        CM(4, 3); LDB(7, 3); CM(4, 2); LDT(7, 3, 0); CM(4, 1); LDT(7, 3, 1); CM(4, 0); PC();
        CM(5, 2); LDB(7, 2); CM(5, 1); LDT(7, 4, 0); CM(5, 0); LDT(7, 4, 1);
        CM(6, 1); LDB(7, 1); CM(6, 0); PC();
        CM(7, 0); LDB(7, 0);
        DGV(3);
        CM(0, 3); LDT(7, 5, 0); CM(0, 2); LDT(7, 5, 1); CM(1, 2); LDT(7, 6, 0); CM(1, 1); LDT(7, 6, 1); CM(2, 1); LDT(7, 7, 0); CM(2, 0); LDT(7, 7, 1);
        CM(3, 0); HF(); CM(0, 1); ZFL(); CM(0, 0); W3(0); CM(1, 0); ZF0(2); W3(1); CM(0, 5); ZF1(2); W3(2); CM(0, 4); ZF2(2); W3(3);
        CM(1, 4); ZF3(2); W3(4); CM(1, 3); ZF4(2); W3(5); CM(2, 3); ZF5(2); W3(6); CM(2, 2); ZF0(3); W3(7); CM(3, 2); ZF1(3);  CM(3, 1); ZF2(3);
        CM(4, 1); ZF3(3);  CM(4, 0); ZF4(3);  CM(5, 0); ZF5(3);  CM(0, 7); ZST(1);  CM(0, 6); ZF0(0);  CM(1, 6); ZF1(0);
        CM(1, 5); ZF2(0);  CM(2, 5); ZF3(0);  CM(2, 4); ZF4(0);  CM(3, 4); ZF5(0);  CM(3, 3); ZF0(1);  CM(4, 3); ZF1(1);
        CM(4, 2); ZF2(1);  CM(5, 2); ZF3(1);  CM(5, 1); ZF4(1);  CM(6, 1); ZF5(1);  CM(6, 0); ZST(0);  CM(7, 0); ZF0(4);
        DG(4);
        ZF1(4); ZF2(4); ZF3(4); ZF4(4); ZF5(4); ZF0(5); ZF1(5); ZF2(5); ZF3(5); ZF4(5); ZF5(5); ZST(2); ZF0(6); ZF1(6); ZF2(6); ZF3(6); ZF4(6); ZF5(6); ZF0(7); ZF1(7); ZF2(7); ZF3(7); ZF4(7); ZF5(7); ZST(3);
        DG(10);
        __builtin_amdgcn_s_setprio(3);
        while ((unsigned)__builtin_amdgcn_readfirstlane(*(volatile LAS unsigned*)(fl + C_OL)) < ZL_GATE) __builtin_amdgcn_s_sleep(4);
        CFENCE();
        const int arow = idx * 64 + (cw >> 1) * 32 + rg * 4, acol = xcd * 256 + (cw & 1) * 128 + cq * 16;
        u32x4 qr[4];
#pragma unroll
        for (int i = 0; i < 4; ++i) qr[i] = gld128((unsigned)(arow * 2048 + acol), Aq + i * 2048);
        {
            const lds_ptr tr = cv + CV_KV + 480;
            u32x4 t[4];
            const int ck = (cw >> 1) * 4 + ((lane ^ ((lane >> 5) * 2)) & 3);
            const unsigned zb = (unsigned)WS_ZT + (unsigned)(xcd * 128 + (cw & 1) * 64 + (lane >> 4) * 4) * 4096u + (ZN ? idx * 128 + ck * 16 : (idx >> 3) * 1024 +
                                ((idx & 3) * 4 + ((idx >> 2) & 1) * 16 + (ck >> 1) + (ck & 1) * 32) * 16);
#pragma unroll 1
            for (unsigned spins = 0; spins < (1u << 20); ++spins) {
#pragma unroll
                for (int i = 0; i < 4; ++i) t[i] = ld_sc1s(rs, zb + ((((lane >> 2) & 3) ^ i) << 12), i * 65536);
                if (!missing(mx4(mx4(mx8(t[0], t[1]), t[2]), t[3]))) break;
                __builtin_amdgcn_s_sleep(Z_SLEEP);
            }
            DG(13);
            __builtin_amdgcn_s_setprio(ZE_PRIO);
#pragma unroll
            for (int i = 0; i < 4; ++i) *(LAS u32x4*)(tr + i * 1024 + lane * 16) = t[i];
#pragma unroll
            for (int i = 0; i < 4; ++i) asm volatile("" : "+v"(qr[i]));
            u32x2 zc[8];
#pragma unroll
            for (int c = 0; c < 8; ++c)
                zc[c] = *(const LAS u32x2*)(tr + cq * 512 + (c >> 2) * 256 + ((((c & 3) * 4 + (rg >> 1)) ^ (cq * 2)) << 4) + (rg & 1) * 8);
#pragma unroll
            for (int i = 0; i < 4; ++i) {
                u32x4 o;
#pragma unroll
                for (int c = 0; c < 4; ++c) {
                    const unsigned z0 = zc[2 * c][i >> 1], z1 = zc[2 * c + 1][i >> 1];
                    o[c] = cvtpk(mul_vv(bflo(qr[i][c]), (i & 1) ? bfhi(z0) : bflo(z0)), mul_vv(bfhi(qr[i][c]), (i & 1) ? bfhi(z1) : bflo(z1)));
                }
                __builtin_amdgcn_raw_buffer_store_b128(o, ro, arow * 4096 + acol, i * 4096, OUT_AUX);
            }
        }
        asm volatile(".Lcp_k2z:");
        DG_END(14);
        return;
    }

    const int half = w >> 2;
    const int TA = qb * 8 + (w & 3) * 2 + half, TB = TA ^ 1;
    bf16x8 sel;
    {
        const short one = (((lane >> 2) & 1) == ((lane >> 4) & 1) && (lane & 11) == 0) ? (short)0x3f80 : (short)0;
#pragma unroll
        for (int i = 0; i < 8; ++i) sel[i] = SPM ? ((lane & 15) == (i & 4) ? (short)0x3f80 : (short)0) : one;
    }
    int sidx = (int)0xdddddddd;
#define LO8(x) __builtin_bit_cast(bfx16_t, __builtin_shufflevector(x, x, 0, 1, 2, 3, 4, 5, 6, 7))
#define HI8(x) __builtin_bit_cast(bfx16_t, __builtin_shufflevector(x, x, 8, 9, 10, 11, 12, 13, 14, 15))
#define SPV(v, p, o) __builtin_amdgcn_smfmac_f32_32x32x32_bf16(__builtin_bit_cast(bfx8_t, v), p, o, sidx, 0, 0)
#define SSUM(p, l) __builtin_amdgcn_smfmac_f32_16x16x64_bf16(__builtin_bit_cast(bfx8_t, sel), p, l, sidx, 0, 0)
    f32x16 oA = f32x16{}, oB = f32x16{};
    f32x4 lA = f32x4{}, lB = f32x4{};
    f32x16 negm, sA, sB;
    lds_ptr lp = lds + half * 16384 + lane * 16;
    bf16x8 k0[2], k1[2], v0[2], v1[2];
    u32x4 p0, p1 = {0u, 0u, 0u, 0u};
#define EX(X, i) X[i] = __builtin_amdgcn_exp2f(X[i])
    lds_ptr qpa = lds + K2_SF + (TA & 7) * 2048 + lane * 16, qpb = lds + K2_SF + (TB & 7) * 2048 + lane * 16;
    asm volatile("" : "+v"(qpa), "+v"(qpb), "+v"(lp), "+v"(sel), "+v"(oA), "+v"(oB), "+v"(lA), "+v"(lB), "+v"(sidx));
    WAIT_BAR(0);
    DG(3);
    const bf16x8 qa0 = lds_rd128(qpa), qb0 = lds_rd128(qpb);
    const bf16x8 x0 = lds_rd128(lds + lane * 16), x1 = lds_rd128(lds + lane * 16 + 1024);
    const bf16x8 qa1 = lds_rd128(qpa + 1024), qb1 = lds_rd128(qpb + 1024);
    {
        k0[0] = lds_rd128(lp); k1[0] = lds_rd128(lp + 1024);
        v0[0] = lds_rd128(lp + 2048); v1[0] = lds_rd128(lp + 3072);
        sA = MFMA32(x0, qa0, f32x16{});
        sB = MFMA32(x0, qb0, f32x16{});
        sA = MFMA32(x1, qa1, sA);
        sB = MFMA32(x1, qb1, sB);
        float m = fmx(sA[0], sA[1]), m2 = fmx(sB[0], sB[1]);
#pragma unroll
        for (int i = 2; i < 16; i += 2) m = fmx(fmx(m, sA[i]), sA[i + 1]);
#pragma unroll
        for (int i = 2; i < 16; i += 2) m2 = fmx(fmx(m2, sB[i]), sB[i + 1]);
        m = fmx(m, m2);
        auto rr = __builtin_amdgcn_permlane32_swap(__float_as_uint(m), __float_as_uint(m), false, false);
        m = fmx(__uint_as_float(rr[0]), __uint_as_float(rr[1]));
#pragma unroll
        for (int i = 0; i < 16; ++i) negm[i] = -m;
        sA = MFMA32(k0[0], qa0, negm);
        sA = MFMA32(k1[0], qa1, sA);
        v1[1] = v1[0];
#pragma unroll
        for (int i = 8; i < 16; ++i) sB[i] = 0.f;
        asm volatile("" : "+v"(sB));
        SBAR();
        RAW_BAR();
        SBAR();
        EX(sA, 0); EX(sA, 1);
    }
    DG(10);
#define SUM16(l, p) asm volatile("v_mfma_f32_16x16x32_bf16 %0, %1, %2, %0" : "+v"(l) : "v"(sel), "v"(p))
#define HALF(C, oC, lC, N, qx, qy, ka, kb, oP, lP, vprev, vcur0, PRE) do { \
        oP = SPV(vprev, HI8(N), oP); SBAR(); \
        PRE; EX(C, 2); EX(C, 3); EX(C, 4); SBAR(); \
        lP = SSUM(HI8(N), lP); SBAR(); \
        EX(C, 5); SBAR(); \
        N = MFMA32(ka, qx, negm); SBAR(); \
        EX(C, 6); EX(C, 7); EX(C, 8); SBAR(); \
        N = MFMA32(kb, qy, N); SBAR(); \
        EX(C, 9); EX(C, 10); EX(C, 11); SBAR(); \
        oC = SPV(vcur0, LO8(C), oC); SBAR(); \
        EX(C, 12); EX(C, 13); EX(C, 14); SBAR(); \
        lC = SSUM(LO8(C), lC); SBAR(); \
        EX(C, 15); EX(N, 0); EX(N, 1); SBAR(); \
    } while (0)
#define RD4(Y, nx) do { v1[Y] = lds_rd128((nx) + 3072); v0[Y] = lds_rd128((nx) + 2048); \
        k1[Y] = lds_rd128((nx) + 1024); k0[Y] = lds_rd128(nx); } while (0)
#define KT(X, nx, PRE0) do { \
        HALF(sA, oA, lA, sB, qb0, qb1, k0[X], k1[X], oB, lB, v1[X ^ 1], v0[X], PRE0; RD4(X ^ 1, nx)); \
        HALF(sB, oB, lB, sA, qa0, qa1, k0[X ^ 1], k1[X ^ 1], oA, lA, v1[X], v0[X], (void)0); \
    } while (0)
#define RING_BAR() do { unsigned long long sv_; \
        asm volatile("s_cmp_eq_u32 %1, 7\n\ts_cbranch_scc1 1f\n\ts_barrier\n1:\n\ts_mov_b64 %0, exec\n\ts_mov_b64 exec, 1\n\tds_add_u32 %2, %3\n\ts_mov_b64 exec, %0" \
                     : "=&s"(sv_) : "s"(st), "v"(fl + C_LD), "v"(1u) : "memory", "scc"); } while (0)
    SBAR();
#pragma unroll 1
    for (int st = 0; st < 8; ++st) {
        const lds_ptr cur = lp + (st & 1) * K2_SLOT;
        const lds_ptr nxt = lp + ((st + 1) & 1) * K2_SLOT;
        PRIO(2);
        KT(0, cur + 4096, (void)0);
        PRIO(1);
        KT(1, cur + 8192, (void)0);
        PRIO(0);
        KT(0, cur + 12288, (void)0);
        PRIO(3);
        KT(1, nxt, RAW_BAR(); SBAR());
    }
    oB = SPV(v1[1], HI8(sB), oB);
    SBAR();
    lB = SSUM(HI8(sB), lB);
    SBAR();
    DG(4);
    u32x4 w3[4], w2r[4];
    const char* s2 = wsrc(half ^ 1, 32 + (w & 3) * 4);
    const char* s3 = wsrc(half, 48 + (w & 3) * 4);
    auto ldwe = [&]() __attribute__((always_inline)) {
        SBAR();
    };
    if (WEP == 0) ldwe();
    {
        const lds_ptr xo = lds + K2_SLOT + w * 4096 + lane * 16;
        *(LAS float*)(lds + K2_LX + w * 256 + lane * 4) = lB[0];
#pragma unroll
        for (int i = 0; i < 4; ++i) *(LAS f32x4*)(xo + i * 1024) = f32x4{oB[4 * i], oB[4 * i + 1], oB[4 * i + 2], oB[4 * i + 3]};
        lds_add(fl + F_X + w * 4, lane);
        lds_add(fl + C_END + half * 4, lane);
        if (WEP == 1) ldwe();
        lds_wait_ge(fl + F_X + (w ^ 4) * 4, 1u);
        const lds_ptr xi = lds + K2_SLOT + (w ^ 4) * 4096 + lane * 16;
        f32x4 t[4];
        const float lx = *(const LAS float*)(lds + K2_LX + (w ^ 4) * 256 + (lane & 31) * 4);
        { auto rr = __builtin_amdgcn_permlane32_swap(__float_as_uint(lA[0]), __float_as_uint(lA[0]), false, false); lA[0] = __uint_as_float(rr[0]); }
#pragma unroll
        for (int i = 0; i < 4; ++i) t[i] = *(const LAS f32x4*)(xi + i * 1024);
        SBAR();
        lA[0] += lx;
#pragma unroll
        for (int i = 0; i < 16; ++i) { float o = oA[i]; acc_vv(o, t[i >> 2][i & 3]); oA[i] = o; }
    }
    DG(9);
    const float l = lA[0];
    u32x4 on0, on1;
    auto finish = [&](float ls) {
        float rl = __builtin_amdgcn_rcpf(ls);
        asm volatile("s_nop 1" : "+v"(rl));
        f32x16 o;
#pragma unroll
        for (int i = 0; i < 16; ++i) o[i] = mul_vv(oA[i], rl);
        on0 = nz4(pack8n(o, 0)); on1 = nz4(pack8n(o, 1));
    };
    finish(l);
    {
        float t0, t1;
        asm("v_mul_f32_e32 %0, 0, %2\n\tv_mul_f32_e32 %1, 0, %3\n\tv_fmac_f32_e32 %0, 0, %4\n\tv_fmac_f32_e32 %1, 0, %5\n\tv_fmac_f32_e32 %0, 0, %6\n\tv_fmac_f32_e32 %1, 0, %7\n\tv_fmac_f32_e32 %0, 0, %8\n\tv_fmac_f32_e32 %1, 0, %9\n\t"
            "v_fmac_f32_e32 %0, 0, %10\n\tv_fmac_f32_e32 %1, 0, %11\n\tv_fmac_f32_e32 %0, 0, %12\n\tv_fmac_f32_e32 %1, 0, %13\n\tv_fmac_f32_e32 %0, 0, %14\n\tv_fmac_f32_e32 %1, 0, %15\n\tv_fmac_f32_e32 %0, 0, %16\n\tv_fmac_f32_e32 %1, 0, %17"
            : "=&v"(t0), "=&v"(t1) : "v"(oA[0]), "v"(oA[1]), "v"(oA[2]), "v"(oA[3]), "v"(oA[4]), "v"(oA[5]), "v"(oA[6]), "v"(oA[7]), "v"(oA[8]), "v"(oA[9]), "v"(oA[10]), "v"(oA[11]), "v"(oA[12]), "v"(oA[13]), "v"(oA[14]), "v"(oA[15]));
        const bool ok = (l <= 1.0e30f) && (l >= 1.0e-18f) && (t0 + t1 == 0.f);
        if (__builtin_expect(__any(!ok), 0)) {
            float m = -INFINITY, ls = 0.f;
            oA = f32x16{};
#pragma unroll 1
            for (int t = 0; t < 64; ++t) {
                const char* g = kvg + t * 4096 + lane * 16;
                const bf16x8 a0 = *(const bf16x8*)g, a1 = *(const bf16x8*)(g + 1024);
                const bf16x8 b0 = *(const bf16x8*)(g + 2048), b1 = *(const bf16x8*)(g + 3072);
                f32x16 s = MFMA32(a0, qa0, f32x16{});
                s = MFMA32(a1, qa1, s);
                float tm = s[0];
#pragma unroll
                for (int i = 1; i < 16; ++i) tm = __builtin_fmaxf(tm, s[i]);
                auto rr = __builtin_amdgcn_permlane32_swap(__float_as_uint(tm), __float_as_uint(tm), false, false);
                tm = __builtin_fmaxf(__uint_as_float(rr[0]), __uint_as_float(rr[1]));
                const float mn = __builtin_fmaxf(m, tm);
                const float alpha = __builtin_amdgcn_exp2f(m - mn);
                m = mn;
                float ps = 0.f;
#pragma unroll
                for (int i = 0; i < 16; ++i) { s[i] = bfhi(__float_as_uint(__builtin_amdgcn_exp2f(s[i] - mn))); ps += s[i]; oA[i] *= alpha; }
                ls = ls * alpha + ps;
                oA = SPV(b0, LO8(s), oA);
                oA = SPV(b1, HI8(s), oA);
            }
            auto rr = __builtin_amdgcn_permlane32_swap(__float_as_uint(ls), __float_as_uint(ls), false, false);
            asm volatile("s_nop 7\n\ts_nop 7" : "+v"(oA));
            finish(__uint_as_float(rr[0]) + __uint_as_float(rr[1]));
        }
    }
    auto ldw2 = [&]() {
#pragma unroll
        for (int i = 0; i < 4; ++i)
            glds16p<K2_WNT>(wvoff, s2 + i * 16384, lds0 + K2_SLOT + ((w ^ 4) * 4 + i) * 1024);
    };
    auto ldw3 = [&]() {
#pragma unroll
        for (int i = 0; i < 4; ++i) w3[i] = gld128(wvoff, s3 + i * 16384);
    };
    {
        const unsigned og = (unsigned)WS_O + (unsigned)(TA * 32 + h) * 2048u + lane * 16;
        st_sc1(on0, rs, og);
        st_sc1(on1, rs, og + 1024);
        lds_add(fl + C_S1, lane);
        lds_wait_ge(fl + C_S1, 12u);
        DG(5);
        if (!(WE & 1)) ldw2();
        if (!(WE & 2)) ldw3();
    }
    {
        const int half = w & 1;
        const int T3 = tt * 4 + (w >> 1);
        const unsigned og = (unsigned)WS_O + (unsigned)T3 * 65536u + half * 32768 + lane * 16;
        const int r = lane & 31, hh = lane >> 5;
        const int g = lane >> 4, q = (lane & 15) >> 2, p = lane & 3;
        int woff[2];
#pragma unroll
        for (int n = 0; n < 2; ++n) {
            const int c = 4 * (n ^ half) + 2 * (p & 1) + (g & 1);
            woff[n] = (4 * hh + q) * 128 + ((c ^ ((q >> 1) << 2)) << 4) + (p >> 1) * 8;
        }
        f32x16 acc[2] = {f32x16{}, f32x16{}};
        lds_ptr wl[3][2] = {{lds + half * 16384 + woff[0], lds + half * 16384 + woff[1]},
                            {lds + K2_SF + half * 16384 + woff[0], lds + K2_SF + half * 16384 + woff[1]},
                            {lds + K2_CV + half * 2 * CV_BYTES + woff[0], lds + K2_CV + half * 2 * CV_BYTES + woff[1]}};
        asm volatile("" : "+v"(wl[0][0]), "+v"(wl[0][1]), "+v"(wl[1][0]), "+v"(wl[1][1]), "+v"(wl[2][0]), "+v"(wl[2][1]));
#define MS(m) ((m) < 64 - 2 * TL ? (m) >> 1 : 32 - TL + ((m) - (64 - 2 * TL)) % TL)
#define MN(m) ((m) < 64 - 2 * TL ? (m) & 1 : (m) < 64 - TL)
        constexpr int WR = 2 * WD + 2;
        s16x4 wa[WR], wb[WR];
#define LDW(m) do { const int s_ = MS(m), f_ = s_ < 8 ? 1 : s_ < 24 ? 0 : 2; \
            const int o_ = f_ == 2 ? ((s_ >> 2) & 1) * CV_BYTES + (s_ & 3) * 2048 : (f_ ? 0 : ((s_ >> 3) - 1) * K2_SLOT) + (s_ & 7) * 2048; \
            wa[(m) % WR] = lds_tr(wl[f_][MN(m)] + o_); wb[(m) % WR] = lds_tr(wl[f_][MN(m)] + o_ + 1024); SBAR(); } while (0)
#pragma unroll
        for (int m = 0; m < 2 * WD; ++m) LDW(m);
        u32x4 of[OWIN];
#define LDO(i) do { of[(i) % OWIN] = ld_sc1s(rs, og, (((i) + krot) & 31) * 1024); SBAR(); } while (0)
#define LDP(i) do { of[(i) % OWIN] = __builtin_amdgcn_raw_buffer_load_b128(rs, (int)og, (((i) + krot) & 31) * 1024, OPL ? OPL - 1 : AUX_SC1); SBAR(); } while (0)
#define OWAIT(n, a, b)
        for (unsigned spins = 0; spins < (1u << 20); ++spins) {
            {
#pragma unroll
            for (int i = 0; i < OW0; ++i) LDO(i);
            }
            unsigned m = mx8(of[0], of[1]);
#pragma unroll
            for (int i = 2; i < OW0; ++i) m = mx4(m, of[i]);
            if (!missing(m)) break;
            DGC(12);
        }
        lds_add(fl + C_S2, lane);
#pragma unroll
        for (int i = OW0; i < OWIN; ++i) LDP(i);
        DG(6);
        __builtin_amdgcn_s_setprio(P3_PRIO);
        SBAR();
        unsigned fv = 0;
#define FLAG_RD(a) do { fv = *(volatile LAS unsigned*)(a); SBAR(); } while (0)
#define FLAG_CHK(a, n) do { if (__builtin_expect((unsigned)__builtin_amdgcn_readfirstlane(fv) < (n), 0)) lds_wait_ge(a, n); CFENCE(); SBAR(); } while (0)
        lds_ptr xo = lds + K2_SF + w * 4096 + lane * 16, xi = lds + K2_SF + (w ^ 1) * 4096 + lane * 16;
        int bo = (T3 * 32 + (lane & 15)) * 4096 + 2048 + nt * 128 + half * 64 + g * 16;
        lds_ptr w3d = lds + K2_CV + (w >> 1) * CV_BYTES + (w & 1) * 4096 + lane * 16;
        asm volatile("" : "+v"(xo), "+v"(xi), "+v"(bo), "+v"(w3d));
        f32x4 xt[4];
        constexpr int M3 = TL > 8 ? 64 - 2 * TL + TL - 8 - 2 * WD : 48 - 2 * WD;
        sfor(std::make_integer_sequence<int, 64>{}, [&](auto mc) __attribute__((always_inline)) {
            constexpr int m = decltype(mc)::value, s = MS(m), n = MN(m);
            constexpr bool first = m < 64 - 2 * TL ? n == 0 : n == 1;
            constexpr bool last = m < 64 - 2 * TL ? n == 1 : n == 0;
            acc[n] = MFMA32(cat8(wa[m % WR], wb[m % WR]), PB(of[s % OWIN]), acc[n]);
            SBAR();
            if constexpr (m == 32 - 2 * WD) FLAG_CHK(fl + C_S2, 8u);
            if constexpr (m == M3) FLAG_CHK(fl + C_S3, 8u);
            if constexpr (m + 2 * WD < 64) LDW(m + 2 * WD);
            if constexpr (first && (s & 1) && s + 1 >= OW0 && s + 1 < 32) {
                OWAIT((OWIN + s < 32 ? OWIN + s : 32) - 3 - s, s + 1, s + 2);
                if (__builtin_expect(missing(mx8(of[(s + 1) % OWIN], of[(s + 2) % OWIN])), 0)) {
#pragma unroll 1
                    for (unsigned spins = 0; spins < (1u << 20); ++spins) {
                        __builtin_amdgcn_s_sleep(2);
                        LDO(s + 1); LDO(s + 2);
                        OWAIT(0, s + 1, s + 2);
                        DGC(13);
                        if (!missing(mx8(of[(s + 1) % OWIN], of[(s + 2) % OWIN]))) break;
                    }
                    if constexpr (ORL) {
#pragma unroll
                        for (int i = s + 3; i < s + OWIN && i < 32; ++i) LDO(i);
                    }
                }
            }
            if constexpr (last && s + OWIN < 32) LDP(s + OWIN);
            if constexpr (LORD && m == S2M) { asm volatile("s_waitcnt vmcnt(%0)" :: "n"(4 + (m + 1) / 2) : "memory"); lds_add(fl + C_S2, lane); }
            if constexpr (LORD && m == W3M) asm volatile("s_waitcnt vmcnt(%0)" :: "n"((m + 1) / 2 < 16 ? (m + 1) / 2 : 16) : "memory");
            if constexpr (m == 15 - 2 * WD) lds_add(fl + C_U0, lane);
            if constexpr (m == 29 - 2 * WD) FLAG_RD(fl + C_S2);
            if constexpr (m == W3M - 3) FLAG_RD(fl + C_HF);
            if constexpr (m == W3M) FLAG_CHK(fl + C_HF, 4u);
            if constexpr (m >= W3M && m < W3M + 8 && ((m - W3M) & 1) == 0) {
                asm volatile("" : "+v"(w3[((m - W3M) >> 1) & 3]));
                *(LAS u32x4*)(w3d + (((m - W3M) >> 1) & 3) * 1024) = w3[((m - W3M) >> 1) & 3];
            }
            if constexpr (m == W3M + 6) lds_add(fl + C_S3, lane);
            if constexpr (m == M3 - 3) FLAG_RD(fl + C_S3);
            if constexpr (m == 64 - TL - 3) FLAG_RD(fl + C_U0);
            if constexpr (m == 64 - TL) FLAG_CHK(fl + C_U0, 8u);
            if constexpr (m == 64 - TL + 1 || m == 64 - TL + 2) {
                constexpr int i = 2 * (m - (64 - TL + 1));
                *(LAS f32x4*)(xo + i * 1024) = f32x4{acc[1][4 * i], acc[1][4 * i + 1], acc[1][4 * i + 2], acc[1][4 * i + 3]};
                *(LAS f32x4*)(xo + i * 1024 + 1024) = f32x4{acc[1][4 * i + 4], acc[1][4 * i + 5], acc[1][4 * i + 6], acc[1][4 * i + 7]};
            }
            if constexpr (m == 64 - TL + 2) lds_add(fl + F_R + w * 4, lane);
            if constexpr (m == XR_AT - 2) FLAG_RD(fl + F_R + (w ^ 1) * 4);
            if constexpr (m == XR_AT) {
                FLAG_CHK(fl + F_R + (w ^ 1) * 4, 1u);
#pragma unroll
                for (int i = 0; i < 4; ++i) xt[i] = *(const LAS f32x4*)(xi + i * 1024);
            }
            if constexpr (ZL_GATE && last && s + OWIN == ZL_AT) lds_add(fl + C_OL, lane);
            if constexpr (m == 2 * P3_T1 + 1) __builtin_amdgcn_s_setprio(P3_PRIO - 1);
            SBAR();
            if constexpr (m == 31) DG(11);
            if constexpr (m == 47) DG(14);
        });
        asm volatile("s_nop 11" : "+v"(acc[0]));
        DG(7);
#pragma unroll
        for (int i = 0; i < 16; ++i) { float o = xt[i >> 2][i & 3]; acc_vv(o, acc[0][i]); acc[1][i] = o; }
        u32x4 pa = pack8n(acc[1], 0), pb = pack8n(acc[1], 1);
#pragma unroll
        for (int i = 0; i < 4; ++i) {
            auto sw = __builtin_amdgcn_permlane16_swap(pa[i], pb[i], false, false);
            pa[i] = sw[0]; pb[i] = sw[1];
        }
        asm volatile("" :: "v"(acc[0]));
        __builtin_amdgcn_raw_buffer_store_b128(pa, ro, bo, 0, OUT_AUX);
        __builtin_amdgcn_raw_buffer_store_b128(pb, ro, bo, 65536, OUT_AUX);
    }
    DG_END(8);
}

extern "C" void kernel_launch(void* const* d_in, const int* in_sizes, int n_in,
                              void* d_out, int out_size, void* d_ws, size_t ws_size,
                              hipStream_t stream) {
    const void* A_q = d_in[0];
    const void* A_k = d_in[1];
    const void* A_v = d_in[2];
    const void* A_shift = d_in[3];
    const void* A_ssmk = d_in[4];
    const void* A_D = d_in[5];
    const void* B_x = d_in[6];
    const void* B_wqkv = d_in[7];
    const void* B_wo = d_in[8];
    char* ws = (char*)d_ws;
    k1_qkv<<<256, 512, 0, stream>>>((const char*)B_x, (const char*)B_wqkv, ws, (const char*)B_wo, (const char*)A_q,
                                    (const char*)A_k, (const char*)A_v, (const char*)A_ssmk);
    k2_attn<<<256, 768, 0, stream>>>(ws, (const char*)A_q, (const char*)A_k, (const char*)A_v, (const char*)A_shift,
                                     (const char*)A_ssmk, (const unsigned*)A_D, (const char*)B_wo, (char*)d_out);
}
```

```cpp
#include <hip/hip_runtime.h>
#include <stdint.h>
#include <utility>

typedef short bf16x8 __attribute__((ext_vector_type(8)));
typedef short s16x4 __attribute__((ext_vector_type(4)));
typedef float f32x16 __attribute__((ext_vector_type(16)));
typedef float f32x4 __attribute__((ext_vector_type(4)));
typedef float f32x2 __attribute__((ext_vector_type(2)));
typedef unsigned u32x4 __attribute__((ext_vector_type(4)));
typedef unsigned u32x2 __attribute__((ext_vector_type(2)));
typedef __bf16 bf16x2_t __attribute__((ext_vector_type(2)));

#define LAS __attribute__((address_space(3)))
typedef LAS char* lds_ptr;
#define DI __device__ __forceinline__
#define SBAR() __builtin_amdgcn_sched_barrier(0)
#define MFMA32(a, b, c) __builtin_amdgcn_mfma_f32_32x32x16_bf16((a), (b), (c), 0, 0, 0)
#define MFMA16(a, b, c) __builtin_amdgcn_mfma_f32_16x16x32_bf16((a), (b), (c), 0, 0, 0)
typedef __bf16 bfx8_t __attribute__((ext_vector_type(8)));
typedef __bf16 bfx16_t __attribute__((ext_vector_type(16)));
#define WAIT_BAR(N) asm volatile("s_waitcnt vmcnt(" #N ") lgkmcnt(0)\n\ts_barrier" ::: "memory")

constexpr unsigned SPIN_MAX = 1u << 18;
constexpr size_t WS_KV = 4u << 20;
constexpr size_t WS_O = 12u << 20;
constexpr size_t WS_KR = 16u << 20;
constexpr size_t WS_ZT = 20u << 20;
constexpr size_t WS_CNT = 24u << 20;

template <int... I, class F> DI void sfor(std::integer_sequence<int, I...>, F&& f) { (f(std::integral_constant<int, I>{}), ...); }
DI unsigned cvtpk(float lo, float hi) {
    f32x2 v = {lo, hi};
    return __builtin_bit_cast(unsigned, __builtin_convertvector(v, bf16x2_t));
}
DI float bflo(unsigned u) { return __uint_as_float(u << 16); }
DI float bfhi(unsigned u) { return __uint_as_float(u & 0xffff0000u); }

DI void glds16(unsigned voff, const void* sbase, unsigned lds_dst) {
    unsigned sv;
    asm volatile("s_mov_b32 %0, m0\n\ts_mov_b32 m0, %3\n\ts_nop 0\n\tglobal_load_lds_dwordx4 %1, %2\n\ts_mov_b32 m0, %0"
                 : "=&s"(sv) : "v"(voff), "s"(sbase), "s"(lds_dst) : "memory");
}
DI void glds16nt(unsigned voff, const void* sbase, unsigned lds_dst) {
    unsigned sv;
    asm volatile("s_mov_b32 %0, m0\n\ts_mov_b32 m0, %3\n\ts_nop 0\n\tglobal_load_lds_dwordx4 %1, %2 nt\n\ts_mov_b32 m0, %0"
                 : "=&s"(sv) : "v"(voff), "s"(sbase), "s"(lds_dst) : "memory");
}
DI bf16x8 lds_rd128(lds_ptr p) { return *(const LAS bf16x8*)p; }
DI s16x4 lds_tr(lds_ptr p) {
    return __builtin_bit_cast(s16x4, __builtin_amdgcn_ds_read_tr16_b64_v4i16((LAS s16x4*)p));
}
DI bf16x8 cat8(s16x4 a, s16x4 b) { return __builtin_shufflevector(a, b, 0, 1, 2, 3, 4, 5, 6, 7); }
DI u32x4 pack8n(const f32x16& x, int s) {
    u32x4 p;
    p[0] = cvtpk(x[8 * s + 0], x[8 * s + 1]);
    p[1] = cvtpk(x[8 * s + 2], x[8 * s + 3]);
    p[2] = cvtpk(x[8 * s + 4], x[8 * s + 5]);
    p[3] = cvtpk(x[8 * s + 6], x[8 * s + 7]);
    return p;
}

#define RAW_BAR() asm volatile("s_barrier" ::: "memory")
DI unsigned gld32(unsigned voff, const void* sbase) {
    unsigned r;
    asm volatile("global_load_dword %0, %1, %2" : "=v"(r) : "v"(voff), "s"(sbase) : "memory");
    return r;
}
DI u32x4 gld128(unsigned voff, const void* sbase) {
    u32x4 r;
    asm volatile("global_load_dwordx4 %0, %1, %2" : "=v"(r) : "v"(voff), "s"(sbase) : "memory");
    return r;
}
DI float mul_sv(float s, float v) { float r; asm("v_mul_f32_e32 %0, %1, %2" : "=v"(r) : "s"(s), "v"(v)); return r; }
DI float mul_vv(float a, float b) { float r; asm("v_mul_f32_e32 %0, %1, %2" : "=v"(r) : "v"(a), "v"(b)); return r; }
DI float add_vv(float a, float b) { float r; asm("v_add_f32_e32 %0, %1, %2" : "=v"(r) : "v"(a), "v"(b)); return r; }
DI void acc_vv(float& a, float b) { asm("v_add_f32_e32 %0, %0, %1" : "+v"(a) : "v"(b)); }
typedef __attribute__((address_space(1))) unsigned gu32;
#define RLX_AGENT __ATOMIC_RELAXED, __HIP_MEMORY_SCOPE_AGENT
#define CFENCE() asm volatile("" ::: "memory")
constexpr int AUX_SC1 = 16;
DI u32x4 ld_sc1s(__amdgpu_buffer_rsrc_t rs, unsigned off, int soff) { return __builtin_amdgcn_raw_buffer_load_b128(rs, (int)off, soff, AUX_SC1); }
DI void st_sc1(u32x4 v, __amdgpu_buffer_rsrc_t rs, unsigned off) { __builtin_amdgcn_raw_buffer_store_b128(v, rs, (int)off, 0, AUX_SC1); }
constexpr unsigned EMPTY = 0xffffffffu;
DI unsigned nz(unsigned x) { return x < EMPTY - 1u ? x : EMPTY - 1u; }
DI u32x4 nz4(u32x4 v) { return u32x4{nz(v[0]), nz(v[1]), nz(v[2]), nz(v[3])}; }
DI float fmx(float a, float b) { return __builtin_elementwise_maximum(a, b); }
DI unsigned umax(unsigned a, unsigned b) { return a > b ? a : b; }
DI unsigned umax3(unsigned a, unsigned b, unsigned c) { return umax(umax(a, b), c); }
DI unsigned mx4(unsigned m, u32x4 v) { return umax3(umax3(m, v[0], v[1]), v[2], v[3]); }
DI unsigned mx8(u32x4 a, u32x4 b) { return umax(umax3(umax3(umax3(a[0], a[1], a[2]), a[3], b[0]), b[1], b[2]), b[3]); }
DI bool missing(unsigned m) { return __builtin_amdgcn_ballot_w64(m == EMPTY) != 0; }
DI void lds_add(lds_ptr p, unsigned one = 1u) {
    unsigned long long sv;
    asm volatile("s_mov_b64 %0, exec\n\ts_mov_b64 exec, 1\n\tds_add_u32 %1, %2\n\ts_mov_b64 exec, %0"
                 : "=&s"(sv) : "v"(p), "v"(one) : "memory");
}
DI void lds_wait_ge(lds_ptr p, unsigned v) {
    CFENCE();
    while ((unsigned)__builtin_amdgcn_readfirstlane(*(volatile LAS unsigned*)p) < v) __builtin_amdgcn_s_sleep(1);
    CFENCE();
}
DI void poll_ge(gu32* word, unsigned want) {
    CFENCE();
    for (unsigned spins = 0; spins < SPIN_MAX; ++spins) {
        if ((unsigned)__builtin_amdgcn_readfirstlane(__hip_atomic_load(word, RLX_AGENT)) >= want) break;
        __builtin_amdgcn_s_sleep(1);
    }
    __builtin_amdgcn_fence(__ATOMIC_ACQUIRE, "wavefront");
    CFENCE();
}
#define PB(x) __builtin_bit_cast(bf16x8, x)
#define PRIO(x) do { __builtin_amdgcn_s_setprio(x); SBAR(); } while (0)

constexpr int K1_STAGE = 40960, K1_LA = 5;
constexpr int K2_SLOT = 40960;
constexpr int K2_LX = 32768;
constexpr int K2_FL = K2_LX + 2048;
constexpr int K2_SCR = K2_FL + 256;
constexpr int K2_PK = K2_SCR + 1024;
constexpr int K2_SF = K2_SLOT + 32768;
constexpr int K2_CV = K2_SF + 32768;
constexpr int K2_Q = K2_CV;
constexpr int CV_HA = 0, CV_HB = 4160, CV_KV = 8320, CV_BYTES = 8320 + 480 + 4096;
static_assert(K2_CV + 4 * CV_BYTES <= 4 * K1_STAGE, "LDS");
constexpr int F_X = 0, F_R = 32, F_A = 64, C_OL = 72, C_S1 = 76, C_S2 = 80, C_HF = 84, C_KV0 = 88, C_KST = 96, C_U0 = 100,
              C_S3 = 128;
constexpr int OWIN = 16, OW0 = 8, WD = 2, TL = 8, XR_AT = 62, W3M = 13;
constexpr int HBAR = 0x3802, CPX = 4032, SIG0 = 27, SIG0C = 29, BEAT0 = 4;

__global__ __launch_bounds__(768, 3) void k_all(const char* __restrict__ X, const char* __restrict__ W, char* __restrict__ ws,
                                                const char* __restrict__ Wo, char* __restrict__ out,
                                                const char* __restrict__ Aq, const char* __restrict__ Ak,
                                                const char* __restrict__ Av, const char* __restrict__ shiftk,
                                                const char* __restrict__ ssmk, const unsigned* __restrict__ ssmD) {
    __shared__ __attribute__((aligned(16))) char lds_raw[4 * K1_STAGE];
    const lds_ptr lds = (lds_ptr)lds_raw;
    const unsigned lds0 = (unsigned)(uintptr_t)lds;
    const int tid = threadIdx.x, lane = tid & 63;
    const int w = __builtin_amdgcn_readfirstlane(tid >> 6);
    const int b = blockIdx.x, xcd = b & 7, idx = b >> 3;
    const int hp = (xcd >> 1) * 4 + (idx & 3), tb = (xcd & 1) * 8 + (idx >> 2);

    const int lw = w & 3;
    const int rot = (((tb + (xcd >> 1)) & 3) * 4 - ((hp & 7) == 3 ? (tb & 4) >> 1 : 0)) & 15;
#define XROW(pp) (64 * (lw >> 1) + 16 * (lw & 1) + 8 * ((pp) & 1) + 32 * ((pp) >> 1))
    unsigned voffX[4], voffW[2];
#pragma unroll
    for (int pp = 0; pp < 4; ++pp) {
        const int row = XROW(pp) + (lane >> 3);
        const int ch = (lane & 7) ^ ((row >> 1) & 7);
        voffX[pp] = (unsigned)(tb * 128 + row) * 2048u + ch * 16;
    }
#pragma unroll
    for (int pp = 0; pp < 2; ++pp) {
        const int krow = 16 * lw + 8 * pp + (lane >> 3);
        const int ch = (lane & 7) ^ (((lane >> 4) & 1) << 2) ^ (pp << 1);
        voffW[pp] = (unsigned)krow * 6144u + ch * 16;
    }
    auto issuem = [&](int kt, int mask) {
        const int kr = (kt + rot) & 15;
        const char* xb = X + kr * 128;
        const char* wb = W + (size_t)kr * (64 * 6144) + hp * 128;
        const unsigned st = lds0 + (kt & 3) * K1_STAGE;
#pragma unroll
        for (int pp = 0; pp < 4; ++pp)
            if (mask >> pp & 1) glds16(voffX[pp], xb, st + XROW(pp) * 128);
#pragma unroll
        for (int sec = 0; sec < 3; ++sec)
#pragma unroll
            for (int pp = 0; pp < 2; ++pp)
                if (mask >> (4 + 2 * sec + pp) & 1)
                    glds16(voffW[pp], wb + sec * 2048, st + 16384 + sec * 8192 + (2 * lw + pp) * 1024);
    };
    auto issue = [&](int kt, int lo, int hi) { issuem(kt, (1 << hi) - (1 << lo)); };
    if (w < 4) { issue(0, 0, 5); issue(1, 0, 5); }
    int w1 = w;
    asm volatile("" : "+s"(w1));
    if ((w1 >> 2) == 1) { issue(0, 5, 10); issue(1, 5, 10); }
    asm volatile("" : "+s"(w1));
    unsigned dd = 0;
    if (w1 >= 8) {
        unsigned long long pc; unsigned hi, cpf;
        asm volatile("s_getpc_b64 %0\n.Lcp_k1a:\n\ts_mov_b32 %1, .Lcp_k2z-.Lcp_k1a+%2" : "=s"(pc), "=s"(hi) : "n"(CPX));
        unsigned off = (unsigned)(idx + 32 * (lane & 7) + 256 * lw) * 64u;
        off = off < hi ? off : hi;
        asm volatile("global_load_dword %0, %1, %2" : "=v"(cpf) : "v"(off), "s"(pc) : "memory");
        const char* ctl = ws + WS_CNT + 65536;
        const int id = b * 4 + lw;
        if (b == 0 && lw) {
#pragma unroll 1
            for (int i = lw - 1; i < 5; i += 3)
                asm volatile("global_store_dwordx2 %0, %1, %2 sc1" :: "v"((i < 4 ? lane : lane & 15) * 256), "v"(0ull), "s"(ws + WS_CNT + (i < 4 ? i * 16384 : 65536 + 4096)) : "memory");
        }
        unsigned gv, n, cur, g0 = 0, left = lw == 0 ? 0 : id == 1 ? BEAT0 : 99, spins = SPIN_MAX;
        asm volatile("s_load_dword %0, %1, 0" : "=s"(dd) : "s"(ssmD + (xcd * 64 + idx * 2 + (lw >> 1))) : "memory");
        asm volatile(
            "s_mov_b32 %[n], 0\n\t"
            "s_mov_b64 exec, %[em]\n\t"
            "s_cmp_lg_u32 %[left], 0\n\t"
            "s_cbranch_scc1 1f\n\t"
            "global_load_dword %[gv], %[l256], %[src] sc1\n"
            "1:\n\t"
            "s_barrier\n\t"
            "s_cmp_lg_u32 %[n], %[b0] - 1\n\t"
            "s_cbranch_scc1 5f\n\t"
            "s_waitcnt vmcnt(0)\n"
            "5:\n\t"
            "s_cmp_lg_u32 %[n], 13\n\t"
            "s_cbranch_scc1 6f\n\t"
            "s_mov_b64 exec, -1\n\t"
            "ds_write_b32 %[fl], %[zero]\n\t"
            "s_mov_b64 exec, %[em]\n\t"
            "s_waitcnt lgkmcnt(0)\n"
            "6:\n\t"
            "s_sub_u32 %[left], %[left], 1\n\t"
            "s_cbranch_scc0 3f\n"
            "7:\n\t"
            "s_waitcnt vmcnt(0)\n\t"
            "v_readfirstlane_b32 %[cur], %[gv]\n\t"
            "s_movk_i32 %[left], 99\n\t"
            "s_cmp_eq_u32 %[id], 1\n\t"
            "s_cbranch_scc1 2f\n\t"
            "s_cmp_eq_u32 %[n], 0\n\t"
            "s_cselect_b32 %[g0], %[cur], %[g0]\n\t"
            "s_cmp_eq_u32 %[cur], %[g0]\n\t"
            "s_cbranch_scc1 4f\n\t"
            "global_atomic_add %[zero], %[one], %[ack]\n\t"
            "s_branch 3f\n"
            "2:\n\t"
            "v_cmp_gt_u32 vcc, 16, %[gv]\n\t"
            "s_cmp_eq_u32 %[n], %[b0]\n\t"
            "s_cselect_b32 %[cur], 1, vcc_lo\n\t"
            "s_cmp_eq_u32 %[cur], 0\n\t"
            "s_cbranch_scc1 3f\n\t"
            "global_atomic_add %[l256], %[one], %[ctl]\n"
            "4:\n\t"
            "global_load_dword %[gv], %[l256], %[src] sc1\n\t"
            "s_cmp_lt_u32 %[n], 15\n\t"
            "s_cselect_b32 %[left], 2, %[left]\n\t"
            "s_cbranch_scc1 3f\n\t"
            "s_sub_u32 %[spins], %[spins], 1\n\t"
            "s_cbranch_scc0 7b\n"
            "3:\n\t"
            "s_add_u32 %[n], %[n], 1\n\t"
            "s_cmp_lt_u32 %[n], 19\n\t"
            "s_cbranch_scc1 1b\n\t"
            "s_mov_b64 exec, -1\n\t"
            "s_waitcnt vmcnt(0)"
            : [gv] "=&v"(gv), [n] "=&s"(n), [cur] "=&s"(cur), [g0] "+s"(g0), [left] "+s"(left), [spins] "+s"(spins), [cpf] "+v"(cpf)
            : [zero] "v"(0u), [l256] "v"(lane * 256), [one] "v"(1u), [fl] "v"(lds0 + K2_FL + lane * 4),
              [src] "s"(ctl + (id == 1 ? 16 : b & 15) * 256), [ctl] "s"(ctl), [ack] "s"(ctl + 4096 + (b & 15) * 256), [id] "s"(id),
              [em] "s"(id == 1 ? 0xffffull : 1ull), [b0] "n"(BEAT0)
            : "memory", "scc", "vcc");
        asm volatile("" : "+s"(dd));
    }
    int w2 = w;
    asm volatile("" : "+s"(w2));
    const lds_ptr fl = lds + K2_FL;
    const int cw = lw;
    const int h = 2 * hp + (w & 1);
    const int tt = tb, nt = hp;
    const __amdgpu_buffer_rsrc_t rs = __builtin_amdgcn_make_buffer_rsrc(ws, 0, 25 << 20, 0x00020000);
    gu32* const cnt = (gu32*)(ws + WS_CNT);
    const int krot = (~nt & 4) * 4;
#define WODEF(lane) unsigned wvoff = (unsigned)(lane >> 3) * 2048u + nt * 128 + (((lane & 7) ^ (((lane >> 4) & 1) << 2)) << 4); \
    auto wsrc = [&](int hf, int lp) { return Wo + (size_t)(hf * 64 + ((lp + krot * 2) & 63)) * 16384; };

    if (w2 >= 8) {
        const unsigned voff = lane * 16;
#define LD_SC1(r, a) asm volatile("s_mov_b64 exec, 1\n\tglobal_load_dword %0, %1, %2 sc1\n\ts_mov_b64 exec, -1" : "=v"(r) : "v"(voff), "s"(a) : "memory")
        const int ch = xcd * 128 + idx * 4 + cw;
        const lds_ptr cv = lds + K2_CV + cw * CV_BYTES;
        const char* kvg = ws + WS_KV + (size_t)h * 64 * 4096;
        const __amdgpu_buffer_rsrc_t ro = __builtin_amdgcn_make_buffer_rsrc(out, 0, 8 << 20, 0x00020000);
        u32x4 kr8[2][8];
        unsigned seen = 0, pv = 0;
        const int pt = ((lane + (lane >= (tb & 7))) ^ (tb & 8)) & 15;
        const unsigned fa = (unsigned)((((pt & 7) * 4 + (idx & 3)) * 8 + (xcd & 6) + (pt >> 3)) * 256 + 4);
#define SEE(N) do { asm volatile("s_waitcnt vmcnt(%1)" : "+v"(pv) : "n"(N) : "memory"); \
            seen = ((unsigned)__builtin_amdgcn_ballot_w64(pv != 0) & 0x7fffu) | (unsigned)__builtin_amdgcn_ballot_w64(pv > 1) << 15; \
            asm volatile("" : "+s"(seen)); } while (0)
#define POLLM(M) asm volatile("s_mov_b64 exec, %3\n\tglobal_load_dword %0, %1, %2 sc1\n\ts_mov_b64 exec, -1" : "+v"(pv) : "v"(fa), "s"(cnt), "n"(M) : "memory")
#define POLL() POLLM(0x7fff)
#define NEED(j0, nj) do { constexpr unsigned bits_ = ((1u << (nj)) - 1u) << (j0); \
            if (__builtin_expect((~seen & bits_) != 0, 0)) { \
                _Pragma("unroll 1") for (unsigned spins = 0; spins < SPIN_MAX; ++spins) { \
                    POLLM((bits_ | bits_ >> 15) & 0x7fffu); \
                    SEE(0); \
                    if ((~seen & bits_) == 0) break; \
                    __builtin_amdgcn_s_sleep(1); \
                } \
            } \
            CFENCE(); } while (0)
        auto ld1 = [&](int s, int i) __attribute__((always_inline)) {
            const int j = 2 * s - 2 + (i >> 2);
            const int pj = (j % 15 + (j % 15 >= (tb & 7))) ^ (tb & 8);
            const char* src = kvg + (pj * 4 + (cw >> 1) * 2 + j / 15) * 4096;
            asm volatile("global_load_dwordx4 %0, %1, %2 offset:%3 sc1" : "=v"(kr8[s & 1][i]) : "v"(voff), "s"(src), "n"((i & 3) * 1024) : "memory");
        };
        auto ldq = [&](int s) __attribute__((always_inline)) {
#pragma unroll
            for (int i = 0; i < 8; ++i) ld1(s, i);
            SBAR();
        };
        lds_ptr qd = lds + cw * 8192 + lane * 16;
        asm volatile("" : "+v"(qd));
#define HAVE(s, N) do { u32x4* r_ = kr8[(s) & 1]; \
            asm volatile("s_waitcnt vmcnt(%8)" : "+v"(r_[0]), "+v"(r_[1]), "+v"(r_[2]), "+v"(r_[3]), "+v"(r_[4]), "+v"(r_[5]), "+v"(r_[6]), "+v"(r_[7]) : "n"(N) : "memory"); } while (0)
#define MOVE(s, LD) do { \
            _Pragma("unroll") for (int i_ = 0; i_ < 8; ++i_) { *(LAS u32x4*)(qd + ((s) & 1) * K2_SLOT + i_ * 1024) = kr8[(s) & 1][i_]; if (LD) ld1((s) + 2, i_); } \
            SBAR(); } while (0)
#define PUT(s, N) do { HAVE(s, N); MOVE(s, false); } while (0)
#define STEP(s, N) do { HAVE(s, N); NEED(2 * (s) + 2, 2); MOVE(s, true); } while (0)
#define LBAR() asm volatile("s_waitcnt lgkmcnt(0)\n\ts_barrier" ::: "memory")
        u32x4 e4 = {EMPTY, EMPTY, EMPTY, EMPTY};
        auto blank = [&](int i) {
            CFENCE();
            __builtin_amdgcn_raw_buffer_store_b128(e4, rs, (int)voff, (i & 3) * 1024 + (i < 4 ? (int)WS_O + ((tb * 4 + cw) * 32 + 2 * hp) * 2048 : (int)WS_ZT + ch * 4096), AUX_SC1);
            CFENCE();
        };
        const int c0 = xcd * 128 + cw * 32;
        const int row0 = idx * 64 + (lane >> 2) * 4;
        u32x4 kr[7], vr[4];
        const __amdgpu_buffer_rsrc_t rk = __builtin_amdgcn_make_buffer_rsrc((void*)Ak, 0, 4 << 20, 0x00020000);
        unsigned roff = (unsigned)row0 * 2048u + (c0 + (lane & 3) * 8) * 2, hoff = roff - 6144u;
        int ln = lane;
        asm volatile("" : "+v"(ln));
        unsigned ska = (unsigned)(c0 + (ln & 3) * 8) * 8u;
        lds_ptr cvl = cv + ln * 16, kvw[2];
#pragma unroll
        for (int i = 0; i < 2; ++i) kvw[i] = cv + CV_KV + 480 + (ln & 3) * 1024 + (((ln >> 3) ^ ((ln & 3) * 2 + i)) << 4) + (~ln & 4) * 2;
        {
            WODEF(ln)
            const int r1 = ln & 15, q = ln >> 4;
            const unsigned sg0 = (unsigned)(ln * 8 + xcd) * 256u, sg1 = (unsigned)(((tb & 7) * 4 + (ln & 3)) * 8 + ((ln >> 2) & 3) * 2 + (tb >> 3)) * 256u;
            const int toff = (r1 & 1) ? CV_HA + 2 * (16 + r1 + 9 - 8 * q) : CV_HB + 2 * (16 + r1 + 10 - 8 * q);
            *(LAS u32x4*)(lds + K2_PK + ln * 16) = u32x4{wvoff, ln < 32 ? sg0 : sg1, (unsigned)((ln >> 3) * 16384 + (ln & 7) * 2048),
                                                           (unsigned)(((ln >> 1) & 15) * 2048 + (ln & 1) * 128)};
            *(LAS u32x4*)(lds + K2_PK + 1024 + ln * 16) = u32x4{(unsigned)((ln >> 3) * 16384 + (ln & 7) * 16), (unsigned)toff, (unsigned)(CV_KV + 32 * r1 + 16 * q),
                                                                  (unsigned)((ln >> 3) * 128 + (((ln & 7) ^ (ln >> 5)) << 4))};
        }
        asm volatile("" : "+v"(roff), "+v"(hoff), "+v"(e4), "+v"(ska), "+v"(cvl), "+v"(kvw[0]), "+v"(kvw[1]) :: "memory");
        PRIO(3);
        lds_wait_ge(fl + C_KV0, 4u);
        NEED(0, 4);
        POLL(); ldq(1); ldq(2);
        PUT(1, 8);
        SEE(8);
        LBAR();
        NEED(4, 2);
        POLL();
        HAVE(2, 1);
#pragma unroll
        for (int i = 0; i < 8; ++i) { *(LAS u32x4*)(qd + i * 1024) = kr8[0][i]; ld1(3, i); }
        SBAR();
        SEE(8);
        NEED(6, 2);
        ldq(4);
        LBAR();
        STEP(3, 8);
        if (cw == 0) asm volatile("s_mov_b64 exec, 1\n\tglobal_store_dword %0, %1, %2 offset:4 sc1\n\ts_mov_b64 exec, -1" :: "v"(voff), "v"(e4[0]), "s"(cnt + b * 64) : "memory");
#define LD128(r, vo, base, off) asm volatile("global_load_dwordx4 %0, %1, %2 offset:%3 nt" : "=v"(r) : "v"(vo), "s"(base), "n"(off) : "memory")
#define HLD(i) glds16nt(voff, ssmk + (size_t)ch * 4096 + (i) * 1024, lds0 + K2_CV + cw * CV_BYTES + CV_HA + 32 + (i) * 1024)
        u32x4 sk[4];
#pragma unroll
        for (int i = 0; i < 4; ++i) sk[i] = gld128(ska, shiftk + i * 16);
#pragma unroll
        for (int i = 0; i < 4; ++i) LD128(kr[3 + i], roff, Ak + 4096, i * 2048 - 4096);
        blank(0);
        LBAR();
        STEP(4, 17);
#pragma unroll
        for (int i = 0; i < 4; ++i) LD128(vr[i], roff, Av + 4096, i * 2048 - 4096);
        blank(1);
        LBAR();
        HAVE(5, 22);
        NEED(12, 2);
        POLL();
        MOVE(5, true);
#pragma unroll
        for (int i = 0; i < 3; ++i)
            asm volatile("buffer_load_dwordx4 %0, %1, %2, %3 offen nt" : "=v"(kr[i]) : "v"(hoff), "s"(rk), "s"(i * 2048) : "memory");
        blank(2); blank(3);
        SEE(13);
        LBAR();
        STEP(6, 13);
        HLD(0); HLD(1);
        blank(4);
        asm volatile("s_waitcnt vmcnt(13)" ::: "memory");
        asm volatile("" : "+v"(kr[0]), "+v"(kr[1]), "+v"(kr[2]), "+v"(kr[3]), "+v"(kr[4]), "+v"(kr[5]), "+v"(kr[6]),
                     "+v"(vr[0]), "+v"(vr[1]), "+v"(vr[2]), "+v"(vr[3]), "+v"(sk[0]), "+v"(sk[1]), "+v"(sk[2]), "+v"(sk[3]));
        auto kvc = [&](int c) __attribute__((always_inline)) {
            unsigned kp[2][6];
            float kv[8], vf[8];
#pragma unroll
            for (int j = 0; j < 12; ++j) kp[j & 1][j >> 1] = __builtin_amdgcn_perm(kr[j >> 1][c], kr[(j >> 1) + 1][c], (j & 1) ? 0x07060302u : 0x05040100u);
            SBAR();
#pragma unroll
            for (int i = 0; i < 8; ++i) asm("v_dot2_f32_bf16 %0, %1, %2, 0" : "=v"(kv[i]) : "v"(kp[i >> 2][i & 3]), "v"(sk[c][(i >> 2) * 2 + 1]));
            SBAR();
#pragma unroll
            for (int i = 0; i < 8; ++i) asm("v_dot2_f32_bf16 %0, %1, %2, %0" : "+v"(kv[i]) : "v"(kp[i >> 2][(i & 3) + 2]), "v"(sk[c][(i >> 2) * 2]));
            SBAR();
#pragma unroll
            for (int i = 0; i < 8; ++i) vf[i] = (i >> 2) ? bfhi(vr[i & 3][c]) : bflo(vr[i & 3][c]);
            SBAR();
#pragma unroll
            for (int i = 0; i < 8; ++i) kv[i] = mul_vv(kv[i], vf[i]);
#pragma unroll
            for (int e = 0; e < 2; ++e)
                { *(LAS u32x2*)(kvw[c >> 1] + (2 * c + e) * 128) = u32x2{cvtpk(kv[4 * e + 3], kv[4 * e + 2]), cvtpk(kv[4 * e + 1], kv[4 * e])}; CFENCE(); }
            SBAR();
        };
        auto warm = [&](unsigned vo, const void* base) {
            unsigned sv;
            asm volatile("s_mov_b32 %0, m0\n\ts_mov_b32 m0, %3\n\ts_nop 0\n\tglobal_load_lds_dword %1, %2\n\ts_mov_b32 m0, %0"
                         : "=&s"(sv) : "v"(vo), "s"(base), "s"(lds0 + K2_SCR + cw * 256) : "memory");
        };
        kvc(0);
        LBAR();
        STEP(7, 13);
        HLD(2); HLD(3);
        blank(5);
        kvc(1);
        LBAR();
        STEP(8, 14);
        blank(6); blank(7);
        kvc(2);
        LBAR();
        STEP(9, 13);
        const u32x4 pk0 = *(const LAS u32x4*)(lds + K2_PK + voff), pk1 = *(const LAS u32x4*)(lds + K2_PK + 1024 + voff);
        kvc(3);
#pragma unroll
        for (int i = 0; i < 4; ++i) {
            const u32x4 t = *(const LAS u32x4*)(cvl + CV_KV + 480 + i * 1024);
            __builtin_amdgcn_raw_buffer_store_b128(t, rs, (int)(pk1[3] ^ (i << 5)), (int)WS_KR + (xcd * 32 + idx) * 16384 + cw * 4096 + i * 1024, AUX_SC1);
        }
        CFENCE();
        LBAR();
        STEP(10, 14);
        {
            const u32x4 z4 = {0u, 0u, 0u, 0u};
            asm volatile("s_mov_b64 exec, 3\n\tds_write_b128 %0, %1 offset:%2\n\tds_write_b128 %0, %1 offset:%3\n\t"
                         "s_mov_b64 exec, 0x3fffffff\n\tds_write_b128 %0, %1 offset:%4\n\ts_mov_b64 exec, -1"
                         :: "v"(cvl), "v"(z4), "n"(CV_HA), "n"(CV_HB), "n"(CV_KV) : "memory");
        }
        {
#pragma unroll
            for (int i = 0; i < 4; ++i) {
                const int off = 32 + i * 1024;
                const u32x4 hv = *(const LAS u32x4*)(cvl + CV_HA + off);
                const unsigned pv = *(const volatile LAS unsigned*)(cvl + CV_HA + off - 4);
                u32x4 sh;
                sh[0] = __builtin_amdgcn_alignbit(hv[0], pv, 16);
                sh[1] = __builtin_amdgcn_alignbit(hv[1], hv[0], 16);
                sh[2] = __builtin_amdgcn_alignbit(hv[2], hv[1], 16);
                sh[3] = __builtin_amdgcn_alignbit(hv[3], hv[2], 16);
                *(LAS u32x4*)(cvl + CV_HB + off) = sh;
            }
        }
        LBAR();
        STEP(11, 8);
        const unsigned wvoff = pk0[0];
        unsigned one = 1;
        asm volatile("" : "+v"(one));
        auto wsrc = [&](int hf, int lp) { return Wo + (size_t)(hf * 64 + ((lp + krot * 2) & 63)) * 16384; };
        {
            unsigned o_, s_;
            asm volatile("s_mov_b64 exec, 1\n\tds_add_rtn_u32 %0, %2, %3 offset:%6\n\ts_waitcnt lgkmcnt(0)\n\tv_readfirstlane_b32 %1, %0\n\t"
                         "s_cmp_eq_u32 %1, 3\n\ts_cselect_b32 exec_lo, -1, 0\n\ts_cselect_b32 exec_hi, 0xffff, 0\n\t"
                         "global_atomic_add %4, %3, %5\n\ts_mov_b64 exec, -1"
                         : "=&v"(o_), "=&s"(s_) : "v"(lds + voff), "v"(one), "v"(pk0[1]), "s"(cnt), "n"(K2_FL + C_KST) : "memory", "scc");
        }
        warm(pk0[2], wsrc(tb & 1, 32 + cw * 8) + nt * 128);
        LBAR();
        HAVE(12, 9);
        NEED(26, 4);
        MOVE(12, true);
#pragma unroll
        for (int i = 0; i < 8; ++i)
            glds16(wvoff, wsrc(i >> 2, cw * 4 + (i & 3)), lds0 + K2_SF + (i >> 2) * 16384 + (cw * 4 + (i & 3)) * 1024);
        LBAR();
        HAVE(13, 17);
        unsigned cv_;
        LD_SC1(cv_, cnt + b * 64);
        MOVE(13, true);
        warm(pk0[3], Aq + (idx * 64 + cw * 16) * 2048 + xcd * 256);
        HAVE(14, 9);
        asm volatile("" : "+v"(cv_));
        cv_ = __builtin_amdgcn_readfirstlane(cv_);
        asm volatile("" : "+s"(cv_));
        LBAR();
        if (__builtin_expect(cv_ < 48u, 0)) poll_ge(cnt + b * 64, 48u);
        CFENCE();
        u32x4 kt4[4];
#pragma unroll
        for (int i = 0; i < 8; ++i) {
            *(LAS u32x4*)(qd + i * 1024) = kr8[0][i];
            if (!(i & 1)) asm volatile("global_load_dwordx4 %0, %1, %2 sc1" : "=v"(kt4[i >> 1]) : "v"(pk1[0]), "s"(ws + WS_KR + xcd * 524288 + (idx * 4 + cw) * 128 + (i >> 1) * 131072) : "memory");
            SBAR();
        }
        f32x4 y[8];
        u32x4 tf[8];
        bf16x8 bf[8];
        lds_ptr bp = cv + pk1[2], kd4 = cvl + CV_KV + 480;
        lds_ptr tp[4];
#pragma unroll
        for (int i = 0; i < 4; ++i) { tp[i] = cv + 1024 * i + pk1[1]; asm volatile("" : "+v"(tp[i])); }
        asm volatile("" : "+v"(bp));
        LBAR();
        __builtin_amdgcn_s_sleep(2);
        PUT(15, 5);
        __builtin_amdgcn_s_sleep(6);
        asm volatile("s_waitcnt vmcnt(0)" : "+v"(kt4[0]), "+v"(kt4[1]), "+v"(kt4[2]), "+v"(kt4[3]) :: "memory");
#pragma unroll
        for (int i = 0; i < 4; ++i) *(LAS u32x4*)(kd4 + i * 1024) = kt4[i];
        LBAR();
#define LDT(bb, a, hf) do { const LAS unsigned* t_ = (const LAS unsigned*)(tp[(a) >> 1] + 64 * (bb) + 512 * ((a) & 1) + 8 * (hf)); \
            tf[a][2 * (hf)] = t_[0]; tf[a][2 * (hf) + 1] = t_[1]; SBAR(); } while (0)
#define LDB(bb, a) do { bf[a] = lds_rd128(bp + 448 - 64 * (bb) + 512 * (a)); SBAR(); } while (0)
#define CM(a, ww) do { asm volatile("v_mfma_f32_16x16x32_bf16 %0, %1, %2, %0" : "+v"(y[(a) + (ww)]) : "v"(tf[a]), "v"(bf[ww])); SBAR(); } while (0)
#define CM0(a, ww) do { asm volatile("v_mfma_f32_16x16x32_bf16 %0, %1, %2, 0" : "=&v"(y[(a) + (ww)]) : "v"(tf[a]), "v"(bf[ww])); SBAR(); } while (0)
#define PC() do { asm volatile("s_nop 1"); SBAR(); } while (0)
        __builtin_amdgcn_s_sleep(1);
#pragma unroll
        for (int a = 0; a < 8; ++a) { if (a < 5) { LDT(0, a, 0); LDT(0, a, 1); } LDB(0, 7 - a); }
#pragma unroll
        for (int i = 0; i < 8; ++i)
            glds16(wvoff, wsrc(i >> 2, 16 + cw * 4 + (i & 3)), lds0 + (i >> 2) * 16384 + (cw * 4 + (i & 3)) * 1024);
        RAW_BAR();
        asm volatile("s_waitcnt vmcnt(0)" ::: "memory");
        lds_add(fl + C_S1, one);
        __builtin_amdgcn_s_setprio(2);
        const float dch = (ch & 1) ? bfhi(dd) : bflo(dd);
        const lds_ptr kdp = cv + CV_KV + 480 + 32 * (lane & 15) + 16 * (lane >> 5) + (~lane & 16) / 2;
        float kd[8][4];
        u32x2 kk;
        unsigned kc0, kc1, zo[8][2];
#define KD_RD(t) do { kk = *(const LAS u32x2*)(kdp + 512 * (t)); SBAR(); } while (0)
#define KD_UP0(t) do { kd[t][0] = bfhi(kk[1]); kd[t][1] = bflo(kk[1]); SBAR(); } while (0)
#define KD_UP1(t) do { kd[t][2] = bfhi(kk[0]); kd[t][3] = bflo(kk[0]); SBAR(); } while (0)
#define KD_MU0(t) do { kd[t][0] = mul_sv(dch, kd[t][0]); kd[t][1] = mul_sv(dch, kd[t][1]); SBAR(); } while (0)
#define KD_MU1(t) do { kd[t][2] = mul_sv(dch, kd[t][2]); kd[t][3] = mul_sv(dch, kd[t][3]); SBAR(); } while (0)
#define KD_CV(t) do { kc0 = cvtpk(kd[t][0], kd[t][1]); kc1 = cvtpk(kd[t][2], kd[t][3]); SBAR(); } while (0)
#define KD_X0(t) do { kd[t][0] = bflo(kc0); kd[t][1] = bfhi(kc0); SBAR(); } while (0)
#define KD_X1(t) do { kd[t][2] = bflo(kc1); kd[t][3] = bfhi(kc1); SBAR(); } while (0)
#define ZF0(t) do { kc0 = cvtpk(y[t][0], y[t][1]); kc1 = cvtpk(y[t][2], y[t][3]); SBAR(); } while (0)
#define ZF1(t) do { y[t][0] = bflo(kc0); y[t][1] = bfhi(kc0); SBAR(); } while (0)
#define ZF2(t) do { y[t][2] = bflo(kc1); y[t][3] = bfhi(kc1); SBAR(); } while (0)
#define ZF3(t) do { y[t][0] = add_vv(y[t][0], kd[t][0]); y[t][1] = add_vv(y[t][1], kd[t][1]); SBAR(); } while (0)
#define ZF4(t) do { y[t][2] = add_vv(y[t][2], kd[t][2]); y[t][3] = add_vv(y[t][3], kd[t][3]); SBAR(); } while (0)
#define ZF5(t) do { zo[t][0] = nz(cvtpk(y[t][0], y[t][1])); zo[t][1] = nz(cvtpk(y[t][2], y[t][3])); SBAR(); } while (0)
#define ZST(pr) do { auto s0 = __builtin_amdgcn_permlane16_swap(zo[2 * (pr)][0], zo[2 * (pr) + 1][0], false, false); \
            auto s1 = __builtin_amdgcn_permlane16_swap(zo[2 * (pr)][1], zo[2 * (pr) + 1][1], false, false); \
            st_sc1(u32x4{s0[0], s1[0], s0[1], s1[1]}, rs, (unsigned)WS_ZT + ch * 4096 + (pr) * 1024 + voff); SBAR(); } while (0)
#define HF() do { lds_add(fl + C_HF, one); SBAR(); } while (0)
        CM0(0, 7); LDB(1, 7); CM0(0, 6); LDT(0, 5, 0); CM0(0, 5); LDT(0, 5, 1); CM0(0, 4); LDT(0, 6, 0); CM0(0, 3); LDT(0, 6, 1); CM0(0, 2); LDT(0, 7, 0); CM0(0, 1); LDT(0, 7, 1); CM0(0, 0); KD_RD(0);
        CM(1, 6); LDB(1, 6); CM(1, 5); LDT(1, 0, 0); CM(1, 4); LDT(1, 0, 1); CM(1, 3); PC(); CM(1, 2); PC(); CM(1, 1); KD_UP0(0); CM(1, 0); KD_UP1(0);
        CM(2, 5); LDB(1, 5); CM(2, 4); LDT(1, 1, 0); CM(2, 3); LDT(1, 1, 1); CM(2, 2); KD_MU0(0); CM(2, 1); KD_MU1(0); CM(2, 0); KD_CV(0);
        CM(3, 4); LDB(1, 4); CM(3, 3); LDT(1, 2, 0); CM(3, 2); LDT(1, 2, 1); CM(3, 1); KD_X0(0); CM(3, 0); KD_X1(0);
        CM(4, 3); LDB(1, 3); CM(4, 2); LDT(1, 3, 0); CM(4, 1); LDT(1, 3, 1); CM(4, 0); KD_RD(1);
        CM(5, 2); LDB(1, 2); CM(5, 1); LDT(1, 4, 0); CM(5, 0); LDT(1, 4, 1);
        CM(6, 1); LDB(1, 1); CM(6, 0); PC();
        CM(7, 0); LDB(1, 0);
        CM(0, 7); LDB(2, 7); CM(0, 6); LDT(1, 5, 0); CM(0, 5); LDT(1, 5, 1); CM(0, 4); LDT(1, 6, 0); CM(0, 3); LDT(1, 6, 1); CM(0, 2); LDT(1, 7, 0); CM(0, 1); LDT(1, 7, 1); CM(0, 0); PC();
        CM(1, 6); LDB(2, 6); CM(1, 5); LDT(2, 0, 0); CM(1, 4); LDT(2, 0, 1); CM(1, 3); KD_UP0(1); CM(1, 2); KD_UP1(1); CM(1, 1); KD_MU0(1); CM(1, 0); KD_MU1(1);
        CM(2, 5); LDB(2, 5); CM(2, 4); LDT(2, 1, 0); CM(2, 3); LDT(2, 1, 1); CM(2, 2); KD_CV(1); CM(2, 1); KD_X0(1); CM(2, 0); KD_X1(1);
        CM(3, 4); LDB(2, 4); CM(3, 3); LDT(2, 2, 0); CM(3, 2); LDT(2, 2, 1); CM(3, 1); KD_RD(2); CM(3, 0); PC();
        CM(4, 3); LDB(2, 3); CM(4, 2); LDT(2, 3, 0); CM(4, 1); LDT(2, 3, 1); CM(4, 0); PC();
        CM(5, 2); LDB(2, 2); CM(5, 1); LDT(2, 4, 0); CM(5, 0); LDT(2, 4, 1);
        CM(6, 1); LDB(2, 1); CM(6, 0); KD_UP0(2);
        CM(7, 0); LDB(2, 0);
        CM(0, 7); LDB(3, 7); CM(0, 6); LDT(2, 5, 0); CM(0, 5); LDT(2, 5, 1); CM(0, 4); LDT(2, 6, 0); CM(0, 3); LDT(2, 6, 1); CM(0, 2); LDT(2, 7, 0); CM(0, 1); LDT(2, 7, 1); CM(0, 0); KD_UP1(2);
        CM(1, 6); LDB(3, 6); CM(1, 5); LDT(3, 0, 0); CM(1, 4); LDT(3, 0, 1); CM(1, 3); KD_MU0(2); CM(1, 2); KD_MU1(2); CM(1, 1); KD_CV(2); CM(1, 0); KD_X0(2);
        CM(2, 5); LDB(3, 5); CM(2, 4); LDT(3, 1, 0); CM(2, 3); LDT(3, 1, 1); CM(2, 2); KD_X1(2); CM(2, 1); KD_RD(3); CM(2, 0); PC();
        CM(3, 4); LDB(3, 4); CM(3, 3); LDT(3, 2, 0); CM(3, 2); LDT(3, 2, 1); CM(3, 1); PC(); CM(3, 0); KD_UP0(3);
        CM(4, 3); LDB(3, 3); CM(4, 2); LDT(3, 3, 0); CM(4, 1); LDT(3, 3, 1); CM(4, 0); KD_UP1(3);
        CM(5, 2); LDB(3, 2); CM(5, 1); LDT(3, 4, 0); CM(5, 0); LDT(3, 4, 1);
        CM(6, 1); LDB(3, 1); CM(6, 0); KD_MU0(3);
        CM(7, 0); LDB(3, 0);
        CM(0, 7); LDB(4, 7); CM(0, 6); LDT(3, 5, 0); CM(0, 5); LDT(3, 5, 1); CM(0, 4); LDT(3, 6, 0); CM(0, 3); LDT(3, 6, 1); CM(0, 2); LDT(3, 7, 0); CM(0, 1); LDT(3, 7, 1); CM(0, 0); KD_MU1(3);
        CM(1, 6); LDB(4, 6); CM(1, 5); LDT(4, 0, 0); CM(1, 4); LDT(4, 0, 1); CM(1, 3); KD_CV(3); CM(1, 2); KD_X0(3); CM(1, 1); KD_X1(3); CM(1, 0); KD_RD(4);
        CM(2, 5); LDB(4, 5); CM(2, 4); LDT(4, 1, 0); CM(2, 3); LDT(4, 1, 1); CM(2, 2); PC(); CM(2, 1); PC(); CM(2, 0); KD_UP0(4);
        CM(3, 4); LDB(4, 4); CM(3, 3); LDT(4, 2, 0); CM(3, 2); LDT(4, 2, 1); CM(3, 1); KD_UP1(4); CM(3, 0); KD_MU0(4);
        CM(4, 3); LDB(4, 3); CM(4, 2); LDT(4, 3, 0); CM(4, 1); LDT(4, 3, 1); CM(4, 0); KD_MU1(4);
        CM(5, 2); LDB(4, 2); CM(5, 1); LDT(4, 4, 0); CM(5, 0); LDT(4, 4, 1);
        CM(6, 1); LDB(4, 1); CM(6, 0); KD_CV(4);
        CM(7, 0); LDB(4, 0);
        CM(0, 7); LDB(5, 7); CM(0, 6); LDT(4, 5, 0); CM(0, 5); LDT(4, 5, 1); CM(0, 4); LDT(4, 6, 0); CM(0, 3); LDT(4, 6, 1); CM(0, 2); LDT(4, 7, 0); CM(0, 1); LDT(4, 7, 1); CM(0, 0); KD_X0(4);
        CM(1, 6); LDB(5, 6); CM(1, 5); LDT(5, 0, 0); CM(1, 4); LDT(5, 0, 1); CM(1, 3); KD_X1(4); CM(1, 2); KD_RD(5); CM(1, 1); PC(); CM(1, 0); PC();
        CM(2, 5); LDB(5, 5); CM(2, 4); LDT(5, 1, 0); CM(2, 3); LDT(5, 1, 1); CM(2, 2); KD_UP0(5); CM(2, 1); KD_UP1(5); CM(2, 0); KD_MU0(5);
        CM(3, 4); LDB(5, 4); CM(3, 3); LDT(5, 2, 0); CM(3, 2); LDT(5, 2, 1); CM(3, 1); KD_MU1(5); CM(3, 0); KD_CV(5);
        CM(4, 3); LDB(5, 3); CM(4, 2); LDT(5, 3, 0); CM(4, 1); LDT(5, 3, 1); CM(4, 0); KD_X0(5);
        CM(5, 2); LDB(5, 2); CM(5, 1); LDT(5, 4, 0); CM(5, 0); LDT(5, 4, 1);
        CM(6, 1); LDB(5, 1); CM(6, 0); KD_X1(5);
        CM(7, 0); LDB(5, 0);
        CM(0, 7); LDB(6, 7); CM(0, 6); LDT(5, 5, 0); CM(0, 5); LDT(5, 5, 1); CM(0, 4); LDT(5, 6, 0); CM(0, 3); LDT(5, 6, 1); CM(0, 2); LDT(5, 7, 0); CM(0, 1); LDT(5, 7, 1); CM(0, 0); KD_RD(6);
        CM(1, 6); LDB(6, 6); CM(1, 5); LDT(6, 0, 0); CM(1, 4); LDT(6, 0, 1); CM(1, 3); PC(); CM(1, 2); PC(); CM(1, 1); KD_UP0(6); CM(1, 0); KD_UP1(6);
        CM(2, 5); LDB(6, 5); CM(2, 4); LDT(6, 1, 0); CM(2, 3); LDT(6, 1, 1); CM(2, 2); KD_MU0(6); CM(2, 1); KD_MU1(6); CM(2, 0); KD_CV(6);
        CM(3, 4); LDB(6, 4); CM(3, 3); LDT(6, 2, 0); CM(3, 2); LDT(6, 2, 1); CM(3, 1); KD_X0(6); CM(3, 0); KD_X1(6);
        CM(4, 3); LDB(6, 3); CM(4, 2); LDT(6, 3, 0); CM(4, 1); LDT(6, 3, 1); CM(4, 0); KD_RD(7);
        CM(5, 2); LDB(6, 2); CM(5, 1); LDT(6, 4, 0); CM(5, 0); LDT(6, 4, 1);
        CM(6, 1); LDB(6, 1); CM(6, 0); PC();
        CM(7, 0); LDB(6, 0);
        CM(0, 7); LDB(7, 7); CM(0, 6); LDT(6, 5, 0); CM(0, 5); LDT(6, 5, 1); CM(0, 4); LDT(6, 6, 0); CM(0, 3); LDT(6, 6, 1); CM(0, 2); LDT(6, 7, 0); CM(0, 1); LDT(6, 7, 1); CM(0, 0); PC();
        CM(1, 6); LDB(7, 6); CM(1, 5); LDT(7, 0, 0); CM(1, 4); LDT(7, 0, 1); CM(1, 3); KD_UP0(7); CM(1, 2); KD_UP1(7); CM(1, 1); KD_MU0(7); CM(1, 0); KD_MU1(7);
        CM(2, 5); LDB(7, 5); CM(2, 4); LDT(7, 1, 0); CM(2, 3); LDT(7, 1, 1); CM(2, 2); KD_CV(7); CM(2, 1); KD_X0(7); CM(2, 0); KD_X1(7);
        CM(3, 4); LDB(7, 4); CM(3, 3); LDT(7, 2, 0); CM(3, 2); LDT(7, 2, 1); CM(3, 1); PC(); CM(3, 0); PC();
        CM(4, 3); LDB(7, 3); CM(4, 2); LDT(7, 3, 0); CM(4, 1); LDT(7, 3, 1); CM(4, 0); PC();
        CM(5, 2); LDB(7, 2); CM(5, 1); LDT(7, 4, 0); CM(5, 0); LDT(7, 4, 1);
        CM(6, 1); LDB(7, 1); CM(6, 0); PC();
        CM(7, 0); LDB(7, 0);
        CM(0, 3); LDT(7, 5, 0); CM(0, 2); LDT(7, 5, 1); CM(1, 2); LDT(7, 6, 0); CM(1, 1); LDT(7, 6, 1); CM(2, 1); LDT(7, 7, 0); CM(2, 0); LDT(7, 7, 1);
        CM(3, 0); HF(); CM(0, 1); PC(); CM(0, 0); PC(); CM(1, 0); ZF0(2); CM(0, 5); ZF1(2); CM(0, 4); ZF2(2);
        CM(1, 4); ZF3(2); CM(1, 3); ZF4(2); CM(2, 3); ZF5(2); CM(2, 2); ZF0(3); CM(3, 2); ZF1(3); CM(3, 1); ZF2(3);
        CM(4, 1); ZF3(3); CM(4, 0); ZF4(3); CM(5, 0); ZF5(3); CM(0, 7); ZST(1); CM(0, 6); ZF0(0); CM(1, 6); ZF1(0);
        CM(1, 5); ZF2(0); CM(2, 5); ZF3(0); CM(2, 4); ZF4(0); CM(3, 4); ZF5(0); CM(3, 3); ZF0(1); CM(4, 3); ZF1(1);
        CM(4, 2); ZF2(1); CM(5, 2); ZF3(1); CM(5, 1); ZF4(1); CM(6, 1); ZF5(1); CM(6, 0); ZST(0); CM(7, 0); ZF0(4);
        ZF1(4); ZF2(4); ZF3(4); ZF4(4); ZF5(4); ZF0(5); ZF1(5); ZF2(5); ZF3(5); ZF4(5); ZF5(5); ZST(2); ZF0(6); ZF1(6); ZF2(6); ZF3(6); ZF4(6); ZF5(6); ZF0(7); ZF1(7); ZF2(7); ZF3(7); ZF4(7); ZF5(7); ZST(3);
        __builtin_amdgcn_s_setprio(3);
        while ((unsigned)__builtin_amdgcn_readfirstlane(*(volatile LAS unsigned*)(fl + C_OL)) < 4u) __builtin_amdgcn_s_sleep(4);
        CFENCE();
        const int cq = lane & 7, rg = lane >> 3;
        const int arow = idx * 64 + (cw >> 1) * 32 + rg * 4, acol = xcd * 256 + (cw & 1) * 128 + cq * 16;
        u32x4 qr[4];
#pragma unroll
        for (int i = 0; i < 4; ++i) qr[i] = gld128((unsigned)(arow * 2048 + acol), Aq + i * 2048);
        {
            const lds_ptr tr = cv + CV_KV + 480;
            u32x4 t[4];
            const int ck = (cw >> 1) * 4 + ((lane ^ ((lane >> 5) * 2)) & 3);
            const unsigned zb = (unsigned)WS_ZT + (unsigned)(xcd * 128 + (cw & 1) * 64 + (lane >> 4) * 4) * 4096u + ((idx >> 3) * 1024 +
                                ((idx & 3) * 4 + ((idx >> 2) & 1) * 16 + (ck >> 1) + (ck & 1) * 32) * 16);
#pragma unroll 1
            for (unsigned spins = 0; spins < SPIN_MAX; ++spins) {
#pragma unroll
                for (int i = 0; i < 4; ++i) t[i] = ld_sc1s(rs, zb + ((((lane >> 2) & 3) ^ i) << 12), i * 65536);
                if (!missing(mx4(mx4(mx8(t[0], t[1]), t[2]), t[3]))) break;
                __builtin_amdgcn_s_sleep(8);
            }
            __builtin_amdgcn_s_setprio(3);
#pragma unroll
            for (int i = 0; i < 4; ++i) *(LAS u32x4*)(tr + i * 1024 + lane * 16) = t[i];
#pragma unroll
            for (int i = 0; i < 4; ++i) asm volatile("" : "+v"(qr[i]));
            u32x2 zc[8];
#pragma unroll
            for (int c = 0; c < 8; ++c)
                zc[c] = *(const LAS u32x2*)(tr + cq * 512 + (c >> 2) * 256 + ((((c & 3) * 4 + (rg >> 1)) ^ (cq * 2)) << 4) + (rg & 1) * 8);
#pragma unroll
            for (int i = 0; i < 4; ++i) {
                u32x4 o;
#pragma unroll
                for (int c = 0; c < 4; ++c) {
                    const unsigned z0 = zc[2 * c][i >> 1], z1 = zc[2 * c + 1][i >> 1];
                    o[c] = cvtpk(mul_vv(bflo(qr[i][c]), (i & 1) ? bfhi(z0) : bflo(z0)), mul_vv(bfhi(qr[i][c]), (i & 1) ? bfhi(z1) : bflo(z1)));
                }
                __builtin_amdgcn_raw_buffer_store_b128(o, ro, arow * 4096 + acol, i * 4096, AUX_SC1);
            }
        }
        asm volatile(".Lcp_k2z:");
        return;
    }

    if (w2 >= 4) {
#define WAITN_BAR(N) asm volatile("s_waitcnt vmcnt(%0) lgkmcnt(0)\n\ts_barrier" :: "n"(N) : "memory")
        issue(2, 0, K1_LA);
        asm volatile("s_waitcnt vmcnt(%0)" :: "n"(5 + K1_LA) : "memory");
#pragma unroll 1
        for (int kt = 0; kt < 11; ++kt) {
            WAITN_BAR(10 + K1_LA);
            issue(kt + 2, K1_LA, 10);
            issue(kt + 3, 0, K1_LA);
        }
        {
            constexpr int E = 0x3c3, EA = 0x1c3, XL = 0x00c, WL = 0x030;
            WAIT_BAR(15);
            issue(13, 5, 10); issuem(14, EA);
            WAIT_BAR(15);
            issuem(14, E - EA); issuem(15, E); issuem(14, XL); issuem(15, 4);
            WAIT_BAR(15);
            issuem(15, 8); issuem(14, WL); issuem(15, WL);
            WAIT_BAR(14);
            WAIT_BAR(8);
            WAIT_BAR(6);
            WAIT_BAR(4);
            WAIT_BAR(0);
        }
    } else {

    const int wm = w >> 1, wn = w & 1;
    const int c16 = lane & 15, g4 = lane >> 4, q = c16 >> 2, p = lane & 3;
    lds_ptr xb[4], wb[4];
#pragma unroll
    for (int u = 0; u < 4; ++u) {
        xb[u] = lds + (u & 2) * K1_STAGE + (64 * wm + c16) * 128 + (((4 * (u & 1) + g4) ^ (c16 >> 1)) << 4);
        wb[u] = lds + (u & 2) * K1_STAGE + 16384 + (8 * g4 + q) * 128 +
                (((4 * wn + 2 * (u & 1) + (p >> 1)) ^ ((q >> 1) << 2) ^ ((g4 & 1) << 1)) << 4) + (p & 1) * 8;
        asm volatile("" : "+v"(xb[u]), "+v"(wb[u]));
    }
    auto rdx = [&](int n, int i) __attribute__((always_inline)) { return lds_rd128(xb[(n & 1) + (n >> 1 & 2)] + (n >> 1 & 1) * K1_STAGE + i * 2048); };
    auto rdw = [&](int n, int j, int hf) __attribute__((always_inline)) {
        return lds_tr(wb[(j & 1) + (n >> 1 & 2)] + (n >> 1 & 1) * K1_STAGE + (j >> 1) * 8192 + (n & 1) * 4096 + hf * 512);
    };
    f32x4 acc[4][6];
    auto mm = [&](int n, int i, int j, bf16x8 x, bf16x8 wv) __attribute__((always_inline)) {
        const bf16x8 a = j < 4 ? wv : x, c = j < 4 ? x : wv;
        if (n == 0) asm volatile("v_mfma_f32_16x16x32_bf16 %0, %1, %2, 0" : "=&v"(acc[i][j]) : "v"(a), "v"(c));
        else if (n < 31) asm volatile("v_mfma_f32_16x16x32_bf16 %0, %1, %2, %0" : "+v"(acc[i][j]) : "v"(a), "v"(c));
        else acc[i][j] = MFMA16(a, c, acc[i][j]);
        SBAR();
    };
    bf16x8 xf[2][4];
    s16x4 wf[4][2];
    const int T0 = tb * 4 + wm * 2;
    const int vl = g4 * 512 + c16 * 16, kl = g4 * 512 + q * 128 + p * 16;
    unsigned dk = (unsigned)WS_KV + (h * 64 + T0) * 4096 + vl;
    unsigned dkk = (unsigned)WS_KV + (h * 64 + T0) * 4096 + kl;
    lds_ptr lk = lds + w * 8192 + vl, lkk = lds + w * 8192 + kl, lq = lds + K2_Q + w * 4096 + vl;
    asm volatile("" : "+v"(dk), "+v"(dkk), "+v"(lk), "+v"(lkk), "+v"(lq));

    WAIT_BAR(5);
    xf[0][0] = rdx(0, 0); wf[0][0] = rdw(0, 0, 0); wf[0][1] = rdw(0, 0, 1);
#pragma unroll
    for (int i = 1; i < 4; ++i) xf[0][i] = rdx(0, i);
#pragma unroll
    for (int j = 2; j < 6; ++j) wf[j >> 1][j & 1] = rdw(0, j >> 1, j & 1);
    SBAR();
    constexpr int NJ = 9, JOB[NJ] = {13, 29, 14, 15, 45, 30, 31, 46, 47}, JBAR = 0xec;
    constexpr int KT0 = 16 - NJ / 3;
    bf16x8 jx[4][2];
    s16x4 jw[4][4];
    auto jrd = [&](auto gc, int part) __attribute__((always_inline)) {
        constexpr int g = decltype(gc)::value;
        if constexpr (g < 4 * NJ) {
            constexpr int pr = JOB[g >> 2] >> 4, n = (JOB[g >> 2] & 15) * 2 + ((g >> 1) & 1), sc = g & 1;
            if constexpr ((g & 3) == 0 && (JBAR >> (g >> 2) & 1)) if (part == 0) { RAW_BAR(); SBAR(); }
            if constexpr (pr < 2) {
                if (sc == 0 && !(part & 1)) jx[g & 3][part >> 1] = rdx(n, 2 * pr + (part >> 1));
                jw[g & 3][part] = rdw(n, 2 + 2 * sc + (part >> 1), part & 1);
            } else {
                if (!(part & 1)) jx[g & 3][part >> 1] = rdx(n, 2 * sc + (part >> 1));
                if (sc == 0) jw[g & 3][part] = rdw(n, part >> 1, part & 1);
            }
            SBAR();
        }
    };
#define IC(x) std::integral_constant<int, (x)>{}
    sfor(std::make_integer_sequence<int, 12 * KT0>{}, [&](auto gc) __attribute__((always_inline)) {
        constexpr int G = decltype(gc)::value, n = G / 6, j = G % 6, n3 = (G + 3) / 6, j3 = (G + 3) % 6;
        constexpr bool more = n3 < 2 * KT0;
        const bf16x8 wv = cat8(wf[G & 3][0], wf[G & 3][1]);
        mm(n, 0, j, xf[n & 1][0], wv);
        if constexpr (j == 3 && (n & 1)) {
            if constexpr (HBAR >> (n + 1) / 2 & 1) WAIT_BAR(0); else RAW_BAR();
            SBAR();
        }
        if constexpr (more && (j == 3 || j == 4)) { xf[~n & 1][2 * (j - 3)] = rdx(n + 1, 2 * (j - 3)); SBAR(); }
        if constexpr (!more && j > 3) jrd(IC(j - 4), 0);
        mm(n, 1, j, xf[n & 1][1], wv);
        if constexpr (more) { wf[(G + 3) & 3][0] = rdw(n3, j3, 0); SBAR(); }
        if constexpr (!more && j > 3) jrd(IC(j - 4), 1);
        mm(n, 2, j, xf[n & 1][2], wv);
        if constexpr (more) { wf[(G + 3) & 3][1] = rdw(n3, j3, 1); SBAR(); }
        if constexpr (!more && j > 3) jrd(IC(j - 4), 2);
        mm(n, 3, j, xf[n & 1][3], wv);
        if constexpr (more && (j == 3 || j == 4)) { xf[~n & 1][2 * (j - 3) + 1] = rdx(n + 1, 2 * (j - 3) + 1); SBAR(); }
        if constexpr (!more && j > 3) jrd(IC(j - 4), 3);
    });
    const float qscale = 0.17677669529663687f * 1.4426950408889634f;
    unsigned old;
#define ARRIVE(N, a) asm volatile("s_waitcnt vmcnt(%3)\n\ts_mov_b64 exec, 1\n\tds_add_rtn_u32 %0, %1, %2\n\ts_mov_b64 exec, -1" \
                                  : "=&v"(old) : "v"(a), "v"(1u), "n"(N) : "memory")
#define ANNOUNCE() do { unsigned o_; asm volatile("s_waitcnt lgkmcnt(0)\n\tv_readfirstlane_b32 %0, %1\n\ts_cmp_lg_u32 %0, 3\n\ts_cbranch_scc1 1f\n\t" \
        "s_mov_b64 exec, 1\n\tglobal_store_dword %2, %3, %4 sc1\n\ts_mov_b64 exec, -1\n1:" : "=&s"(o_) : "v"(old), "v"(4u), "v"(1u), "s"(cnt + b * 64) : "memory", "scc"); } while (0)
    auto sig0 = [&](int g) __attribute__((always_inline)) {
        if (g == SIG0) { ARRIVE(0, fl + C_KV0); SBAR(); }
        if (g == SIG0C) { ANNOUNCE(); SBAR(); }
    };
    u32x4 pk[4];
    auto jst = [&](int pr, int k) __attribute__((always_inline)) {
        const int i = k < 12 ? k / 3 : k - 12, hf = k % 3;
        const int o = i < 2 ? i * 64 : 2048 + (i - 2) * 256;
        if (k >= 12) { if (pr < 2) *(LAS u32x4*)((i < 2 ? lkk : lk) + pr * 4096 + o) = pk[i]; }
        else if (hf < 2) {
            const f32x4 a = pr == 2 ? acc[i][hf] * qscale : i < 2 ? acc[2 * pr + i][2 + hf] : acc[2 * pr + hf][2 + i];
            pk[i][2 * hf] = cvtpk(a[0], a[1]); pk[i][2 * hf + 1] = cvtpk(a[2], a[3]);
        } else if (pr < 2) __builtin_amdgcn_raw_buffer_store_b128(pk[i], rs, (int)(i < 2 ? dkk : dk) + o, pr * 4096, AUX_SC1);
        else *(LAS u32x4*)(lq + (i >> 1) * 2048 + (i & 1) * 256) = pk[i];
        SBAR();
    };
    sfor(std::make_integer_sequence<int, 4 * NJ>{}, [&](auto gc) __attribute__((always_inline)) {
        constexpr int g = decltype(gc)::value, pr = JOB[g >> 2] >> 4, sc = g & 1;
        constexpr int dn = g >= 3 && (JOB[(g >= 3 ? g - 3 : 0) >> 2] & 15) == 15 ? JOB[(g >= 3 ? g - 3 : 0) >> 2] >> 4 : -1;
        constexpr int gx = pr < 2 ? g - sc : g, gw = pr < 2 ? g : g - sc;
        sfor(std::make_integer_sequence<int, 4>{}, [&](auto mc) __attribute__((always_inline)) {
            constexpr int m = decltype(mc)::value, ii = pr < 2 && sc ? m & 1 : m >> 1, dd = pr < 2 && sc ? m >> 1 : m & 1;
            mm((JOB[g >> 2] & 15) * 2 + (g >> 1 & 1), (pr < 2 ? 2 * pr : 2 * sc) + ii, (pr < 2 ? 2 + 2 * sc : 0) + dd, jx[gx & 3][ii],
               cat8(jw[gw & 3][2 * dd], jw[gw & 3][2 * dd + 1]));
            if constexpr (m == 0) sig0(g);
            jrd(IC(g + 2), m);
            if constexpr (dn >= 0) jst(dn, ((g + 1) & 3) * 4 + m);
        });
    });
#pragma unroll
    for (int k = 4; k < 12; ++k) jst(2, k);
    lds_add(fl + F_A);
    }
    const __amdgpu_buffer_rsrc_t ro = __builtin_amdgcn_make_buffer_rsrc(out, 0, 8 << 20, 0x00020000);
    WODEF(lane)

    const int half = w >> 2;
    const int TA = tb * 4 + (cw >> 1) * 2 + half;
    bf16x8 sel;
#pragma unroll
    for (int i = 0; i < 8; ++i) sel[i] = (lane & 15) == (i & 4) ? (short)0x3f80 : (short)0;
    int sidx = (int)0xdddddddd;
#define LO8(x) __builtin_bit_cast(bfx16_t, __builtin_shufflevector(x, x, 0, 1, 2, 3, 4, 5, 6, 7))
#define HI8(x) __builtin_bit_cast(bfx16_t, __builtin_shufflevector(x, x, 8, 9, 10, 11, 12, 13, 14, 15))
#define SPV(v, p, o) __builtin_amdgcn_smfmac_f32_32x32x32_bf16(__builtin_bit_cast(bfx8_t, v), p, o, sidx, 0, 0)
#define SSUM(p, l) __builtin_amdgcn_smfmac_f32_16x16x64_bf16(__builtin_bit_cast(bfx8_t, sel), p, l, sidx, 0, 0)
    f32x16 oA = f32x16{}, oB = f32x16{};
    f32x4 lA = f32x4{}, lB = f32x4{};
    f32x16 negm, sA, sB;
    lds_ptr lp = lds + half * 16384 + (w & 1) * 8192 + lane * 16;
    bf16x8 k0[2], k1[2], v0[2], v1[2];
#define EX(X, i) X[i] = __builtin_amdgcn_exp2f(X[i])
    lds_ptr qpa = lds + K2_Q + cw * 4096 + half * 2048 + lane * 16, qpb = lds + K2_Q + cw * 4096 + (half ^ 1) * 2048 + lane * 16;
    asm volatile("" : "+v"(qpa), "+v"(qpb), "+v"(lp), "+v"(sel), "+v"(oA), "+v"(oB), "+v"(lA), "+v"(lB), "+v"(sidx));
    lds_wait_ge(fl + F_A, 4u);
    const bf16x8 qa0 = lds_rd128(qpa), qb0 = lds_rd128(qpb);
    const bf16x8 x0 = lds_rd128(lds + (w & 1) * 8192 + lane * 16), x1 = lds_rd128(lds + (w & 1) * 8192 + lane * 16 + 1024);
    const bf16x8 qa1 = lds_rd128(qpa + 1024), qb1 = lds_rd128(qpb + 1024);
    {
        k0[0] = lds_rd128(lp); k1[0] = lds_rd128(lp + 1024);
        v0[0] = lds_rd128(lp + 2048); v1[0] = lds_rd128(lp + 3072);
        sA = MFMA32(x0, qa0, f32x16{});
        sB = MFMA32(x0, qb0, f32x16{});
        sA = MFMA32(x1, qa1, sA);
        sB = MFMA32(x1, qb1, sB);
        float m = fmx(sA[0], sA[1]), m2 = fmx(sB[0], sB[1]);
#pragma unroll
        for (int i = 2; i < 16; i += 2) m = fmx(fmx(m, sA[i]), sA[i + 1]);
#pragma unroll
        for (int i = 2; i < 16; i += 2) m2 = fmx(fmx(m2, sB[i]), sB[i + 1]);
        m = fmx(m, m2);
        auto rr = __builtin_amdgcn_permlane32_swap(__float_as_uint(m), __float_as_uint(m), false, false);
        m = fmx(__uint_as_float(rr[0]), __uint_as_float(rr[1]));
#pragma unroll
        for (int i = 0; i < 16; ++i) negm[i] = -m;
        sA = MFMA32(k0[0], qa0, negm);
        sA = MFMA32(k1[0], qa1, sA);
        v1[1] = v1[0];
#pragma unroll
        for (int i = 8; i < 16; ++i) sB[i] = 0.f;
        asm volatile("" : "+v"(sB));
        SBAR();
        EX(sA, 0); EX(sA, 1);
    }
#define HALF(C, oC, lC, N, qx, qy, ka, kb, oP, lP, vprev, vcur0, PRE) do { \
        oP = SPV(vprev, HI8(N), oP); SBAR(); \
        PRE; EX(C, 2); EX(C, 3); EX(C, 4); SBAR(); \
        lP = SSUM(HI8(N), lP); SBAR(); \
        EX(C, 5); SBAR(); \
        N = MFMA32(ka, qx, negm); SBAR(); \
        EX(C, 6); EX(C, 7); EX(C, 8); SBAR(); \
        N = MFMA32(kb, qy, N); SBAR(); \
        EX(C, 9); EX(C, 10); EX(C, 11); SBAR(); \
        oC = SPV(vcur0, LO8(C), oC); SBAR(); \
        EX(C, 12); EX(C, 13); EX(C, 14); SBAR(); \
        lC = SSUM(LO8(C), lC); SBAR(); \
        EX(C, 15); EX(N, 0); EX(N, 1); SBAR(); \
    } while (0)
#define RD4(Y, nx) do { v1[Y] = lds_rd128((nx) + 3072); v0[Y] = lds_rd128((nx) + 2048); \
        k1[Y] = lds_rd128((nx) + 1024); k0[Y] = lds_rd128(nx); } while (0)
#define KT(X, nx, PRE0, PA, PB_) do { PRIO(PA); \
        HALF(sA, oA, lA, sB, qb0, qb1, k0[X], k1[X], oB, lB, v1[X ^ 1], v0[X], PRE0; RD4(X ^ 1, nx)); \
        if (PA != PB_) PRIO(PB_); \
        HALF(sB, oB, lB, sA, qa0, qa1, k0[X ^ 1], k1[X ^ 1], oA, lA, v1[X], v0[X], (void)0); \
    } while (0)
    SBAR();
    u32x4 w3[4];
    const char* s2 = wsrc(half ^ 1, 32 + (w & 3) * 4);
    const char* s3 = wsrc(half, 48 + (w & 3) * 4);
#pragma unroll 1
    for (int st = 0; st < 8; ++st) {
        KT(0, lp + 4096, (void)0, 1, 0);
        KT(1, lp + K2_SLOT, RAW_BAR(); SBAR(), 3, 2);
        KT(0, lp + K2_SLOT + 4096, (void)0, 1, 0);
        KT(1, lp, asm volatile("s_waitcnt vmcnt(0)\n\ts_barrier" ::: "memory"); SBAR(), 3, 2);
    }
    PRIO(3);
    oB = SPV(v1[1], HI8(sB), oB);
    SBAR();
    lB = SSUM(HI8(sB), lB);
    SBAR();
    {
        const lds_ptr xo = lds + K2_SLOT + w * 4096 + lane * 16;
        *(LAS float*)(lds + K2_LX + w * 256 + lane * 4) = lB[0];
#pragma unroll
        for (int i = 0; i < 4; ++i) *(LAS f32x4*)(xo + i * 1024) = f32x4{oB[4 * i], oB[4 * i + 1], oB[4 * i + 2], oB[4 * i + 3]};
        lds_add(fl + F_X + w * 4);
        lds_wait_ge(fl + F_X + (w ^ 4) * 4, 1u);
        const lds_ptr xi = lds + K2_SLOT + (w ^ 4) * 4096 + lane * 16;
        f32x4 t[4];
        const float lx = *(const LAS float*)(lds + K2_LX + (w ^ 4) * 256 + (lane & 31) * 4);
        { auto rr = __builtin_amdgcn_permlane32_swap(__float_as_uint(lA[0]), __float_as_uint(lA[0]), false, false); lA[0] = __uint_as_float(rr[0]); }
#pragma unroll
        for (int i = 0; i < 4; ++i) t[i] = *(const LAS f32x4*)(xi + i * 1024);
        SBAR();
        lA[0] += lx;
#pragma unroll
        for (int i = 0; i < 16; ++i) { float o = oA[i]; acc_vv(o, t[i >> 2][i & 3]); oA[i] = o; }
    }
    const float l = lA[0];
    u32x4 on0, on1;
    auto finish = [&](float ls) {
        float rl = __builtin_amdgcn_rcpf(ls);
        asm volatile("s_nop 1" : "+v"(rl));
        f32x16 o;
#pragma unroll
        for (int i = 0; i < 16; ++i) o[i] = mul_vv(oA[i], rl);
        on0 = nz4(pack8n(o, 0)); on1 = nz4(pack8n(o, 1));
    };
    finish(l);
    {
        float t0, t1;
        asm("v_mul_f32_e32 %0, 0, %2\n\tv_mul_f32_e32 %1, 0, %3\n\tv_fmac_f32_e32 %0, 0, %4\n\tv_fmac_f32_e32 %1, 0, %5\n\tv_fmac_f32_e32 %0, 0, %6\n\tv_fmac_f32_e32 %1, 0, %7\n\tv_fmac_f32_e32 %0, 0, %8\n\tv_fmac_f32_e32 %1, 0, %9\n\t"
            "v_fmac_f32_e32 %0, 0, %10\n\tv_fmac_f32_e32 %1, 0, %11\n\tv_fmac_f32_e32 %0, 0, %12\n\tv_fmac_f32_e32 %1, 0, %13\n\tv_fmac_f32_e32 %0, 0, %14\n\tv_fmac_f32_e32 %1, 0, %15\n\tv_fmac_f32_e32 %0, 0, %16\n\tv_fmac_f32_e32 %1, 0, %17"
            : "=&v"(t0), "=&v"(t1) : "v"(oA[0]), "v"(oA[1]), "v"(oA[2]), "v"(oA[3]), "v"(oA[4]), "v"(oA[5]), "v"(oA[6]), "v"(oA[7]), "v"(oA[8]), "v"(oA[9]), "v"(oA[10]), "v"(oA[11]), "v"(oA[12]), "v"(oA[13]), "v"(oA[14]), "v"(oA[15]));
        const bool ok = (l <= 1.0e30f) && (l >= 1.0e-18f) && (t0 + t1 == 0.f);
        if (__builtin_expect(__any(!ok), 0)) {
            float m = -INFINITY, ls = 0.f;
            oA = f32x16{};
#pragma unroll 1
            for (int t = 0; t < 64; ++t) {
                const unsigned g = (unsigned)WS_KV + (h * 64 + t) * 4096 + lane * 16;
                const bf16x8 a0 = PB(ld_sc1s(rs, g, 0)), a1 = PB(ld_sc1s(rs, g, 1024));
                const bf16x8 b0 = PB(ld_sc1s(rs, g, 2048)), b1 = PB(ld_sc1s(rs, g, 3072));
                f32x16 s = MFMA32(a0, qa0, f32x16{});
                s = MFMA32(a1, qa1, s);
                float tm = s[0];
#pragma unroll
                for (int i = 1; i < 16; ++i) tm = __builtin_fmaxf(tm, s[i]);
                auto rr = __builtin_amdgcn_permlane32_swap(__float_as_uint(tm), __float_as_uint(tm), false, false);
                tm = __builtin_fmaxf(__uint_as_float(rr[0]), __uint_as_float(rr[1]));
                const float mn = __builtin_fmaxf(m, tm);
                const float alpha = __builtin_amdgcn_exp2f(m - mn);
                m = mn;
                float ps = 0.f;
#pragma unroll
                for (int i = 0; i < 16; ++i) { s[i] = bfhi(__float_as_uint(__builtin_amdgcn_exp2f(s[i] - mn))); ps += s[i]; oA[i] *= alpha; }
                ls = ls * alpha + ps;
                oA = SPV(b0, LO8(s), oA);
                oA = SPV(b1, HI8(s), oA);
            }
            auto rr = __builtin_amdgcn_permlane32_swap(__float_as_uint(ls), __float_as_uint(ls), false, false);
            asm volatile("s_nop 7\n\ts_nop 7" : "+v"(oA));
            finish(__uint_as_float(rr[0]) + __uint_as_float(rr[1]));
        }
    }
    {
        const unsigned og = (unsigned)WS_O + (unsigned)(TA * 32 + h) * 2048u + lane * 16;
        st_sc1(on0, rs, og);
        st_sc1(on1, rs, og + 1024);
        lds_add(fl + C_S1);
        lds_wait_ge(fl + C_S1, 12u);
#pragma unroll
        for (int i = 0; i < 4; ++i)
            glds16(wvoff, s2 + i * 16384, lds0 + K2_SLOT + ((w ^ 4) * 4 + i) * 1024);
#pragma unroll
        for (int i = 0; i < 4; ++i) w3[i] = gld128(wvoff, s3 + i * 16384);
    }
    {
        const int half = w & 1;
        const int T3 = tt * 4 + (w >> 1);
        const unsigned og = (unsigned)WS_O + (unsigned)T3 * 65536u + half * 32768 + lane * 16;
        const int hh = lane >> 5;
        const int g = lane >> 4, q = (lane & 15) >> 2, p = lane & 3;
        int woff[2];
#pragma unroll
        for (int n = 0; n < 2; ++n) {
            const int c = 4 * (n ^ half) + 2 * (p & 1) + (g & 1);
            woff[n] = (4 * hh + q) * 128 + ((c ^ ((q >> 1) << 2)) << 4) + (p >> 1) * 8;
        }
        f32x16 acc[2] = {f32x16{}, f32x16{}};
        lds_ptr wl[3][2] = {{lds + half * 16384 + woff[0], lds + half * 16384 + woff[1]},
                            {lds + K2_SF + half * 16384 + woff[0], lds + K2_SF + half * 16384 + woff[1]},
                            {lds + K2_CV + half * 2 * CV_BYTES + woff[0], lds + K2_CV + half * 2 * CV_BYTES + woff[1]}};
        asm volatile("" : "+v"(wl[0][0]), "+v"(wl[0][1]), "+v"(wl[1][0]), "+v"(wl[1][1]), "+v"(wl[2][0]), "+v"(wl[2][1]));
#define MS(m) ((m) < 64 - 2 * TL ? (m) >> 1 : 32 - TL + ((m) - (64 - 2 * TL)) % TL)
#define MN(m) ((m) < 64 - 2 * TL ? (m) & 1 : (m) < 64 - TL)
        constexpr int WR = 2 * WD + 2;
        s16x4 wa[WR], wb[WR];
#define LDW(m) do { const int s_ = MS(m), f_ = s_ < 8 ? 1 : s_ < 24 ? 0 : 2; \
            const int o_ = f_ == 2 ? ((s_ >> 2) & 1) * CV_BYTES + (s_ & 3) * 2048 : (f_ ? 0 : ((s_ >> 3) - 1) * K2_SLOT) + (s_ & 7) * 2048; \
            wa[(m) % WR] = lds_tr(wl[f_][MN(m)] + o_); wb[(m) % WR] = lds_tr(wl[f_][MN(m)] + o_ + 1024); SBAR(); } while (0)
#pragma unroll
        for (int m = 0; m < 2 * WD; ++m) LDW(m);
        u32x4 of[OWIN];
#define LDO(i) do { of[(i) % OWIN] = ld_sc1s(rs, og, (((i) + krot) & 31) * 1024); SBAR(); } while (0)
        for (unsigned spins = 0; spins < SPIN_MAX; ++spins) {
#pragma unroll
            for (int i = 0; i < OW0; ++i) LDO(i);
            unsigned m = mx8(of[0], of[1]);
#pragma unroll
            for (int i = 2; i < OW0; ++i) m = mx4(m, of[i]);
            if (!missing(m)) break;
        }
#pragma unroll
        for (int i = OW0; i < OWIN; ++i) LDO(i);
        lds_add(fl + C_S2);
        if (w < 4) __builtin_amdgcn_s_setprio(0); else __builtin_amdgcn_s_setprio(1);
        SBAR();
        unsigned fv = 0;
#define FLAG_RD(a) do { fv = *(volatile LAS unsigned*)(a); SBAR(); } while (0)
#define FLAG_CHK(a, n) do { if (__builtin_expect((unsigned)__builtin_amdgcn_readfirstlane(fv) < (n), 0)) lds_wait_ge(a, n); CFENCE(); SBAR(); } while (0)
        lds_ptr xo = lds + K2_SF + w * 4096 + lane * 16, xi = lds + K2_SF + (w ^ 1) * 4096 + lane * 16;
        int bo = (T3 * 32 + (lane & 15)) * 4096 + 2048 + nt * 128 + half * 64 + g * 16;
        lds_ptr w3d = lds + K2_CV + (w >> 1) * CV_BYTES + (w & 1) * 4096 + lane * 16;
        asm volatile("" : "+v"(xo), "+v"(xi), "+v"(bo), "+v"(w3d));
        f32x4 xt[4];
        constexpr int M3 = 48 - 2 * WD;
        sfor(std::make_integer_sequence<int, 64>{}, [&](auto mc) __attribute__((always_inline)) {
            constexpr int m = decltype(mc)::value, s = MS(m), n = MN(m);
            constexpr bool first = m < 64 - 2 * TL ? n == 0 : n == 1;
            constexpr bool last = m < 64 - 2 * TL ? n == 1 : n == 0;
            acc[n] = MFMA32(cat8(wa[m % WR], wb[m % WR]), PB(of[s % OWIN]), acc[n]);
            SBAR();
            if constexpr (m == 32 - 2 * WD) FLAG_CHK(fl + C_S2, 8u);
            if constexpr (m == M3) FLAG_CHK(fl + C_S3, 8u);
            if constexpr (m + 2 * WD < 64) LDW(m + 2 * WD);
            if constexpr (first && (s & 1) && s + 1 >= OW0 && s + 1 < 32) {
                if (__builtin_expect(missing(mx8(of[(s + 1) % OWIN], of[(s + 2) % OWIN])), 0)) {
#pragma unroll 1
                    for (unsigned spins = 0; spins < SPIN_MAX; ++spins) {
                        __builtin_amdgcn_s_sleep(2);
                        LDO(s + 1); LDO(s + 2);
                        if (!missing(mx8(of[(s + 1) % OWIN], of[(s + 2) % OWIN]))) break;
                    }
                }
            }
            if constexpr (last && s + OWIN < 32) LDO(s + OWIN);
            if constexpr (m == 15 - 2 * WD) lds_add(fl + C_U0);
            if constexpr (m == 29 - 2 * WD) FLAG_RD(fl + C_S2);
            if constexpr (m == W3M - 3) FLAG_RD(fl + C_HF);
            if constexpr (m == W3M) FLAG_CHK(fl + C_HF, 4u);
            if constexpr (m >= W3M && m < W3M + 8 && ((m - W3M) & 1) == 0) {
                asm volatile("" : "+v"(w3[((m - W3M) >> 1) & 3]));
                *(LAS u32x4*)(w3d + (((m - W3M) >> 1) & 3) * 1024) = w3[((m - W3M) >> 1) & 3];
            }
            if constexpr (m == W3M + 6) lds_add(fl + C_S3);
            if constexpr (m == M3 - 3) FLAG_RD(fl + C_S3);
            if constexpr (m == 64 - TL - 3) FLAG_RD(fl + C_U0);
            if constexpr (m == 64 - TL) FLAG_CHK(fl + C_U0, 8u);
            if constexpr (m == 64 - TL + 1 || m == 64 - TL + 2) {
                constexpr int i = 2 * (m - (64 - TL + 1));
                *(LAS f32x4*)(xo + i * 1024) = f32x4{acc[1][4 * i], acc[1][4 * i + 1], acc[1][4 * i + 2], acc[1][4 * i + 3]};
                *(LAS f32x4*)(xo + i * 1024 + 1024) = f32x4{acc[1][4 * i + 4], acc[1][4 * i + 5], acc[1][4 * i + 6], acc[1][4 * i + 7]};
            }
            if constexpr (m == 64 - TL + 2) lds_add(fl + F_R + w * 4);
            if constexpr (m == XR_AT - 2) FLAG_RD(fl + F_R + (w ^ 1) * 4);
            if constexpr (m == XR_AT) {
                FLAG_CHK(fl + F_R + (w ^ 1) * 4, 1u);
#pragma unroll
                for (int i = 0; i < 4; ++i) xt[i] = *(const LAS f32x4*)(xi + i * 1024);
            }
            if constexpr (last && s + OWIN == 31) lds_add(fl + C_OL);
            SBAR();
        });
        asm volatile("s_nop 11" : "+v"(acc[0]));
#pragma unroll
        for (int i = 0; i < 16; ++i) { float o = xt[i >> 2][i & 3]; acc_vv(o, acc[0][i]); acc[1][i] = o; }
        u32x4 pa = pack8n(acc[1], 0), pb = pack8n(acc[1], 1);
#pragma unroll
        for (int i = 0; i < 4; ++i) {
            auto sw = __builtin_amdgcn_permlane16_swap(pa[i], pb[i], false, false);
            pa[i] = sw[0]; pb[i] = sw[1];
        }
        asm volatile("" :: "v"(acc[0]));
        __builtin_amdgcn_raw_buffer_store_b128(pa, ro, bo, 0, AUX_SC1);
        __builtin_amdgcn_raw_buffer_store_b128(pb, ro, bo, 65536, AUX_SC1);
    }
}

extern "C" void kernel_launch(void* const* d_in, const int* in_sizes, int n_in,
                              void* d_out, int out_size, void* d_ws, size_t ws_size,
                              hipStream_t stream) {
    const void* A_q = d_in[0];
    const void* A_k = d_in[1];
    const void* A_v = d_in[2];
    const void* A_shift = d_in[3];
    const void* A_ssmk = d_in[4];
    const void* A_D = d_in[5];
    const void* B_x = d_in[6];
    const void* B_wqkv = d_in[7];
    const void* B_wo = d_in[8];
    char* ws = (char*)d_ws;
    k_all<<<256, 768, 0, stream>>>((const char*)B_x, (const char*)B_wqkv, ws, (const char*)B_wo, (char*)d_out, (const char*)A_q,
                                   (const char*)A_k, (const char*)A_v, (const char*)A_shift, (const char*)A_ssmk,
                                   (const unsigned*)A_D);
}
```
